```python
import torch

def _softmax(x, axis=-1):
    return torch.softmax(x, dim=axis)
def _gelu(x):
    return torch.nn.functional.gelu(x, approximate="tanh")
def _silu(x):
    return torch.nn.functional.silu(x)
def _sigmoid(x):
    return torch.sigmoid(x)
def _layernorm(x, axis=-1, eps=1e-5):
    m = x.mean(dim=axis, keepdim=True)
    v = x.var(dim=axis, keepdim=True, unbiased=False)
    return (x - m) / torch.sqrt(v + eps)
def _scatter_add(tgt, idx, src, axis=0):
    return tgt.clone().index_add_(axis, idx, src)
def _segment_sum(src, seg, n):
    out = torch.zeros((n,) + src.shape[1:], dtype=src.dtype)
    return out.index_add_(0, seg, src)
def _pad(x, pw):
    flat = tuple(v for pair in reversed(pw) for v in pair)
    return torch.nn.functional.pad(x, flat)

kernel_name = 'sequential_preLN__compressed_kv_cla__dense_activation_silu__prompt'

def setup_inputs():

    _w = (lambda a: (lambda t: t.to(torch.bfloat16) if t.is_floating_point() else t)(torch.as_tensor(a)))
    def _A_setup():
        B, H, Sq, Sk, D, M = 1, 32, 2048, 2048, 128, 1024
        Dc = 4 * D
        return {
            "x": _w((lambda *s: torch.randn(*s, dtype=torch.bfloat16) * 0.05)(B, Sq, M)),
            "c_kv": _w((lambda *s: torch.randn(*s, dtype=torch.bfloat16) * 0.4)(B, Sk, Dc)),
            "w_q": _w((lambda *s: torch.randn(*s, dtype=torch.bfloat16) * 1.6)(M, H, D) * (M ** -0.5)),
            "w_uk": _w((lambda *s: torch.randn(*s, dtype=torch.bfloat16) * 1.6)(Dc, H, D) * (Dc ** -0.5)),
            "w_uv": _w((lambda *s: torch.randn(*s, dtype=torch.bfloat16) * 0.4)(Dc, H, D) * (Dc ** -0.5)),
            "w_o": _w((lambda *s: torch.randn(*s, dtype=torch.bfloat16) * 0.4)(H, D, M) * ((H * D) ** -0.5)),
        }
    _A_in = _A_setup()

    def _B_setup():
        B, S, D, F = 1, 2048, 1024, 3584
        return {
            "x": _w((lambda *s: torch.randn(*s, dtype=torch.bfloat16) * 0.05)(B, S, D)),
            "w1": _w((lambda *s: torch.randn(*s, dtype=torch.bfloat16) * 0.8)(D, F) * (D ** -0.5)),
            "w2": _w((lambda *s: torch.randn(*s, dtype=torch.bfloat16) * 0.1)(F, D) * (F ** -0.5)),
        }
    _B_in = _B_setup()

    return {
        "A_x": _A_in["x"],
        "A_c_kv": _A_in["c_kv"],
        "A_w_q": _A_in["w_q"],
        "A_w_uk": _A_in["w_uk"],
        "A_w_uv": _A_in["w_uv"],
        "A_w_o": _A_in["w_o"],
        "B_x": _B_in["x"],
        "B_w1": _B_in["w1"],
        "B_w2": _B_in["w2"],
    }

def reference(A_x, A_c_kv, A_w_q, A_w_uk, A_w_uv, A_w_o, B_x, B_w1, B_w2):
    def _A_ref(x, c_kv, w_q, w_uk, w_uv, w_o):
        B, Sq, M = x.shape[0], x.shape[1], x.shape[2]
        Sk = c_kv.shape[1]
        H, D = w_q.shape[1], w_q.shape[2]
        q = torch.einsum("bqm,mhd->bhqd", x, w_q)
        k = torch.einsum("bkc,chd->bhkd", c_kv, w_uk)
        v = torch.einsum("bkc,chd->bhkd", c_kv, w_uv)
        scores = torch.einsum("bhqd,bhkd->bhqk", q, k) * (D ** -0.5)
        off = Sk - Sq
        qi = torch.arange(Sq)[:, None]
        kj = torch.arange(Sk)[None, :]
        cmask = kj <= (qi + off)
        scores = torch.where(cmask[None, None], scores, -1e9)
        p = _softmax(scores, axis=-1)
        ctx = torch.einsum("bhqk,bhkd->bhqd", p, v)
        return torch.einsum("bhqd,hdm->bqm", ctx, w_o)

    def _B_ref(x, w1, w2):
        h = torch.einsum("bsd,df->bsf", x, w1)
        h = _silu(h)
        out = torch.einsum("bsf,fd->bsd", h, w2)
        return out

    x = A_x
    h = x + _A_ref(_layernorm(x, axis=-1), A_c_kv, A_w_q, A_w_uk, A_w_uv, A_w_o)
    h2 = h + _B_ref(_layernorm(h, axis=-1), B_w1, B_w2)
    return h2
```

```cpp
#include <hip/hip_runtime.h>
#include <hip/hip_bf16.h>
typedef unsigned short u16;
typedef short bf16x8 __attribute__((ext_vector_type(8)));
typedef float f32x4 __attribute__((ext_vector_type(4)));
__device__ inline float bf2f(u16 b){ return __uint_as_float(((unsigned)b)<<16); }
__device__ inline u16 f2bf(float f){ unsigned u=__float_as_uint(f); u += 0x7FFFu + ((u>>16)&1u); return (u16)(u>>16); }

__global__ void ln_kernel(const u16* x, u16* y, int n){
  __shared__ float red[8];
  int row=blockIdx.x, t=threadIdx.x;
  float v[4]; float s=0;
  for(int i=0;i<4;i++){ v[i]=bf2f(x[(size_t)row*n+t*4+i]); s+=v[i]; }
  for(int o=32;o>=1;o>>=1) s+=__shfl_xor(s,o);
  if((t&63)==0) red[t>>6]=s; __syncthreads();
  float m=(red[0]+red[1]+red[2]+red[3])/n; __syncthreads();
  float q=0; for(int i=0;i<4;i++){ float d=v[i]-m; q+=d*d; }
  for(int o=32;o>=1;o>>=1) q+=__shfl_xor(q,o);
  if((t&63)==0) red[t>>6]=q; __syncthreads();
  float var=(red[0]+red[1]+red[2]+red[3])/n;
  float r=1.0f/sqrtf(var+1e-5f);
  for(int i=0;i<4;i++) y[(size_t)row*n+t*4+i]=f2bf((v[i]-m)*r);
}
template<int MODE>
__global__ __launch_bounds__(256) void gemm_kernel(const u16* A,const u16* B,u16* C,const u16* res,int M,int N,int K){
  __shared__ u16 As[64][40]; __shared__ u16 Bs[64][40];
  int t=threadIdx.x, w=t>>6, l=t&63, fr=l&15, fq=l>>4;
  int m0=blockIdx.y*64, n0=blockIdx.x*64;
  f32x4 acc[4]={};
  for(int k0=0;k0<K;k0+=32){
    { int r=t>>2, c=(t&3)*8; *(bf16x8*)&As[r][c]=*(const bf16x8*)&A[(size_t)(m0+r)*K+k0+c]; }
    { int kk=t>>3, c=(t&7)*8; bf16x8 v=*(const bf16x8*)&B[(size_t)(k0+kk)*N+n0+c]; for(int j=0;j<8;j++) Bs[c+j][kk]=v[j]; }
    __syncthreads();
    bf16x8 a=*(bf16x8*)&As[w*16+fr][fq*8];
    for(int n=0;n<4;n++){ bf16x8 b=*(bf16x8*)&Bs[n*16+fr][fq*8]; acc[n]=__builtin_amdgcn_mfma_f32_16x16x32_bf16(a,b,acc[n],0,0,0);}
    __syncthreads();
  }
  for(int n=0;n<4;n++) for(int j=0;j<4;j++){ size_t o=(size_t)(m0+w*16+fq*4+j)*N+n0+n*16+fr; float v=acc[n][j];
    if(MODE==1) v=v/(1.f+__expf(-v)); if(MODE==2) v+=bf2f(res[o]); C[o]=f2bf(v); }
}
__global__ __launch_bounds__(256) void attn_kernel(const u16* Q,const u16* Kk,const u16* V,u16* O){
  __shared__ float S[2048]; __shared__ float q[128]; __shared__ float red[4]; __shared__ float part[2][128];
  int h=blockIdx.y, t=threadIdx.x;
  for(int rr=0;rr<8;rr++){
    int row=blockIdx.x*8+rr;
    if(t<128) q[t]=bf2f(Q[(size_t)row*4096+h*128+t]); __syncthreads();
    float mx=-1e30f;
    for(int k=t;k<=row;k+=256){ const u16* kp=Kk+(size_t)k*4096+h*128; float s=0; for(int d=0;d<128;d+=8){ bf16x8 v=*(const bf16x8*)(kp+d); for(int j=0;j<8;j++) s+=q[d+j]*bf2f(v[j]); } s*=0.08838834764831845f; S[k]=s; mx=fmaxf(mx,s);}
    for(int o=32;o>=1;o>>=1) mx=fmaxf(mx,__shfl_xor(mx,o));
    if((t&63)==0) red[t>>6]=mx; __syncthreads(); mx=fmaxf(fmaxf(red[0],red[1]),fmaxf(red[2],red[3])); __syncthreads();
    float sum=0; for(int k=t;k<=row;k+=256){ float p=__expf(S[k]-mx); S[k]=p; sum+=p; }
    for(int o=32;o>=1;o>>=1) sum+=__shfl_xor(sum,o);
    if((t&63)==0) red[t>>6]=sum; __syncthreads(); sum=red[0]+red[1]+red[2]+red[3];
    int d=t&127, hf=t>>7; float a=0; for(int k=hf;k<=row;k+=2) a+=S[k]*bf2f(V[(size_t)k*4096+h*128+d]);
    part[hf][d]=a; __syncthreads();
    if(t<128) O[(size_t)row*4096+h*128+t]=f2bf((part[0][t]+part[1][t])/sum);
    __syncthreads();
  }
}
extern "C" void kernel_launch(void* const* d_in, const int* in_sizes, int n_in, void* d_out, int out_size, void* d_ws, size_t ws_size, hipStream_t stream){
  const u16* x=(const u16*)d_in[0]; const u16* ckv=(const u16*)d_in[1]; const u16* wq=(const u16*)d_in[2]; const u16* wuk=(const u16*)d_in[3];
  const u16* wuv=(const u16*)d_in[4]; const u16* wo=(const u16*)d_in[5]; const u16* w1=(const u16*)d_in[7]; const u16* w2=(const u16*)d_in[8];
  u16* ws=(u16*)d_ws; size_t MB=1<<20;
  u16* xn=ws; u16* Q=ws+4*MB; u16* K=ws+16*MB; u16* V=ws+28*MB; u16* ctx=ws+40*MB; u16* h=ws+52*MB; u16* hn=ws+56*MB; u16* h1=ws+60*MB;
  ln_kernel<<<2048,256,0,stream>>>(x,xn,1024);
  gemm_kernel<0><<<dim3(64,32),256,0,stream>>>(xn,wq,Q,nullptr,2048,4096,1024);
  gemm_kernel<0><<<dim3(64,32),256,0,stream>>>(ckv,wuk,K,nullptr,2048,4096,512);
  gemm_kernel<0><<<dim3(64,32),256,0,stream>>>(ckv,wuv,V,nullptr,2048,4096,512);
  attn_kernel<<<dim3(256,32),256,0,stream>>>(Q,K,V,ctx);
  gemm_kernel<2><<<dim3(16,32),256,0,stream>>>(ctx,wo,h,x,2048,1024,4096);
  ln_kernel<<<2048,256,0,stream>>>(h,hn,1024);
  gemm_kernel<1><<<dim3(56,32),256,0,stream>>>(hn,w1,h1,nullptr,2048,3584,1024);
  gemm_kernel<2><<<dim3(16,32),256,0,stream>>>(h1,w2,(u16*)d_out,h,2048,1024,3584);
}
```

```cpp
#include <hip/hip_runtime.h>

extern "C" __global__ __launch_bounds__(256, 1) void k_attn(const void* p0, const void* p1, const void* p2, const void* p3, const void* p4, const void* p5, const void* p6, const void* p7, const void* p8, const void* p9) {
  __shared__ __attribute__((aligned(16))) char lds[163840];
  asm volatile(
    "s_mov_b32 s30, %1\n"
    "v_mov_b32 v0, %2\n"
    "s_load_dwordx16 s[8:23], %0, 0\n"
    "s_load_dwordx4 s[24:27], %0, 0x40\n"
    "v_lshrrev_b32 v26, 6, v0\n"
    "v_and_b32 v1, 63, v0\n"
    "v_readfirstlane_b32 s78, v26\n"
    "v_readfirstlane_b32 s100, v26\n"
    "s_and_b32 s81, s30, 7\n"
    "s_lshr_b32 s82, s30, 3\n"
    "s_lshl_b32 s79, s81, 2\n"
    "s_lshr_b32 s83, s82, 3\n"
    "s_add_u32 s79, s79, s83\n"
    "s_and_b32 s80, s82, 7\n"
    "s_getpc_b64 s[64:65]\n s_add_u32 s64, s64, k_attn@rel32@lo+4\n s_addc_u32 s65, s65, k_attn@rel32@hi+12\n"
    "s_mov_b32 s66, k_attn_code_end-k_attn\n"
    "s_mov_b32 s67, 0x20000\n"
    "v_lshlrev_b32 v30, 7, v1\n"
    "s_cmp_eq_u32 s78, 0\n"
    "s_cselect_b32 s66, s66, 0\n"
    "buffer_load_dword v0, v30, s[64:67], 0 offen\n"
    "v_and_b32 v26, 7, v1\n"
    "v_lshrrev_b32 v27, 3, v1\n"
    "v_lshlrev_b32 v96, 4, v26\n"
    "v_lshl_add_u32 v96, v27, 8, v96\n"
    "v_lshlrev_b32 v97, 5, v26\n"
    "v_lshrrev_b32 v26, 4, v1\n"
    "v_bfe_u32 v27, v1, 1, 3\n"
    "v_and_b32 v28, 1, v1\n"
    "v_lshlrev_b32 v28, 4, v28\n"
    "v_lshl_add_u32 v28, v26, 13, v28\n"
    "v_xor_b32 v27, v27, v26\n"
    "v_xor_b32 v29, 4, v27\n"
    "s_lshl_b32 s81, s79, 8\n"
    "v_add_u32 v28, s81, v28\n"
    "v_lshl_add_u32 v8, v27, 5, v28\n"
    "v_lshl_add_u32 v9, v29, 5, v28\n"
    "v_add_u32 v10, 0x1f800, v8\n"
    "v_add_u32 v11, 0x2f400, v9\n"
    "v_add_u32 v9, 0xfc00, v9\n"
    "s_lshr_b32 s73, s78, 1\n"
    "s_lshl_b32 s73, s73, 3\n"
    "s_and_b32 s81, s78, 1\n"
    "s_add_u32 s73, s73, s81\n"
    "s_lshl_b32 s73, s73, 15\n"
    "s_mov_b32 s86, 0x8000\n"
    "s_lshl_b32 s84, s78, 12\n"
    "s_add_u32 s85, s84, 0x4000\n"
    "s_mov_b32 s74, 0\n"
    "s_mov_b32 s72, 0\n"
    "s_and_b32 s81, s30, 3\n"
    "s_lshl_b32 s72, s81, 8\n"
    "s_add_u32 s86, s72, 0x8000\n"
    "s_lshl_b32 s81, s72, 12\n"
    "s_add_u32 s73, s73, s81\n"
    "s_lshl_b32 s81, s80, 4\n"
    "s_lshl_b32 s82, s78, 2\n"
    "s_add_u32 s81, s81, s82\n"
    "s_lshl_b32 s81, s81, 14\n"
    "v_lshlrev_b32 v24, 4, v1\n"
    "v_lshrrev_b32 v26, 2, v1\n"
    "v_and_b32 v27, 48, v24\n"
    "v_lshl_add_u32 v26, v26, 10, v27\n"
    "v_add_u32 v2, s81, v26\n"
    "v_add_u32 v3, 0x8000, v2\n"
    "v_lshrrev_b32 v26, 4, v1\n"
    "v_xor_b32 v26, v26, v1\n"
    "v_and_b32 v26, 3, v26\n"
    "v_lshrrev_b32 v27, 2, v1\n"
    "v_lshlrev_b32 v27, 10, v27\n"
    "v_lshl_add_u32 v26, v26, 4, v27\n"
    "s_add_u32 s81, s81, 0x1000\n"
    "v_add_u32 v2, s81, v26\n"
    "v_add_u32 v3, 0xfffffc40, v2\n"
    "v_add_u32 v99, 0x3800, v2\n"
    "v_add_u32 v100, 0x3440, v2\n"
    "s_waitcnt lgkmcnt(0)\n"
    "s_mov_b64 s[32:33], s[8:9]\n"
    "s_mov_b64 s[34:35], s[10:11]\n"
    "s_mov_b64 s[36:37], s[12:13]\n"
    "s_mov_b64 s[38:39], s[14:15]\n"
    "s_mov_b32 s48, s36\n"
    "s_mov_b32 s49, s37\n"
    "s_mov_b32 s50, 0x400000\n"
    "s_mov_b32 s51, 0x20000\n"
    "s_mov_b32 s52, s38\n"
    "s_mov_b32 s53, s39\n"
    "s_mov_b32 s54, 0x400000\n"
    "s_mov_b32 s55, 0x20000\n"
    "s_mov_b64 s[40:41], s[26:27]\n"
    "s_mov_b32 s42, 0x201000\n"
    "s_mov_b32 s43, 0x20000\n"
    "s_sub_u32 s40, s40, 0x1000\n"
    "s_subb_u32 s41, s41, 0\n"
    "s_load_dwordx2 s[94:95], s[32:33], 8192\n"
    "s_add_u32 m0, s84, 0x20000\n"
    "s_nop 0\n"
    "buffer_load_dwordx4 v2, s[40:43], s72 offen lds\n"
    "buffer_load_dwordx4 v3, s[40:43], s72 offen offset:1024 lds\n"
    "buffer_load_dwordx4 v99, s[40:43], s72 offen offset:2048 lds\n"
    "buffer_load_dwordx4 v100, s[40:43], s72 offen offset:3072 lds\n"
    "s_add_u32 m0, s85, 0x20000\n"
    "s_nop 0\n"
    "buffer_load_dwordx4 v2, s[40:43], s86 offen lds\n"
    "buffer_load_dwordx4 v3, s[40:43], s86 offen offset:1024 lds\n"
    "buffer_load_dwordx4 v99, s[40:43], s86 offen offset:2048 lds\n"
    "buffer_load_dwordx4 v100, s[40:43], s86 offen offset:3072 lds\n"
    "s_add_u32 s72, s72, 0x80\n"
    "s_and_b32 s72, s72, 0x3ff\n"
    "s_add_u32 s86, s72, 0x8000\n"
    "s_add_u32 m0, s74, s84\n"
    "s_nop 0\n"
    "buffer_load_dwordx4 v8, s[48:51], s73 offen lds\n"
    "buffer_load_dwordx4 v9, s[48:51], s73 offen offset:1024 lds\n"
    "buffer_load_dwordx4 v10, s[48:51], s73 offen offset:2048 lds\n"
    "buffer_load_dwordx4 v11, s[48:51], s73 offen offset:3072 lds\n"
    "s_add_u32 m0, s74, s85\n"
    "s_nop 0\n"
    "buffer_load_dwordx4 v8, s[52:55], s73 offen lds\n"
    "buffer_load_dwordx4 v9, s[52:55], s73 offen offset:1024 lds\n"
    "buffer_load_dwordx4 v10, s[52:55], s73 offen offset:2048 lds\n"
    "buffer_load_dwordx4 v11, s[52:55], s73 offen offset:3072 lds\n"
    "s_add_u32 s73, s73, 0x80000\n"
    "s_and_b32 s73, s73, 0x3fffff\n"
    "s_add_u32 s74, s74, 0x8000\n"
    "s_cmp_lt_u32 s74, 0x28000\n"
    "s_cselect_b32 s74, s74, 0\n"
    "s_getpc_b64 s[64:65]\n s_add_u32 s64, s64, k_attn@rel32@lo+4\n s_addc_u32 s65, s65, k_attn@rel32@hi+12\n"
    "s_mov_b32 s66, k_attn_code_end-k_attn\n"
    "s_mov_b32 s67, 0x20000\n"
    "s_lshr_b32 s82, s30, 3\n"
    "s_lshl_b32 s82, s82, 2\n"
    "s_add_u32 s82, s82, s78\n"
    "s_lshl_b32 s82, s82, 7\n"
    "v_lshlrev_b32 v30, 14, v1\n"
    "s_mov_b64 exec, 15\n buffer_load_dword v0, v30, s[64:67], s82 offen\n s_mov_b64 exec, -1\n"
    "s_add_u32 s44, s32, 0x100000\n"
    "s_addc_u32 s45, s33, 0\n"
    "s_mov_b32 s46, 0x400000\n"
    "s_mov_b32 s47, 0x20000\n"
    "s_mov_b32 s68, s34\n"
    "s_mov_b32 s69, s35\n"
    "s_mov_b32 s70, 0x800000\n"
    "s_mov_b32 s71, 0x20000\n"
    "s_add_u32 s60, s32, 0x2c00000\n"
    "s_addc_u32 s61, s33, 0\n"
    "s_mov_b32 s62, 0x1000000\n"
    "s_mov_b32 s63, 0x20000\n"
    "s_mov_b32 s64, s32\n"
    "s_mov_b32 s65, s33\n"
    "s_mov_b32 s66, 0x20000\n"
    "s_mov_b32 s67, 0x20000\n"
    "s_lshl_b32 s81, s79, 20\n"
    "s_add_u32 s81, s81, 0xc00000\n"
    "s_add_u32 s56, s32, s81\n"
    "s_addc_u32 s57, s33, 0\n"
    "s_mov_b32 s58, 0x100000\n"
    "s_mov_b32 s59, 0x20000\n"
    "s_lshl_b32 s89, s80, 2\n"
    "s_add_u32 s89, s89, s78\n"
    "s_lshl_b32 s89, s89, 15\n"
    "s_lshl_b32 s81, s80, 3\n"
    "s_sub_u32 s82, 0x78, s81\n"
    "s_sub_u32 s83, 7, s78\n"
    "s_add_u32 s87, s81, s78\n"
    "s_lshl_b32 s87, s87, 11\n"
    "v_add_u32 v4, s87, v24\n"
    "s_add_u32 s87, s81, s83\n"
    "s_lshl_b32 s87, s87, 11\n"
    "v_add_u32 v5, s87, v24\n"
    "s_add_u32 s87, s82, s78\n"
    "s_lshl_b32 s87, s87, 11\n"
    "v_add_u32 v6, s87, v24\n"
    "s_add_u32 s87, s82, s83\n"
    "s_lshl_b32 s87, s87, 11\n"
    "v_add_u32 v7, s87, v24\n"
    "s_lshl_b32 s81, s78, 12\n"
    "v_add_u32 v21, s81, v24\n"
    "v_add_u32 v20, 0x8000, v21\n"
    "v_add_u32 v21, 0x4000, v21\n"
    "v_lshrrev_b32 v26, 2, v1\n"
    "v_and_b32 v27, 7, v26\n"
    "v_and_b32 v28, 3, v1\n"
    "v_lshlrev_b32 v28, 3, v28\n"
    "v_lshl_add_u32 v28, v26, 8, v28\n"
    "v_xor_b32 v29, 0, v27\n"
    "v_lshl_add_u32 v12, v29, 5, v28\n"
    "v_xor_b32 v29, 1, v27\n"
    "v_lshl_add_u32 v13, v29, 5, v28\n"
    "v_xor_b32 v29, 2, v27\n"
    "v_lshl_add_u32 v14, v29, 5, v28\n"
    "v_xor_b32 v29, 3, v27\n"
    "v_lshl_add_u32 v15, v29, 5, v28\n"
    "v_xor_b32 v29, 4, v27\n"
    "v_lshl_add_u32 v16, v29, 5, v28\n"
    "v_xor_b32 v29, 5, v27\n"
    "v_lshl_add_u32 v17, v29, 5, v28\n"
    "v_xor_b32 v29, 6, v27\n"
    "v_lshl_add_u32 v18, v29, 5, v28\n"
    "v_xor_b32 v29, 7, v27\n"
    "v_lshl_add_u32 v19, v29, 5, v28\n"
    "s_mov_b32 s75, 0\n"
    "s_mov_b32 s77, 0xfffe0000\n"
    "s_mov_b32 s88, 0\n"
    "v_and_b32 v26, 15, v1\n"
    "v_lshrrev_b32 v27, 4, v1\n"
    "v_lshl_add_u32 v22, v26, 2, v27\n"
    "v_lshlrev_b32 v22, 2, v22\n"
    "v_lshrrev_b32 v28, 2, v26\n"
    "v_xor_b32 v27, v27, v28\n"
    "v_lshlrev_b32 v27, 4, v27\n"
    "v_lshl_add_u32 v22, v26, 6, v27\n"
    "s_waitcnt lgkmcnt(0)\n"
    "s_add_u32 s94, s94, 0x7f4a7c15\n"
    "s_addc_u32 s95, s95, 0x9e3779b9\n"
    "s_lshl_b32 s81, s78, 3\n"
    "s_add_u32 s81, s81, 0x7c00\n"
    "v_add_u32 v98, s81, v97\n"
    "v_add_u32 v97, 0x7c00, v97\n"
    "s_add_u32 s91, s84, 0x20000\n"
    "v_add_u32 v101, s91, v22\n"
    "s_waitcnt vmcnt(13)\n"
    "ds_read_b128 a[128:131], v101\n"
    "ds_read_b128 a[132:135], v101 offset:1024\n"
    "ds_read_b128 a[136:139], v101 offset:2048\n"
    "ds_read_b128 a[140:143], v101 offset:3072\n"
    "s_add_u32 m0, s74, s84\n"
    "s_nop 0\n"
    "buffer_load_dwordx4 v8, s[48:51], s73 offen lds\n"
    "buffer_load_dwordx4 v9, s[48:51], s73 offen offset:1024 lds\n"
    "buffer_load_dwordx4 v10, s[48:51], s73 offen offset:2048 lds\n"
    "buffer_load_dwordx4 v11, s[48:51], s73 offen offset:3072 lds\n"
    "s_add_u32 m0, s74, s85\n"
    "s_nop 0\n"
    "buffer_load_dwordx4 v8, s[52:55], s73 offen lds\n"
    "buffer_load_dwordx4 v9, s[52:55], s73 offen offset:1024 lds\n"
    "buffer_load_dwordx4 v10, s[52:55], s73 offen offset:2048 lds\n"
    "buffer_load_dwordx4 v11, s[52:55], s73 offen offset:3072 lds\n"
    "s_add_u32 s73, s73, 0x80000\n"
    "s_and_b32 s73, s73, 0x3fffff\n"
    "s_add_u32 s74, s74, 0x8000\n"
    "s_cmp_lt_u32 s74, 0x28000\n"
    "s_cselect_b32 s74, s74, 0\n"
    "s_waitcnt vmcnt(17)\n"
    "ds_read_b128 a[144:147], v101 offset:16384\n"
    "ds_read_b128 a[148:151], v101 offset:17408\n"
    "ds_read_b128 a[152:155], v101 offset:18432\n"
    "ds_read_b128 a[156:159], v101 offset:19456\n"
    "s_add_u32 m0, s84, 0x10000\n"
    "s_nop 0\n"
    "buffer_load_dwordx4 v2, s[40:43], s72 offen lds\n"
    "buffer_load_dwordx4 v3, s[40:43], s72 offen offset:1024 lds\n"
    "buffer_load_dwordx4 v99, s[40:43], s72 offen offset:2048 lds\n"
    "buffer_load_dwordx4 v100, s[40:43], s72 offen offset:3072 lds\n"
    "s_add_u32 m0, s85, 0x10000\n"
    "s_nop 0\n"
    "buffer_load_dwordx4 v2, s[40:43], s86 offen lds\n"
    "buffer_load_dwordx4 v3, s[40:43], s86 offen offset:1024 lds\n"
    "buffer_load_dwordx4 v99, s[40:43], s86 offen offset:2048 lds\n"
    "buffer_load_dwordx4 v100, s[40:43], s86 offen offset:3072 lds\n"
    "s_add_u32 s72, s72, 0x80\n"
    "s_and_b32 s72, s72, 0x3ff\n"
    "s_add_u32 s86, s72, 0x8000\n"
    "s_lshr_b32 s81, s30, 3\n"
    "s_lshl_b32 s81, s81, 2\n"
    "s_add_u32 s81, s81, s78\n"
    "s_cmp_lt_u32 s81, 64\n"
    "s_cselect_b32 s96, s48, s52\n"
    "s_cselect_b32 s97, s49, s53\n"
    "s_mov_b32 s99, 0x20000\n"
    "s_and_b32 s81, s81, 63\n"
    "s_lshr_b32 s82, s72, 4\n"
    "s_sub_u32 s82, s81, s82\n"
    "s_add_u32 s82, s82, 16\n"
    "s_and_b32 s82, s82, 63\n"
    "s_cmp_lt_u32 s82, 24\n"
    "s_cselect_b32 s98, 0, s50\n"
    "v_lshrrev_b32 v30, 3, v1\n"
    "v_and_b32 v29, 7, v1\n"
    "v_lshlrev_b32 v29, 7, v29\n"
    "v_lshl_add_u32 v30, v30, 13, v29\n"
    "s_lshl_b32 s81, s81, 16\n"
    "s_and_b32 s82, s30, 7\n"
    "s_lshl_b32 s82, s82, 10\n"
    "s_add_u32 s81, s81, s82\n"
    "buffer_load_dword v0, v30, s[96:99], s81 offen\n"
    "s_waitcnt vmcnt(18) lgkmcnt(1)\n"
    "s_barrier\n"
    "ds_read_b64_tr_b16 a[224:225], v12\n"
    "ds_read_b64_tr_b16 a[226:227], v12 offset:4096\n"
    "ds_read_b64_tr_b16 a[228:229], v12 offset:8192\n"
    "ds_read_b64_tr_b16 a[230:231], v12 offset:12288\n"
    "ds_read_b64_tr_b16 a[232:233], v13\n"
    "ds_read_b64_tr_b16 a[234:235], v13 offset:4096\n"
    "ds_read_b64_tr_b16 a[236:237], v13 offset:8192\n"
    "ds_read_b64_tr_b16 a[238:239], v13 offset:12288\n"
    "s_waitcnt lgkmcnt(4)\n"
    "v_mfma_f32_16x16x32_bf16 v[128:131], a[224:227], a[128:131], 0\n"
    "s_add_u32 m0, s84, 0x18000\n"
    "ds_read_b64_tr_b16 a[240:241], v14\n"
    "v_mfma_f32_16x16x32_bf16 v[128:131], a[228:231], a[132:135], v[128:131]\n"
    "buffer_load_dwordx4 v2, s[40:43], s72 offen lds\n"
    "v_mfma_f32_16x16x32_bf16 v[160:163], a[224:227], a[136:139], 0\n"
    "s_add_u32 s91, s84, 0x10000\n"
    "ds_read_b64_tr_b16 a[242:243], v14 offset:4096\n"
    "v_mfma_f32_16x16x32_bf16 v[160:163], a[228:231], a[140:143], v[160:163]\n"
    "v_add_u32 v101, s91, v22\n"
    "v_mfma_f32_16x16x32_bf16 v[192:195], a[224:227], a[144:147], 0\n"
    "s_waitcnt vmcnt(6)\n"
    "ds_read_b64_tr_b16 a[244:245], v14 offset:8192\n"
    "v_mfma_f32_16x16x32_bf16 v[192:195], a[228:231], a[148:151], v[192:195]\n"
    "v_mfma_f32_16x16x32_bf16 v[224:227], a[224:227], a[152:155], 0\n"
    "ds_read_b64_tr_b16 a[246:247], v14 offset:12288\n"
    "v_mfma_f32_16x16x32_bf16 v[224:227], a[228:231], a[156:159], v[224:227]\n"
    "s_waitcnt lgkmcnt(4)\n"
    "ds_read_b128 a[160:163], v101\n"
    "v_mfma_f32_16x16x32_bf16 v[132:135], a[232:235], a[128:131], 0\n"
    "ds_read_b64_tr_b16 a[248:249], v15\n"
    "v_mfma_f32_16x16x32_bf16 v[132:135], a[236:239], a[132:135], v[132:135]\n"
    "buffer_load_dwordx4 v3, s[40:43], s72 offen offset:1024 lds\n"
    "v_mfma_f32_16x16x32_bf16 v[164:167], a[232:235], a[136:139], 0\n"
    "ds_read_b64_tr_b16 a[250:251], v15 offset:4096\n"
    "v_mfma_f32_16x16x32_bf16 v[164:167], a[236:239], a[140:143], v[164:167]\n"
    "ds_read_b128 a[164:167], v101 offset:1024\n"
    "v_mfma_f32_16x16x32_bf16 v[196:199], a[232:235], a[144:147], 0\n"
    "ds_read_b64_tr_b16 a[252:253], v15 offset:8192\n"
    "v_mfma_f32_16x16x32_bf16 v[196:199], a[236:239], a[148:151], v[196:199]\n"
    "ds_read_b128 a[168:171], v101 offset:2048\n"
    "v_mfma_f32_16x16x32_bf16 v[228:231], a[232:235], a[152:155], 0\n"
    "ds_read_b64_tr_b16 a[254:255], v15 offset:12288\n"
    "v_mfma_f32_16x16x32_bf16 v[228:231], a[236:239], a[156:159], v[228:231]\n"
    "s_waitcnt lgkmcnt(7)\n"
    "ds_read_b128 a[172:175], v101 offset:3072\n"
    "v_mfma_f32_16x16x32_bf16 v[136:139], a[240:243], a[128:131], 0\n"
    "ds_read_b64_tr_b16 a[224:225], v16\n"
    "v_mfma_f32_16x16x32_bf16 v[136:139], a[244:247], a[132:135], v[136:139]\n"
    "buffer_load_dwordx4 v99, s[40:43], s72 offen offset:2048 lds\n"
    "v_mfma_f32_16x16x32_bf16 v[168:171], a[240:243], a[136:139], 0\n"
    "ds_read_b64_tr_b16 a[226:227], v16 offset:4096\n"
    "v_mfma_f32_16x16x32_bf16 v[168:171], a[244:247], a[140:143], v[168:171]\n"
    "v_mfma_f32_16x16x32_bf16 v[200:203], a[240:243], a[144:147], 0\n"
    "ds_read_b64_tr_b16 a[228:229], v16 offset:8192\n"
    "v_mfma_f32_16x16x32_bf16 v[200:203], a[244:247], a[148:151], v[200:203]\n"
    "v_mfma_f32_16x16x32_bf16 v[232:235], a[240:243], a[152:155], 0\n"
    "ds_read_b64_tr_b16 a[230:231], v16 offset:12288\n"
    "v_mfma_f32_16x16x32_bf16 v[232:235], a[244:247], a[156:159], v[232:235]\n"
    "s_waitcnt lgkmcnt(5)\n"
    "v_mfma_f32_16x16x32_bf16 v[140:143], a[248:251], a[128:131], 0\n"
    "ds_read_b64_tr_b16 a[232:233], v17\n"
    "v_mfma_f32_16x16x32_bf16 v[140:143], a[252:255], a[132:135], v[140:143]\n"
    "buffer_load_dwordx4 v100, s[40:43], s72 offen offset:3072 lds\n"
    "v_mfma_f32_16x16x32_bf16 v[172:175], a[248:251], a[136:139], 0\n"
    "ds_read_b64_tr_b16 a[234:235], v17 offset:4096\n"
    "v_mfma_f32_16x16x32_bf16 v[172:175], a[252:255], a[140:143], v[172:175]\n"
    "v_mfma_f32_16x16x32_bf16 v[204:207], a[248:251], a[144:147], 0\n"
    "ds_read_b64_tr_b16 a[236:237], v17 offset:8192\n"
    "v_mfma_f32_16x16x32_bf16 v[204:207], a[252:255], a[148:151], v[204:207]\n"
    "v_mfma_f32_16x16x32_bf16 v[236:239], a[248:251], a[152:155], 0\n"
    "ds_read_b64_tr_b16 a[238:239], v17 offset:12288\n"
    "v_mfma_f32_16x16x32_bf16 v[236:239], a[252:255], a[156:159], v[236:239]\n"
    "s_waitcnt lgkmcnt(4)\n"
    "s_add_u32 m0, s74, s84\n"
    "v_mfma_f32_16x16x32_bf16 v[144:147], a[224:227], a[128:131], 0\n"
    "ds_read_b64_tr_b16 a[240:241], v18\n"
    "v_mfma_f32_16x16x32_bf16 v[144:147], a[228:231], a[132:135], v[144:147]\n"
    "buffer_load_dwordx4 v8, s[48:51], s73 offen lds\n"
    "v_mfma_f32_16x16x32_bf16 v[176:179], a[224:227], a[136:139], 0\n"
    "ds_read_b64_tr_b16 a[242:243], v18 offset:4096\n"
    "v_mfma_f32_16x16x32_bf16 v[176:179], a[228:231], a[140:143], v[176:179]\n"
    "v_mfma_f32_16x16x32_bf16 v[208:211], a[224:227], a[144:147], 0\n"
    "ds_read_b64_tr_b16 a[244:245], v18 offset:8192\n"
    "v_mfma_f32_16x16x32_bf16 v[208:211], a[228:231], a[148:151], v[208:211]\n"
    "v_mfma_f32_16x16x32_bf16 v[240:243], a[224:227], a[152:155], 0\n"
    "ds_read_b64_tr_b16 a[246:247], v18 offset:12288\n"
    "v_mfma_f32_16x16x32_bf16 v[240:243], a[228:231], a[156:159], v[240:243]\n"
    "s_waitcnt lgkmcnt(4)\n"
    "v_mfma_f32_16x16x32_bf16 v[148:151], a[232:235], a[128:131], 0\n"
    "ds_read_b64_tr_b16 a[248:249], v19\n"
    "v_mfma_f32_16x16x32_bf16 v[148:151], a[236:239], a[132:135], v[148:151]\n"
    "buffer_load_dwordx4 v9, s[48:51], s73 offen offset:1024 lds\n"
    "v_mfma_f32_16x16x32_bf16 v[180:183], a[232:235], a[136:139], 0\n"
    "ds_read_b64_tr_b16 a[250:251], v19 offset:4096\n"
    "v_mfma_f32_16x16x32_bf16 v[180:183], a[236:239], a[140:143], v[180:183]\n"
    "v_mfma_f32_16x16x32_bf16 v[212:215], a[232:235], a[144:147], 0\n"
    "ds_read_b64_tr_b16 a[252:253], v19 offset:8192\n"
    "v_mfma_f32_16x16x32_bf16 v[212:215], a[236:239], a[148:151], v[212:215]\n"
    "v_mfma_f32_16x16x32_bf16 v[244:247], a[232:235], a[152:155], 0\n"
    "ds_read_b64_tr_b16 a[254:255], v19 offset:12288\n"
    "v_mfma_f32_16x16x32_bf16 v[244:247], a[236:239], a[156:159], v[244:247]\n"
    "s_waitcnt lgkmcnt(4)\n"
    "v_mfma_f32_16x16x32_bf16 v[152:155], a[240:243], a[128:131], 0\n"
    "ds_read_b64_tr_b16 a[224:225], v12 offset:16384\n"
    "v_mfma_f32_16x16x32_bf16 v[152:155], a[244:247], a[132:135], v[152:155]\n"
    "buffer_load_dwordx4 v10, s[48:51], s73 offen offset:2048 lds\n"
    "v_mfma_f32_16x16x32_bf16 v[184:187], a[240:243], a[136:139], 0\n"
    "s_add_u32 s75, s75, 1\n"
    "ds_read_b64_tr_b16 a[226:227], v12 offset:20480\n"
    "v_mfma_f32_16x16x32_bf16 v[184:187], a[244:247], a[140:143], v[184:187]\n"
    "v_mfma_f32_16x16x32_bf16 v[216:219], a[240:243], a[144:147], 0\n"
    "ds_read_b64_tr_b16 a[228:229], v12 offset:24576\n"
    "v_mfma_f32_16x16x32_bf16 v[216:219], a[244:247], a[148:151], v[216:219]\n"
    "s_cmp_lt_u32 s75, 5\n"
    "s_cselect_b32 s75, s75, 0\n"
    "s_cselect_b32 s76, 0x8000, s77\n"
    "v_mfma_f32_16x16x32_bf16 v[248:251], a[240:243], a[152:155], 0\n"
    "ds_read_b64_tr_b16 a[230:231], v12 offset:28672\n"
    "v_add_u32 v12, s76, v12\n"
    "v_mfma_f32_16x16x32_bf16 v[248:251], a[244:247], a[156:159], v[248:251]\n"
    "s_waitcnt lgkmcnt(4)\n"
    "v_mfma_f32_16x16x32_bf16 v[156:159], a[248:251], a[128:131], 0\n"
    "ds_read_b64_tr_b16 a[232:233], v13 offset:16384\n"
    "v_mfma_f32_16x16x32_bf16 v[156:159], a[252:255], a[132:135], v[156:159]\n"
    "buffer_load_dwordx4 v11, s[48:51], s73 offen offset:3072 lds\n"
    "v_mfma_f32_16x16x32_bf16 v[188:191], a[248:251], a[136:139], 0\n"
    "ds_read_b64_tr_b16 a[234:235], v13 offset:20480\n"
    "v_mfma_f32_16x16x32_bf16 v[188:191], a[252:255], a[140:143], v[188:191]\n"
    "v_mfma_f32_16x16x32_bf16 v[220:223], a[248:251], a[144:147], 0\n"
    "s_mov_b64 s[96:97], s[22:23]\n"
    "ds_read_b64_tr_b16 a[236:237], v13 offset:24576\n"
    "v_mfma_f32_16x16x32_bf16 v[220:223], a[252:255], a[148:151], v[220:223]\n"
    "v_mfma_f32_16x16x32_bf16 v[252:255], a[248:251], a[152:155], 0\n"
    "ds_read_b64_tr_b16 a[238:239], v13 offset:28672\n"
    "v_add_u32 v13, s76, v13\n"
    "v_mfma_f32_16x16x32_bf16 v[252:255], a[252:255], a[156:159], v[252:255]\n"
    "s_waitcnt lgkmcnt(4)\n"
    "s_add_u32 m0, s85, 0x18000\n"
    "s_mov_b32 s98, 0x400000\n"
    "v_mfma_f32_16x16x32_bf16 a[0:3], a[128:131], a[224:227], 0\n"
    "ds_read_b64_tr_b16 a[240:241], v14 offset:16384\n"
    "v_mfma_f32_16x16x32_bf16 a[0:3], a[132:135], a[228:231], a[0:3]\n"
    "buffer_load_dwordx4 v2, s[40:43], s86 offen lds\n"
    "v_mfma_f32_16x16x32_bf16 a[4:7], a[136:139], a[224:227], 0\n"
    "s_waitcnt vmcnt(10)\n"
    "ds_read_b64_tr_b16 a[242:243], v14 offset:20480\n"
    "v_mfma_f32_16x16x32_bf16 a[4:7], a[140:143], a[228:231], a[4:7]\n"
    "s_mov_b32 s99, 0x20000\n"
    "v_mfma_f32_16x16x32_bf16 a[8:11], a[144:147], a[224:227], 0\n"
    "ds_read_b64_tr_b16 a[244:245], v14 offset:24576\n"
    "v_mfma_f32_16x16x32_bf16 a[8:11], a[148:151], a[228:231], a[8:11]\n"
    "ds_read_b128 a[176:179], v101 offset:16384\n"
    "v_mfma_f32_16x16x32_bf16 a[12:15], a[152:155], a[224:227], 0\n"
    "ds_read_b64_tr_b16 a[246:247], v14 offset:28672\n"
    "v_add_u32 v14, s76, v14\n"
    "v_mfma_f32_16x16x32_bf16 a[12:15], a[156:159], a[228:231], a[12:15]\n"
    "s_waitcnt lgkmcnt(5)\n"
    "ds_read_b128 a[180:183], v101 offset:17408\n"
    "v_mfma_f32_16x16x32_bf16 a[16:19], a[128:131], a[232:235], 0\n"
    "ds_read_b64_tr_b16 a[248:249], v15 offset:16384\n"
    "v_and_b32 v26, 7, v1\n"
    "v_mfma_f32_16x16x32_bf16 a[16:19], a[132:135], a[236:239], a[16:19]\n"
    "buffer_load_dwordx4 v3, s[40:43], s86 offen offset:1024 lds\n"
    "v_mfma_f32_16x16x32_bf16 a[20:23], a[136:139], a[232:235], 0\n"
    "ds_read_b64_tr_b16 a[250:251], v15 offset:20480\n"
    "v_lshrrev_b32 v27, 3, v1\n"
    "v_mfma_f32_16x16x32_bf16 a[20:23], a[140:143], a[236:239], a[20:23]\n"
    "ds_read_b128 a[184:187], v101 offset:18432\n"
    "v_mfma_f32_16x16x32_bf16 a[24:27], a[144:147], a[232:235], 0\n"
    "ds_read_b64_tr_b16 a[252:253], v15 offset:24576\n"
    "v_mfma_f32_16x16x32_bf16 a[24:27], a[148:151], a[236:239], a[24:27]\n"
    "ds_read_b128 a[188:191], v101 offset:19456\n"
    "v_lshlrev_b32 v28, 4, v27\n"
    "v_mfma_f32_16x16x32_bf16 a[28:31], a[152:155], a[232:235], 0\n"
    "ds_read_b64_tr_b16 a[254:255], v15 offset:28672\n"
    "v_add_u32 v15, s76, v15\n"
    "v_mfma_f32_16x16x32_bf16 a[28:31], a[156:159], a[236:239], a[28:31]\n"
    "s_waitcnt lgkmcnt(7)\n"
    "v_mfma_f32_16x16x32_bf16 a[32:35], a[128:131], a[240:243], 0\n"
    "ds_read_b64_tr_b16 a[224:225], v16 offset:16384\n"
    "v_mfma_f32_16x16x32_bf16 a[32:35], a[132:135], a[244:247], a[32:35]\n"
    "buffer_load_dwordx4 v99, s[40:43], s86 offen offset:2048 lds\n"
    "v_mfma_f32_16x16x32_bf16 a[36:39], a[136:139], a[240:243], 0\n"
    "ds_read_b64_tr_b16 a[226:227], v16 offset:20480\n"
    "v_lshl_add_u32 v28, v26, 11, v28\n"
    "v_mfma_f32_16x16x32_bf16 a[36:39], a[140:143], a[244:247], a[36:39]\n"
    "v_mfma_f32_16x16x32_bf16 a[40:43], a[144:147], a[240:243], 0\n"
    "ds_read_b64_tr_b16 a[228:229], v16 offset:24576\n"
    "v_mfma_f32_16x16x32_bf16 a[40:43], a[148:151], a[244:247], a[40:43]\n"
    "s_lshl_b32 s81, s30, 14\n"
    "v_mfma_f32_16x16x32_bf16 a[44:47], a[152:155], a[240:243], 0\n"
    "ds_read_b64_tr_b16 a[230:231], v16 offset:28672\n"
    "v_add_u32 v16, s76, v16\n"
    "v_mfma_f32_16x16x32_bf16 a[44:47], a[156:159], a[244:247], a[44:47]\n"
    "s_waitcnt lgkmcnt(4)\n"
    "v_mfma_f32_16x16x32_bf16 a[48:51], a[128:131], a[248:251], 0\n"
    "s_lshl_b32 s82, s78, 9\n"
    "ds_read_b64_tr_b16 a[232:233], v17 offset:16384\n"
    "v_mfma_f32_16x16x32_bf16 a[48:51], a[132:135], a[252:255], a[48:51]\n"
    "buffer_load_dwordx4 v100, s[40:43], s86 offen offset:3072 lds\n"
    "v_mfma_f32_16x16x32_bf16 a[52:55], a[136:139], a[248:251], 0\n"
    "ds_read_b64_tr_b16 a[234:235], v17 offset:20480\n"
    "v_mfma_f32_16x16x32_bf16 a[52:55], a[140:143], a[252:255], a[52:55]\n"
    "v_mfma_f32_16x16x32_bf16 a[56:59], a[144:147], a[248:251], 0\n"
    "s_add_u32 s81, s81, s82\n"
    "ds_read_b64_tr_b16 a[236:237], v17 offset:24576\n"
    "v_mfma_f32_16x16x32_bf16 a[56:59], a[148:151], a[252:255], a[56:59]\n"
    "v_mfma_f32_16x16x32_bf16 a[60:63], a[152:155], a[248:251], 0\n"
    "ds_read_b64_tr_b16 a[238:239], v17 offset:28672\n"
    "v_add_u32 v17, s76, v17\n"
    "v_mfma_f32_16x16x32_bf16 a[60:63], a[156:159], a[252:255], a[60:63]\n"
    "s_waitcnt lgkmcnt(4)\n"
    "s_add_u32 m0, s74, s85\n"
    "v_mfma_f32_16x16x32_bf16 a[64:67], a[128:131], a[224:227], 0\n"
    "ds_read_b64_tr_b16 a[240:241], v18 offset:16384\n"
    "v_mfma_f32_16x16x32_bf16 a[64:67], a[132:135], a[228:231], a[64:67]\n"
    "buffer_load_dwordx4 v8, s[52:55], s73 offen lds\n"
    "v_mfma_f32_16x16x32_bf16 a[68:71], a[136:139], a[224:227], 0\n"
    "ds_read_b64_tr_b16 a[242:243], v18 offset:20480\n"
    "v_mfma_f32_16x16x32_bf16 a[68:71], a[140:143], a[228:231], a[68:71]\n"
    "buffer_load_dwordx4 v[32:35], v28, s[96:99], s81 offen\n"
    "v_mfma_f32_16x16x32_bf16 a[72:75], a[144:147], a[224:227], 0\n"
    "ds_read_b64_tr_b16 a[244:245], v18 offset:24576\n"
    "v_mfma_f32_16x16x32_bf16 a[72:75], a[148:151], a[228:231], a[72:75]\n"
    "buffer_load_dwordx4 v[36:39], v28, s[96:99], s81 offen offset:128\n"
    "v_mfma_f32_16x16x32_bf16 a[76:79], a[152:155], a[224:227], 0\n"
    "ds_read_b64_tr_b16 a[246:247], v18 offset:28672\n"
    "v_add_u32 v18, s76, v18\n"
    "v_mfma_f32_16x16x32_bf16 a[76:79], a[156:159], a[228:231], a[76:79]\n"
    "s_waitcnt lgkmcnt(4)\n"
    "buffer_load_dwordx4 v[40:43], v28, s[96:99], s81 offen offset:256\n"
    "v_mfma_f32_16x16x32_bf16 a[80:83], a[128:131], a[232:235], 0\n"
    "ds_read_b64_tr_b16 a[248:249], v19 offset:16384\n"
    "v_mfma_f32_16x16x32_bf16 a[80:83], a[132:135], a[236:239], a[80:83]\n"
    "buffer_load_dwordx4 v9, s[52:55], s73 offen offset:1024 lds\n"
    "v_mfma_f32_16x16x32_bf16 a[84:87], a[136:139], a[232:235], 0\n"
    "ds_read_b64_tr_b16 a[250:251], v19 offset:20480\n"
    "v_mfma_f32_16x16x32_bf16 a[84:87], a[140:143], a[236:239], a[84:87]\n"
    "buffer_load_dwordx4 v[44:47], v28, s[96:99], s81 offen offset:384\n"
    "v_mfma_f32_16x16x32_bf16 a[88:91], a[144:147], a[232:235], 0\n"
    "ds_read_b64_tr_b16 a[252:253], v19 offset:24576\n"
    "v_mfma_f32_16x16x32_bf16 a[88:91], a[148:151], a[236:239], a[88:91]\n"
    "v_mfma_f32_16x16x32_bf16 a[92:95], a[152:155], a[232:235], 0\n"
    "ds_read_b64_tr_b16 a[254:255], v19 offset:28672\n"
    "v_add_u32 v19, s76, v19\n"
    "v_mfma_f32_16x16x32_bf16 a[92:95], a[156:159], a[236:239], a[92:95]\n"
    "s_waitcnt lgkmcnt(4)\n"
    "s_barrier\n"
    "v_mfma_f32_16x16x32_bf16 a[96:99], a[128:131], a[240:243], 0\n"
    "ds_read_b64_tr_b16 a[224:225], v12\n"
    "v_mfma_f32_16x16x32_bf16 a[96:99], a[132:135], a[244:247], a[96:99]\n"
    "buffer_load_dwordx4 v10, s[52:55], s73 offen offset:2048 lds\n"
    "v_mfma_f32_16x16x32_bf16 a[100:103], a[136:139], a[240:243], 0\n"
    "ds_read_b64_tr_b16 a[226:227], v12 offset:4096\n"
    "v_mfma_f32_16x16x32_bf16 a[100:103], a[140:143], a[244:247], a[100:103]\n"
    "v_mfma_f32_16x16x32_bf16 a[104:107], a[144:147], a[240:243], 0\n"
    "ds_read_b64_tr_b16 a[228:229], v12 offset:8192\n"
    "v_mfma_f32_16x16x32_bf16 a[104:107], a[148:151], a[244:247], a[104:107]\n"
    "v_mfma_f32_16x16x32_bf16 a[108:111], a[152:155], a[240:243], 0\n"
    "ds_read_b64_tr_b16 a[230:231], v12 offset:12288\n"
    "v_mfma_f32_16x16x32_bf16 a[108:111], a[156:159], a[244:247], a[108:111]\n"
    "s_waitcnt lgkmcnt(4)\n"
    "v_mfma_f32_16x16x32_bf16 a[112:115], a[128:131], a[248:251], 0\n"
    "ds_read_b64_tr_b16 a[232:233], v13\n"
    "v_mfma_f32_16x16x32_bf16 a[112:115], a[132:135], a[252:255], a[112:115]\n"
    "buffer_load_dwordx4 v11, s[52:55], s73 offen offset:3072 lds\n"
    "v_mfma_f32_16x16x32_bf16 a[116:119], a[136:139], a[248:251], 0\n"
    "s_add_u32 s73, s73, 0x80000\n"
    "ds_read_b64_tr_b16 a[234:235], v13 offset:4096\n"
    "v_mfma_f32_16x16x32_bf16 a[116:119], a[140:143], a[252:255], a[116:119]\n"
    "s_and_b32 s73, s73, 0x3fffff\n"
    "s_add_u32 s74, s74, 0x8000\n"
    "v_mfma_f32_16x16x32_bf16 a[120:123], a[144:147], a[248:251], 0\n"
    "ds_read_b64_tr_b16 a[236:237], v13 offset:8192\n"
    "v_mfma_f32_16x16x32_bf16 a[120:123], a[148:151], a[252:255], a[120:123]\n"
    "s_cmp_lt_u32 s74, 0x28000\n"
    "s_cselect_b32 s74, s74, 0\n"
    "s_add_u32 s72, s72, 0x80\n"
    "v_mfma_f32_16x16x32_bf16 a[124:127], a[152:155], a[248:251], 0\n"
    "s_and_b32 s72, s72, 0x3ff\n"
    "ds_read_b64_tr_b16 a[238:239], v13 offset:12288\n"
    "v_mfma_f32_16x16x32_bf16 a[124:127], a[156:159], a[252:255], a[124:127]\n"
    "s_add_u32 s86, s72, 0x8000\n"
    "s_waitcnt lgkmcnt(4)\n"
    "v_mfma_f32_16x16x32_bf16 v[128:131], a[224:227], a[160:163], v[128:131]\n"
    "s_add_u32 m0, s84, 0x20000\n"
    "ds_read_b64_tr_b16 a[240:241], v14\n"
    "v_mfma_f32_16x16x32_bf16 v[128:131], a[228:231], a[164:167], v[128:131]\n"
    "buffer_load_dwordx4 v2, s[40:43], s72 offen lds\n"
    "v_mfma_f32_16x16x32_bf16 v[160:163], a[224:227], a[168:171], v[160:163]\n"
    "s_add_u32 s91, s84, 0x18000\n"
    "ds_read_b64_tr_b16 a[242:243], v14 offset:4096\n"
    "v_mfma_f32_16x16x32_bf16 v[160:163], a[228:231], a[172:175], v[160:163]\n"
    "v_add_u32 v101, s91, v22\n"
    "v_mfma_f32_16x16x32_bf16 v[192:195], a[224:227], a[176:179], v[192:195]\n"
    "s_waitcnt vmcnt(17)\n"
    "ds_read_b64_tr_b16 a[244:245], v14 offset:8192\n"
    "v_mfma_f32_16x16x32_bf16 v[192:195], a[228:231], a[180:183], v[192:195]\n"
    "v_mfma_f32_16x16x32_bf16 v[224:227], a[224:227], a[184:187], v[224:227]\n"
    "ds_read_b64_tr_b16 a[246:247], v14 offset:12288\n"
    "v_mfma_f32_16x16x32_bf16 v[224:227], a[228:231], a[188:191], v[224:227]\n"
    "s_waitcnt lgkmcnt(4)\n"
    "ds_read_b128 a[192:195], v101\n"
    "v_mfma_f32_16x16x32_bf16 v[132:135], a[232:235], a[160:163], v[132:135]\n"
    "ds_read_b64_tr_b16 a[248:249], v15\n"
    "v_mfma_f32_16x16x32_bf16 v[132:135], a[236:239], a[164:167], v[132:135]\n"
    "buffer_load_dwordx4 v3, s[40:43], s72 offen offset:1024 lds\n"
    "v_mfma_f32_16x16x32_bf16 v[164:167], a[232:235], a[168:171], v[164:167]\n"
    "ds_read_b64_tr_b16 a[250:251], v15 offset:4096\n"
    "v_mfma_f32_16x16x32_bf16 v[164:167], a[236:239], a[172:175], v[164:167]\n"
    "ds_read_b128 a[196:199], v101 offset:1024\n"
    "v_mfma_f32_16x16x32_bf16 v[196:199], a[232:235], a[176:179], v[196:199]\n"
    "ds_read_b64_tr_b16 a[252:253], v15 offset:8192\n"
    "v_mfma_f32_16x16x32_bf16 v[196:199], a[236:239], a[180:183], v[196:199]\n"
    "ds_read_b128 a[200:203], v101 offset:2048\n"
    "v_mfma_f32_16x16x32_bf16 v[228:231], a[232:235], a[184:187], v[228:231]\n"
    "ds_read_b64_tr_b16 a[254:255], v15 offset:12288\n"
    "v_mfma_f32_16x16x32_bf16 v[228:231], a[236:239], a[188:191], v[228:231]\n"
    "s_waitcnt lgkmcnt(7)\n"
    "ds_read_b128 a[204:207], v101 offset:3072\n"
    "v_mfma_f32_16x16x32_bf16 v[136:139], a[240:243], a[160:163], v[136:139]\n"
    "ds_read_b64_tr_b16 a[224:225], v16\n"
    "v_mfma_f32_16x16x32_bf16 v[136:139], a[244:247], a[164:167], v[136:139]\n"
    "buffer_load_dwordx4 v99, s[40:43], s72 offen offset:2048 lds\n"
    "v_mfma_f32_16x16x32_bf16 v[168:171], a[240:243], a[168:171], v[168:171]\n"
    "ds_read_b64_tr_b16 a[226:227], v16 offset:4096\n"
    "v_mfma_f32_16x16x32_bf16 v[168:171], a[244:247], a[172:175], v[168:171]\n"
    "v_mfma_f32_16x16x32_bf16 v[200:203], a[240:243], a[176:179], v[200:203]\n"
    "ds_read_b64_tr_b16 a[228:229], v16 offset:8192\n"
    "v_mfma_f32_16x16x32_bf16 v[200:203], a[244:247], a[180:183], v[200:203]\n"
    "v_mfma_f32_16x16x32_bf16 v[232:235], a[240:243], a[184:187], v[232:235]\n"
    "ds_read_b64_tr_b16 a[230:231], v16 offset:12288\n"
    "v_mfma_f32_16x16x32_bf16 v[232:235], a[244:247], a[188:191], v[232:235]\n"
    "s_waitcnt lgkmcnt(5)\n"
    "v_mfma_f32_16x16x32_bf16 v[140:143], a[248:251], a[160:163], v[140:143]\n"
    "ds_read_b64_tr_b16 a[232:233], v17\n"
    "v_mfma_f32_16x16x32_bf16 v[140:143], a[252:255], a[164:167], v[140:143]\n"
    "buffer_load_dwordx4 v100, s[40:43], s72 offen offset:3072 lds\n"
    "v_mfma_f32_16x16x32_bf16 v[172:175], a[248:251], a[168:171], v[172:175]\n"
    "ds_read_b64_tr_b16 a[234:235], v17 offset:4096\n"
    "v_mfma_f32_16x16x32_bf16 v[172:175], a[252:255], a[172:175], v[172:175]\n"
    "v_mfma_f32_16x16x32_bf16 v[204:207], a[248:251], a[176:179], v[204:207]\n"
    "ds_read_b64_tr_b16 a[236:237], v17 offset:8192\n"
    "v_mfma_f32_16x16x32_bf16 v[204:207], a[252:255], a[180:183], v[204:207]\n"
    "v_mfma_f32_16x16x32_bf16 v[236:239], a[248:251], a[184:187], v[236:239]\n"
    "ds_read_b64_tr_b16 a[238:239], v17 offset:12288\n"
    "v_mfma_f32_16x16x32_bf16 v[236:239], a[252:255], a[188:191], v[236:239]\n"
    "s_waitcnt lgkmcnt(4)\n"
    "s_add_u32 m0, s74, s84\n"
    "v_mfma_f32_16x16x32_bf16 v[144:147], a[224:227], a[160:163], v[144:147]\n"
    "ds_read_b64_tr_b16 a[240:241], v18\n"
    "v_mfma_f32_16x16x32_bf16 v[144:147], a[228:231], a[164:167], v[144:147]\n"
    "buffer_load_dwordx4 v8, s[48:51], s73 offen lds\n"
    "v_mfma_f32_16x16x32_bf16 v[176:179], a[224:227], a[168:171], v[176:179]\n"
    "ds_read_b64_tr_b16 a[242:243], v18 offset:4096\n"
    "v_mfma_f32_16x16x32_bf16 v[176:179], a[228:231], a[172:175], v[176:179]\n"
    "v_mfma_f32_16x16x32_bf16 v[208:211], a[224:227], a[176:179], v[208:211]\n"
    "ds_read_b64_tr_b16 a[244:245], v18 offset:8192\n"
    "v_mfma_f32_16x16x32_bf16 v[208:211], a[228:231], a[180:183], v[208:211]\n"
    "v_mfma_f32_16x16x32_bf16 v[240:243], a[224:227], a[184:187], v[240:243]\n"
    "ds_read_b64_tr_b16 a[246:247], v18 offset:12288\n"
    "v_mfma_f32_16x16x32_bf16 v[240:243], a[228:231], a[188:191], v[240:243]\n"
    "s_waitcnt lgkmcnt(4)\n"
    "v_mfma_f32_16x16x32_bf16 v[148:151], a[232:235], a[160:163], v[148:151]\n"
    "s_waitcnt vmcnt(11)\n"
    "ds_read_b64_tr_b16 a[248:249], v19\n"
    "v_mfma_f32_16x16x32_bf16 v[148:151], a[236:239], a[164:167], v[148:151]\n"
    "buffer_load_dwordx4 v9, s[48:51], s73 offen offset:1024 lds\n"
    "v_mfma_f32_16x16x32_bf16 v[180:183], a[232:235], a[168:171], v[180:183]\n"
    "ds_read_b64_tr_b16 a[250:251], v19 offset:4096\n"
    "v_lshlrev_b32 v48, 16, v32\n"
    "v_mfma_f32_16x16x32_bf16 v[180:183], a[236:239], a[172:175], v[180:183]\n"
    "v_and_b32 v49, 0xffff0000, v32\n"
    "v_lshlrev_b32 v50, 16, v33\n"
    "v_mfma_f32_16x16x32_bf16 v[212:215], a[232:235], a[176:179], v[212:215]\n"
    "ds_read_b64_tr_b16 a[252:253], v19 offset:8192\n"
    "v_and_b32 v51, 0xffff0000, v33\n"
    "v_mfma_f32_16x16x32_bf16 v[212:215], a[236:239], a[180:183], v[212:215]\n"
    "v_lshlrev_b32 v52, 16, v34\n"
    "v_and_b32 v53, 0xffff0000, v34\n"
    "v_mfma_f32_16x16x32_bf16 v[244:247], a[232:235], a[184:187], v[244:247]\n"
    "ds_read_b64_tr_b16 a[254:255], v19 offset:12288\n"
    "v_lshlrev_b32 v54, 16, v35\n"
    "v_mfma_f32_16x16x32_bf16 v[244:247], a[236:239], a[188:191], v[244:247]\n"
    "s_waitcnt lgkmcnt(4)\n"
    "v_and_b32 v55, 0xffff0000, v35\n"
    "v_mfma_f32_16x16x32_bf16 v[152:155], a[240:243], a[160:163], v[152:155]\n"
    "ds_read_b64_tr_b16 a[224:225], v12 offset:16384\n"
    "v_add_f32 v80, v48, v50\n"
    "v_mfma_f32_16x16x32_bf16 v[152:155], a[244:247], a[164:167], v[152:155]\n"
    "buffer_load_dwordx4 v10, s[48:51], s73 offen offset:2048 lds\n"
    "v_mfma_f32_16x16x32_bf16 v[184:187], a[240:243], a[168:171], v[184:187]\n"
    "s_add_u32 s75, s75, 1\n"
    "ds_read_b64_tr_b16 a[226:227], v12 offset:20480\n"
    "v_mfma_f32_16x16x32_bf16 v[184:187], a[244:247], a[172:175], v[184:187]\n"
    "v_add_f32 v81, v49, v51\n"
    "v_add_f32 v80, v80, v52\n"
    "v_mfma_f32_16x16x32_bf16 v[216:219], a[240:243], a[176:179], v[216:219]\n"
    "ds_read_b64_tr_b16 a[228:229], v12 offset:24576\n"
    "v_add_f32 v81, v81, v53\n"
    "v_add_f32 v80, v80, v54\n"
    "v_mfma_f32_16x16x32_bf16 v[216:219], a[244:247], a[180:183], v[216:219]\n"
    "s_cmp_lt_u32 s75, 5\n"
    "s_cselect_b32 s75, s75, 0\n"
    "s_cselect_b32 s76, 0x8000, s77\n"
    "v_add_f32 v81, v81, v55\n"
    "v_mfma_f32_16x16x32_bf16 v[248:251], a[240:243], a[184:187], v[248:251]\n"
    "ds_read_b64_tr_b16 a[230:231], v12 offset:28672\n"
    "v_add_u32 v12, s76, v12\n"
    "v_mfma_f32_16x16x32_bf16 v[248:251], a[244:247], a[188:191], v[248:251]\n"
    "s_waitcnt lgkmcnt(4)\n"
    "s_waitcnt vmcnt(12)\n"
    "v_mfma_f32_16x16x32_bf16 v[156:159], a[248:251], a[160:163], v[156:159]\n"
    "ds_read_b64_tr_b16 a[232:233], v13 offset:16384\n"
    "v_lshlrev_b32 v56, 16, v36\n"
    "v_and_b32 v57, 0xffff0000, v36\n"
    "v_mfma_f32_16x16x32_bf16 v[156:159], a[252:255], a[164:167], v[156:159]\n"
    "buffer_load_dwordx4 v11, s[48:51], s73 offen offset:3072 lds\n"
    "v_mfma_f32_16x16x32_bf16 v[188:191], a[248:251], a[168:171], v[188:191]\n"
    "ds_read_b64_tr_b16 a[234:235], v13 offset:20480\n"
    "v_lshlrev_b32 v58, 16, v37\n"
    "v_mfma_f32_16x16x32_bf16 v[188:191], a[252:255], a[172:175], v[188:191]\n"
    "v_and_b32 v59, 0xffff0000, v37\n"
    "v_lshlrev_b32 v60, 16, v38\n"
    "v_mfma_f32_16x16x32_bf16 v[220:223], a[248:251], a[176:179], v[220:223]\n"
    "ds_read_b64_tr_b16 a[236:237], v13 offset:24576\n"
    "v_and_b32 v61, 0xffff0000, v38\n"
    "v_mfma_f32_16x16x32_bf16 v[220:223], a[252:255], a[180:183], v[220:223]\n"
    "v_lshlrev_b32 v62, 16, v39\n"
    "v_and_b32 v63, 0xffff0000, v39\n"
    "v_mfma_f32_16x16x32_bf16 v[252:255], a[248:251], a[184:187], v[252:255]\n"
    "ds_read_b64_tr_b16 a[238:239], v13 offset:28672\n"
    "v_add_u32 v13, s76, v13\n"
    "v_mfma_f32_16x16x32_bf16 v[252:255], a[252:255], a[188:191], v[252:255]\n"
    "s_waitcnt lgkmcnt(4)\n"
    "s_add_u32 m0, s85, 0x20000\n"
    "v_add_f32 v80, v80, v56\n"
    "v_mfma_f32_16x16x32_bf16 a[0:3], a[160:163], a[224:227], a[0:3]\n"
    "ds_read_b64_tr_b16 a[240:241], v14 offset:16384\n"
    "v_add_f32 v81, v81, v57\n"
    "v_add_f32 v80, v80, v58\n"
    "v_mfma_f32_16x16x32_bf16 a[0:3], a[164:167], a[228:231], a[0:3]\n"
    "buffer_load_dwordx4 v2, s[40:43], s86 offen lds\n"
    "v_add_f32 v81, v81, v59\n"
    "v_mfma_f32_16x16x32_bf16 a[4:7], a[168:171], a[224:227], a[4:7]\n"
    "ds_read_b64_tr_b16 a[242:243], v14 offset:20480\n"
    "v_mfma_f32_16x16x32_bf16 a[4:7], a[172:175], a[228:231], a[4:7]\n"
    "v_add_f32 v80, v80, v60\n"
    "v_add_f32 v81, v81, v61\n"
    "v_mfma_f32_16x16x32_bf16 a[8:11], a[176:179], a[224:227], a[8:11]\n"
    "ds_read_b64_tr_b16 a[244:245], v14 offset:24576\n"
    "v_add_f32 v80, v80, v62\n"
    "v_mfma_f32_16x16x32_bf16 a[8:11], a[180:183], a[228:231], a[8:11]\n"
    "ds_read_b128 a[208:211], v101 offset:16384\n"
    "v_add_f32 v81, v81, v63\n"
    "v_mfma_f32_16x16x32_bf16 a[12:15], a[184:187], a[224:227], a[12:15]\n"
    "s_waitcnt vmcnt(13)\n"
    "ds_read_b64_tr_b16 a[246:247], v14 offset:28672\n"
    "v_add_u32 v14, s76, v14\n"
    "v_mfma_f32_16x16x32_bf16 a[12:15], a[188:191], a[228:231], a[12:15]\n"
    "s_waitcnt lgkmcnt(5)\n"
    "ds_read_b128 a[212:215], v101 offset:17408\n"
    "v_mfma_f32_16x16x32_bf16 a[16:19], a[160:163], a[232:235], a[16:19]\n"
    "ds_read_b64_tr_b16 a[248:249], v15 offset:16384\n"
    "v_lshlrev_b32 v64, 16, v40\n"
    "v_and_b32 v65, 0xffff0000, v40\n"
    "v_mfma_f32_16x16x32_bf16 a[16:19], a[164:167], a[236:239], a[16:19]\n"
    "buffer_load_dwordx4 v3, s[40:43], s86 offen offset:1024 lds\n"
    "v_lshlrev_b32 v66, 16, v41\n"
    "v_mfma_f32_16x16x32_bf16 a[20:23], a[168:171], a[232:235], a[20:23]\n"
    "ds_read_b64_tr_b16 a[250:251], v15 offset:20480\n"
    "v_and_b32 v67, 0xffff0000, v41\n"
    "v_mfma_f32_16x16x32_bf16 a[20:23], a[172:175], a[236:239], a[20:23]\n"
    "ds_read_b128 a[216:219], v101 offset:18432\n"
    "v_lshlrev_b32 v68, 16, v42\n"
    "v_mfma_f32_16x16x32_bf16 a[24:27], a[176:179], a[232:235], a[24:27]\n"
    "ds_read_b64_tr_b16 a[252:253], v15 offset:24576\n"
    "v_and_b32 v69, 0xffff0000, v42\n"
    "v_mfma_f32_16x16x32_bf16 a[24:27], a[180:183], a[236:239], a[24:27]\n"
    "ds_read_b128 a[220:223], v101 offset:19456\n"
    "v_lshlrev_b32 v70, 16, v43\n"
    "v_and_b32 v71, 0xffff0000, v43\n"
    "v_mfma_f32_16x16x32_bf16 a[28:31], a[184:187], a[232:235], a[28:31]\n"
    "ds_read_b64_tr_b16 a[254:255], v15 offset:28672\n"
    "v_add_u32 v15, s76, v15\n"
    "v_mfma_f32_16x16x32_bf16 a[28:31], a[188:191], a[236:239], a[28:31]\n"
    "s_waitcnt lgkmcnt(7)\n"
    "v_add_f32 v80, v80, v64\n"
    "v_add_f32 v81, v81, v65\n"
    "v_mfma_f32_16x16x32_bf16 a[32:35], a[160:163], a[240:243], a[32:35]\n"
    "ds_read_b64_tr_b16 a[224:225], v16 offset:16384\n"
    "v_add_f32 v80, v80, v66\n"
    "v_mfma_f32_16x16x32_bf16 a[32:35], a[164:167], a[244:247], a[32:35]\n"
    "buffer_load_dwordx4 v99, s[40:43], s86 offen offset:2048 lds\n"
    "v_mfma_f32_16x16x32_bf16 a[36:39], a[168:171], a[240:243], a[36:39]\n"
    "ds_read_b64_tr_b16 a[226:227], v16 offset:20480\n"
    "v_add_f32 v81, v81, v67\n"
    "v_mfma_f32_16x16x32_bf16 a[36:39], a[172:175], a[244:247], a[36:39]\n"
    "v_add_f32 v80, v80, v68\n"
    "v_add_f32 v81, v81, v69\n"
    "v_mfma_f32_16x16x32_bf16 a[40:43], a[176:179], a[240:243], a[40:43]\n"
    "ds_read_b64_tr_b16 a[228:229], v16 offset:24576\n"
    "v_add_f32 v80, v80, v70\n"
    "v_mfma_f32_16x16x32_bf16 a[40:43], a[180:183], a[244:247], a[40:43]\n"
    "v_add_f32 v81, v81, v71\n"
    "v_mfma_f32_16x16x32_bf16 a[44:47], a[184:187], a[240:243], a[44:47]\n"
    "s_waitcnt vmcnt(13)\n"
    "ds_read_b64_tr_b16 a[230:231], v16 offset:28672\n"
    "v_add_u32 v16, s76, v16\n"
    "v_mfma_f32_16x16x32_bf16 a[44:47], a[188:191], a[244:247], a[44:47]\n"
    "s_waitcnt lgkmcnt(4)\n"
    "v_lshlrev_b32 v72, 16, v44\n"
    "v_and_b32 v73, 0xffff0000, v44\n"
    "v_mfma_f32_16x16x32_bf16 a[48:51], a[160:163], a[248:251], a[48:51]\n"
    "ds_read_b64_tr_b16 a[232:233], v17 offset:16384\n"
    "v_lshlrev_b32 v74, 16, v45\n"
    "v_mfma_f32_16x16x32_bf16 a[48:51], a[164:167], a[252:255], a[48:51]\n"
    "buffer_load_dwordx4 v100, s[40:43], s86 offen offset:3072 lds\n"
    "v_mfma_f32_16x16x32_bf16 a[52:55], a[168:171], a[248:251], a[52:55]\n"
    "ds_read_b64_tr_b16 a[234:235], v17 offset:20480\n"
    "v_and_b32 v75, 0xffff0000, v45\n"
    "v_mfma_f32_16x16x32_bf16 a[52:55], a[172:175], a[252:255], a[52:55]\n"
    "v_lshlrev_b32 v76, 16, v46\n"
    "v_and_b32 v77, 0xffff0000, v46\n"
    "v_mfma_f32_16x16x32_bf16 a[56:59], a[176:179], a[248:251], a[56:59]\n"
    "ds_read_b64_tr_b16 a[236:237], v17 offset:24576\n"
    "v_lshlrev_b32 v78, 16, v47\n"
    "v_mfma_f32_16x16x32_bf16 a[56:59], a[180:183], a[252:255], a[56:59]\n"
    "v_and_b32 v79, 0xffff0000, v47\n"
    "v_add_f32 v80, v80, v72\n"
    "v_mfma_f32_16x16x32_bf16 a[60:63], a[184:187], a[248:251], a[60:63]\n"
    "ds_read_b64_tr_b16 a[238:239], v17 offset:28672\n"
    "v_add_u32 v17, s76, v17\n"
    "v_mfma_f32_16x16x32_bf16 a[60:63], a[188:191], a[252:255], a[60:63]\n"
    "s_waitcnt lgkmcnt(4)\n"
    "s_add_u32 m0, s74, s85\n"
    "v_add_f32 v81, v81, v73\n"
    "v_mfma_f32_16x16x32_bf16 a[64:67], a[160:163], a[224:227], a[64:67]\n"
    "ds_read_b64_tr_b16 a[240:241], v18 offset:16384\n"
    "v_add_f32 v80, v80, v74\n"
    "v_add_f32 v81, v81, v75\n"
    "v_mfma_f32_16x16x32_bf16 a[64:67], a[164:167], a[228:231], a[64:67]\n"
    "buffer_load_dwordx4 v8, s[52:55], s73 offen lds\n"
    "v_mfma_f32_16x16x32_bf16 a[68:71], a[168:171], a[224:227], a[68:71]\n"
    "ds_read_b64_tr_b16 a[242:243], v18 offset:20480\n"
    "v_add_f32 v80, v80, v76\n"
    "v_mfma_f32_16x16x32_bf16 a[68:71], a[172:175], a[228:231], a[68:71]\n"
    "v_add_f32 v81, v81, v77\n"
    "v_add_f32 v80, v80, v78\n"
    "v_mfma_f32_16x16x32_bf16 a[72:75], a[176:179], a[224:227], a[72:75]\n"
    "ds_read_b64_tr_b16 a[244:245], v18 offset:24576\n"
    "v_add_f32 v81, v81, v79\n"
    "v_mfma_f32_16x16x32_bf16 a[72:75], a[180:183], a[228:231], a[72:75]\n"
    "v_add_f32 v80, v80, v81\n"
    "v_mfma_f32_16x16x32_bf16 a[76:79], a[184:187], a[224:227], a[76:79]\n"
    "ds_read_b64_tr_b16 a[246:247], v18 offset:28672\n"
    "v_add_u32 v18, s76, v18\n"
    "v_mfma_f32_16x16x32_bf16 a[76:79], a[188:191], a[228:231], a[76:79]\n"
    "s_waitcnt lgkmcnt(4)\n"
    "s_nop 0\n"
    "v_mfma_f32_16x16x32_bf16 a[80:83], a[160:163], a[232:235], a[80:83]\n"
    "ds_read_b64_tr_b16 a[248:249], v19 offset:16384\n"
    "v_add_f32_dpp v80, v80, v80 row_ror:8 row_mask:0xf bank_mask:0xf\n"
    "v_mov_b32 v81, v80\n"
    "v_mfma_f32_16x16x32_bf16 a[80:83], a[164:167], a[236:239], a[80:83]\n"
    "s_nop 0\n"
    "buffer_load_dwordx4 v9, s[52:55], s73 offen offset:1024 lds\n"
    "v_mfma_f32_16x16x32_bf16 a[84:87], a[168:171], a[232:235], a[84:87]\n"
    "ds_read_b64_tr_b16 a[250:251], v19 offset:20480\n"
    "v_permlane32_swap_b32 v80, v81\n"
    "v_mfma_f32_16x16x32_bf16 a[84:87], a[172:175], a[236:239], a[84:87]\n"
    "s_nop 0\n"
    "v_mfma_f32_16x16x32_bf16 a[88:91], a[176:179], a[232:235], a[88:91]\n"
    "ds_read_b64_tr_b16 a[252:253], v19 offset:24576\n"
    "v_add_f32 v80, v80, v81\n"
    "v_mfma_f32_16x16x32_bf16 a[88:91], a[180:183], a[236:239], a[88:91]\n"
    "v_mov_b32 v81, v80\n"
    "v_mfma_f32_16x16x32_bf16 a[92:95], a[184:187], a[232:235], a[92:95]\n"
    "s_nop 0\n"
    "ds_read_b64_tr_b16 a[254:255], v19 offset:28672\n"
    "v_add_u32 v19, s76, v19\n"
    "v_mfma_f32_16x16x32_bf16 a[92:95], a[188:191], a[236:239], a[92:95]\n"
    "s_waitcnt vmcnt(14) lgkmcnt(4)\n"
    "s_barrier\n"
    "v_permlane16_swap_b32 v80, v81\n"
    "v_mfma_f32_16x16x32_bf16 a[96:99], a[160:163], a[240:243], a[96:99]\n"
    "s_nop 0\n"
    "ds_read_b64_tr_b16 a[224:225], v12\n"
    "v_mfma_f32_16x16x32_bf16 a[96:99], a[164:167], a[244:247], a[96:99]\n"
    "buffer_load_dwordx4 v10, s[52:55], s73 offen offset:2048 lds\n"
    "v_add_f32 v80, v80, v81\n"
    "v_mfma_f32_16x16x32_bf16 a[100:103], a[168:171], a[240:243], a[100:103]\n"
    "ds_read_b64_tr_b16 a[226:227], v12 offset:4096\n"
    "v_mul_f32 v80, 0x3b800000, v80\n"
    "v_mfma_f32_16x16x32_bf16 a[100:103], a[172:175], a[244:247], a[100:103]\n"
    "v_sub_f32 v48, v48, v80\n"
    "v_sub_f32 v49, v49, v80\n"
    "v_mfma_f32_16x16x32_bf16 a[104:107], a[176:179], a[240:243], a[104:107]\n"
    "ds_read_b64_tr_b16 a[228:229], v12 offset:8192\n"
    "v_mul_f32 v82, v48, v48\n"
    "v_mfma_f32_16x16x32_bf16 a[104:107], a[180:183], a[244:247], a[104:107]\n"
    "v_sub_f32 v50, v50, v80\n"
    "v_mul_f32 v81, v49, v49\n"
    "v_mfma_f32_16x16x32_bf16 a[108:111], a[184:187], a[240:243], a[108:111]\n"
    "ds_read_b64_tr_b16 a[230:231], v12 offset:12288\n"
    "v_sub_f32 v51, v51, v80\n"
    "v_mfma_f32_16x16x32_bf16 a[108:111], a[188:191], a[244:247], a[108:111]\n"
    "s_waitcnt lgkmcnt(4)\n"
    "v_fmac_f32 v82, v50, v50\n"
    "v_sub_f32 v52, v52, v80\n"
    "v_mfma_f32_16x16x32_bf16 a[112:115], a[160:163], a[248:251], a[112:115]\n"
    "ds_read_b64_tr_b16 a[232:233], v13\n"
    "v_fmac_f32 v81, v51, v51\n"
    "v_mfma_f32_16x16x32_bf16 a[112:115], a[164:167], a[252:255], a[112:115]\n"
    "buffer_load_dwordx4 v11, s[52:55], s73 offen offset:3072 lds\n"
    "v_mfma_f32_16x16x32_bf16 a[116:119], a[168:171], a[248:251], a[116:119]\n"
    "s_add_u32 s73, s73, 0x80000\n"
    "ds_read_b64_tr_b16 a[234:235], v13 offset:4096\n"
    "v_mfma_f32_16x16x32_bf16 a[116:119], a[172:175], a[252:255], a[116:119]\n"
    "s_and_b32 s73, s73, 0x3fffff\n"
    "s_add_u32 s74, s74, 0x8000\n"
    "v_sub_f32 v53, v53, v80\n"
    "v_mfma_f32_16x16x32_bf16 a[120:123], a[176:179], a[248:251], a[120:123]\n"
    "ds_read_b64_tr_b16 a[236:237], v13 offset:8192\n"
    "v_fmac_f32 v82, v52, v52\n"
    "v_sub_f32 v54, v54, v80\n"
    "v_mfma_f32_16x16x32_bf16 a[120:123], a[180:183], a[252:255], a[120:123]\n"
    "s_cmp_lt_u32 s74, 0x28000\n"
    "s_cselect_b32 s74, s74, 0\n"
    "s_add_u32 s72, s72, 0x80\n"
    "v_mfma_f32_16x16x32_bf16 a[124:127], a[184:187], a[248:251], a[124:127]\n"
    "s_and_b32 s72, s72, 0x3ff\n"
    "ds_read_b64_tr_b16 a[238:239], v13 offset:12288\n"
    "v_mfma_f32_16x16x32_bf16 a[124:127], a[188:191], a[252:255], a[124:127]\n"
    "s_add_u32 s86, s72, 0x8000\n"
    "s_waitcnt lgkmcnt(4)\n"
    "v_mfma_f32_16x16x32_bf16 v[128:131], a[224:227], a[192:195], v[128:131]\n"
    "s_add_u32 m0, s84, 0\n"
    "ds_read_b64_tr_b16 a[240:241], v14\n"
    "v_fmac_f32 v81, v53, v53\n"
    "v_mfma_f32_16x16x32_bf16 v[128:131], a[228:231], a[196:199], v[128:131]\n"
    "buffer_load_dwordx4 v2, s[40:43], s72 offen lds\n"
    "v_mfma_f32_16x16x32_bf16 v[160:163], a[224:227], a[200:203], v[160:163]\n"
    "s_add_u32 s91, s84, 0x20000\n"
    "ds_read_b64_tr_b16 a[242:243], v14 offset:4096\n"
    "v_mfma_f32_16x16x32_bf16 v[160:163], a[228:231], a[204:207], v[160:163]\n"
    "v_add_u32 v101, s91, v22\n"
    "v_sub_f32 v55, v55, v80\n"
    "v_fmac_f32 v82, v54, v54\n"
    "v_mfma_f32_16x16x32_bf16 v[192:195], a[224:227], a[208:211], v[192:195]\n"
    "s_waitcnt vmcnt(13)\n"
    "ds_read_b64_tr_b16 a[244:245], v14 offset:8192\n"
    "v_mfma_f32_16x16x32_bf16 v[192:195], a[228:231], a[212:215], v[192:195]\n"
    "v_sub_f32 v56, v56, v80\n"
    "v_fmac_f32 v81, v55, v55\n"
    "v_mfma_f32_16x16x32_bf16 v[224:227], a[224:227], a[216:219], v[224:227]\n"
    "ds_read_b64_tr_b16 a[246:247], v14 offset:12288\n"
    "v_sub_f32 v57, v57, v80\n"
    "v_mfma_f32_16x16x32_bf16 v[224:227], a[228:231], a[220:223], v[224:227]\n"
    "s_waitcnt lgkmcnt(4)\n"
    "ds_read_b128 a[128:131], v101\n"
    "v_mfma_f32_16x16x32_bf16 v[132:135], a[232:235], a[192:195], v[132:135]\n"
    "ds_read_b64_tr_b16 a[248:249], v15\n"
    "v_fmac_f32 v82, v56, v56\n"
    "v_mfma_f32_16x16x32_bf16 v[132:135], a[236:239], a[196:199], v[132:135]\n"
    "buffer_load_dwordx4 v3, s[40:43], s72 offen offset:1024 lds\n"
    "v_mfma_f32_16x16x32_bf16 v[164:167], a[232:235], a[200:203], v[164:167]\n"
    "ds_read_b64_tr_b16 a[250:251], v15 offset:4096\n"
    "v_sub_f32 v58, v58, v80\n"
    "v_mfma_f32_16x16x32_bf16 v[164:167], a[236:239], a[204:207], v[164:167]\n"
    "ds_read_b128 a[132:135], v101 offset:1024\n"
    "v_fmac_f32 v81, v57, v57\n"
    "v_mfma_f32_16x16x32_bf16 v[196:199], a[232:235], a[208:211], v[196:199]\n"
    "ds_read_b64_tr_b16 a[252:253], v15 offset:8192\n"
    "v_sub_f32 v59, v59, v80\n"
    "v_mfma_f32_16x16x32_bf16 v[196:199], a[236:239], a[212:215], v[196:199]\n"
    "ds_read_b128 a[136:139], v101 offset:2048\n"
    "v_fmac_f32 v82, v58, v58\n"
    "v_mfma_f32_16x16x32_bf16 v[228:231], a[232:235], a[216:219], v[228:231]\n"
    "ds_read_b64_tr_b16 a[254:255], v15 offset:12288\n"
    "v_sub_f32 v60, v60, v80\n"
    "v_mfma_f32_16x16x32_bf16 v[228:231], a[236:239], a[220:223], v[228:231]\n"
    "s_waitcnt lgkmcnt(7)\n"
    "ds_read_b128 a[140:143], v101 offset:3072\n"
    "v_mfma_f32_16x16x32_bf16 v[136:139], a[240:243], a[192:195], v[136:139]\n"
    "ds_read_b64_tr_b16 a[224:225], v16\n"
    "v_fmac_f32 v81, v59, v59\n"
    "v_mfma_f32_16x16x32_bf16 v[136:139], a[244:247], a[196:199], v[136:139]\n"
    "buffer_load_dwordx4 v99, s[40:43], s72 offen offset:2048 lds\n"
    "v_mfma_f32_16x16x32_bf16 v[168:171], a[240:243], a[200:203], v[168:171]\n"
    "ds_read_b64_tr_b16 a[226:227], v16 offset:4096\n"
    "v_sub_f32 v61, v61, v80\n"
    "v_mfma_f32_16x16x32_bf16 v[168:171], a[244:247], a[204:207], v[168:171]\n"
    "v_fmac_f32 v82, v60, v60\n"
    "v_sub_f32 v62, v62, v80\n"
    "v_mfma_f32_16x16x32_bf16 v[200:203], a[240:243], a[208:211], v[200:203]\n"
    "ds_read_b64_tr_b16 a[228:229], v16 offset:8192\n"
    "v_mfma_f32_16x16x32_bf16 v[200:203], a[244:247], a[212:215], v[200:203]\n"
    "v_fmac_f32 v81, v61, v61\n"
    "v_mfma_f32_16x16x32_bf16 v[232:235], a[240:243], a[216:219], v[232:235]\n"
    "ds_read_b64_tr_b16 a[230:231], v16 offset:12288\n"
    "v_sub_f32 v63, v63, v80\n"
    "v_mfma_f32_16x16x32_bf16 v[232:235], a[244:247], a[220:223], v[232:235]\n"
    "s_waitcnt lgkmcnt(5)\n"
    "v_fmac_f32 v82, v62, v62\n"
    "v_mfma_f32_16x16x32_bf16 v[140:143], a[248:251], a[192:195], v[140:143]\n"
    "ds_read_b64_tr_b16 a[232:233], v17\n"
    "v_sub_f32 v64, v64, v80\n"
    "v_mfma_f32_16x16x32_bf16 v[140:143], a[252:255], a[196:199], v[140:143]\n"
    "buffer_load_dwordx4 v100, s[40:43], s72 offen offset:3072 lds\n"
    "v_mfma_f32_16x16x32_bf16 v[172:175], a[248:251], a[200:203], v[172:175]\n"
    "ds_read_b64_tr_b16 a[234:235], v17 offset:4096\n"
    "v_fmac_f32 v81, v63, v63\n"
    "v_mfma_f32_16x16x32_bf16 v[172:175], a[252:255], a[204:207], v[172:175]\n"
    "v_sub_f32 v65, v65, v80\n"
    "v_mfma_f32_16x16x32_bf16 v[204:207], a[248:251], a[208:211], v[204:207]\n"
    "ds_read_b64_tr_b16 a[236:237], v17 offset:8192\n"
    "v_fmac_f32 v82, v64, v64\n"
    "v_mfma_f32_16x16x32_bf16 v[204:207], a[252:255], a[212:215], v[204:207]\n"
    "v_mfma_f32_16x16x32_bf16 v[236:239], a[248:251], a[216:219], v[236:239]\n"
    "ds_read_b64_tr_b16 a[238:239], v17 offset:12288\n"
    "v_sub_f32 v66, v66, v80\n"
    "v_mfma_f32_16x16x32_bf16 v[236:239], a[252:255], a[220:223], v[236:239]\n"
    "s_waitcnt lgkmcnt(4)\n"
    "s_add_u32 m0, s74, s84\n"
    "v_fmac_f32 v81, v65, v65\n"
    "v_mfma_f32_16x16x32_bf16 v[144:147], a[224:227], a[192:195], v[144:147]\n"
    "ds_read_b64_tr_b16 a[240:241], v18\n"
    "v_sub_f32 v67, v67, v80\n"
    "v_mfma_f32_16x16x32_bf16 v[144:147], a[228:231], a[196:199], v[144:147]\n"
    "buffer_load_dwordx4 v8, s[48:51], s73 offen lds\n"
    "v_mfma_f32_16x16x32_bf16 v[176:179], a[224:227], a[200:203], v[176:179]\n"
    "ds_read_b64_tr_b16 a[242:243], v18 offset:4096\n"
    "v_fmac_f32 v82, v66, v66\n"
    "v_mfma_f32_16x16x32_bf16 v[176:179], a[228:231], a[204:207], v[176:179]\n"
    "v_sub_f32 v68, v68, v80\n"
    "v_mfma_f32_16x16x32_bf16 v[208:211], a[224:227], a[208:211], v[208:211]\n"
    "s_waitcnt vmcnt(9)\n"
    "ds_read_b64_tr_b16 a[244:245], v18 offset:8192\n"
    "v_mfma_f32_16x16x32_bf16 v[208:211], a[228:231], a[212:215], v[208:211]\n"
    "v_fmac_f32 v81, v67, v67\n"
    "v_mfma_f32_16x16x32_bf16 v[240:243], a[224:227], a[216:219], v[240:243]\n"
    "ds_read_b64_tr_b16 a[246:247], v18 offset:12288\n"
    "v_sub_f32 v69, v69, v80\n"
    "v_mfma_f32_16x16x32_bf16 v[240:243], a[228:231], a[220:223], v[240:243]\n"
    "s_waitcnt lgkmcnt(4)\n"
    "ds_read_b128 a[144:147], v101 offset:16384\n"
    "v_mfma_f32_16x16x32_bf16 v[148:151], a[232:235], a[192:195], v[148:151]\n"
    "ds_read_b64_tr_b16 a[248:249], v19\n"
    "v_fmac_f32 v82, v68, v68\n"
    "v_mfma_f32_16x16x32_bf16 v[148:151], a[236:239], a[196:199], v[148:151]\n"
    "buffer_load_dwordx4 v9, s[48:51], s73 offen offset:1024 lds\n"
    "v_mfma_f32_16x16x32_bf16 v[180:183], a[232:235], a[200:203], v[180:183]\n"
    "ds_read_b64_tr_b16 a[250:251], v19 offset:4096\n"
    "v_sub_f32 v70, v70, v80\n"
    "v_mfma_f32_16x16x32_bf16 v[180:183], a[236:239], a[204:207], v[180:183]\n"
    "ds_read_b128 a[148:151], v101 offset:17408\n"
    "v_fmac_f32 v81, v69, v69\n"
    "v_mfma_f32_16x16x32_bf16 v[212:215], a[232:235], a[208:211], v[212:215]\n"
    "ds_read_b64_tr_b16 a[252:253], v19 offset:8192\n"
    "v_sub_f32 v71, v71, v80\n"
    "v_mfma_f32_16x16x32_bf16 v[212:215], a[236:239], a[212:215], v[212:215]\n"
    "ds_read_b128 a[152:155], v101 offset:18432\n"
    "v_fmac_f32 v82, v70, v70\n"
    "v_mfma_f32_16x16x32_bf16 v[244:247], a[232:235], a[216:219], v[244:247]\n"
    "ds_read_b64_tr_b16 a[254:255], v19 offset:12288\n"
    "v_sub_f32 v72, v72, v80\n"
    "v_mfma_f32_16x16x32_bf16 v[244:247], a[236:239], a[220:223], v[244:247]\n"
    "s_waitcnt lgkmcnt(7)\n"
    "ds_read_b128 a[156:159], v101 offset:19456\n"
    "v_mfma_f32_16x16x32_bf16 v[152:155], a[240:243], a[192:195], v[152:155]\n"
    "ds_read_b64_tr_b16 a[224:225], v12 offset:16384\n"
    "v_fmac_f32 v81, v71, v71\n"
    "v_sub_f32 v73, v73, v80\n"
    "v_mfma_f32_16x16x32_bf16 v[152:155], a[244:247], a[196:199], v[152:155]\n"
    "buffer_load_dwordx4 v10, s[48:51], s73 offen offset:2048 lds\n"
    "v_mfma_f32_16x16x32_bf16 v[184:187], a[240:243], a[200:203], v[184:187]\n"
    "s_add_u32 s75, s75, 1\n"
    "ds_read_b64_tr_b16 a[226:227], v12 offset:20480\n"
    "v_mfma_f32_16x16x32_bf16 v[184:187], a[244:247], a[204:207], v[184:187]\n"
    "v_fmac_f32 v82, v72, v72\n"
    "v_sub_f32 v74, v74, v80\n"
    "v_mfma_f32_16x16x32_bf16 v[216:219], a[240:243], a[208:211], v[216:219]\n"
    "ds_read_b64_tr_b16 a[228:229], v12 offset:24576\n"
    "v_fmac_f32 v81, v73, v73\n"
    "v_mfma_f32_16x16x32_bf16 v[216:219], a[244:247], a[212:215], v[216:219]\n"
    "s_cmp_lt_u32 s75, 5\n"
    "s_cselect_b32 s75, s75, 0\n"
    "s_cselect_b32 s76, 0x8000, s77\n"
    "v_mfma_f32_16x16x32_bf16 v[248:251], a[240:243], a[216:219], v[248:251]\n"
    "ds_read_b64_tr_b16 a[230:231], v12 offset:28672\n"
    "v_add_u32 v12, s76, v12\n"
    "v_mfma_f32_16x16x32_bf16 v[248:251], a[244:247], a[220:223], v[248:251]\n"
    "s_waitcnt lgkmcnt(5)\n"
    "v_sub_f32 v75, v75, v80\n"
    "v_fmac_f32 v82, v74, v74\n"
    "v_mfma_f32_16x16x32_bf16 v[156:159], a[248:251], a[192:195], v[156:159]\n"
    "ds_read_b64_tr_b16 a[232:233], v13 offset:16384\n"
    "v_sub_f32 v76, v76, v80\n"
    "v_mfma_f32_16x16x32_bf16 v[156:159], a[252:255], a[196:199], v[156:159]\n"
    "buffer_load_dwordx4 v11, s[48:51], s73 offen offset:3072 lds\n"
    "v_mfma_f32_16x16x32_bf16 v[188:191], a[248:251], a[200:203], v[188:191]\n"
    "ds_read_b64_tr_b16 a[234:235], v13 offset:20480\n"
    "v_fmac_f32 v81, v75, v75\n"
    "v_mfma_f32_16x16x32_bf16 v[188:191], a[252:255], a[204:207], v[188:191]\n"
    "v_sub_f32 v77, v77, v80\n"
    "v_mfma_f32_16x16x32_bf16 v[220:223], a[248:251], a[208:211], v[220:223]\n"
    "ds_read_b64_tr_b16 a[236:237], v13 offset:24576\n"
    "v_fmac_f32 v82, v76, v76\n"
    "v_mfma_f32_16x16x32_bf16 v[220:223], a[252:255], a[212:215], v[220:223]\n"
    "v_sub_f32 v78, v78, v80\n"
    "v_mfma_f32_16x16x32_bf16 v[252:255], a[248:251], a[216:219], v[252:255]\n"
    "ds_read_b64_tr_b16 a[238:239], v13 offset:28672\n"
    "v_add_u32 v13, s76, v13\n"
    "v_mfma_f32_16x16x32_bf16 v[252:255], a[252:255], a[220:223], v[252:255]\n"
    "s_waitcnt lgkmcnt(4)\n"
    "s_add_u32 m0, s74, s85\n"
    "v_fmac_f32 v81, v77, v77\n"
    "v_mfma_f32_16x16x32_bf16 a[0:3], a[192:195], a[224:227], a[0:3]\n"
    "ds_read_b64_tr_b16 a[240:241], v14 offset:16384\n"
    "v_sub_f32 v79, v79, v80\n"
    "v_mfma_f32_16x16x32_bf16 a[0:3], a[196:199], a[228:231], a[0:3]\n"
    "buffer_load_dwordx4 v8, s[52:55], s73 offen lds\n"
    "v_mfma_f32_16x16x32_bf16 a[4:7], a[200:203], a[224:227], a[4:7]\n"
    "ds_read_b64_tr_b16 a[242:243], v14 offset:20480\n"
    "v_fmac_f32 v82, v78, v78\n"
    "v_mfma_f32_16x16x32_bf16 a[4:7], a[204:207], a[228:231], a[4:7]\n"
    "v_fmac_f32 v81, v79, v79\n"
    "v_mfma_f32_16x16x32_bf16 a[8:11], a[208:211], a[224:227], a[8:11]\n"
    "ds_read_b64_tr_b16 a[244:245], v14 offset:24576\n"
    "v_add_f32 v81, v81, v82\n"
    "v_mfma_f32_16x16x32_bf16 a[8:11], a[212:215], a[228:231], a[8:11]\n"
    "s_nop 0\n"
    "v_mfma_f32_16x16x32_bf16 a[12:15], a[216:219], a[224:227], a[12:15]\n"
    "ds_read_b64_tr_b16 a[246:247], v14 offset:28672\n"
    "v_add_u32 v14, s76, v14\n"
    "v_mfma_f32_16x16x32_bf16 a[12:15], a[220:223], a[228:231], a[12:15]\n"
    "s_waitcnt lgkmcnt(4)\n"
    "v_add_f32_dpp v81, v81, v81 row_ror:8 row_mask:0xf bank_mask:0xf\n"
    "v_mfma_f32_16x16x32_bf16 a[16:19], a[192:195], a[232:235], a[16:19]\n"
    "ds_read_b64_tr_b16 a[248:249], v15 offset:16384\n"
    "v_mov_b32 v82, v81\n"
    "v_mfma_f32_16x16x32_bf16 a[16:19], a[196:199], a[236:239], a[16:19]\n"
    "buffer_load_dwordx4 v9, s[52:55], s73 offen offset:1024 lds\n"
    "v_mfma_f32_16x16x32_bf16 a[20:23], a[200:203], a[232:235], a[20:23]\n"
    "s_nop 0\n"
    "ds_read_b64_tr_b16 a[250:251], v15 offset:20480\n"
    "v_mfma_f32_16x16x32_bf16 a[20:23], a[204:207], a[236:239], a[20:23]\n"
    "v_permlane32_swap_b32 v81, v82\n"
    "v_mfma_f32_16x16x32_bf16 a[24:27], a[208:211], a[232:235], a[24:27]\n"
    "s_nop 0\n"
    "ds_read_b64_tr_b16 a[252:253], v15 offset:24576\n"
    "v_mfma_f32_16x16x32_bf16 a[24:27], a[212:215], a[236:239], a[24:27]\n"
    "v_add_f32 v81, v81, v82\n"
    "v_mfma_f32_16x16x32_bf16 a[28:31], a[216:219], a[232:235], a[28:31]\n"
    "ds_read_b64_tr_b16 a[254:255], v15 offset:28672\n"
    "v_add_u32 v15, s76, v15\n"
    "v_mfma_f32_16x16x32_bf16 a[28:31], a[220:223], a[236:239], a[28:31]\n"
    "s_waitcnt lgkmcnt(4)\n"
    "v_mov_b32 v82, v81\n"
    "v_mfma_f32_16x16x32_bf16 a[32:35], a[192:195], a[240:243], a[32:35]\n"
    "s_nop 0\n"
    "ds_read_b64_tr_b16 a[224:225], v16 offset:16384\n"
    "v_mfma_f32_16x16x32_bf16 a[32:35], a[196:199], a[244:247], a[32:35]\n"
    "buffer_load_dwordx4 v10, s[52:55], s73 offen offset:2048 lds\n"
    "v_mfma_f32_16x16x32_bf16 a[36:39], a[200:203], a[240:243], a[36:39]\n"
    "ds_read_b64_tr_b16 a[226:227], v16 offset:20480\n"
    "v_permlane16_swap_b32 v81, v82\n"
    "v_mfma_f32_16x16x32_bf16 a[36:39], a[204:207], a[244:247], a[36:39]\n"
    "s_nop 0\n"
    "v_mfma_f32_16x16x32_bf16 a[40:43], a[208:211], a[240:243], a[40:43]\n"
    "ds_read_b64_tr_b16 a[228:229], v16 offset:24576\n"
    "v_add_f32 v81, v81, v82\n"
    "v_mfma_f32_16x16x32_bf16 a[40:43], a[212:215], a[244:247], a[40:43]\n"
    "s_mov_b64 exec, 0xff\n ds_write_b64 v98, v[80:81]\n s_mov_b64 exec, -1\n"
    "v_mfma_f32_16x16x32_bf16 a[44:47], a[216:219], a[240:243], a[44:47]\n"
    "ds_read_b64_tr_b16 a[230:231], v16 offset:28672\n"
    "v_add_u32 v16, s76, v16\n"
    "v_mfma_f32_16x16x32_bf16 a[44:47], a[220:223], a[244:247], a[44:47]\n"
    "s_waitcnt lgkmcnt(5)\n"
    "s_nop 0\n"
    "v_mfma_f32_16x16x32_bf16 a[48:51], a[192:195], a[248:251], a[48:51]\n"
    "s_waitcnt lgkmcnt(1)\n"
    "s_barrier\n"
    "ds_read_b64_tr_b16 a[232:233], v17 offset:16384\n"
    "v_mfma_f32_16x16x32_bf16 a[48:51], a[196:199], a[252:255], a[48:51]\n"
    "buffer_load_dwordx4 v11, s[52:55], s73 offen offset:3072 lds\n"
    "v_mfma_f32_16x16x32_bf16 a[52:55], a[200:203], a[248:251], a[52:55]\n"
    "ds_read_b64_tr_b16 a[234:235], v17 offset:20480\n"
    "v_mfma_f32_16x16x32_bf16 a[52:55], a[204:207], a[252:255], a[52:55]\n"
    "ds_read_b128 v[84:87], v97\n"
    "v_mfma_f32_16x16x32_bf16 a[56:59], a[208:211], a[248:251], a[56:59]\n"
    "ds_read_b64_tr_b16 a[236:237], v17 offset:24576\n"
    "ds_read_b128 v[88:91], v97 offset:16\n"
    "v_mfma_f32_16x16x32_bf16 a[56:59], a[212:215], a[252:255], a[56:59]\n"
    "v_mfma_f32_16x16x32_bf16 a[60:63], a[216:219], a[248:251], a[60:63]\n"
    "ds_read_b64_tr_b16 a[238:239], v17 offset:28672\n"
    "v_add_u32 v17, s76, v17\n"
    "v_mfma_f32_16x16x32_bf16 a[60:63], a[220:223], a[252:255], a[60:63]\n"
    "s_waitcnt lgkmcnt(6)\n"
    "s_add_u32 m0, s85, 0\n"
    "v_mfma_f32_16x16x32_bf16 a[64:67], a[192:195], a[224:227], a[64:67]\n"
    "ds_read_b64_tr_b16 a[240:241], v18 offset:16384\n"
    "v_mfma_f32_16x16x32_bf16 a[64:67], a[196:199], a[228:231], a[64:67]\n"
    "buffer_load_dwordx4 v2, s[40:43], s86 offen lds\n"
    "v_mfma_f32_16x16x32_bf16 a[68:71], a[200:203], a[224:227], a[68:71]\n"
    "ds_read_b64_tr_b16 a[242:243], v18 offset:20480\n"
    "v_mfma_f32_16x16x32_bf16 a[68:71], a[204:207], a[228:231], a[68:71]\n"
    "v_mfma_f32_16x16x32_bf16 a[72:75], a[208:211], a[224:227], a[72:75]\n"
    "s_waitcnt lgkmcnt(3)\n"
    "ds_read_b64_tr_b16 a[244:245], v18 offset:24576\n"
    "v_mfma_f32_16x16x32_bf16 a[72:75], a[212:215], a[228:231], a[72:75]\n"
    "v_add_f32 v82, v84, v86\n"
    "v_mfma_f32_16x16x32_bf16 a[76:79], a[216:219], a[224:227], a[76:79]\n"
    "ds_read_b64_tr_b16 a[246:247], v18 offset:28672\n"
    "v_add_u32 v18, s76, v18\n"
    "v_mfma_f32_16x16x32_bf16 a[76:79], a[220:223], a[228:231], a[76:79]\n"
    "s_waitcnt lgkmcnt(4)\n"
    "v_add_f32 v83, v88, v90\n"
    "v_mfma_f32_16x16x32_bf16 a[80:83], a[192:195], a[232:235], a[80:83]\n"
    "ds_read_b64_tr_b16 a[248:249], v19 offset:16384\n"
    "v_add_f32 v85, v85, v87\n"
    "v_mfma_f32_16x16x32_bf16 a[80:83], a[196:199], a[236:239], a[80:83]\n"
    "buffer_load_dwordx4 v3, s[40:43], s86 offen offset:1024 lds\n"
    "v_mfma_f32_16x16x32_bf16 a[84:87], a[200:203], a[232:235], a[84:87]\n"
    "ds_read_b64_tr_b16 a[250:251], v19 offset:20480\n"
    "v_add_f32 v82, v82, v83\n"
    "v_mfma_f32_16x16x32_bf16 a[84:87], a[204:207], a[236:239], a[84:87]\n"
    "v_add_f32 v89, v89, v91\n"
    "v_mfma_f32_16x16x32_bf16 a[88:91], a[208:211], a[232:235], a[88:91]\n"
    "ds_read_b64_tr_b16 a[252:253], v19 offset:24576\n"
    "v_mul_f32 v82, 0x3e800000, v82\n"
    "v_mfma_f32_16x16x32_bf16 a[88:91], a[212:215], a[236:239], a[88:91]\n"
    "v_add_f32 v85, v85, v89\n"
    "v_mfma_f32_16x16x32_bf16 a[92:95], a[216:219], a[232:235], a[92:95]\n"
    "ds_read_b64_tr_b16 a[254:255], v19 offset:28672\n"
    "v_add_u32 v19, s76, v19\n"
    "v_mfma_f32_16x16x32_bf16 a[92:95], a[220:223], a[236:239], a[92:95]\n"
    "s_waitcnt vmcnt(14) lgkmcnt(4)\n"
    "s_barrier\n"
    "v_sub_f32 v84, v84, v82\n"
    "v_mfma_f32_16x16x32_bf16 a[96:99], a[192:195], a[240:243], a[96:99]\n"
    "ds_read_b64_tr_b16 a[224:225], v12\n"
    "v_sub_f32 v86, v86, v82\n"
    "v_mfma_f32_16x16x32_bf16 a[96:99], a[196:199], a[244:247], a[96:99]\n"
    "buffer_load_dwordx4 v99, s[40:43], s86 offen offset:2048 lds\n"
    "v_mfma_f32_16x16x32_bf16 a[100:103], a[200:203], a[240:243], a[100:103]\n"
    "ds_read_b64_tr_b16 a[226:227], v12 offset:4096\n"
    "v_sub_f32 v88, v88, v82\n"
    "v_mfma_f32_16x16x32_bf16 a[100:103], a[204:207], a[244:247], a[100:103]\n"
    "v_sub_f32 v90, v90, v82\n"
    "v_mfma_f32_16x16x32_bf16 a[104:107], a[208:211], a[240:243], a[104:107]\n"
    "ds_read_b64_tr_b16 a[228:229], v12 offset:8192\n"
    "v_mul_f32 v87, v84, v84\n"
    "v_mfma_f32_16x16x32_bf16 a[104:107], a[212:215], a[244:247], a[104:107]\n"
    "v_fmac_f32 v87, v86, v86\n"
    "v_mfma_f32_16x16x32_bf16 a[108:111], a[216:219], a[240:243], a[108:111]\n"
    "ds_read_b64_tr_b16 a[230:231], v12 offset:12288\n"
    "v_fmac_f32 v87, v88, v88\n"
    "v_mfma_f32_16x16x32_bf16 a[108:111], a[220:223], a[244:247], a[108:111]\n"
    "s_waitcnt lgkmcnt(4)\n"
    "v_mfma_f32_16x16x32_bf16 a[112:115], a[192:195], a[248:251], a[112:115]\n"
    "ds_read_b64_tr_b16 a[232:233], v13\n"
    "v_fmac_f32 v87, v90, v90\n"
    "v_mfma_f32_16x16x32_bf16 a[112:115], a[196:199], a[252:255], a[112:115]\n"
    "buffer_load_dwordx4 v100, s[40:43], s86 offen offset:3072 lds\n"
    "v_mfma_f32_16x16x32_bf16 a[116:119], a[200:203], a[248:251], a[116:119]\n"
    "s_add_u32 s73, s73, 0x80000\n"
    "ds_read_b64_tr_b16 a[234:235], v13 offset:4096\n"
    "v_mfma_f32_16x16x32_bf16 a[116:119], a[204:207], a[252:255], a[116:119]\n"
    "s_and_b32 s73, s73, 0x3fffff\n"
    "s_add_u32 s74, s74, 0x8000\n"
    "v_mov_b32 v89, 0x43800000\n"
    "v_mfma_f32_16x16x32_bf16 a[120:123], a[208:211], a[248:251], a[120:123]\n"
    "ds_read_b64_tr_b16 a[236:237], v13 offset:8192\n"
    "v_fmac_f32 v85, v87, v89\n"
    "v_mfma_f32_16x16x32_bf16 a[120:123], a[212:215], a[252:255], a[120:123]\n"
    "s_cmp_lt_u32 s74, 0x28000\n"
    "s_cselect_b32 s74, s74, 0\n"
    "s_add_u32 s72, s72, 0x80\n"
    "v_mfma_f32_16x16x32_bf16 a[124:127], a[216:219], a[248:251], a[124:127]\n"
    "s_and_b32 s72, s72, 0x3ff\n"
    "ds_read_b64_tr_b16 a[238:239], v13 offset:12288\n"
    "v_mfma_f32_16x16x32_bf16 a[124:127], a[220:223], a[252:255], a[124:127]\n"
    "s_add_u32 s86, s72, 0x8000\n"
    "s_waitcnt lgkmcnt(4)\n"
    "v_mfma_f32_16x16x32_bf16 v[128:131], a[224:227], a[128:131], v[128:131]\n"
    "s_add_u32 m0, s84, 0x8000\n"
    "ds_read_b64_tr_b16 a[240:241], v14\n"
    "v_mov_b32 v89, 0x3727c5ac\n"
    "v_mfma_f32_16x16x32_bf16 v[128:131], a[228:231], a[132:135], v[128:131]\n"
    "buffer_load_dwordx4 v2, s[40:43], s72 offen lds\n"
    "v_mfma_f32_16x16x32_bf16 v[160:163], a[224:227], a[136:139], v[160:163]\n"
    "s_add_u32 s91, s84, 0\n"
    "ds_read_b64_tr_b16 a[242:243], v14 offset:4096\n"
    "v_mfma_f32_16x16x32_bf16 v[160:163], a[228:231], a[140:143], v[160:163]\n"
    "v_add_u32 v101, s91, v22\n"
    "v_mov_b32 v91, 0x3a800000\n"
    "v_fmac_f32 v89, v85, v91\n"
    "v_mfma_f32_16x16x32_bf16 v[192:195], a[224:227], a[144:147], v[192:195]\n"
    "s_waitcnt vmcnt(13)\n"
    "ds_read_b64_tr_b16 a[244:245], v14 offset:8192\n"
    "v_mfma_f32_16x16x32_bf16 v[192:195], a[228:231], a[148:151], v[192:195]\n"
    "v_rsq_f32 v83, v89\n"
    "v_mfma_f32_16x16x32_bf16 v[224:227], a[224:227], a[152:155], v[224:227]\n"
    "ds_read_b64_tr_b16 a[246:247], v14 offset:12288\n"
    "v_sub_f32 v92, v80, v82\n"
    "v_mfma_f32_16x16x32_bf16 v[224:227], a[228:231], a[156:159], v[224:227]\n"
    "s_waitcnt lgkmcnt(4)\n"
    "ds_read_b128 a[160:163], v101\n"
    "v_mfma_f32_16x16x32_bf16 v[132:135], a[232:235], a[128:131], v[132:135]\n"
    "ds_read_b64_tr_b16 a[248:249], v15\n"
    "v_mul_f32 v83, 0x3e0293ee, v83\n"
    "v_mul_f32 v92, v92, v83\n"
    "v_mfma_f32_16x16x32_bf16 v[132:135], a[236:239], a[132:135], v[132:135]\n"
    "buffer_load_dwordx4 v3, s[40:43], s72 offen offset:1024 lds\n"
    "v_mfma_f32_16x16x32_bf16 v[164:167], a[232:235], a[136:139], v[164:167]\n"
    "s_lshr_b32 s87, s30, 1\n"
    "ds_read_b64_tr_b16 a[250:251], v15 offset:4096\n"
    "v_mfma_f32_16x16x32_bf16 v[164:167], a[236:239], a[140:143], v[164:167]\n"
    "s_lshl_b32 s87, s87, 4\n"
    "ds_read_b128 a[164:167], v101 offset:1024\n"
    "v_mfma_f32_16x16x32_bf16 v[196:199], a[232:235], a[144:147], v[196:199]\n"
    "s_and_b32 s90, s30, 1\n"
    "ds_read_b64_tr_b16 a[252:253], v15 offset:8192\n"
    "v_mfma_f32_16x16x32_bf16 v[196:199], a[236:239], a[148:151], v[196:199]\n"
    "s_add_u32 s87, s87, s90\n"
    "ds_read_b128 a[168:171], v101 offset:2048\n"
    "v_mfma_f32_16x16x32_bf16 v[228:231], a[232:235], a[152:155], v[228:231]\n"
    "s_lshl_b32 s87, s87, 7\n"
    "ds_read_b64_tr_b16 a[254:255], v15 offset:12288\n"
    "v_mfma_f32_16x16x32_bf16 v[228:231], a[236:239], a[156:159], v[228:231]\n"
    "s_waitcnt lgkmcnt(7)\n"
    "ds_read_b128 a[172:175], v101 offset:3072\n"
    "v_mfma_f32_16x16x32_bf16 v[136:139], a[240:243], a[128:131], v[136:139]\n"
    "s_lshl_b32 s90, s78, 20\n"
    "ds_read_b64_tr_b16 a[224:225], v16\n"
    "v_mfma_f32_16x16x32_bf16 v[136:139], a[244:247], a[132:135], v[136:139]\n"
    "buffer_load_dwordx4 v99, s[40:43], s72 offen offset:2048 lds\n"
    "v_mfma_f32_16x16x32_bf16 v[168:171], a[240:243], a[136:139], v[168:171]\n"
    "s_add_u32 s87, s87, s90\n"
    "ds_read_b64_tr_b16 a[226:227], v16 offset:4096\n"
    "v_mfma_f32_16x16x32_bf16 v[168:171], a[244:247], a[140:143], v[168:171]\n"
    "v_fma_f32 v48, v48, v83, v92\n"
    "v_fma_f32 v49, v49, v83, v92\n"
    "v_mfma_f32_16x16x32_bf16 v[200:203], a[240:243], a[144:147], v[200:203]\n"
    "ds_read_b64_tr_b16 a[228:229], v16 offset:8192\n"
    "v_fma_f32 v50, v50, v83, v92\n"
    "v_mfma_f32_16x16x32_bf16 v[200:203], a[244:247], a[148:151], v[200:203]\n"
    "v_mfma_f32_16x16x32_bf16 v[232:235], a[240:243], a[152:155], v[232:235]\n"
    "ds_read_b64_tr_b16 a[230:231], v16 offset:12288\n"
    "v_fma_f32 v51, v51, v83, v92\n"
    "v_mfma_f32_16x16x32_bf16 v[232:235], a[244:247], a[156:159], v[232:235]\n"
    "s_waitcnt lgkmcnt(5)\n"
    "v_fma_f32 v52, v52, v83, v92\n"
    "v_mfma_f32_16x16x32_bf16 v[140:143], a[248:251], a[128:131], v[140:143]\n"
    "ds_read_b64_tr_b16 a[232:233], v17\n"
    "v_fma_f32 v53, v53, v83, v92\n"
    "v_mfma_f32_16x16x32_bf16 v[140:143], a[252:255], a[132:135], v[140:143]\n"
    "buffer_load_dwordx4 v100, s[40:43], s72 offen offset:3072 lds\n"
    "v_mfma_f32_16x16x32_bf16 v[172:175], a[248:251], a[136:139], v[172:175]\n"
    "ds_read_b64_tr_b16 a[234:235], v17 offset:4096\n"
    "v_fma_f32 v54, v54, v83, v92\n"
    "v_mfma_f32_16x16x32_bf16 v[172:175], a[252:255], a[140:143], v[172:175]\n"
    "v_fma_f32 v55, v55, v83, v92\n"
    "v_mfma_f32_16x16x32_bf16 v[204:207], a[248:251], a[144:147], v[204:207]\n"
    "ds_read_b64_tr_b16 a[236:237], v17 offset:8192\n"
    "v_cvt_pk_bf16_f32 v32, v48, v49\n"
    "v_mfma_f32_16x16x32_bf16 v[204:207], a[252:255], a[148:151], v[204:207]\n"
    "v_mfma_f32_16x16x32_bf16 v[236:239], a[248:251], a[152:155], v[236:239]\n"
    "ds_read_b64_tr_b16 a[238:239], v17 offset:12288\n"
    "v_cvt_pk_bf16_f32 v33, v50, v51\n"
    "v_mfma_f32_16x16x32_bf16 v[236:239], a[252:255], a[156:159], v[236:239]\n"
    "s_waitcnt lgkmcnt(4)\n"
    "s_add_u32 m0, s74, s84\n"
    "v_cvt_pk_bf16_f32 v34, v52, v53\n"
    "v_mfma_f32_16x16x32_bf16 v[144:147], a[224:227], a[128:131], v[144:147]\n"
    "ds_read_b64_tr_b16 a[240:241], v18\n"
    "v_cvt_pk_bf16_f32 v35, v54, v55\n"
    "v_mfma_f32_16x16x32_bf16 v[144:147], a[228:231], a[132:135], v[144:147]\n"
    "buffer_load_dwordx4 v8, s[48:51], s73 offen lds\n"
    "v_mfma_f32_16x16x32_bf16 v[176:179], a[224:227], a[136:139], v[176:179]\n"
    "ds_read_b64_tr_b16 a[242:243], v18 offset:4096\n"
    "v_mfma_f32_16x16x32_bf16 v[176:179], a[228:231], a[140:143], v[176:179]\n"
    "buffer_store_dwordx4 v[32:35], v96, s[44:47], s87 offen sc1\n"
    "v_mfma_f32_16x16x32_bf16 v[208:211], a[224:227], a[144:147], v[208:211]\n"
    "s_add_u32 s87, s87, 0x40000\n"
    "ds_read_b64_tr_b16 a[244:245], v18 offset:8192\n"
    "v_mfma_f32_16x16x32_bf16 v[208:211], a[228:231], a[148:151], v[208:211]\n"
    "v_fma_f32 v56, v56, v83, v92\n"
    "v_fma_f32 v57, v57, v83, v92\n"
    "v_mfma_f32_16x16x32_bf16 v[240:243], a[224:227], a[152:155], v[240:243]\n"
    "ds_read_b64_tr_b16 a[246:247], v18 offset:12288\n"
    "v_mfma_f32_16x16x32_bf16 v[240:243], a[228:231], a[156:159], v[240:243]\n"
    "s_waitcnt lgkmcnt(4)\n"
    "v_fma_f32 v58, v58, v83, v92\n"
    "v_mfma_f32_16x16x32_bf16 v[148:151], a[232:235], a[128:131], v[148:151]\n"
    "ds_read_b64_tr_b16 a[248:249], v19\n"
    "v_fma_f32 v59, v59, v83, v92\n"
    "v_mfma_f32_16x16x32_bf16 v[148:151], a[236:239], a[132:135], v[148:151]\n"
    "buffer_load_dwordx4 v9, s[48:51], s73 offen offset:1024 lds\n"
    "v_mfma_f32_16x16x32_bf16 v[180:183], a[232:235], a[136:139], v[180:183]\n"
    "ds_read_b64_tr_b16 a[250:251], v19 offset:4096\n"
    "v_fma_f32 v60, v60, v83, v92\n"
    "v_mfma_f32_16x16x32_bf16 v[180:183], a[236:239], a[140:143], v[180:183]\n"
    "v_fma_f32 v61, v61, v83, v92\n"
    "v_mfma_f32_16x16x32_bf16 v[212:215], a[232:235], a[144:147], v[212:215]\n"
    "ds_read_b64_tr_b16 a[252:253], v19 offset:8192\n"
    "v_fma_f32 v62, v62, v83, v92\n"
    "v_mfma_f32_16x16x32_bf16 v[212:215], a[236:239], a[148:151], v[212:215]\n"
    "v_fma_f32 v63, v63, v83, v92\n"
    "v_mfma_f32_16x16x32_bf16 v[244:247], a[232:235], a[152:155], v[244:247]\n"
    "ds_read_b64_tr_b16 a[254:255], v19 offset:12288\n"
    "v_cvt_pk_bf16_f32 v36, v56, v57\n"
    "v_mfma_f32_16x16x32_bf16 v[244:247], a[236:239], a[156:159], v[244:247]\n"
    "s_waitcnt lgkmcnt(4)\n"
    "v_mfma_f32_16x16x32_bf16 v[152:155], a[240:243], a[128:131], v[152:155]\n"
    "ds_read_b64_tr_b16 a[224:225], v12 offset:16384\n"
    "v_cvt_pk_bf16_f32 v37, v58, v59\n"
    "v_mfma_f32_16x16x32_bf16 v[152:155], a[244:247], a[132:135], v[152:155]\n"
    "buffer_load_dwordx4 v10, s[48:51], s73 offen offset:2048 lds\n"
    "v_mfma_f32_16x16x32_bf16 v[184:187], a[240:243], a[136:139], v[184:187]\n"
    "s_add_u32 s75, s75, 1\n"
    "ds_read_b64_tr_b16 a[226:227], v12 offset:20480\n"
    "v_mfma_f32_16x16x32_bf16 v[184:187], a[244:247], a[140:143], v[184:187]\n"
    "v_cvt_pk_bf16_f32 v38, v60, v61\n"
    "v_cvt_pk_bf16_f32 v39, v62, v63\n"
    "v_mfma_f32_16x16x32_bf16 v[216:219], a[240:243], a[144:147], v[216:219]\n"
    "ds_read_b64_tr_b16 a[228:229], v12 offset:24576\n"
    "buffer_store_dwordx4 v[36:39], v96, s[44:47], s87 offen sc1\n"
    "v_mfma_f32_16x16x32_bf16 v[216:219], a[244:247], a[148:151], v[216:219]\n"
    "s_cmp_lt_u32 s75, 5\n"
    "s_cselect_b32 s75, s75, 0\n"
    "s_cselect_b32 s76, 0x8000, s77\n"
    "v_mfma_f32_16x16x32_bf16 v[248:251], a[240:243], a[152:155], v[248:251]\n"
    "ds_read_b64_tr_b16 a[230:231], v12 offset:28672\n"
    "v_add_u32 v12, s76, v12\n"
    "v_mfma_f32_16x16x32_bf16 v[248:251], a[244:247], a[156:159], v[248:251]\n"
    "s_waitcnt lgkmcnt(4)\n"
    "s_add_u32 s87, s87, 0x40000\n"
    "v_mfma_f32_16x16x32_bf16 v[156:159], a[248:251], a[128:131], v[156:159]\n"
    "ds_read_b64_tr_b16 a[232:233], v13 offset:16384\n"
    "v_fma_f32 v64, v64, v83, v92\n"
    "v_mfma_f32_16x16x32_bf16 v[156:159], a[252:255], a[132:135], v[156:159]\n"
    "buffer_load_dwordx4 v11, s[48:51], s73 offen offset:3072 lds\n"
    "v_mfma_f32_16x16x32_bf16 v[188:191], a[248:251], a[136:139], v[188:191]\n"
    "ds_read_b64_tr_b16 a[234:235], v13 offset:20480\n"
    "v_fma_f32 v65, v65, v83, v92\n"
    "v_mfma_f32_16x16x32_bf16 v[188:191], a[252:255], a[140:143], v[188:191]\n"
    "v_fma_f32 v66, v66, v83, v92\n"
    "v_fma_f32 v67, v67, v83, v92\n"
    "v_mfma_f32_16x16x32_bf16 v[220:223], a[248:251], a[144:147], v[220:223]\n"
    "ds_read_b64_tr_b16 a[236:237], v13 offset:24576\n"
    "v_fma_f32 v68, v68, v83, v92\n"
    "v_mfma_f32_16x16x32_bf16 v[220:223], a[252:255], a[148:151], v[220:223]\n"
    "v_fma_f32 v69, v69, v83, v92\n"
    "v_mfma_f32_16x16x32_bf16 v[252:255], a[248:251], a[152:155], v[252:255]\n"
    "ds_read_b64_tr_b16 a[238:239], v13 offset:28672\n"
    "v_add_u32 v13, s76, v13\n"
    "v_mfma_f32_16x16x32_bf16 v[252:255], a[252:255], a[156:159], v[252:255]\n"
    "s_waitcnt lgkmcnt(4)\n"
    "s_add_u32 m0, s85, 0x8000\n"
    "v_fma_f32 v70, v70, v83, v92\n"
    "v_mfma_f32_16x16x32_bf16 a[0:3], a[128:131], a[224:227], a[0:3]\n"
    "ds_read_b64_tr_b16 a[240:241], v14 offset:16384\n"
    "v_fma_f32 v71, v71, v83, v92\n"
    "v_mfma_f32_16x16x32_bf16 a[0:3], a[132:135], a[228:231], a[0:3]\n"
    "buffer_load_dwordx4 v2, s[40:43], s86 offen lds\n"
    "v_mfma_f32_16x16x32_bf16 a[4:7], a[136:139], a[224:227], a[4:7]\n"
    "s_waitcnt vmcnt(11)\n"
    "ds_read_b64_tr_b16 a[242:243], v14 offset:20480\n"
    "v_mfma_f32_16x16x32_bf16 a[4:7], a[140:143], a[228:231], a[4:7]\n"
    "v_cvt_pk_bf16_f32 v40, v64, v65\n"
    "v_cvt_pk_bf16_f32 v41, v66, v67\n"
    "v_mfma_f32_16x16x32_bf16 a[8:11], a[144:147], a[224:227], a[8:11]\n"
    "ds_read_b64_tr_b16 a[244:245], v14 offset:24576\n"
    "v_cvt_pk_bf16_f32 v42, v68, v69\n"
    "v_mfma_f32_16x16x32_bf16 a[8:11], a[148:151], a[228:231], a[8:11]\n"
    "ds_read_b128 a[176:179], v101 offset:16384\n"
    "v_cvt_pk_bf16_f32 v43, v70, v71\n"
    "v_mfma_f32_16x16x32_bf16 a[12:15], a[152:155], a[224:227], a[12:15]\n"
    "ds_read_b64_tr_b16 a[246:247], v14 offset:28672\n"
    "v_add_u32 v14, s76, v14\n"
    "v_mfma_f32_16x16x32_bf16 a[12:15], a[156:159], a[228:231], a[12:15]\n"
    "s_waitcnt lgkmcnt(5)\n"
    "ds_read_b128 a[180:183], v101 offset:17408\n"
    "v_mfma_f32_16x16x32_bf16 a[16:19], a[128:131], a[232:235], a[16:19]\n"
    "ds_read_b64_tr_b16 a[248:249], v15 offset:16384\n"
    "buffer_store_dwordx4 v[40:43], v96, s[44:47], s87 offen sc1\n"
    "v_mfma_f32_16x16x32_bf16 a[16:19], a[132:135], a[236:239], a[16:19]\n"
    "s_add_u32 s87, s87, 0x40000\n"
    "buffer_load_dwordx4 v3, s[40:43], s86 offen offset:1024 lds\n"
    "v_mfma_f32_16x16x32_bf16 a[20:23], a[136:139], a[232:235], a[20:23]\n"
    "ds_read_b64_tr_b16 a[250:251], v15 offset:20480\n"
    "v_fma_f32 v72, v72, v83, v92\n"
    "v_mfma_f32_16x16x32_bf16 a[20:23], a[140:143], a[236:239], a[20:23]\n"
    "ds_read_b128 a[184:187], v101 offset:18432\n"
    "v_fma_f32 v73, v73, v83, v92\n"
    "v_mfma_f32_16x16x32_bf16 a[24:27], a[144:147], a[232:235], a[24:27]\n"
    "ds_read_b64_tr_b16 a[252:253], v15 offset:24576\n"
    "v_fma_f32 v74, v74, v83, v92\n"
    "v_mfma_f32_16x16x32_bf16 a[24:27], a[148:151], a[236:239], a[24:27]\n"
    "ds_read_b128 a[188:191], v101 offset:19456\n"
    "v_fma_f32 v75, v75, v83, v92\n"
    "v_mfma_f32_16x16x32_bf16 a[28:31], a[152:155], a[232:235], a[28:31]\n"
    "ds_read_b64_tr_b16 a[254:255], v15 offset:28672\n"
    "v_add_u32 v15, s76, v15\n"
    "v_mfma_f32_16x16x32_bf16 a[28:31], a[156:159], a[236:239], a[28:31]\n"
    "s_waitcnt lgkmcnt(7)\n"
    "v_fma_f32 v76, v76, v83, v92\n"
    "v_fma_f32 v77, v77, v83, v92\n"
    "v_mfma_f32_16x16x32_bf16 a[32:35], a[128:131], a[240:243], a[32:35]\n"
    "ds_read_b64_tr_b16 a[224:225], v16 offset:16384\n"
    "v_fma_f32 v78, v78, v83, v92\n"
    "v_mfma_f32_16x16x32_bf16 a[32:35], a[132:135], a[244:247], a[32:35]\n"
    "buffer_load_dwordx4 v99, s[40:43], s86 offen offset:2048 lds\n"
    "v_mfma_f32_16x16x32_bf16 a[36:39], a[136:139], a[240:243], a[36:39]\n"
    "ds_read_b64_tr_b16 a[226:227], v16 offset:20480\n"
    "v_fma_f32 v79, v79, v83, v92\n"
    "v_mfma_f32_16x16x32_bf16 a[36:39], a[140:143], a[244:247], a[36:39]\n"
    "v_cvt_pk_bf16_f32 v44, v72, v73\n"
    "v_mfma_f32_16x16x32_bf16 a[40:43], a[144:147], a[240:243], a[40:43]\n"
    "ds_read_b64_tr_b16 a[228:229], v16 offset:24576\n"
    "v_cvt_pk_bf16_f32 v45, v74, v75\n"
    "v_mfma_f32_16x16x32_bf16 a[40:43], a[148:151], a[244:247], a[40:43]\n"
    "v_mfma_f32_16x16x32_bf16 a[44:47], a[152:155], a[240:243], a[44:47]\n"
    "ds_read_b64_tr_b16 a[230:231], v16 offset:28672\n"
    "v_add_u32 v16, s76, v16\n"
    "v_mfma_f32_16x16x32_bf16 a[44:47], a[156:159], a[244:247], a[44:47]\n"
    "s_waitcnt lgkmcnt(4)\n"
    "v_cvt_pk_bf16_f32 v46, v76, v77\n"
    "v_cvt_pk_bf16_f32 v47, v78, v79\n"
    "v_mfma_f32_16x16x32_bf16 a[48:51], a[128:131], a[248:251], a[48:51]\n"
    "ds_read_b64_tr_b16 a[232:233], v17 offset:16384\n"
    "buffer_store_dwordx4 v[44:47], v96, s[44:47], s87 offen sc1\n"
    "v_mfma_f32_16x16x32_bf16 a[48:51], a[132:135], a[252:255], a[48:51]\n"
    "buffer_load_dwordx4 v100, s[40:43], s86 offen offset:3072 lds\n"
    "v_mfma_f32_16x16x32_bf16 a[52:55], a[136:139], a[248:251], a[52:55]\n"
    "ds_read_b64_tr_b16 a[234:235], v17 offset:20480\n"
    "v_mfma_f32_16x16x32_bf16 a[52:55], a[140:143], a[252:255], a[52:55]\n"
    "v_mfma_f32_16x16x32_bf16 a[56:59], a[144:147], a[248:251], a[56:59]\n"
    "ds_read_b64_tr_b16 a[236:237], v17 offset:24576\n"
    "v_mfma_f32_16x16x32_bf16 a[56:59], a[148:151], a[252:255], a[56:59]\n"
    "v_mfma_f32_16x16x32_bf16 a[60:63], a[152:155], a[248:251], a[60:63]\n"
    "ds_read_b64_tr_b16 a[238:239], v17 offset:28672\n"
    "v_add_u32 v17, s76, v17\n"
    "v_mfma_f32_16x16x32_bf16 a[60:63], a[156:159], a[252:255], a[60:63]\n"
    "s_waitcnt lgkmcnt(4)\n"
    "s_add_u32 m0, s74, s85\n"
    "v_mfma_f32_16x16x32_bf16 a[64:67], a[128:131], a[224:227], a[64:67]\n"
    "ds_read_b64_tr_b16 a[240:241], v18 offset:16384\n"
    "v_mfma_f32_16x16x32_bf16 a[64:67], a[132:135], a[228:231], a[64:67]\n"
    "buffer_load_dwordx4 v8, s[52:55], s73 offen lds\n"
    "v_mfma_f32_16x16x32_bf16 a[68:71], a[136:139], a[224:227], a[68:71]\n"
    "ds_read_b64_tr_b16 a[242:243], v18 offset:20480\n"
    "v_mfma_f32_16x16x32_bf16 a[68:71], a[140:143], a[228:231], a[68:71]\n"
    "v_mfma_f32_16x16x32_bf16 a[72:75], a[144:147], a[224:227], a[72:75]\n"
    "ds_read_b64_tr_b16 a[244:245], v18 offset:24576\n"
    "v_mfma_f32_16x16x32_bf16 a[72:75], a[148:151], a[228:231], a[72:75]\n"
    "v_mfma_f32_16x16x32_bf16 a[76:79], a[152:155], a[224:227], a[76:79]\n"
    "ds_read_b64_tr_b16 a[246:247], v18 offset:28672\n"
    "v_add_u32 v18, s76, v18\n"
    "v_mfma_f32_16x16x32_bf16 a[76:79], a[156:159], a[228:231], a[76:79]\n"
    "s_waitcnt lgkmcnt(4)\n"
    "v_mfma_f32_16x16x32_bf16 a[80:83], a[128:131], a[232:235], a[80:83]\n"
    "ds_read_b64_tr_b16 a[248:249], v19 offset:16384\n"
    "v_mfma_f32_16x16x32_bf16 a[80:83], a[132:135], a[236:239], a[80:83]\n"
    "buffer_load_dwordx4 v9, s[52:55], s73 offen offset:1024 lds\n"
    "v_mfma_f32_16x16x32_bf16 a[84:87], a[136:139], a[232:235], a[84:87]\n"
    "ds_read_b64_tr_b16 a[250:251], v19 offset:20480\n"
    "v_mfma_f32_16x16x32_bf16 a[84:87], a[140:143], a[236:239], a[84:87]\n"
    "v_mfma_f32_16x16x32_bf16 a[88:91], a[144:147], a[232:235], a[88:91]\n"
    "ds_read_b64_tr_b16 a[252:253], v19 offset:24576\n"
    "v_mfma_f32_16x16x32_bf16 a[88:91], a[148:151], a[236:239], a[88:91]\n"
    "v_mfma_f32_16x16x32_bf16 a[92:95], a[152:155], a[232:235], a[92:95]\n"
    "ds_read_b64_tr_b16 a[254:255], v19 offset:28672\n"
    "v_add_u32 v19, s76, v19\n"
    "v_mfma_f32_16x16x32_bf16 a[92:95], a[156:159], a[236:239], a[92:95]\n"
    "s_waitcnt lgkmcnt(4)\n"
    "s_barrier\n"
    "v_mfma_f32_16x16x32_bf16 a[96:99], a[128:131], a[240:243], a[96:99]\n"
    "ds_read_b64_tr_b16 a[224:225], v12\n"
    "v_mfma_f32_16x16x32_bf16 a[96:99], a[132:135], a[244:247], a[96:99]\n"
    "buffer_load_dwordx4 v10, s[52:55], s73 offen offset:2048 lds\n"
    "v_mfma_f32_16x16x32_bf16 a[100:103], a[136:139], a[240:243], a[100:103]\n"
    "ds_read_b64_tr_b16 a[226:227], v12 offset:4096\n"
    "v_mfma_f32_16x16x32_bf16 a[100:103], a[140:143], a[244:247], a[100:103]\n"
    "v_mfma_f32_16x16x32_bf16 a[104:107], a[144:147], a[240:243], a[104:107]\n"
    "ds_read_b64_tr_b16 a[228:229], v12 offset:8192\n"
    "v_mfma_f32_16x16x32_bf16 a[104:107], a[148:151], a[244:247], a[104:107]\n"
    "v_mfma_f32_16x16x32_bf16 a[108:111], a[152:155], a[240:243], a[108:111]\n"
    "ds_read_b64_tr_b16 a[230:231], v12 offset:12288\n"
    "v_mfma_f32_16x16x32_bf16 a[108:111], a[156:159], a[244:247], a[108:111]\n"
    "s_waitcnt lgkmcnt(4)\n"
    "v_mfma_f32_16x16x32_bf16 a[112:115], a[128:131], a[248:251], a[112:115]\n"
    "ds_read_b64_tr_b16 a[232:233], v13\n"
    "v_mfma_f32_16x16x32_bf16 a[112:115], a[132:135], a[252:255], a[112:115]\n"
    "buffer_load_dwordx4 v11, s[52:55], s73 offen offset:3072 lds\n"
    "v_mfma_f32_16x16x32_bf16 a[116:119], a[136:139], a[248:251], a[116:119]\n"
    "s_add_u32 s73, s73, 0x80000\n"
    "ds_read_b64_tr_b16 a[234:235], v13 offset:4096\n"
    "v_mfma_f32_16x16x32_bf16 a[116:119], a[140:143], a[252:255], a[116:119]\n"
    "s_and_b32 s73, s73, 0x3fffff\n"
    "s_add_u32 s74, s74, 0x8000\n"
    "v_mfma_f32_16x16x32_bf16 a[120:123], a[144:147], a[248:251], a[120:123]\n"
    "ds_read_b64_tr_b16 a[236:237], v13 offset:8192\n"
    "v_mfma_f32_16x16x32_bf16 a[120:123], a[148:151], a[252:255], a[120:123]\n"
    "s_cmp_lt_u32 s74, 0x28000\n"
    "s_cselect_b32 s74, s74, 0\n"
    "s_add_u32 s72, s72, 0x80\n"
    "v_mfma_f32_16x16x32_bf16 a[124:127], a[152:155], a[248:251], a[124:127]\n"
    "s_and_b32 s72, s72, 0x3ff\n"
    "ds_read_b64_tr_b16 a[238:239], v13 offset:12288\n"
    "v_mfma_f32_16x16x32_bf16 a[124:127], a[156:159], a[252:255], a[124:127]\n"
    "s_add_u32 s86, s72, 0x8000\n"
    "s_waitcnt lgkmcnt(4)\n"
    "v_mfma_f32_16x16x32_bf16 v[128:131], a[224:227], a[160:163], v[128:131]\n"
    "s_add_u32 m0, s84, 0x10000\n"
    "ds_read_b64_tr_b16 a[240:241], v14\n"
    "v_mfma_f32_16x16x32_bf16 v[128:131], a[228:231], a[164:167], v[128:131]\n"
    "buffer_load_dwordx4 v2, s[40:43], s72 offen lds\n"
    "v_mfma_f32_16x16x32_bf16 v[160:163], a[224:227], a[168:171], v[160:163]\n"
    "s_add_u32 s91, s84, 0x8000\n"
    "ds_read_b64_tr_b16 a[242:243], v14 offset:4096\n"
    "v_mfma_f32_16x16x32_bf16 v[160:163], a[228:231], a[172:175], v[160:163]\n"
    "v_add_u32 v101, s91, v22\n"
    "v_mfma_f32_16x16x32_bf16 v[192:195], a[224:227], a[176:179], v[192:195]\n"
    "s_waitcnt vmcnt(17)\n"
    "ds_read_b64_tr_b16 a[244:245], v14 offset:8192\n"
    "v_mfma_f32_16x16x32_bf16 v[192:195], a[228:231], a[180:183], v[192:195]\n"
    "v_lshrrev_b32 v30, 3, v1\n"
    "v_mfma_f32_16x16x32_bf16 v[224:227], a[224:227], a[184:187], v[224:227]\n"
    "ds_read_b64_tr_b16 a[246:247], v14 offset:12288\n"
    "v_lshlrev_b32 v30, 13, v30\n"
    "v_mfma_f32_16x16x32_bf16 v[224:227], a[228:231], a[188:191], v[224:227]\n"
    "s_waitcnt lgkmcnt(4)\n"
    "ds_read_b128 a[192:195], v101\n"
    "v_mfma_f32_16x16x32_bf16 v[132:135], a[232:235], a[160:163], v[132:135]\n"
    "ds_read_b64_tr_b16 a[248:249], v15\n"
    "v_and_b32 v29, 7, v1\n"
    "v_mfma_f32_16x16x32_bf16 v[132:135], a[236:239], a[164:167], v[132:135]\n"
    "buffer_load_dwordx4 v3, s[40:43], s72 offen offset:1024 lds\n"
    "v_mfma_f32_16x16x32_bf16 v[164:167], a[232:235], a[168:171], v[164:167]\n"
    "ds_read_b64_tr_b16 a[250:251], v15 offset:4096\n"
    "v_lshl_add_u32 v30, v29, 7, v30\n"
    "v_mfma_f32_16x16x32_bf16 v[164:167], a[236:239], a[172:175], v[164:167]\n"
    "ds_read_b128 a[196:199], v101 offset:1024\n"
    "v_mfma_f32_16x16x32_bf16 v[196:199], a[232:235], a[176:179], v[196:199]\n"
    "ds_read_b64_tr_b16 a[252:253], v15 offset:8192\n"
    "v_cmp_gt_u32 vcc, 32, v1\n"
    "v_mfma_f32_16x16x32_bf16 v[196:199], a[236:239], a[180:183], v[196:199]\n"
    "ds_read_b128 a[200:203], v101 offset:2048\n"
    "v_mfma_f32_16x16x32_bf16 v[228:231], a[232:235], a[184:187], v[228:231]\n"
    "ds_read_b64_tr_b16 a[254:255], v15 offset:12288\n"
    "v_mov_b32 v29, 0x7ffffff0\n"
    "v_mfma_f32_16x16x32_bf16 v[228:231], a[236:239], a[188:191], v[228:231]\n"
    "s_waitcnt lgkmcnt(7)\n"
    "ds_read_b128 a[204:207], v101 offset:3072\n"
    "v_mfma_f32_16x16x32_bf16 v[136:139], a[240:243], a[160:163], v[136:139]\n"
    "ds_read_b64_tr_b16 a[224:225], v16\n"
    "v_mov_b32 v26, 0\n"
    "v_mfma_f32_16x16x32_bf16 v[136:139], a[244:247], a[164:167], v[136:139]\n"
    "buffer_load_dwordx4 v99, s[40:43], s72 offen offset:2048 lds\n"
    "v_mfma_f32_16x16x32_bf16 v[168:171], a[240:243], a[168:171], v[168:171]\n"
    "ds_read_b64_tr_b16 a[226:227], v16 offset:4096\n"
    "v_cndmask_b32 v30, v29, v30, vcc\n"
    "v_mfma_f32_16x16x32_bf16 v[168:171], a[244:247], a[172:175], v[168:171]\n"
    "v_mfma_f32_16x16x32_bf16 v[200:203], a[240:243], a[176:179], v[200:203]\n"
    "s_lshr_b32 s81, s30, 3\n"
    "ds_read_b64_tr_b16 a[228:229], v16 offset:8192\n"
    "v_mfma_f32_16x16x32_bf16 v[200:203], a[244:247], a[180:183], v[200:203]\n"
    "v_mfma_f32_16x16x32_bf16 v[232:235], a[240:243], a[184:187], v[232:235]\n"
    "ds_read_b64_tr_b16 a[230:231], v16 offset:12288\n"
    "v_mfma_f32_16x16x32_bf16 v[232:235], a[244:247], a[188:191], v[232:235]\n"
    "s_waitcnt lgkmcnt(5)\n"
    "s_lshl_b32 s81, s81, 2\n"
    "v_mfma_f32_16x16x32_bf16 v[140:143], a[248:251], a[160:163], v[140:143]\n"
    "ds_read_b64_tr_b16 a[232:233], v17\n"
    "v_mfma_f32_16x16x32_bf16 v[140:143], a[252:255], a[164:167], v[140:143]\n"
    "buffer_load_dwordx4 v100, s[40:43], s72 offen offset:3072 lds\n"
    "v_mfma_f32_16x16x32_bf16 v[172:175], a[248:251], a[168:171], v[172:175]\n"
    "s_add_u32 s81, s81, s78\n"
    "ds_read_b64_tr_b16 a[234:235], v17 offset:4096\n"
    "v_mfma_f32_16x16x32_bf16 v[172:175], a[252:255], a[172:175], v[172:175]\n"
    "s_mul_i32 s81, s81, 0x8000\n"
    "v_mfma_f32_16x16x32_bf16 v[204:207], a[248:251], a[176:179], v[204:207]\n"
    "ds_read_b64_tr_b16 a[236:237], v17 offset:8192\n"
    "v_mfma_f32_16x16x32_bf16 v[204:207], a[252:255], a[180:183], v[204:207]\n"
    "s_lshl_b32 s87, s30, 4\n"
    "s_add_u32 s87, s87, s78\n"
    "s_and_b32 s82, s30, 7\n"
    "v_mfma_f32_16x16x32_bf16 v[236:239], a[248:251], a[184:187], v[236:239]\n"
    "s_add_u32 m0, s74, s84\n"
    "ds_read_b64_tr_b16 a[238:239], v17 offset:12288\n"
    "v_mfma_f32_16x16x32_bf16 v[236:239], a[252:255], a[188:191], v[236:239]\n"
    "s_lshl_b32 s87, s87, 3\n"
    "s_add_u32 s87, s87, 0x18020\n"
    "s_waitcnt lgkmcnt(4)\n"
    "v_mov_b32 v80, s94\n"
    "v_mov_b32 v81, s95\n"
    "v_mfma_f32_16x16x32_bf16 v[144:147], a[224:227], a[160:163], v[144:147]\n"
    "s_lshl_b32 s82, s82, 10\n"
    "ds_read_b64_tr_b16 a[240:241], v18\n"
    "v_mfma_f32_16x16x32_bf16 v[144:147], a[228:231], a[164:167], v[144:147]\n"
    "s_waitcnt vmcnt(9)\n"
    "s_mov_b64 exec, 1\n buffer_store_dwordx2 v[80:81], off, s[64:67], s87 offset:0 sc1\n s_mov_b64 exec, -1\n"
    "v_mfma_f32_16x16x32_bf16 v[176:179], a[224:227], a[168:171], v[176:179]\n"
    "s_add_u32 s81, s81, s82\n"
    "ds_read_b64_tr_b16 a[242:243], v18 offset:4096\n"
    "v_mfma_f32_16x16x32_bf16 v[176:179], a[228:231], a[172:175], v[176:179]\n"
    "buffer_load_dwordx4 v8, s[48:51], s73 offen lds\n"
    "v_mfma_f32_16x16x32_bf16 v[208:211], a[224:227], a[176:179], v[208:211]\n"
    "ds_read_b64_tr_b16 a[244:245], v18 offset:8192\n"
    "v_mfma_f32_16x16x32_bf16 v[208:211], a[228:231], a[180:183], v[208:211]\n"
    "buffer_load_dword v0, v30, s[68:71], s81 offen\n"
    "v_mfma_f32_16x16x32_bf16 v[240:243], a[224:227], a[184:187], v[240:243]\n"
    "s_add_u32 s81, s81, 0x400000\n"
    "ds_read_b64_tr_b16 a[246:247], v18 offset:12288\n"
    "v_mfma_f32_16x16x32_bf16 v[240:243], a[228:231], a[188:191], v[240:243]\n"
    "s_waitcnt lgkmcnt(4)\n"
    "v_mfma_f32_16x16x32_bf16 v[148:151], a[232:235], a[160:163], v[148:151]\n"
    "ds_read_b64_tr_b16 a[248:249], v19\n"
    "v_mfma_f32_16x16x32_bf16 v[148:151], a[236:239], a[164:167], v[148:151]\n"
    "buffer_load_dwordx4 v9, s[48:51], s73 offen offset:1024 lds\n"
    "v_mfma_f32_16x16x32_bf16 v[180:183], a[232:235], a[168:171], v[180:183]\n"
    "ds_read_b64_tr_b16 a[250:251], v19 offset:4096\n"
    "v_mfma_f32_16x16x32_bf16 v[180:183], a[236:239], a[172:175], v[180:183]\n"
    "v_mfma_f32_16x16x32_bf16 v[212:215], a[232:235], a[176:179], v[212:215]\n"
    "ds_read_b64_tr_b16 a[252:253], v19 offset:8192\n"
    "v_mfma_f32_16x16x32_bf16 v[212:215], a[236:239], a[180:183], v[212:215]\n"
    "v_mfma_f32_16x16x32_bf16 v[244:247], a[232:235], a[184:187], v[244:247]\n"
    "ds_read_b64_tr_b16 a[254:255], v19 offset:12288\n"
    "v_mfma_f32_16x16x32_bf16 v[244:247], a[236:239], a[188:191], v[244:247]\n"
    "s_waitcnt lgkmcnt(4)\n"
    "v_mfma_f32_16x16x32_bf16 v[152:155], a[240:243], a[160:163], v[152:155]\n"
    "ds_read_b64_tr_b16 a[224:225], v12 offset:16384\n"
    "v_mfma_f32_16x16x32_bf16 v[152:155], a[244:247], a[164:167], v[152:155]\n"
    "buffer_load_dwordx4 v10, s[48:51], s73 offen offset:2048 lds\n"
    "v_mfma_f32_16x16x32_bf16 v[184:187], a[240:243], a[168:171], v[184:187]\n"
    "s_add_u32 s75, s75, 1\n"
    "ds_read_b64_tr_b16 a[226:227], v12 offset:20480\n"
    "v_mfma_f32_16x16x32_bf16 v[184:187], a[244:247], a[172:175], v[184:187]\n"
    "v_mfma_f32_16x16x32_bf16 v[216:219], a[240:243], a[176:179], v[216:219]\n"
    "ds_read_b64_tr_b16 a[228:229], v12 offset:24576\n"
    "v_mfma_f32_16x16x32_bf16 v[216:219], a[244:247], a[180:183], v[216:219]\n"
    "s_cmp_lt_u32 s75, 5\n"
    "s_cselect_b32 s75, s75, 0\n"
    "s_cselect_b32 s76, 0x8000, s77\n"
    "v_mfma_f32_16x16x32_bf16 v[248:251], a[240:243], a[184:187], v[248:251]\n"
    "ds_read_b64_tr_b16 a[230:231], v12 offset:28672\n"
    "v_add_u32 v12, s76, v12\n"
    "v_mfma_f32_16x16x32_bf16 v[248:251], a[244:247], a[188:191], v[248:251]\n"
    "s_waitcnt lgkmcnt(4)\n"
    "v_mfma_f32_16x16x32_bf16 v[156:159], a[248:251], a[160:163], v[156:159]\n"
    "ds_read_b64_tr_b16 a[232:233], v13 offset:16384\n"
    "v_mfma_f32_16x16x32_bf16 v[156:159], a[252:255], a[164:167], v[156:159]\n"
    "buffer_load_dwordx4 v11, s[48:51], s73 offen offset:3072 lds\n"
    "v_mfma_f32_16x16x32_bf16 v[188:191], a[248:251], a[168:171], v[188:191]\n"
    "ds_read_b64_tr_b16 a[234:235], v13 offset:20480\n"
    "v_mfma_f32_16x16x32_bf16 v[188:191], a[252:255], a[172:175], v[188:191]\n"
    "v_mfma_f32_16x16x32_bf16 v[220:223], a[248:251], a[176:179], v[220:223]\n"
    "s_lshl_b32 s87, s80, 4\n"
    "ds_read_b64_tr_b16 a[236:237], v13 offset:24576\n"
    "v_mfma_f32_16x16x32_bf16 v[220:223], a[252:255], a[180:183], v[220:223]\n"
    "v_mfma_f32_16x16x32_bf16 v[252:255], a[248:251], a[184:187], v[252:255]\n"
    "ds_read_b64_tr_b16 a[238:239], v13 offset:28672\n"
    "v_add_u32 v13, s76, v13\n"
    "v_mfma_f32_16x16x32_bf16 v[252:255], a[252:255], a[188:191], v[252:255]\n"
    "s_waitcnt lgkmcnt(4)\n"
    "s_add_u32 m0, s85, 0x10000\n"
    "s_sub_u32 s90, 0xe0, s87\n"
    "v_mfma_f32_16x16x32_bf16 a[0:3], a[160:163], a[224:227], a[0:3]\n"
    "ds_read_b64_tr_b16 a[240:241], v14 offset:16384\n"
    "v_mfma_f32_16x16x32_bf16 a[0:3], a[164:167], a[228:231], a[0:3]\n"
    "buffer_load_dwordx4 v2, s[40:43], s86 offen lds\n"
    "v_mfma_f32_16x16x32_bf16 a[4:7], a[168:171], a[224:227], a[4:7]\n"
    "s_waitcnt vmcnt(15)\n"
    "ds_read_b64_tr_b16 a[242:243], v14 offset:20480\n"
    "v_mfma_f32_16x16x32_bf16 a[4:7], a[172:175], a[228:231], a[4:7]\n"
    "v_lshrrev_b32 v82, 1, v1\n"
    "v_mfma_f32_16x16x32_bf16 a[8:11], a[176:179], a[224:227], a[8:11]\n"
    "ds_read_b64_tr_b16 a[244:245], v14 offset:24576\n"
    "v_mfma_f32_16x16x32_bf16 a[8:11], a[180:183], a[228:231], a[8:11]\n"
    "ds_read_b128 a[208:211], v101 offset:16384\n"
    "v_mfma_f32_16x16x32_bf16 a[12:15], a[184:187], a[224:227], a[12:15]\n"
    "ds_read_b64_tr_b16 a[246:247], v14 offset:28672\n"
    "v_add_u32 v14, s76, v14\n"
    "v_mfma_f32_16x16x32_bf16 a[12:15], a[188:191], a[228:231], a[12:15]\n"
    "s_waitcnt lgkmcnt(5)\n"
    "ds_read_b128 a[212:215], v101 offset:17408\n"
    "v_mfma_f32_16x16x32_bf16 a[16:19], a[160:163], a[232:235], a[16:19]\n"
    "ds_read_b64_tr_b16 a[248:249], v15 offset:16384\n"
    "v_add_u32 v83, s87, v82\n"
    "v_mfma_f32_16x16x32_bf16 a[16:19], a[164:167], a[236:239], a[16:19]\n"
    "buffer_load_dwordx4 v3, s[40:43], s86 offen offset:1024 lds\n"
    "v_mfma_f32_16x16x32_bf16 a[20:23], a[168:171], a[232:235], a[20:23]\n"
    "ds_read_b64_tr_b16 a[250:251], v15 offset:20480\n"
    "v_add_u32 v84, s90, v82\n"
    "v_mfma_f32_16x16x32_bf16 a[20:23], a[172:175], a[236:239], a[20:23]\n"
    "ds_read_b128 a[216:219], v101 offset:18432\n"
    "v_mfma_f32_16x16x32_bf16 a[24:27], a[176:179], a[232:235], a[24:27]\n"
    "ds_read_b64_tr_b16 a[252:253], v15 offset:24576\n"
    "v_mfma_f32_16x16x32_bf16 a[24:27], a[180:183], a[236:239], a[24:27]\n"
    "ds_read_b128 a[220:223], v101 offset:19456\n"
    "v_mfma_f32_16x16x32_bf16 a[28:31], a[184:187], a[232:235], a[28:31]\n"
    "ds_read_b64_tr_b16 a[254:255], v15 offset:28672\n"
    "v_add_u32 v15, s76, v15\n"
    "v_mfma_f32_16x16x32_bf16 a[28:31], a[188:191], a[236:239], a[28:31]\n"
    "s_waitcnt lgkmcnt(7)\n"
    "v_cmp_gt_u32 vcc, 32, v1\n"
    "v_mfma_f32_16x16x32_bf16 a[32:35], a[160:163], a[240:243], a[32:35]\n"
    "ds_read_b64_tr_b16 a[224:225], v16 offset:16384\n"
    "v_mfma_f32_16x16x32_bf16 a[32:35], a[164:167], a[244:247], a[32:35]\n"
    "buffer_load_dwordx4 v99, s[40:43], s86 offen offset:2048 lds\n"
    "v_mfma_f32_16x16x32_bf16 a[36:39], a[168:171], a[240:243], a[36:39]\n"
    "s_mov_b32 s87, 0x18020\n"
    "ds_read_b64_tr_b16 a[226:227], v16 offset:20480\n"
    "v_mfma_f32_16x16x32_bf16 a[36:39], a[172:175], a[244:247], a[36:39]\n"
    "v_mfma_f32_16x16x32_bf16 a[40:43], a[176:179], a[240:243], a[40:43]\n"
    "ds_read_b64_tr_b16 a[228:229], v16 offset:24576\n"
    "v_mfma_f32_16x16x32_bf16 a[40:43], a[180:183], a[244:247], a[40:43]\n"
    "v_and_b32 v82, 1, v1\n"
    "v_mfma_f32_16x16x32_bf16 a[44:47], a[184:187], a[240:243], a[44:47]\n"
    "ds_read_b64_tr_b16 a[230:231], v16 offset:28672\n"
    "v_add_u32 v16, s76, v16\n"
    "v_mfma_f32_16x16x32_bf16 a[44:47], a[188:191], a[244:247], a[44:47]\n"
    "s_waitcnt lgkmcnt(4)\n"
    "v_mfma_f32_16x16x32_bf16 a[48:51], a[160:163], a[248:251], a[48:51]\n"
    "ds_read_b64_tr_b16 a[232:233], v17 offset:16384\n"
    "v_mfma_f32_16x16x32_bf16 a[48:51], a[164:167], a[252:255], a[48:51]\n"
    "buffer_load_dwordx4 v100, s[40:43], s86 offen offset:3072 lds\n"
    "v_mfma_f32_16x16x32_bf16 a[52:55], a[168:171], a[248:251], a[52:55]\n"
    "ds_read_b64_tr_b16 a[234:235], v17 offset:20480\n"
    "v_lshlrev_b32 v82, 4, v82\n"
    "v_mfma_f32_16x16x32_bf16 a[52:55], a[172:175], a[252:255], a[52:55]\n"
    "v_mfma_f32_16x16x32_bf16 a[56:59], a[176:179], a[248:251], a[56:59]\n"
    "ds_read_b64_tr_b16 a[236:237], v17 offset:24576\n"
    "v_cndmask_b32 v83, v84, v83, vcc\n"
    "v_mfma_f32_16x16x32_bf16 a[56:59], a[180:183], a[252:255], a[56:59]\n"
    "v_mfma_f32_16x16x32_bf16 a[60:63], a[184:187], a[248:251], a[60:63]\n"
    "ds_read_b64_tr_b16 a[238:239], v17 offset:28672\n"
    "v_add_u32 v17, s76, v17\n"
    "v_mfma_f32_16x16x32_bf16 a[60:63], a[188:191], a[252:255], a[60:63]\n"
    "s_waitcnt lgkmcnt(4)\n"
    "s_add_u32 m0, s74, s85\n"
    "v_mfma_f32_16x16x32_bf16 a[64:67], a[160:163], a[224:227], a[64:67]\n"
    "ds_read_b64_tr_b16 a[240:241], v18 offset:16384\n"
    "v_lshl_add_u32 v23, v83, 7, v82\n"
    "v_mfma_f32_16x16x32_bf16 a[64:67], a[164:167], a[228:231], a[64:67]\n"
    "buffer_load_dwordx4 v8, s[52:55], s73 offen lds\n"
    "v_mfma_f32_16x16x32_bf16 a[68:71], a[168:171], a[224:227], a[68:71]\n"
    "ds_read_b64_tr_b16 a[242:243], v18 offset:20480\n"
    "v_mfma_f32_16x16x32_bf16 a[68:71], a[172:175], a[228:231], a[68:71]\n"
    "v_mfma_f32_16x16x32_bf16 a[72:75], a[176:179], a[224:227], a[72:75]\n"
    "ds_read_b64_tr_b16 a[244:245], v18 offset:24576\n"
    "v_mfma_f32_16x16x32_bf16 a[72:75], a[180:183], a[228:231], a[72:75]\n"
    "v_mfma_f32_16x16x32_bf16 a[76:79], a[184:187], a[224:227], a[76:79]\n"
    "ds_read_b64_tr_b16 a[246:247], v18 offset:28672\n"
    "v_add_u32 v18, s76, v18\n"
    "v_mfma_f32_16x16x32_bf16 a[76:79], a[188:191], a[228:231], a[76:79]\n"
    "s_waitcnt lgkmcnt(4)\n"
    "v_mfma_f32_16x16x32_bf16 a[80:83], a[160:163], a[232:235], a[80:83]\n"
    "ds_read_b64_tr_b16 a[248:249], v19 offset:16384\n"
    "v_mfma_f32_16x16x32_bf16 a[80:83], a[164:167], a[236:239], a[80:83]\n"
    "buffer_load_dwordx4 v9, s[52:55], s73 offen offset:1024 lds\n"
    "v_mfma_f32_16x16x32_bf16 a[84:87], a[168:171], a[232:235], a[84:87]\n"
    "ds_read_b64_tr_b16 a[250:251], v19 offset:20480\n"
    "v_mfma_f32_16x16x32_bf16 a[84:87], a[172:175], a[236:239], a[84:87]\n"
    "v_mfma_f32_16x16x32_bf16 a[88:91], a[176:179], a[232:235], a[88:91]\n"
    "ds_read_b64_tr_b16 a[252:253], v19 offset:24576\n"
    "v_mfma_f32_16x16x32_bf16 a[88:91], a[180:183], a[236:239], a[88:91]\n"
    "v_mfma_f32_16x16x32_bf16 a[92:95], a[184:187], a[232:235], a[92:95]\n"
    "ds_read_b64_tr_b16 a[254:255], v19 offset:28672\n"
    "v_add_u32 v19, s76, v19\n"
    "v_mfma_f32_16x16x32_bf16 a[92:95], a[188:191], a[236:239], a[92:95]\n"
    "s_waitcnt vmcnt(16) lgkmcnt(4)\n"
    "s_barrier\n"
    "v_mfma_f32_16x16x32_bf16 a[96:99], a[160:163], a[240:243], a[96:99]\n"
    "ds_read_b64_tr_b16 a[224:225], v12\n"
    "v_mfma_f32_16x16x32_bf16 a[96:99], a[164:167], a[244:247], a[96:99]\n"
    "buffer_load_dwordx4 v10, s[52:55], s73 offen offset:2048 lds\n"
    "v_mfma_f32_16x16x32_bf16 a[100:103], a[168:171], a[240:243], a[100:103]\n"
    "ds_read_b64_tr_b16 a[226:227], v12 offset:4096\n"
    "v_mfma_f32_16x16x32_bf16 a[100:103], a[172:175], a[244:247], a[100:103]\n"
    "v_mfma_f32_16x16x32_bf16 a[104:107], a[176:179], a[240:243], a[104:107]\n"
    "ds_read_b64_tr_b16 a[228:229], v12 offset:8192\n"
    "v_mfma_f32_16x16x32_bf16 a[104:107], a[180:183], a[244:247], a[104:107]\n"
    "v_mfma_f32_16x16x32_bf16 a[108:111], a[184:187], a[240:243], a[108:111]\n"
    "ds_read_b64_tr_b16 a[230:231], v12 offset:12288\n"
    "v_mfma_f32_16x16x32_bf16 a[108:111], a[188:191], a[244:247], a[108:111]\n"
    "s_waitcnt lgkmcnt(4)\n"
    "v_mfma_f32_16x16x32_bf16 a[112:115], a[160:163], a[248:251], a[112:115]\n"
    "ds_read_b64_tr_b16 a[232:233], v13\n"
    "v_mfma_f32_16x16x32_bf16 a[112:115], a[164:167], a[252:255], a[112:115]\n"
    "buffer_load_dwordx4 v11, s[52:55], s73 offen offset:3072 lds\n"
    "v_mfma_f32_16x16x32_bf16 a[116:119], a[168:171], a[248:251], a[116:119]\n"
    "s_add_u32 s73, s73, 0x80000\n"
    "ds_read_b64_tr_b16 a[234:235], v13 offset:4096\n"
    "v_mfma_f32_16x16x32_bf16 a[116:119], a[172:175], a[252:255], a[116:119]\n"
    "s_and_b32 s73, s73, 0x3fffff\n"
    "s_add_u32 s74, s74, 0x8000\n"
    "v_mfma_f32_16x16x32_bf16 a[120:123], a[176:179], a[248:251], a[120:123]\n"
    "ds_read_b64_tr_b16 a[236:237], v13 offset:8192\n"
    "v_mfma_f32_16x16x32_bf16 a[120:123], a[180:183], a[252:255], a[120:123]\n"
    "s_cmp_lt_u32 s74, 0x28000\n"
    "s_cselect_b32 s74, s74, 0\n"
    "s_add_u32 s72, s72, 0x80\n"
    "v_mfma_f32_16x16x32_bf16 a[124:127], a[184:187], a[248:251], a[124:127]\n"
    "s_and_b32 s72, s72, 0x3ff\n"
    "ds_read_b64_tr_b16 a[238:239], v13 offset:12288\n"
    "v_mfma_f32_16x16x32_bf16 a[124:127], a[188:191], a[252:255], a[124:127]\n"
    "s_add_u32 s86, s72, 0x8000\n"
    "s_waitcnt lgkmcnt(4)\n"
    "v_mfma_f32_16x16x32_bf16 v[128:131], a[224:227], a[192:195], v[128:131]\n"
    "s_lshl1_add_u32 m0, s84, 0x18000\n"
    "ds_read_b64_tr_b16 a[240:241], v14\n"
    "v_mfma_f32_16x16x32_bf16 v[128:131], a[228:231], a[196:199], v[128:131]\n"
    "buffer_load_dwordx4 v2, s[40:43], s72 offen lds\n"
    "v_mfma_f32_16x16x32_bf16 v[160:163], a[224:227], a[200:203], v[160:163]\n"
    "s_add_u32 s91, s84, 0x10000\n"
    "ds_read_b64_tr_b16 a[242:243], v14 offset:4096\n"
    "v_mfma_f32_16x16x32_bf16 v[160:163], a[228:231], a[204:207], v[160:163]\n"
    "v_add_u32 v101, s91, v22\n"
    "v_mfma_f32_16x16x32_bf16 v[192:195], a[224:227], a[208:211], v[192:195]\n"
    "s_waitcnt vmcnt(15)\n"
    "ds_read_b64_tr_b16 a[244:245], v14 offset:8192\n"
    "v_mfma_f32_16x16x32_bf16 v[192:195], a[228:231], a[212:215], v[192:195]\n"
    "v_mfma_f32_16x16x32_bf16 v[224:227], a[224:227], a[216:219], v[224:227]\n"
    "ds_read_b64_tr_b16 a[246:247], v14 offset:12288\n"
    "v_mfma_f32_16x16x32_bf16 v[224:227], a[228:231], a[220:223], v[224:227]\n"
    "s_waitcnt lgkmcnt(4)\n"
    "ds_read_b128 a[128:131], v101\n"
    "v_mfma_f32_16x16x32_bf16 v[132:135], a[232:235], a[192:195], v[132:135]\n"
    "ds_read_b64_tr_b16 a[248:249], v15\n"
    "v_mfma_f32_16x16x32_bf16 v[132:135], a[236:239], a[196:199], v[132:135]\n"
    "buffer_load_dwordx4 v3, s[40:43], s72 offen offset:1024 lds\n"
    "v_mfma_f32_16x16x32_bf16 v[164:167], a[232:235], a[200:203], v[164:167]\n"
    "ds_read_b64_tr_b16 a[250:251], v15 offset:4096\n"
    "v_mfma_f32_16x16x32_bf16 v[164:167], a[236:239], a[204:207], v[164:167]\n"
    "ds_read_b128 a[132:135], v101 offset:1024\n"
    "v_mfma_f32_16x16x32_bf16 v[196:199], a[232:235], a[208:211], v[196:199]\n"
    "ds_read_b64_tr_b16 a[252:253], v15 offset:8192\n"
    "v_mfma_f32_16x16x32_bf16 v[196:199], a[236:239], a[212:215], v[196:199]\n"
    "ds_read_b128 a[136:139], v101 offset:2048\n"
    "v_mfma_f32_16x16x32_bf16 v[228:231], a[232:235], a[216:219], v[228:231]\n"
    "ds_read_b64_tr_b16 a[254:255], v15 offset:12288\n"
    "v_mfma_f32_16x16x32_bf16 v[228:231], a[236:239], a[220:223], v[228:231]\n"
    "s_waitcnt lgkmcnt(7)\n"
    "ds_read_b128 a[140:143], v101 offset:3072\n"
    "v_mfma_f32_16x16x32_bf16 v[136:139], a[240:243], a[192:195], v[136:139]\n"
    "ds_read_b64_tr_b16 a[224:225], v16\n"
    "v_mfma_f32_16x16x32_bf16 v[136:139], a[244:247], a[196:199], v[136:139]\n"
    "buffer_load_dwordx4 v99, s[40:43], s72 offen offset:2048 lds\n"
    "v_mfma_f32_16x16x32_bf16 v[168:171], a[240:243], a[200:203], v[168:171]\n"
    "ds_read_b64_tr_b16 a[226:227], v16 offset:4096\n"
    "v_mfma_f32_16x16x32_bf16 v[168:171], a[244:247], a[204:207], v[168:171]\n"
    "v_mfma_f32_16x16x32_bf16 v[200:203], a[240:243], a[208:211], v[200:203]\n"
    "ds_read_b64_tr_b16 a[228:229], v16 offset:8192\n"
    "v_mfma_f32_16x16x32_bf16 v[200:203], a[244:247], a[212:215], v[200:203]\n"
    "v_mfma_f32_16x16x32_bf16 v[232:235], a[240:243], a[216:219], v[232:235]\n"
    "ds_read_b64_tr_b16 a[230:231], v16 offset:12288\n"
    "v_mfma_f32_16x16x32_bf16 v[232:235], a[244:247], a[220:223], v[232:235]\n"
    "s_waitcnt lgkmcnt(5)\n"
    "v_mfma_f32_16x16x32_bf16 v[140:143], a[248:251], a[192:195], v[140:143]\n"
    "ds_read_b64_tr_b16 a[232:233], v17\n"
    "v_mfma_f32_16x16x32_bf16 v[140:143], a[252:255], a[196:199], v[140:143]\n"
    "buffer_load_dwordx4 v100, s[40:43], s72 offen offset:3072 lds\n"
    "v_mfma_f32_16x16x32_bf16 v[172:175], a[248:251], a[200:203], v[172:175]\n"
    "ds_read_b64_tr_b16 a[234:235], v17 offset:4096\n"
    "v_mfma_f32_16x16x32_bf16 v[172:175], a[252:255], a[204:207], v[172:175]\n"
    "v_mfma_f32_16x16x32_bf16 v[204:207], a[248:251], a[208:211], v[204:207]\n"
    "ds_read_b64_tr_b16 a[236:237], v17 offset:8192\n"
    "v_mfma_f32_16x16x32_bf16 v[204:207], a[252:255], a[212:215], v[204:207]\n"
    "v_mfma_f32_16x16x32_bf16 v[236:239], a[248:251], a[216:219], v[236:239]\n"
    "ds_read_b64_tr_b16 a[238:239], v17 offset:12288\n"
    "v_mfma_f32_16x16x32_bf16 v[236:239], a[252:255], a[220:223], v[236:239]\n"
    "s_waitcnt lgkmcnt(4)\n"
    "s_add_u32 m0, s74, s84\n"
    "v_mfma_f32_16x16x32_bf16 v[144:147], a[224:227], a[192:195], v[144:147]\n"
    "ds_read_b64_tr_b16 a[240:241], v18\n"
    "v_mfma_f32_16x16x32_bf16 v[144:147], a[228:231], a[196:199], v[144:147]\n"
    "buffer_load_dwordx4 v8, s[48:51], s73 offen lds\n"
    "v_mfma_f32_16x16x32_bf16 v[176:179], a[224:227], a[200:203], v[176:179]\n"
    "ds_read_b64_tr_b16 a[242:243], v18 offset:4096\n"
    "v_mfma_f32_16x16x32_bf16 v[176:179], a[228:231], a[204:207], v[176:179]\n"
    "v_mfma_f32_16x16x32_bf16 v[208:211], a[224:227], a[208:211], v[208:211]\n"
    "ds_read_b64_tr_b16 a[244:245], v18 offset:8192\n"
    "v_mfma_f32_16x16x32_bf16 v[208:211], a[228:231], a[212:215], v[208:211]\n"
    "v_mfma_f32_16x16x32_bf16 v[240:243], a[224:227], a[216:219], v[240:243]\n"
    "ds_read_b64_tr_b16 a[246:247], v18 offset:12288\n"
    "v_mfma_f32_16x16x32_bf16 v[240:243], a[228:231], a[220:223], v[240:243]\n"
    "s_waitcnt lgkmcnt(4)\n"
    "v_mfma_f32_16x16x32_bf16 v[148:151], a[232:235], a[192:195], v[148:151]\n"
    "ds_read_b64_tr_b16 a[248:249], v19\n"
    "v_mfma_f32_16x16x32_bf16 v[148:151], a[236:239], a[196:199], v[148:151]\n"
    "buffer_load_dwordx4 v9, s[48:51], s73 offen offset:1024 lds\n"
    "v_mfma_f32_16x16x32_bf16 v[180:183], a[232:235], a[200:203], v[180:183]\n"
    "ds_read_b64_tr_b16 a[250:251], v19 offset:4096\n"
    "v_mfma_f32_16x16x32_bf16 v[180:183], a[236:239], a[204:207], v[180:183]\n"
    "v_mfma_f32_16x16x32_bf16 v[212:215], a[232:235], a[208:211], v[212:215]\n"
    "ds_read_b64_tr_b16 a[252:253], v19 offset:8192\n"
    "v_mfma_f32_16x16x32_bf16 v[212:215], a[236:239], a[212:215], v[212:215]\n"
    "v_mfma_f32_16x16x32_bf16 v[244:247], a[232:235], a[216:219], v[244:247]\n"
    "ds_read_b64_tr_b16 a[254:255], v19 offset:12288\n"
    "v_mfma_f32_16x16x32_bf16 v[244:247], a[236:239], a[220:223], v[244:247]\n"
    "s_waitcnt lgkmcnt(4)\n"
    "v_mfma_f32_16x16x32_bf16 v[152:155], a[240:243], a[192:195], v[152:155]\n"
    "ds_read_b64_tr_b16 a[224:225], v12 offset:16384\n"
    "v_mfma_f32_16x16x32_bf16 v[152:155], a[244:247], a[196:199], v[152:155]\n"
    "buffer_load_dwordx4 v[80:83], v23, s[64:67], s87 offen sc1\n"
    "v_mfma_f32_16x16x32_bf16 v[184:187], a[240:243], a[200:203], v[184:187]\n"
    "s_add_u32 s75, s75, 1\n"
    "ds_read_b64_tr_b16 a[226:227], v12 offset:20480\n"
    "v_mfma_f32_16x16x32_bf16 v[184:187], a[244:247], a[204:207], v[184:187]\n"
    "buffer_load_dwordx4 v10, s[48:51], s73 offen offset:2048 lds\n"
    "v_mfma_f32_16x16x32_bf16 v[216:219], a[240:243], a[208:211], v[216:219]\n"
    "ds_read_b64_tr_b16 a[228:229], v12 offset:24576\n"
    "v_mfma_f32_16x16x32_bf16 v[216:219], a[244:247], a[212:215], v[216:219]\n"
    "s_cmp_lt_u32 s75, 5\n"
    "s_cselect_b32 s75, s75, 0\n"
    "s_cselect_b32 s76, 0x8000, s77\n"
    "v_mfma_f32_16x16x32_bf16 v[248:251], a[240:243], a[216:219], v[248:251]\n"
    "ds_read_b64_tr_b16 a[230:231], v12 offset:28672\n"
    "v_add_u32 v12, s76, v12\n"
    "v_mfma_f32_16x16x32_bf16 v[248:251], a[244:247], a[220:223], v[248:251]\n"
    "s_waitcnt lgkmcnt(4)\n"
    "v_mfma_f32_16x16x32_bf16 v[156:159], a[248:251], a[192:195], v[156:159]\n"
    "ds_read_b64_tr_b16 a[232:233], v13 offset:16384\n"
    "v_mfma_f32_16x16x32_bf16 v[156:159], a[252:255], a[196:199], v[156:159]\n"
    "buffer_load_dwordx4 v11, s[48:51], s73 offen offset:3072 lds\n"
    "v_mfma_f32_16x16x32_bf16 v[188:191], a[248:251], a[200:203], v[188:191]\n"
    "ds_read_b64_tr_b16 a[234:235], v13 offset:20480\n"
    "v_mfma_f32_16x16x32_bf16 v[188:191], a[252:255], a[204:207], v[188:191]\n"
    "v_mfma_f32_16x16x32_bf16 v[220:223], a[248:251], a[208:211], v[220:223]\n"
    "ds_read_b64_tr_b16 a[236:237], v13 offset:24576\n"
    "v_mfma_f32_16x16x32_bf16 v[220:223], a[252:255], a[212:215], v[220:223]\n"
    "v_mfma_f32_16x16x32_bf16 v[252:255], a[248:251], a[216:219], v[252:255]\n"
    "ds_read_b64_tr_b16 a[238:239], v13 offset:28672\n"
    "v_add_u32 v13, s76, v13\n"
    "v_mfma_f32_16x16x32_bf16 v[252:255], a[252:255], a[220:223], v[252:255]\n"
    "s_waitcnt lgkmcnt(4)\n"
    "s_lshl1_add_u32 m0, s84, 0x19000\n"
    "v_mfma_f32_16x16x32_bf16 a[0:3], a[192:195], a[224:227], a[0:3]\n"
    "ds_read_b64_tr_b16 a[240:241], v14 offset:16384\n"
    "v_mfma_f32_16x16x32_bf16 a[0:3], a[196:199], a[228:231], a[0:3]\n"
    "buffer_load_dwordx4 v2, s[40:43], s86 offen lds\n"
    "v_mfma_f32_16x16x32_bf16 a[4:7], a[200:203], a[224:227], a[4:7]\n"
    "s_waitcnt vmcnt(14)\n"
    "ds_read_b64_tr_b16 a[242:243], v14 offset:20480\n"
    "v_mfma_f32_16x16x32_bf16 a[4:7], a[204:207], a[228:231], a[4:7]\n"
    "v_mfma_f32_16x16x32_bf16 a[8:11], a[208:211], a[224:227], a[8:11]\n"
    "ds_read_b64_tr_b16 a[244:245], v14 offset:24576\n"
    "v_mfma_f32_16x16x32_bf16 a[8:11], a[212:215], a[228:231], a[8:11]\n"
    "ds_read_b128 a[144:147], v101 offset:16384\n"
    "v_mfma_f32_16x16x32_bf16 a[12:15], a[216:219], a[224:227], a[12:15]\n"
    "ds_read_b64_tr_b16 a[246:247], v14 offset:28672\n"
    "v_add_u32 v14, s76, v14\n"
    "v_mfma_f32_16x16x32_bf16 a[12:15], a[220:223], a[228:231], a[12:15]\n"
    "s_waitcnt lgkmcnt(5)\n"
    "ds_read_b128 a[148:151], v101 offset:17408\n"
    "v_mfma_f32_16x16x32_bf16 a[16:19], a[192:195], a[232:235], a[16:19]\n"
    "ds_read_b64_tr_b16 a[248:249], v15 offset:16384\n"
    "v_mfma_f32_16x16x32_bf16 a[16:19], a[196:199], a[236:239], a[16:19]\n"
    "buffer_load_dwordx4 v3, s[40:43], s86 offen offset:1024 lds\n"
    "v_mfma_f32_16x16x32_bf16 a[20:23], a[200:203], a[232:235], a[20:23]\n"
    "ds_read_b64_tr_b16 a[250:251], v15 offset:20480\n"
    "v_mfma_f32_16x16x32_bf16 a[20:23], a[204:207], a[236:239], a[20:23]\n"
    "ds_read_b128 a[152:155], v101 offset:18432\n"
    "v_mfma_f32_16x16x32_bf16 a[24:27], a[208:211], a[232:235], a[24:27]\n"
    "ds_read_b64_tr_b16 a[252:253], v15 offset:24576\n"
    "v_mfma_f32_16x16x32_bf16 a[24:27], a[212:215], a[236:239], a[24:27]\n"
    "ds_read_b128 a[156:159], v101 offset:19456\n"
    "v_mfma_f32_16x16x32_bf16 a[28:31], a[216:219], a[232:235], a[28:31]\n"
    "ds_read_b64_tr_b16 a[254:255], v15 offset:28672\n"
    "v_add_u32 v15, s76, v15\n"
    "v_mfma_f32_16x16x32_bf16 a[28:31], a[220:223], a[236:239], a[28:31]\n"
    "s_waitcnt lgkmcnt(7)\n"
    "v_mfma_f32_16x16x32_bf16 a[32:35], a[192:195], a[240:243], a[32:35]\n"
    "ds_read_b64_tr_b16 a[224:225], v16 offset:16384\n"
    "v_mfma_f32_16x16x32_bf16 a[32:35], a[196:199], a[244:247], a[32:35]\n"
    "buffer_load_dwordx4 v99, s[40:43], s86 offen offset:2048 lds\n"
    "v_mfma_f32_16x16x32_bf16 a[36:39], a[200:203], a[240:243], a[36:39]\n"
    "ds_read_b64_tr_b16 a[226:227], v16 offset:20480\n"
    "v_mfma_f32_16x16x32_bf16 a[36:39], a[204:207], a[244:247], a[36:39]\n"
    "v_mfma_f32_16x16x32_bf16 a[40:43], a[208:211], a[240:243], a[40:43]\n"
    "ds_read_b64_tr_b16 a[228:229], v16 offset:24576\n"
    "v_mfma_f32_16x16x32_bf16 a[40:43], a[212:215], a[244:247], a[40:43]\n"
    "v_mfma_f32_16x16x32_bf16 a[44:47], a[216:219], a[240:243], a[44:47]\n"
    "ds_read_b64_tr_b16 a[230:231], v16 offset:28672\n"
    "v_add_u32 v16, s76, v16\n"
    "v_mfma_f32_16x16x32_bf16 a[44:47], a[220:223], a[244:247], a[44:47]\n"
    "s_waitcnt lgkmcnt(4)\n"
    "v_mfma_f32_16x16x32_bf16 a[48:51], a[192:195], a[248:251], a[48:51]\n"
    "ds_read_b64_tr_b16 a[232:233], v17 offset:16384\n"
    "v_mfma_f32_16x16x32_bf16 a[48:51], a[196:199], a[252:255], a[48:51]\n"
    "buffer_load_dwordx4 v100, s[40:43], s86 offen offset:3072 lds\n"
    "v_mfma_f32_16x16x32_bf16 a[52:55], a[200:203], a[248:251], a[52:55]\n"
    "ds_read_b64_tr_b16 a[234:235], v17 offset:20480\n"
    "v_mfma_f32_16x16x32_bf16 a[52:55], a[204:207], a[252:255], a[52:55]\n"
    "v_mfma_f32_16x16x32_bf16 a[56:59], a[208:211], a[248:251], a[56:59]\n"
    "ds_read_b64_tr_b16 a[236:237], v17 offset:24576\n"
    "v_mfma_f32_16x16x32_bf16 a[56:59], a[212:215], a[252:255], a[56:59]\n"
    "v_mfma_f32_16x16x32_bf16 a[60:63], a[216:219], a[248:251], a[60:63]\n"
    "ds_read_b64_tr_b16 a[238:239], v17 offset:28672\n"
    "v_add_u32 v17, s76, v17\n"
    "v_mfma_f32_16x16x32_bf16 a[60:63], a[220:223], a[252:255], a[60:63]\n"
    "s_waitcnt lgkmcnt(4)\n"
    "s_add_u32 m0, s74, s85\n"
    "v_mfma_f32_16x16x32_bf16 a[64:67], a[192:195], a[224:227], a[64:67]\n"
    "ds_read_b64_tr_b16 a[240:241], v18 offset:16384\n"
    "v_mfma_f32_16x16x32_bf16 a[64:67], a[196:199], a[228:231], a[64:67]\n"
    "buffer_load_dwordx4 v8, s[52:55], s73 offen lds\n"
    "v_mfma_f32_16x16x32_bf16 a[68:71], a[200:203], a[224:227], a[68:71]\n"
    "ds_read_b64_tr_b16 a[242:243], v18 offset:20480\n"
    "v_mfma_f32_16x16x32_bf16 a[68:71], a[204:207], a[228:231], a[68:71]\n"
    "v_mfma_f32_16x16x32_bf16 a[72:75], a[208:211], a[224:227], a[72:75]\n"
    "ds_read_b64_tr_b16 a[244:245], v18 offset:24576\n"
    "v_mfma_f32_16x16x32_bf16 a[72:75], a[212:215], a[228:231], a[72:75]\n"
    "v_mfma_f32_16x16x32_bf16 a[76:79], a[216:219], a[224:227], a[76:79]\n"
    "ds_read_b64_tr_b16 a[246:247], v18 offset:28672\n"
    "v_add_u32 v18, s76, v18\n"
    "v_mfma_f32_16x16x32_bf16 a[76:79], a[220:223], a[228:231], a[76:79]\n"
    "s_waitcnt lgkmcnt(4)\n"
    "v_mfma_f32_16x16x32_bf16 a[80:83], a[192:195], a[232:235], a[80:83]\n"
    "ds_read_b64_tr_b16 a[248:249], v19 offset:16384\n"
    "v_mfma_f32_16x16x32_bf16 a[80:83], a[196:199], a[236:239], a[80:83]\n"
    "buffer_load_dwordx4 v9, s[52:55], s73 offen offset:1024 lds\n"
    "v_mfma_f32_16x16x32_bf16 a[84:87], a[200:203], a[232:235], a[84:87]\n"
    "ds_read_b64_tr_b16 a[250:251], v19 offset:20480\n"
    "v_mfma_f32_16x16x32_bf16 a[84:87], a[204:207], a[236:239], a[84:87]\n"
    "v_mfma_f32_16x16x32_bf16 a[88:91], a[208:211], a[232:235], a[88:91]\n"
    "ds_read_b64_tr_b16 a[252:253], v19 offset:24576\n"
    "v_mfma_f32_16x16x32_bf16 a[88:91], a[212:215], a[236:239], a[88:91]\n"
    "v_mfma_f32_16x16x32_bf16 a[92:95], a[216:219], a[232:235], a[92:95]\n"
    "ds_read_b64_tr_b16 a[254:255], v19 offset:28672\n"
    "v_add_u32 v19, s76, v19\n"
    "v_mfma_f32_16x16x32_bf16 a[92:95], a[220:223], a[236:239], a[92:95]\n"
    "s_waitcnt vmcnt(15) lgkmcnt(4)\n"
    "s_barrier\n"
    "v_mfma_f32_16x16x32_bf16 a[96:99], a[192:195], a[240:243], a[96:99]\n"
    "ds_read_b64_tr_b16 a[224:225], v12\n"
    "v_mfma_f32_16x16x32_bf16 a[96:99], a[196:199], a[244:247], a[96:99]\n"
    "buffer_load_dwordx4 v10, s[52:55], s73 offen offset:2048 lds\n"
    "v_mfma_f32_16x16x32_bf16 a[100:103], a[200:203], a[240:243], a[100:103]\n"
    "ds_read_b64_tr_b16 a[226:227], v12 offset:4096\n"
    "v_mfma_f32_16x16x32_bf16 a[100:103], a[204:207], a[244:247], a[100:103]\n"
    "v_mfma_f32_16x16x32_bf16 a[104:107], a[208:211], a[240:243], a[104:107]\n"
    "ds_read_b64_tr_b16 a[228:229], v12 offset:8192\n"
    "v_mfma_f32_16x16x32_bf16 a[104:107], a[212:215], a[244:247], a[104:107]\n"
    "v_mfma_f32_16x16x32_bf16 a[108:111], a[216:219], a[240:243], a[108:111]\n"
    "ds_read_b64_tr_b16 a[230:231], v12 offset:12288\n"
    "v_mfma_f32_16x16x32_bf16 a[108:111], a[220:223], a[244:247], a[108:111]\n"
    "s_waitcnt lgkmcnt(4)\n"
    "v_mfma_f32_16x16x32_bf16 a[112:115], a[192:195], a[248:251], a[112:115]\n"
    "ds_read_b64_tr_b16 a[232:233], v13\n"
    "v_mfma_f32_16x16x32_bf16 a[112:115], a[196:199], a[252:255], a[112:115]\n"
    "buffer_load_dwordx4 v11, s[52:55], s73 offen offset:3072 lds\n"
    "v_mfma_f32_16x16x32_bf16 a[116:119], a[200:203], a[248:251], a[116:119]\n"
    "s_add_u32 s73, s73, 0x80000\n"
    "ds_read_b64_tr_b16 a[234:235], v13 offset:4096\n"
    "v_mfma_f32_16x16x32_bf16 a[116:119], a[204:207], a[252:255], a[116:119]\n"
    "s_and_b32 s73, s73, 0x3fffff\n"
    "s_add_u32 s74, s74, 0x8000\n"
    "v_mfma_f32_16x16x32_bf16 a[120:123], a[208:211], a[248:251], a[120:123]\n"
    "ds_read_b64_tr_b16 a[236:237], v13 offset:8192\n"
    "v_mfma_f32_16x16x32_bf16 a[120:123], a[212:215], a[252:255], a[120:123]\n"
    "s_cmp_lt_u32 s74, 0x28000\n"
    "s_cselect_b32 s74, s74, 0\n"
    "s_add_u32 s72, s72, 0x80\n"
    "v_mfma_f32_16x16x32_bf16 a[124:127], a[216:219], a[248:251], a[124:127]\n"
    "s_and_b32 s72, s72, 0x3ff\n"
    "ds_read_b64_tr_b16 a[238:239], v13 offset:12288\n"
    "v_mfma_f32_16x16x32_bf16 a[124:127], a[220:223], a[252:255], a[124:127]\n"
    "s_add_u32 s86, s72, 0x8000\n"
    "s_mov_b32 s90, 1048577\n"
    "s_waitcnt vmcnt(10)\n"
    "v_cmp_ne_u64 s[92:93], s[94:95], v[80:81]\n"
    "v_cmp_ne_u64 vcc, s[94:95], v[82:83]\n"
    "s_sub_u32 s90, s90, 1\n"
    "s_or_b64 vcc, vcc, s[92:93]\n"
    "s_cbranch_scc0 ln_go\n"
    "ln_spin:\n"
    "buffer_load_dwordx4 v[80:83], v23, s[64:67], s87 offen sc1\n"
    "s_waitcnt vmcnt(0)\n"
    "v_cmp_ne_u64 s[92:93], s[94:95], v[80:81]\n"
    "v_cmp_ne_u64 vcc, s[94:95], v[82:83]\n"
    "s_sub_u32 s90, s90, 1\n"
    "s_or_b64 vcc, vcc, s[92:93]\n"
    "s_cbranch_scc0 ln_go\n"
    "s_sleep 4\n"
    "s_cmp_lg_u32 s90, 0\n"
    "s_cbranch_scc1 ln_spin\n"
    "ln_go:\n"
    "s_lshl_b32 s73, s78, 18\n"
    "s_add_u32 s83, s73, 0x8000\n"
    "s_lshl_b32 s84, s78, 13\n"
    "s_add_u32 s85, s84, 0x1000\n"
    "s_mov_b32 s72, 0\n"
    "s_add_u32 s91, s84, 0x18000\n"
    "v_add_u32 v101, s91, v22\n"
    "ds_read_b128 a[160:163], v101\n"
    "ds_read_b128 a[164:167], v101 offset:1024\n"
    "ds_read_b128 a[168:171], v101 offset:2048\n"
    "ds_read_b128 a[172:175], v101 offset:3072\n"
    "s_waitcnt lgkmcnt(8)\n"
    "v_mfma_f32_16x16x32_bf16 v[128:131], a[224:227], a[128:131], v[128:131]\n"
    "ds_read_b64_tr_b16 a[240:241], v14\n"
    "v_mfma_f32_16x16x32_bf16 v[128:131], a[228:231], a[132:135], v[128:131]\n"
    "buffer_load_dwordx4 a[192:195], v4, s[44:47], s72 offen sc1\n"
    "v_mfma_f32_16x16x32_bf16 v[160:163], a[224:227], a[136:139], v[160:163]\n"
    "ds_read_b64_tr_b16 a[242:243], v14 offset:4096\n"
    "v_mfma_f32_16x16x32_bf16 v[160:163], a[228:231], a[140:143], v[160:163]\n"
    "v_mfma_f32_16x16x32_bf16 v[192:195], a[224:227], a[144:147], v[192:195]\n"
    "ds_read_b64_tr_b16 a[244:245], v14 offset:8192\n"
    "v_mfma_f32_16x16x32_bf16 v[192:195], a[228:231], a[148:151], v[192:195]\n"
    "s_waitcnt lgkmcnt(3)\n"
    "buffer_load_dword v0, v30, s[68:71], s81 offen\n"
    "v_mfma_f32_16x16x32_bf16 v[224:227], a[224:227], a[152:155], v[224:227]\n"
    "ds_read_b64_tr_b16 a[246:247], v14 offset:12288\n"
    "v_mfma_f32_16x16x32_bf16 v[224:227], a[228:231], a[156:159], v[224:227]\n"
    "s_add_u32 m0, s74, s84\n"
    "v_mfma_f32_16x16x32_bf16 v[132:135], a[232:235], a[128:131], v[132:135]\n"
    "ds_read_b64_tr_b16 a[248:249], v15\n"
    "v_mfma_f32_16x16x32_bf16 v[132:135], a[236:239], a[132:135], v[132:135]\n"
    "buffer_load_dwordx4 v8, s[68:71], s73 offen lds\n"
    "v_mfma_f32_16x16x32_bf16 v[164:167], a[232:235], a[136:139], v[164:167]\n"
    "ds_read_b64_tr_b16 a[250:251], v15 offset:4096\n"
    "v_mfma_f32_16x16x32_bf16 v[164:167], a[236:239], a[140:143], v[164:167]\n"
    "v_mfma_f32_16x16x32_bf16 v[196:199], a[232:235], a[144:147], v[196:199]\n"
    "ds_read_b64_tr_b16 a[252:253], v15 offset:8192\n"
    "v_mfma_f32_16x16x32_bf16 v[196:199], a[236:239], a[148:151], v[196:199]\n"
    "v_mfma_f32_16x16x32_bf16 v[228:231], a[232:235], a[152:155], v[228:231]\n"
    "ds_read_b64_tr_b16 a[254:255], v15 offset:12288\n"
    "v_mfma_f32_16x16x32_bf16 v[228:231], a[236:239], a[156:159], v[228:231]\n"
    "s_waitcnt lgkmcnt(4)\n"
    "v_mfma_f32_16x16x32_bf16 v[136:139], a[240:243], a[128:131], v[136:139]\n"
    "ds_read_b64_tr_b16 a[224:225], v16\n"
    "v_mfma_f32_16x16x32_bf16 v[136:139], a[244:247], a[132:135], v[136:139]\n"
    "buffer_load_dwordx4 a[196:199], v4, s[44:47], s72 offen offset:1024 sc1\n"
    "v_mfma_f32_16x16x32_bf16 v[168:171], a[240:243], a[136:139], v[168:171]\n"
    "ds_read_b64_tr_b16 a[226:227], v16 offset:4096\n"
    "v_mfma_f32_16x16x32_bf16 v[168:171], a[244:247], a[140:143], v[168:171]\n"
    "v_mfma_f32_16x16x32_bf16 v[200:203], a[240:243], a[144:147], v[200:203]\n"
    "ds_read_b64_tr_b16 a[228:229], v16 offset:8192\n"
    "v_mfma_f32_16x16x32_bf16 v[200:203], a[244:247], a[148:151], v[200:203]\n"
    "v_mfma_f32_16x16x32_bf16 v[232:235], a[240:243], a[152:155], v[232:235]\n"
    "s_add_u32 s81, s81, 0x400000\n"
    "ds_read_b64_tr_b16 a[230:231], v16 offset:12288\n"
    "v_mfma_f32_16x16x32_bf16 v[232:235], a[244:247], a[156:159], v[232:235]\n"
    "s_waitcnt lgkmcnt(4)\n"
    "v_mfma_f32_16x16x32_bf16 v[140:143], a[248:251], a[128:131], v[140:143]\n"
    "ds_read_b64_tr_b16 a[232:233], v17\n"
    "v_mfma_f32_16x16x32_bf16 v[140:143], a[252:255], a[132:135], v[140:143]\n"
    "buffer_load_dwordx4 v9, s[68:71], s73 offen offset:1024 lds\n"
    "v_mfma_f32_16x16x32_bf16 v[172:175], a[248:251], a[136:139], v[172:175]\n"
    "ds_read_b64_tr_b16 a[234:235], v17 offset:4096\n"
    "v_mfma_f32_16x16x32_bf16 v[172:175], a[252:255], a[140:143], v[172:175]\n"
    "v_mfma_f32_16x16x32_bf16 v[204:207], a[248:251], a[144:147], v[204:207]\n"
    "ds_read_b64_tr_b16 a[236:237], v17 offset:8192\n"
    "v_mfma_f32_16x16x32_bf16 v[204:207], a[252:255], a[148:151], v[204:207]\n"
    "v_mfma_f32_16x16x32_bf16 v[236:239], a[248:251], a[152:155], v[236:239]\n"
    "ds_read_b64_tr_b16 a[238:239], v17 offset:12288\n"
    "v_mfma_f32_16x16x32_bf16 v[236:239], a[252:255], a[156:159], v[236:239]\n"
    "s_waitcnt lgkmcnt(4)\n"
    "v_mfma_f32_16x16x32_bf16 v[144:147], a[224:227], a[128:131], v[144:147]\n"
    "ds_read_b64_tr_b16 a[240:241], v18\n"
    "v_mfma_f32_16x16x32_bf16 v[144:147], a[228:231], a[132:135], v[144:147]\n"
    "buffer_load_dwordx4 a[200:203], v5, s[44:47], s72 offen sc1\n"
    "v_mfma_f32_16x16x32_bf16 v[176:179], a[224:227], a[136:139], v[176:179]\n"
    "ds_read_b64_tr_b16 a[242:243], v18 offset:4096\n"
    "v_mfma_f32_16x16x32_bf16 v[176:179], a[228:231], a[140:143], v[176:179]\n"
    "v_mfma_f32_16x16x32_bf16 v[208:211], a[224:227], a[144:147], v[208:211]\n"
    "s_waitcnt vmcnt(10)\n"
    "ds_read_b64_tr_b16 a[244:245], v18 offset:8192\n"
    "v_mfma_f32_16x16x32_bf16 v[208:211], a[228:231], a[148:151], v[208:211]\n"
    "v_mfma_f32_16x16x32_bf16 v[240:243], a[224:227], a[152:155], v[240:243]\n"
    "ds_read_b64_tr_b16 a[246:247], v18 offset:12288\n"
    "v_mfma_f32_16x16x32_bf16 v[240:243], a[228:231], a[156:159], v[240:243]\n"
    "s_waitcnt lgkmcnt(4)\n"
    "ds_read_b128 a[176:179], v101 offset:4096\n"
    "v_mfma_f32_16x16x32_bf16 v[148:151], a[232:235], a[128:131], v[148:151]\n"
    "ds_read_b64_tr_b16 a[248:249], v19\n"
    "v_mfma_f32_16x16x32_bf16 v[148:151], a[236:239], a[132:135], v[148:151]\n"
    "buffer_load_dwordx4 v10, s[68:71], s73 offen offset:2048 lds\n"
    "v_mfma_f32_16x16x32_bf16 v[180:183], a[232:235], a[136:139], v[180:183]\n"
    "ds_read_b64_tr_b16 a[250:251], v19 offset:4096\n"
    "v_mfma_f32_16x16x32_bf16 v[180:183], a[236:239], a[140:143], v[180:183]\n"
    "ds_read_b128 a[180:183], v101 offset:5120\n"
    "v_mfma_f32_16x16x32_bf16 v[212:215], a[232:235], a[144:147], v[212:215]\n"
    "ds_read_b64_tr_b16 a[252:253], v19 offset:8192\n"
    "v_mfma_f32_16x16x32_bf16 v[212:215], a[236:239], a[148:151], v[212:215]\n"
    "ds_read_b128 a[184:187], v101 offset:6144\n"
    "v_mfma_f32_16x16x32_bf16 v[244:247], a[232:235], a[152:155], v[244:247]\n"
    "ds_read_b64_tr_b16 a[254:255], v19 offset:12288\n"
    "v_mfma_f32_16x16x32_bf16 v[244:247], a[236:239], a[156:159], v[244:247]\n"
    "s_waitcnt lgkmcnt(7)\n"
    "ds_read_b128 a[188:191], v101 offset:7168\n"
    "v_mfma_f32_16x16x32_bf16 v[152:155], a[240:243], a[128:131], v[152:155]\n"
    "ds_read_b64_tr_b16 a[224:225], v12 offset:16384\n"
    "v_mfma_f32_16x16x32_bf16 v[152:155], a[244:247], a[132:135], v[152:155]\n"
    "buffer_load_dwordx4 a[204:207], v5, s[44:47], s72 offen offset:1024 sc1\n"
    "v_mfma_f32_16x16x32_bf16 v[184:187], a[240:243], a[136:139], v[184:187]\n"
    "s_add_u32 s75, s75, 1\n"
    "ds_read_b64_tr_b16 a[226:227], v12 offset:20480\n"
    "v_mfma_f32_16x16x32_bf16 v[184:187], a[244:247], a[140:143], v[184:187]\n"
    "v_mfma_f32_16x16x32_bf16 v[216:219], a[240:243], a[144:147], v[216:219]\n"
    "ds_read_b64_tr_b16 a[228:229], v12 offset:24576\n"
    "v_mfma_f32_16x16x32_bf16 v[216:219], a[244:247], a[148:151], v[216:219]\n"
    "s_cmp_lt_u32 s75, 5\n"
    "s_cselect_b32 s75, s75, 0\n"
    "s_cselect_b32 s76, 0x8000, s77\n"
    "v_mfma_f32_16x16x32_bf16 v[248:251], a[240:243], a[152:155], v[248:251]\n"
    "ds_read_b64_tr_b16 a[230:231], v12 offset:28672\n"
    "v_add_u32 v12, s76, v12\n"
    "v_mfma_f32_16x16x32_bf16 v[248:251], a[244:247], a[156:159], v[248:251]\n"
    "s_waitcnt lgkmcnt(5)\n"
    "v_mfma_f32_16x16x32_bf16 v[156:159], a[248:251], a[128:131], v[156:159]\n"
    "ds_read_b64_tr_b16 a[232:233], v13 offset:16384\n"
    "v_mfma_f32_16x16x32_bf16 v[156:159], a[252:255], a[132:135], v[156:159]\n"
    "buffer_load_dwordx4 v11, s[68:71], s73 offen offset:3072 lds\n"
    "v_mfma_f32_16x16x32_bf16 v[188:191], a[248:251], a[136:139], v[188:191]\n"
    "ds_read_b64_tr_b16 a[234:235], v13 offset:20480\n"
    "v_mfma_f32_16x16x32_bf16 v[188:191], a[252:255], a[140:143], v[188:191]\n"
    "v_mfma_f32_16x16x32_bf16 v[220:223], a[248:251], a[144:147], v[220:223]\n"
    "ds_read_b64_tr_b16 a[236:237], v13 offset:24576\n"
    "v_mfma_f32_16x16x32_bf16 v[220:223], a[252:255], a[148:151], v[220:223]\n"
    "v_mfma_f32_16x16x32_bf16 v[252:255], a[248:251], a[152:155], v[252:255]\n"
    "ds_read_b64_tr_b16 a[238:239], v13 offset:28672\n"
    "v_add_u32 v13, s76, v13\n"
    "v_mfma_f32_16x16x32_bf16 v[252:255], a[252:255], a[156:159], v[252:255]\n"
    "s_waitcnt lgkmcnt(4)\n"
    "v_mfma_f32_16x16x32_bf16 a[0:3], a[128:131], a[224:227], a[0:3]\n"
    "ds_read_b64_tr_b16 a[240:241], v14 offset:16384\n"
    "v_mfma_f32_16x16x32_bf16 a[0:3], a[132:135], a[228:231], a[0:3]\n"
    "buffer_load_dwordx4 a[208:211], v6, s[44:47], s72 offen sc1\n"
    "v_mfma_f32_16x16x32_bf16 a[4:7], a[136:139], a[224:227], a[4:7]\n"
    "ds_read_b64_tr_b16 a[242:243], v14 offset:20480\n"
    "v_mfma_f32_16x16x32_bf16 a[4:7], a[140:143], a[228:231], a[4:7]\n"
    "v_mfma_f32_16x16x32_bf16 a[8:11], a[144:147], a[224:227], a[8:11]\n"
    "ds_read_b64_tr_b16 a[244:245], v14 offset:24576\n"
    "v_mfma_f32_16x16x32_bf16 a[8:11], a[148:151], a[228:231], a[8:11]\n"
    "v_mfma_f32_16x16x32_bf16 a[12:15], a[152:155], a[224:227], a[12:15]\n"
    "ds_read_b64_tr_b16 a[246:247], v14 offset:28672\n"
    "v_add_u32 v14, s76, v14\n"
    "v_mfma_f32_16x16x32_bf16 a[12:15], a[156:159], a[228:231], a[12:15]\n"
    "s_waitcnt lgkmcnt(4)\n"
    "s_add_u32 m0, s74, s85\n"
    "v_mfma_f32_16x16x32_bf16 a[16:19], a[128:131], a[232:235], a[16:19]\n"
    "ds_read_b64_tr_b16 a[248:249], v15 offset:16384\n"
    "v_mfma_f32_16x16x32_bf16 a[16:19], a[132:135], a[236:239], a[16:19]\n"
    "buffer_load_dwordx4 v8, s[68:71], s83 offen lds\n"
    "v_mfma_f32_16x16x32_bf16 a[20:23], a[136:139], a[232:235], a[20:23]\n"
    "ds_read_b64_tr_b16 a[250:251], v15 offset:20480\n"
    "v_mfma_f32_16x16x32_bf16 a[20:23], a[140:143], a[236:239], a[20:23]\n"
    "v_mfma_f32_16x16x32_bf16 a[24:27], a[144:147], a[232:235], a[24:27]\n"
    "ds_read_b64_tr_b16 a[252:253], v15 offset:24576\n"
    "v_mfma_f32_16x16x32_bf16 a[24:27], a[148:151], a[236:239], a[24:27]\n"
    "v_mfma_f32_16x16x32_bf16 a[28:31], a[152:155], a[232:235], a[28:31]\n"
    "ds_read_b64_tr_b16 a[254:255], v15 offset:28672\n"
    "v_add_u32 v15, s76, v15\n"
    "v_mfma_f32_16x16x32_bf16 a[28:31], a[156:159], a[236:239], a[28:31]\n"
    "s_waitcnt lgkmcnt(4)\n"
    "v_mfma_f32_16x16x32_bf16 a[32:35], a[128:131], a[240:243], a[32:35]\n"
    "ds_read_b64_tr_b16 a[224:225], v16 offset:16384\n"
    "v_mfma_f32_16x16x32_bf16 a[32:35], a[132:135], a[244:247], a[32:35]\n"
    "buffer_load_dwordx4 a[212:215], v6, s[44:47], s72 offen offset:1024 sc1\n"
    "v_mfma_f32_16x16x32_bf16 a[36:39], a[136:139], a[240:243], a[36:39]\n"
    "ds_read_b64_tr_b16 a[226:227], v16 offset:20480\n"
    "v_mfma_f32_16x16x32_bf16 a[36:39], a[140:143], a[244:247], a[36:39]\n"
    "v_mfma_f32_16x16x32_bf16 a[40:43], a[144:147], a[240:243], a[40:43]\n"
    "ds_read_b64_tr_b16 a[228:229], v16 offset:24576\n"
    "v_mfma_f32_16x16x32_bf16 a[40:43], a[148:151], a[244:247], a[40:43]\n"
    "v_mfma_f32_16x16x32_bf16 a[44:47], a[152:155], a[240:243], a[44:47]\n"
    "ds_read_b64_tr_b16 a[230:231], v16 offset:28672\n"
    "v_add_u32 v16, s76, v16\n"
    "v_mfma_f32_16x16x32_bf16 a[44:47], a[156:159], a[244:247], a[44:47]\n"
    "s_waitcnt lgkmcnt(4)\n"
    "v_mfma_f32_16x16x32_bf16 a[48:51], a[128:131], a[248:251], a[48:51]\n"
    "ds_read_b64_tr_b16 a[232:233], v17 offset:16384\n"
    "v_mfma_f32_16x16x32_bf16 a[48:51], a[132:135], a[252:255], a[48:51]\n"
    "buffer_load_dwordx4 v9, s[68:71], s83 offen offset:1024 lds\n"
    "v_mfma_f32_16x16x32_bf16 a[52:55], a[136:139], a[248:251], a[52:55]\n"
    "ds_read_b64_tr_b16 a[234:235], v17 offset:20480\n"
    "v_mfma_f32_16x16x32_bf16 a[52:55], a[140:143], a[252:255], a[52:55]\n"
    "v_mfma_f32_16x16x32_bf16 a[56:59], a[144:147], a[248:251], a[56:59]\n"
    "ds_read_b64_tr_b16 a[236:237], v17 offset:24576\n"
    "v_mfma_f32_16x16x32_bf16 a[56:59], a[148:151], a[252:255], a[56:59]\n"
    "v_mfma_f32_16x16x32_bf16 a[60:63], a[152:155], a[248:251], a[60:63]\n"
    "ds_read_b64_tr_b16 a[238:239], v17 offset:28672\n"
    "v_add_u32 v17, s76, v17\n"
    "v_mfma_f32_16x16x32_bf16 a[60:63], a[156:159], a[252:255], a[60:63]\n"
    "s_waitcnt lgkmcnt(4)\n"
    "v_mfma_f32_16x16x32_bf16 a[64:67], a[128:131], a[224:227], a[64:67]\n"
    "ds_read_b64_tr_b16 a[240:241], v18 offset:16384\n"
    "v_mfma_f32_16x16x32_bf16 a[64:67], a[132:135], a[228:231], a[64:67]\n"
    "buffer_load_dwordx4 a[216:219], v7, s[44:47], s72 offen sc1\n"
    "v_mfma_f32_16x16x32_bf16 a[68:71], a[136:139], a[224:227], a[68:71]\n"
    "ds_read_b64_tr_b16 a[242:243], v18 offset:20480\n"
    "v_mfma_f32_16x16x32_bf16 a[68:71], a[140:143], a[228:231], a[68:71]\n"
    "v_mfma_f32_16x16x32_bf16 a[72:75], a[144:147], a[224:227], a[72:75]\n"
    "ds_read_b64_tr_b16 a[244:245], v18 offset:24576\n"
    "v_mfma_f32_16x16x32_bf16 a[72:75], a[148:151], a[228:231], a[72:75]\n"
    "v_mfma_f32_16x16x32_bf16 a[76:79], a[152:155], a[224:227], a[76:79]\n"
    "ds_read_b64_tr_b16 a[246:247], v18 offset:28672\n"
    "v_add_u32 v18, s76, v18\n"
    "v_mfma_f32_16x16x32_bf16 a[76:79], a[156:159], a[228:231], a[76:79]\n"
    "s_waitcnt lgkmcnt(4)\n"
    "v_mfma_f32_16x16x32_bf16 a[80:83], a[128:131], a[232:235], a[80:83]\n"
    "ds_read_b64_tr_b16 a[248:249], v19 offset:16384\n"
    "v_mfma_f32_16x16x32_bf16 a[80:83], a[132:135], a[236:239], a[80:83]\n"
    "buffer_load_dwordx4 v10, s[68:71], s83 offen offset:2048 lds\n"
    "v_mfma_f32_16x16x32_bf16 a[84:87], a[136:139], a[232:235], a[84:87]\n"
    "ds_read_b64_tr_b16 a[250:251], v19 offset:20480\n"
    "v_mfma_f32_16x16x32_bf16 a[84:87], a[140:143], a[236:239], a[84:87]\n"
    "v_mfma_f32_16x16x32_bf16 a[88:91], a[144:147], a[232:235], a[88:91]\n"
    "ds_read_b64_tr_b16 a[252:253], v19 offset:24576\n"
    "v_mfma_f32_16x16x32_bf16 a[88:91], a[148:151], a[236:239], a[88:91]\n"
    "v_mfma_f32_16x16x32_bf16 a[92:95], a[152:155], a[232:235], a[92:95]\n"
    "ds_read_b64_tr_b16 a[254:255], v19 offset:28672\n"
    "v_add_u32 v19, s76, v19\n"
    "v_mfma_f32_16x16x32_bf16 a[92:95], a[156:159], a[236:239], a[92:95]\n"
    "s_waitcnt vmcnt(15) lgkmcnt(4)\n"
    "s_barrier\n"
    "v_mfma_f32_16x16x32_bf16 a[96:99], a[128:131], a[240:243], a[96:99]\n"
    "ds_read_b64_tr_b16 a[224:225], v12\n"
    "v_mfma_f32_16x16x32_bf16 a[96:99], a[132:135], a[244:247], a[96:99]\n"
    "buffer_load_dwordx4 a[220:223], v7, s[44:47], s72 offen offset:1024 sc1\n"
    "v_mfma_f32_16x16x32_bf16 a[100:103], a[136:139], a[240:243], a[100:103]\n"
    "s_add_u32 s72, s72, 0x40000\n"
    "ds_read_b64_tr_b16 a[226:227], v12 offset:4096\n"
    "v_mfma_f32_16x16x32_bf16 a[100:103], a[140:143], a[244:247], a[100:103]\n"
    "v_mfma_f32_16x16x32_bf16 a[104:107], a[144:147], a[240:243], a[104:107]\n"
    "ds_read_b64_tr_b16 a[228:229], v12 offset:8192\n"
    "v_mfma_f32_16x16x32_bf16 a[104:107], a[148:151], a[244:247], a[104:107]\n"
    "v_mfma_f32_16x16x32_bf16 a[108:111], a[152:155], a[240:243], a[108:111]\n"
    "ds_read_b64_tr_b16 a[230:231], v12 offset:12288\n"
    "v_mfma_f32_16x16x32_bf16 a[108:111], a[156:159], a[244:247], a[108:111]\n"
    "s_waitcnt lgkmcnt(4)\n"
    "v_mfma_f32_16x16x32_bf16 a[112:115], a[128:131], a[248:251], a[112:115]\n"
    "ds_read_b64_tr_b16 a[232:233], v13\n"
    "v_mfma_f32_16x16x32_bf16 a[112:115], a[132:135], a[252:255], a[112:115]\n"
    "buffer_load_dwordx4 v11, s[68:71], s83 offen offset:3072 lds\n"
    "v_mfma_f32_16x16x32_bf16 a[116:119], a[136:139], a[248:251], a[116:119]\n"
    "s_add_u32 s73, s73, 0x100000\n"
    "ds_read_b64_tr_b16 a[234:235], v13 offset:4096\n"
    "v_mfma_f32_16x16x32_bf16 a[116:119], a[140:143], a[252:255], a[116:119]\n"
    "s_add_u32 s83, s83, 0x100000\n"
    "s_add_u32 s74, s74, 0x8000\n"
    "v_mfma_f32_16x16x32_bf16 a[120:123], a[144:147], a[248:251], a[120:123]\n"
    "ds_read_b64_tr_b16 a[236:237], v13 offset:8192\n"
    "v_mfma_f32_16x16x32_bf16 a[120:123], a[148:151], a[252:255], a[120:123]\n"
    "s_cmp_lt_u32 s74, 0x28000\n"
    "s_cselect_b32 s74, s74, 0\n"
    "v_mfma_f32_16x16x32_bf16 a[124:127], a[152:155], a[248:251], a[124:127]\n"
    "ds_read_b64_tr_b16 a[238:239], v13 offset:12288\n"
    "v_mfma_f32_16x16x32_bf16 a[124:127], a[156:159], a[252:255], a[124:127]\n"
    "s_waitcnt lgkmcnt(4)\n"
    "v_mfma_f32_16x16x32_bf16 v[128:131], a[224:227], a[160:163], v[128:131]\n"
    "ds_read_b64_tr_b16 a[240:241], v14\n"
    "v_mfma_f32_16x16x32_bf16 v[128:131], a[228:231], a[164:167], v[128:131]\n"
    "buffer_load_dwordx4 a[128:131], v4, s[44:47], s72 offen sc1\n"
    "v_mfma_f32_16x16x32_bf16 v[160:163], a[224:227], a[168:171], v[160:163]\n"
    "ds_read_b64_tr_b16 a[242:243], v14 offset:4096\n"
    "v_mfma_f32_16x16x32_bf16 v[160:163], a[228:231], a[172:175], v[160:163]\n"
    "v_mfma_f32_16x16x32_bf16 v[192:195], a[224:227], a[176:179], v[192:195]\n"
    "ds_read_b64_tr_b16 a[244:245], v14 offset:8192\n"
    "v_mfma_f32_16x16x32_bf16 v[192:195], a[228:231], a[180:183], v[192:195]\n"
    "s_add_u32 m0, s74, s84\n"
    "v_mfma_f32_16x16x32_bf16 v[224:227], a[224:227], a[184:187], v[224:227]\n"
    "ds_read_b64_tr_b16 a[246:247], v14 offset:12288\n"
    "v_mfma_f32_16x16x32_bf16 v[224:227], a[228:231], a[188:191], v[224:227]\n"
    "s_waitcnt lgkmcnt(4)\n"
    "v_mfma_f32_16x16x32_bf16 v[132:135], a[232:235], a[160:163], v[132:135]\n"
    "ds_read_b64_tr_b16 a[248:249], v15\n"
    "v_mfma_f32_16x16x32_bf16 v[132:135], a[236:239], a[164:167], v[132:135]\n"
    "buffer_load_dwordx4 v8, s[68:71], s73 offen lds\n"
    "v_mfma_f32_16x16x32_bf16 v[164:167], a[232:235], a[168:171], v[164:167]\n"
    "ds_read_b64_tr_b16 a[250:251], v15 offset:4096\n"
    "v_mfma_f32_16x16x32_bf16 v[164:167], a[236:239], a[172:175], v[164:167]\n"
    "buffer_load_dwordx4 v9, s[68:71], s73 offen offset:1024 lds\n"
    "v_mfma_f32_16x16x32_bf16 v[196:199], a[232:235], a[176:179], v[196:199]\n"
    "ds_read_b64_tr_b16 a[252:253], v15 offset:8192\n"
    "v_mfma_f32_16x16x32_bf16 v[196:199], a[236:239], a[180:183], v[196:199]\n"
    "v_mfma_f32_16x16x32_bf16 v[228:231], a[232:235], a[184:187], v[228:231]\n"
    "ds_read_b64_tr_b16 a[254:255], v15 offset:12288\n"
    "v_mfma_f32_16x16x32_bf16 v[228:231], a[236:239], a[188:191], v[228:231]\n"
    "s_waitcnt lgkmcnt(4)\n"
    "v_mfma_f32_16x16x32_bf16 v[136:139], a[240:243], a[160:163], v[136:139]\n"
    "ds_read_b64_tr_b16 a[224:225], v16\n"
    "v_mfma_f32_16x16x32_bf16 v[136:139], a[244:247], a[164:167], v[136:139]\n"
    "buffer_load_dwordx4 a[132:135], v4, s[44:47], s72 offen offset:1024 sc1\n"
    "v_mfma_f32_16x16x32_bf16 v[168:171], a[240:243], a[168:171], v[168:171]\n"
    "ds_read_b64_tr_b16 a[226:227], v16 offset:4096\n"
    "v_mfma_f32_16x16x32_bf16 v[168:171], a[244:247], a[172:175], v[168:171]\n"
    "v_mfma_f32_16x16x32_bf16 v[200:203], a[240:243], a[176:179], v[200:203]\n"
    "ds_read_b64_tr_b16 a[228:229], v16 offset:8192\n"
    "v_mfma_f32_16x16x32_bf16 v[200:203], a[244:247], a[180:183], v[200:203]\n"
    "v_mfma_f32_16x16x32_bf16 v[232:235], a[240:243], a[184:187], v[232:235]\n"
    "ds_read_b64_tr_b16 a[230:231], v16 offset:12288\n"
    "v_mfma_f32_16x16x32_bf16 v[232:235], a[244:247], a[188:191], v[232:235]\n"
    "s_waitcnt lgkmcnt(4)\n"
    "v_mfma_f32_16x16x32_bf16 v[140:143], a[248:251], a[160:163], v[140:143]\n"
    "ds_read_b64_tr_b16 a[232:233], v17\n"
    "v_mfma_f32_16x16x32_bf16 v[140:143], a[252:255], a[164:167], v[140:143]\n"
    "buffer_load_dwordx4 v10, s[68:71], s73 offen offset:2048 lds\n"
    "v_mfma_f32_16x16x32_bf16 v[172:175], a[248:251], a[168:171], v[172:175]\n"
    "ds_read_b64_tr_b16 a[234:235], v17 offset:4096\n"
    "v_mfma_f32_16x16x32_bf16 v[172:175], a[252:255], a[172:175], v[172:175]\n"
    "buffer_load_dwordx4 v11, s[68:71], s73 offen offset:3072 lds\n"
    "v_mfma_f32_16x16x32_bf16 v[204:207], a[248:251], a[176:179], v[204:207]\n"
    "ds_read_b64_tr_b16 a[236:237], v17 offset:8192\n"
    "v_mfma_f32_16x16x32_bf16 v[204:207], a[252:255], a[180:183], v[204:207]\n"
    "v_mfma_f32_16x16x32_bf16 v[236:239], a[248:251], a[184:187], v[236:239]\n"
    "ds_read_b64_tr_b16 a[238:239], v17 offset:12288\n"
    "v_mfma_f32_16x16x32_bf16 v[236:239], a[252:255], a[188:191], v[236:239]\n"
    "s_waitcnt lgkmcnt(4)\n"
    "v_mfma_f32_16x16x32_bf16 v[144:147], a[224:227], a[160:163], v[144:147]\n"
    "ds_read_b64_tr_b16 a[240:241], v18\n"
    "v_mfma_f32_16x16x32_bf16 v[144:147], a[228:231], a[164:167], v[144:147]\n"
    "buffer_load_dwordx4 a[136:139], v5, s[44:47], s72 offen sc1\n"
    "v_mfma_f32_16x16x32_bf16 v[176:179], a[224:227], a[168:171], v[176:179]\n"
    "ds_read_b64_tr_b16 a[242:243], v18 offset:4096\n"
    "v_mfma_f32_16x16x32_bf16 v[176:179], a[228:231], a[172:175], v[176:179]\n"
    "v_mfma_f32_16x16x32_bf16 v[208:211], a[224:227], a[176:179], v[208:211]\n"
    "ds_read_b64_tr_b16 a[244:245], v18 offset:8192\n"
    "v_mfma_f32_16x16x32_bf16 v[208:211], a[228:231], a[180:183], v[208:211]\n"
    "s_add_u32 m0, s74, s85\n"
    "v_mfma_f32_16x16x32_bf16 v[240:243], a[224:227], a[184:187], v[240:243]\n"
    "ds_read_b64_tr_b16 a[246:247], v18 offset:12288\n"
    "v_mfma_f32_16x16x32_bf16 v[240:243], a[228:231], a[188:191], v[240:243]\n"
    "s_waitcnt lgkmcnt(4)\n"
    "v_mfma_f32_16x16x32_bf16 v[148:151], a[232:235], a[160:163], v[148:151]\n"
    "ds_read_b64_tr_b16 a[248:249], v19\n"
    "v_mfma_f32_16x16x32_bf16 v[148:151], a[236:239], a[164:167], v[148:151]\n"
    "buffer_load_dwordx4 v8, s[68:71], s83 offen lds\n"
    "v_mfma_f32_16x16x32_bf16 v[180:183], a[232:235], a[168:171], v[180:183]\n"
    "ds_read_b64_tr_b16 a[250:251], v19 offset:4096\n"
    "v_mfma_f32_16x16x32_bf16 v[180:183], a[236:239], a[172:175], v[180:183]\n"
    "buffer_load_dwordx4 v9, s[68:71], s83 offen offset:1024 lds\n"
    "v_mfma_f32_16x16x32_bf16 v[212:215], a[232:235], a[176:179], v[212:215]\n"
    "ds_read_b64_tr_b16 a[252:253], v19 offset:8192\n"
    "v_mfma_f32_16x16x32_bf16 v[212:215], a[236:239], a[180:183], v[212:215]\n"
    "v_mfma_f32_16x16x32_bf16 v[244:247], a[232:235], a[184:187], v[244:247]\n"
    "ds_read_b64_tr_b16 a[254:255], v19 offset:12288\n"
    "v_mfma_f32_16x16x32_bf16 v[244:247], a[236:239], a[188:191], v[244:247]\n"
    "s_waitcnt lgkmcnt(4)\n"
    "v_mfma_f32_16x16x32_bf16 v[152:155], a[240:243], a[160:163], v[152:155]\n"
    "ds_read_b64_tr_b16 a[224:225], v12 offset:16384\n"
    "v_mfma_f32_16x16x32_bf16 v[152:155], a[244:247], a[164:167], v[152:155]\n"
    "buffer_load_dwordx4 a[140:143], v5, s[44:47], s72 offen offset:1024 sc1\n"
    "v_mfma_f32_16x16x32_bf16 v[184:187], a[240:243], a[168:171], v[184:187]\n"
    "s_add_u32 s75, s75, 1\n"
    "ds_read_b64_tr_b16 a[226:227], v12 offset:20480\n"
    "v_mfma_f32_16x16x32_bf16 v[184:187], a[244:247], a[172:175], v[184:187]\n"
    "v_mfma_f32_16x16x32_bf16 v[216:219], a[240:243], a[176:179], v[216:219]\n"
    "ds_read_b64_tr_b16 a[228:229], v12 offset:24576\n"
    "v_mfma_f32_16x16x32_bf16 v[216:219], a[244:247], a[180:183], v[216:219]\n"
    "s_cmp_lt_u32 s75, 5\n"
    "s_cselect_b32 s75, s75, 0\n"
    "s_cselect_b32 s76, 0x8000, s77\n"
    "v_mfma_f32_16x16x32_bf16 v[248:251], a[240:243], a[184:187], v[248:251]\n"
    "ds_read_b64_tr_b16 a[230:231], v12 offset:28672\n"
    "v_add_u32 v12, s76, v12\n"
    "v_mfma_f32_16x16x32_bf16 v[248:251], a[244:247], a[188:191], v[248:251]\n"
    "s_waitcnt lgkmcnt(4)\n"
    "v_mfma_f32_16x16x32_bf16 v[156:159], a[248:251], a[160:163], v[156:159]\n"
    "ds_read_b64_tr_b16 a[232:233], v13 offset:16384\n"
    "v_mfma_f32_16x16x32_bf16 v[156:159], a[252:255], a[164:167], v[156:159]\n"
    "buffer_load_dwordx4 v10, s[68:71], s83 offen offset:2048 lds\n"
    "v_mfma_f32_16x16x32_bf16 v[188:191], a[248:251], a[168:171], v[188:191]\n"
    "ds_read_b64_tr_b16 a[234:235], v13 offset:20480\n"
    "v_mfma_f32_16x16x32_bf16 v[188:191], a[252:255], a[172:175], v[188:191]\n"
    "buffer_load_dwordx4 v11, s[68:71], s83 offen offset:3072 lds\n"
    "v_mfma_f32_16x16x32_bf16 v[220:223], a[248:251], a[176:179], v[220:223]\n"
    "ds_read_b64_tr_b16 a[236:237], v13 offset:24576\n"
    "v_cvt_pk_bf16_f32 v32, v128, v129\n"
    "v_mfma_f32_16x16x32_bf16 v[220:223], a[252:255], a[180:183], v[220:223]\n"
    "v_cvt_pk_bf16_f32 v33, v130, v131\n"
    "v_mfma_f32_16x16x32_bf16 v[252:255], a[248:251], a[184:187], v[252:255]\n"
    "ds_read_b64_tr_b16 a[238:239], v13 offset:28672\n"
    "v_add_u32 v13, s76, v13\n"
    "v_mfma_f32_16x16x32_bf16 v[252:255], a[252:255], a[188:191], v[252:255]\n"
    "s_waitcnt lgkmcnt(4)\n"
    "v_cvt_pk_bf16_f32 v34, v132, v133\n"
    "v_cvt_pk_bf16_f32 v35, v134, v135\n"
    "v_mfma_f32_16x16x32_bf16 a[0:3], a[160:163], a[224:227], a[0:3]\n"
    "ds_read_b64_tr_b16 a[240:241], v14 offset:16384\n"
    "v_cvt_pk_bf16_f32 v48, v160, v161\n"
    "v_mfma_f32_16x16x32_bf16 a[0:3], a[164:167], a[228:231], a[0:3]\n"
    "buffer_load_dwordx4 a[144:147], v6, s[44:47], s72 offen sc1\n"
    "v_mfma_f32_16x16x32_bf16 a[4:7], a[168:171], a[224:227], a[4:7]\n"
    "ds_read_b64_tr_b16 a[242:243], v14 offset:20480\n"
    "v_cvt_pk_bf16_f32 v49, v162, v163\n"
    "v_mfma_f32_16x16x32_bf16 a[4:7], a[172:175], a[228:231], a[4:7]\n"
    "v_cvt_pk_bf16_f32 v50, v164, v165\n"
    "v_cvt_pk_bf16_f32 v51, v166, v167\n"
    "v_mfma_f32_16x16x32_bf16 a[8:11], a[176:179], a[224:227], a[8:11]\n"
    "ds_read_b64_tr_b16 a[244:245], v14 offset:24576\n"
    "v_cvt_pk_bf16_f32 v64, v192, v193\n"
    "v_mfma_f32_16x16x32_bf16 a[8:11], a[180:183], a[228:231], a[8:11]\n"
    "s_add_u32 s73, s73, 0x100000\n"
    "s_add_u32 s83, s83, 0x100000\n"
    "s_add_u32 s74, s74, 0x8000\n"
    "s_cmp_lt_u32 s74, 0x28000\n"
    "s_cselect_b32 s74, s74, 0\n"
    "s_add_u32 m0, s74, s84\n"
    "v_mfma_f32_16x16x32_bf16 a[12:15], a[184:187], a[224:227], a[12:15]\n"
    "ds_read_b64_tr_b16 a[246:247], v14 offset:28672\n"
    "v_add_u32 v14, s76, v14\n"
    "v_mfma_f32_16x16x32_bf16 a[12:15], a[188:191], a[228:231], a[12:15]\n"
    "s_waitcnt lgkmcnt(4)\n"
    "v_cvt_pk_bf16_f32 v65, v194, v195\n"
    "v_cvt_pk_bf16_f32 v66, v196, v197\n"
    "v_mfma_f32_16x16x32_bf16 a[16:19], a[160:163], a[232:235], a[16:19]\n"
    "ds_read_b64_tr_b16 a[248:249], v15 offset:16384\n"
    "v_cvt_pk_bf16_f32 v67, v198, v199\n"
    "v_mfma_f32_16x16x32_bf16 a[16:19], a[164:167], a[236:239], a[16:19]\n"
    "buffer_load_dwordx4 v8, s[68:71], s73 offen lds\n"
    "v_mfma_f32_16x16x32_bf16 a[20:23], a[168:171], a[232:235], a[20:23]\n"
    "ds_read_b64_tr_b16 a[250:251], v15 offset:20480\n"
    "v_cvt_pk_bf16_f32 v80, v224, v225\n"
    "v_mfma_f32_16x16x32_bf16 a[20:23], a[172:175], a[236:239], a[20:23]\n"
    "buffer_load_dwordx4 v9, s[68:71], s73 offen offset:1024 lds\n"
    "v_mfma_f32_16x16x32_bf16 a[24:27], a[176:179], a[232:235], a[24:27]\n"
    "ds_read_b64_tr_b16 a[252:253], v15 offset:24576\n"
    "v_cvt_pk_bf16_f32 v81, v226, v227\n"
    "v_mfma_f32_16x16x32_bf16 a[24:27], a[180:183], a[236:239], a[24:27]\n"
    "v_cvt_pk_bf16_f32 v82, v228, v229\n"
    "v_cvt_pk_bf16_f32 v83, v230, v231\n"
    "v_mfma_f32_16x16x32_bf16 a[28:31], a[184:187], a[232:235], a[28:31]\n"
    "ds_read_b64_tr_b16 a[254:255], v15 offset:28672\n"
    "v_add_u32 v15, s76, v15\n"
    "v_mfma_f32_16x16x32_bf16 a[28:31], a[188:191], a[236:239], a[28:31]\n"
    "s_waitcnt lgkmcnt(4)\n"
    "v_cvt_pk_bf16_f32 v36, v136, v137\n"
    "v_cvt_pk_bf16_f32 v37, v138, v139\n"
    "v_mfma_f32_16x16x32_bf16 a[32:35], a[160:163], a[240:243], a[32:35]\n"
    "ds_read_b64_tr_b16 a[224:225], v16 offset:16384\n"
    "v_cvt_pk_bf16_f32 v38, v140, v141\n"
    "v_mfma_f32_16x16x32_bf16 a[32:35], a[164:167], a[244:247], a[32:35]\n"
    "buffer_load_dwordx4 a[148:151], v6, s[44:47], s72 offen offset:1024 sc1\n"
    "v_mfma_f32_16x16x32_bf16 a[36:39], a[168:171], a[240:243], a[36:39]\n"
    "ds_read_b64_tr_b16 a[226:227], v16 offset:20480\n"
    "v_cvt_pk_bf16_f32 v39, v142, v143\n"
    "v_mfma_f32_16x16x32_bf16 a[36:39], a[172:175], a[244:247], a[36:39]\n"
    "v_cvt_pk_bf16_f32 v52, v168, v169\n"
    "v_cvt_pk_bf16_f32 v53, v170, v171\n"
    "v_mfma_f32_16x16x32_bf16 a[40:43], a[176:179], a[240:243], a[40:43]\n"
    "ds_read_b64_tr_b16 a[228:229], v16 offset:24576\n"
    "v_cvt_pk_bf16_f32 v54, v172, v173\n"
    "v_mfma_f32_16x16x32_bf16 a[40:43], a[180:183], a[244:247], a[40:43]\n"
    "buffer_load_dwordx4 v10, s[68:71], s73 offen offset:2048 lds\n"
    "v_mfma_f32_16x16x32_bf16 a[44:47], a[184:187], a[240:243], a[44:47]\n"
    "ds_read_b64_tr_b16 a[230:231], v16 offset:28672\n"
    "v_add_u32 v16, s76, v16\n"
    "v_mfma_f32_16x16x32_bf16 a[44:47], a[188:191], a[244:247], a[44:47]\n"
    "s_waitcnt lgkmcnt(4)\n"
    "v_cvt_pk_bf16_f32 v55, v174, v175\n"
    "v_cvt_pk_bf16_f32 v68, v200, v201\n"
    "v_mfma_f32_16x16x32_bf16 a[48:51], a[160:163], a[248:251], a[48:51]\n"
    "ds_read_b64_tr_b16 a[232:233], v17 offset:16384\n"
    "v_cvt_pk_bf16_f32 v69, v202, v203\n"
    "v_mfma_f32_16x16x32_bf16 a[48:51], a[164:167], a[252:255], a[48:51]\n"
    "v_cvt_pk_bf16_f32 v70, v204, v205\n"
    "v_cvt_pk_bf16_f32 v71, v206, v207\n"
    "v_mfma_f32_16x16x32_bf16 a[52:55], a[168:171], a[248:251], a[52:55]\n"
    "ds_read_b64_tr_b16 a[234:235], v17 offset:20480\n"
    "v_cvt_pk_bf16_f32 v84, v232, v233\n"
    "v_mfma_f32_16x16x32_bf16 a[52:55], a[172:175], a[252:255], a[52:55]\n"
    "buffer_load_dwordx4 v11, s[68:71], s73 offen offset:3072 lds\n"
    "v_mfma_f32_16x16x32_bf16 a[56:59], a[176:179], a[248:251], a[56:59]\n"
    "ds_read_b64_tr_b16 a[236:237], v17 offset:24576\n"
    "v_cvt_pk_bf16_f32 v85, v234, v235\n"
    "v_mfma_f32_16x16x32_bf16 a[56:59], a[180:183], a[252:255], a[56:59]\n"
    "v_cvt_pk_bf16_f32 v86, v236, v237\n"
    "v_cvt_pk_bf16_f32 v87, v238, v239\n"
    "v_mfma_f32_16x16x32_bf16 a[60:63], a[184:187], a[248:251], a[60:63]\n"
    "ds_read_b64_tr_b16 a[238:239], v17 offset:28672\n"
    "v_add_u32 v17, s76, v17\n"
    "v_mfma_f32_16x16x32_bf16 a[60:63], a[188:191], a[252:255], a[60:63]\n"
    "s_waitcnt lgkmcnt(4)\n"
    "v_cvt_pk_bf16_f32 v40, v144, v145\n"
    "v_cvt_pk_bf16_f32 v41, v146, v147\n"
    "v_mfma_f32_16x16x32_bf16 a[64:67], a[160:163], a[224:227], a[64:67]\n"
    "ds_read_b64_tr_b16 a[240:241], v18 offset:16384\n"
    "v_cvt_pk_bf16_f32 v42, v148, v149\n"
    "v_mfma_f32_16x16x32_bf16 a[64:67], a[164:167], a[228:231], a[64:67]\n"
    "buffer_load_dwordx4 a[152:155], v7, s[44:47], s72 offen sc1\n"
    "v_mfma_f32_16x16x32_bf16 a[68:71], a[168:171], a[224:227], a[68:71]\n"
    "ds_read_b64_tr_b16 a[242:243], v18 offset:20480\n"
    "v_cvt_pk_bf16_f32 v43, v150, v151\n"
    "v_mfma_f32_16x16x32_bf16 a[68:71], a[172:175], a[228:231], a[68:71]\n"
    "v_cvt_pk_bf16_f32 v56, v176, v177\n"
    "v_cvt_pk_bf16_f32 v57, v178, v179\n"
    "v_mfma_f32_16x16x32_bf16 a[72:75], a[176:179], a[224:227], a[72:75]\n"
    "s_add_u32 m0, s74, s85\n"
    "ds_read_b64_tr_b16 a[244:245], v18 offset:24576\n"
    "v_mfma_f32_16x16x32_bf16 a[72:75], a[180:183], a[228:231], a[72:75]\n"
    "buffer_load_dwordx4 v8, s[68:71], s83 offen lds\n"
    "v_mfma_f32_16x16x32_bf16 a[76:79], a[184:187], a[224:227], a[76:79]\n"
    "ds_read_b64_tr_b16 a[246:247], v18 offset:28672\n"
    "v_add_u32 v18, s76, v18\n"
    "v_mfma_f32_16x16x32_bf16 a[76:79], a[188:191], a[228:231], a[76:79]\n"
    "s_waitcnt lgkmcnt(4)\n"
    "v_cvt_pk_bf16_f32 v58, v180, v181\n"
    "v_cvt_pk_bf16_f32 v59, v182, v183\n"
    "v_mfma_f32_16x16x32_bf16 a[80:83], a[160:163], a[232:235], a[80:83]\n"
    "ds_read_b64_tr_b16 a[248:249], v19 offset:16384\n"
    "v_cvt_pk_bf16_f32 v72, v208, v209\n"
    "v_mfma_f32_16x16x32_bf16 a[80:83], a[164:167], a[236:239], a[80:83]\n"
    "v_cvt_pk_bf16_f32 v73, v210, v211\n"
    "v_cvt_pk_bf16_f32 v74, v212, v213\n"
    "v_mfma_f32_16x16x32_bf16 a[84:87], a[168:171], a[232:235], a[84:87]\n"
    "ds_read_b64_tr_b16 a[250:251], v19 offset:20480\n"
    "v_cvt_pk_bf16_f32 v75, v214, v215\n"
    "v_mfma_f32_16x16x32_bf16 a[84:87], a[172:175], a[236:239], a[84:87]\n"
    "buffer_load_dwordx4 v9, s[68:71], s83 offen offset:1024 lds\n"
    "v_mfma_f32_16x16x32_bf16 a[88:91], a[176:179], a[232:235], a[88:91]\n"
    "ds_read_b64_tr_b16 a[252:253], v19 offset:24576\n"
    "v_cvt_pk_bf16_f32 v88, v240, v241\n"
    "v_mfma_f32_16x16x32_bf16 a[88:91], a[180:183], a[236:239], a[88:91]\n"
    "v_cvt_pk_bf16_f32 v89, v242, v243\n"
    "v_cvt_pk_bf16_f32 v90, v244, v245\n"
    "v_mfma_f32_16x16x32_bf16 a[92:95], a[184:187], a[232:235], a[92:95]\n"
    "ds_read_b64_tr_b16 a[254:255], v19 offset:28672\n"
    "v_add_u32 v19, s76, v19\n"
    "v_mfma_f32_16x16x32_bf16 a[92:95], a[188:191], a[236:239], a[92:95]\n"
    "s_waitcnt vmcnt(21) lgkmcnt(4)\n"
    "s_barrier\n"
    "v_cvt_pk_bf16_f32 v91, v246, v247\n"
    "v_mfma_f32_16x16x32_bf16 a[96:99], a[160:163], a[240:243], a[96:99]\n"
    "ds_read_b64_tr_b16 a[224:225], v12\n"
    "v_cvt_pk_bf16_f32 v44, v152, v153\n"
    "v_mfma_f32_16x16x32_bf16 a[96:99], a[164:167], a[244:247], a[96:99]\n"
    "buffer_load_dwordx4 a[156:159], v7, s[44:47], s72 offen offset:1024 sc1\n"
    "v_mfma_f32_16x16x32_bf16 a[100:103], a[168:171], a[240:243], a[100:103]\n"
    "s_add_u32 s72, s72, 0x40000\n"
    "ds_read_b64_tr_b16 a[226:227], v12 offset:4096\n"
    "v_mfma_f32_16x16x32_bf16 a[100:103], a[172:175], a[244:247], a[100:103]\n"
    "v_cvt_pk_bf16_f32 v45, v154, v155\n"
    "v_cvt_pk_bf16_f32 v46, v156, v157\n"
    "v_mfma_f32_16x16x32_bf16 a[104:107], a[176:179], a[240:243], a[104:107]\n"
    "ds_read_b64_tr_b16 a[228:229], v12 offset:8192\n"
    "v_cvt_pk_bf16_f32 v47, v158, v159\n"
    "v_mfma_f32_16x16x32_bf16 a[104:107], a[180:183], a[244:247], a[104:107]\n"
    "buffer_load_dwordx4 v10, s[68:71], s83 offen offset:2048 lds\n"
    "v_mfma_f32_16x16x32_bf16 a[108:111], a[184:187], a[240:243], a[108:111]\n"
    "ds_read_b64_tr_b16 a[230:231], v12 offset:12288\n"
    "v_cvt_pk_bf16_f32 v60, v184, v185\n"
    "v_mfma_f32_16x16x32_bf16 a[108:111], a[188:191], a[244:247], a[108:111]\n"
    "s_waitcnt lgkmcnt(4)\n"
    "v_cvt_pk_bf16_f32 v61, v186, v187\n"
    "v_cvt_pk_bf16_f32 v62, v188, v189\n"
    "v_mfma_f32_16x16x32_bf16 a[112:115], a[160:163], a[248:251], a[112:115]\n"
    "ds_read_b64_tr_b16 a[232:233], v13\n"
    "v_cvt_pk_bf16_f32 v63, v190, v191\n"
    "v_mfma_f32_16x16x32_bf16 a[112:115], a[164:167], a[252:255], a[112:115]\n"
    "v_cvt_pk_bf16_f32 v76, v216, v217\n"
    "v_cvt_pk_bf16_f32 v77, v218, v219\n"
    "v_mfma_f32_16x16x32_bf16 a[116:119], a[168:171], a[248:251], a[116:119]\n"
    "ds_read_b64_tr_b16 a[234:235], v13 offset:4096\n"
    "v_cvt_pk_bf16_f32 v78, v220, v221\n"
    "v_mfma_f32_16x16x32_bf16 a[116:119], a[172:175], a[252:255], a[116:119]\n"
    "buffer_load_dwordx4 v11, s[68:71], s83 offen offset:3072 lds\n"
    "v_mfma_f32_16x16x32_bf16 a[120:123], a[176:179], a[248:251], a[120:123]\n"
    "s_add_u32 s73, s73, 0x100000\n"
    "ds_read_b64_tr_b16 a[236:237], v13 offset:8192\n"
    "v_mfma_f32_16x16x32_bf16 a[120:123], a[180:183], a[252:255], a[120:123]\n"
    "s_add_u32 s83, s83, 0x100000\n"
    "s_add_u32 s74, s74, 0x8000\n"
    "v_cvt_pk_bf16_f32 v79, v222, v223\n"
    "v_mfma_f32_16x16x32_bf16 a[124:127], a[184:187], a[248:251], a[124:127]\n"
    "ds_read_b64_tr_b16 a[238:239], v13 offset:12288\n"
    "v_cvt_pk_bf16_f32 v92, v248, v249\n"
    "v_mfma_f32_16x16x32_bf16 a[124:127], a[188:191], a[252:255], a[124:127]\n"
    "s_cmp_lt_u32 s74, 0x28000\n"
    "s_cselect_b32 s74, s74, 0\n"
    "v_cvt_pk_bf16_f32 v93, v250, v251\n"
    "v_cvt_pk_bf16_f32 v94, v252, v253\n"
    "v_cvt_pk_bf16_f32 v95, v254, v255\n"
    "s_waitcnt lgkmcnt(4)\n"
    "v_mfma_f32_16x16x32_bf16 v[128:131], a[224:227], a[192:195], 0\n"
    "ds_read_b64_tr_b16 a[240:241], v14\n"
    "v_mfma_f32_16x16x32_bf16 v[128:131], a[228:231], a[196:199], v[128:131]\n"
    "buffer_load_dwordx4 a[160:163], v4, s[44:47], s72 offen sc1\n"
    "v_mfma_f32_16x16x32_bf16 v[160:163], a[224:227], a[200:203], 0\n"
    "ds_read_b64_tr_b16 a[242:243], v14 offset:4096\n"
    "v_mfma_f32_16x16x32_bf16 v[160:163], a[228:231], a[204:207], v[160:163]\n"
    "buffer_store_dwordx4 v[32:35], v24, s[56:59], s89 offen sc1\n"
    "v_mfma_f32_16x16x32_bf16 v[192:195], a[224:227], a[208:211], 0\n"
    "ds_read_b64_tr_b16 a[244:245], v14 offset:8192\n"
    "v_mfma_f32_16x16x32_bf16 v[192:195], a[228:231], a[212:215], v[192:195]\n"
    "v_mfma_f32_16x16x32_bf16 v[224:227], a[224:227], a[216:219], 0\n"
    "ds_read_b64_tr_b16 a[246:247], v14 offset:12288\n"
    "v_mfma_f32_16x16x32_bf16 v[224:227], a[228:231], a[220:223], v[224:227]\n"
    "s_waitcnt lgkmcnt(4)\n"
    "v_mfma_f32_16x16x32_bf16 v[132:135], a[232:235], a[192:195], 0\n"
    "ds_read_b64_tr_b16 a[248:249], v15\n"
    "v_mfma_f32_16x16x32_bf16 v[132:135], a[236:239], a[196:199], v[132:135]\n"
    "buffer_load_dwordx4 a[164:167], v4, s[44:47], s72 offen offset:1024 sc1\n"
    "v_mfma_f32_16x16x32_bf16 v[164:167], a[232:235], a[200:203], 0\n"
    "s_add_u32 m0, s74, s84\n"
    "ds_read_b64_tr_b16 a[250:251], v15 offset:4096\n"
    "v_mfma_f32_16x16x32_bf16 v[164:167], a[236:239], a[204:207], v[164:167]\n"
    "buffer_load_dwordx4 v8, s[68:71], s73 offen lds\n"
    "v_mfma_f32_16x16x32_bf16 v[196:199], a[232:235], a[208:211], 0\n"
    "ds_read_b64_tr_b16 a[252:253], v15 offset:8192\n"
    "v_mfma_f32_16x16x32_bf16 v[196:199], a[236:239], a[212:215], v[196:199]\n"
    "v_mfma_f32_16x16x32_bf16 v[228:231], a[232:235], a[216:219], 0\n"
    "ds_read_b64_tr_b16 a[254:255], v15 offset:12288\n"
    "v_mfma_f32_16x16x32_bf16 v[228:231], a[236:239], a[220:223], v[228:231]\n"
    "s_waitcnt lgkmcnt(4)\n"
    "v_mfma_f32_16x16x32_bf16 v[136:139], a[240:243], a[192:195], 0\n"
    "ds_read_b64_tr_b16 a[224:225], v16\n"
    "v_mfma_f32_16x16x32_bf16 v[136:139], a[244:247], a[196:199], v[136:139]\n"
    "buffer_load_dwordx4 a[168:171], v5, s[44:47], s72 offen sc1\n"
    "v_mfma_f32_16x16x32_bf16 v[168:171], a[240:243], a[200:203], 0\n"
    "ds_read_b64_tr_b16 a[226:227], v16 offset:4096\n"
    "v_mfma_f32_16x16x32_bf16 v[168:171], a[244:247], a[204:207], v[168:171]\n"
    "buffer_store_dwordx4 v[36:39], v24, s[56:59], s89 offen offset:1024 sc1\n"
    "v_mfma_f32_16x16x32_bf16 v[200:203], a[240:243], a[208:211], 0\n"
    "ds_read_b64_tr_b16 a[228:229], v16 offset:8192\n"
    "v_mfma_f32_16x16x32_bf16 v[200:203], a[244:247], a[212:215], v[200:203]\n"
    "v_mfma_f32_16x16x32_bf16 v[232:235], a[240:243], a[216:219], 0\n"
    "ds_read_b64_tr_b16 a[230:231], v16 offset:12288\n"
    "v_mfma_f32_16x16x32_bf16 v[232:235], a[244:247], a[220:223], v[232:235]\n"
    "s_waitcnt lgkmcnt(4)\n"
    "v_mfma_f32_16x16x32_bf16 v[140:143], a[248:251], a[192:195], 0\n"
    "ds_read_b64_tr_b16 a[232:233], v17\n"
    "v_mfma_f32_16x16x32_bf16 v[140:143], a[252:255], a[196:199], v[140:143]\n"
    "buffer_load_dwordx4 a[172:175], v5, s[44:47], s72 offen offset:1024 sc1\n"
    "v_mfma_f32_16x16x32_bf16 v[172:175], a[248:251], a[200:203], 0\n"
    "ds_read_b64_tr_b16 a[234:235], v17 offset:4096\n"
    "v_mfma_f32_16x16x32_bf16 v[172:175], a[252:255], a[204:207], v[172:175]\n"
    "buffer_load_dwordx4 v9, s[68:71], s73 offen offset:1024 lds\n"
    "v_mfma_f32_16x16x32_bf16 v[204:207], a[248:251], a[208:211], 0\n"
    "ds_read_b64_tr_b16 a[236:237], v17 offset:8192\n"
    "v_mfma_f32_16x16x32_bf16 v[204:207], a[252:255], a[212:215], v[204:207]\n"
    "v_mfma_f32_16x16x32_bf16 v[236:239], a[248:251], a[216:219], 0\n"
    "ds_read_b64_tr_b16 a[238:239], v17 offset:12288\n"
    "v_mfma_f32_16x16x32_bf16 v[236:239], a[252:255], a[220:223], v[236:239]\n"
    "s_waitcnt lgkmcnt(4)\n"
    "v_mfma_f32_16x16x32_bf16 v[144:147], a[224:227], a[192:195], 0\n"
    "ds_read_b64_tr_b16 a[240:241], v18\n"
    "v_mfma_f32_16x16x32_bf16 v[144:147], a[228:231], a[196:199], v[144:147]\n"
    "buffer_load_dwordx4 a[176:179], v6, s[44:47], s72 offen sc1\n"
    "v_mfma_f32_16x16x32_bf16 v[176:179], a[224:227], a[200:203], 0\n"
    "ds_read_b64_tr_b16 a[242:243], v18 offset:4096\n"
    "v_mfma_f32_16x16x32_bf16 v[176:179], a[228:231], a[204:207], v[176:179]\n"
    "buffer_store_dwordx4 v[40:43], v24, s[56:59], s89 offen offset:2048 sc1\n"
    "v_mfma_f32_16x16x32_bf16 v[208:211], a[224:227], a[208:211], 0\n"
    "ds_read_b64_tr_b16 a[244:245], v18 offset:8192\n"
    "v_mfma_f32_16x16x32_bf16 v[208:211], a[228:231], a[212:215], v[208:211]\n"
    "buffer_load_dwordx4 a[180:183], v6, s[44:47], s72 offen offset:1024 sc1\n"
    "v_mfma_f32_16x16x32_bf16 v[240:243], a[224:227], a[216:219], 0\n"
    "ds_read_b64_tr_b16 a[246:247], v18 offset:12288\n"
    "v_mfma_f32_16x16x32_bf16 v[240:243], a[228:231], a[220:223], v[240:243]\n"
    "s_waitcnt lgkmcnt(4)\n"
    "v_mfma_f32_16x16x32_bf16 v[148:151], a[232:235], a[192:195], 0\n"
    "ds_read_b64_tr_b16 a[248:249], v19\n"
    "v_mfma_f32_16x16x32_bf16 v[148:151], a[236:239], a[196:199], v[148:151]\n"
    "v_mfma_f32_16x16x32_bf16 v[180:183], a[232:235], a[200:203], 0\n"
    "ds_read_b64_tr_b16 a[250:251], v19 offset:4096\n"
    "v_mfma_f32_16x16x32_bf16 v[180:183], a[236:239], a[204:207], v[180:183]\n"
    "buffer_load_dwordx4 v10, s[68:71], s73 offen offset:2048 lds\n"
    "v_mfma_f32_16x16x32_bf16 v[212:215], a[232:235], a[208:211], 0\n"
    "ds_read_b64_tr_b16 a[252:253], v19 offset:8192\n"
    "v_mfma_f32_16x16x32_bf16 v[212:215], a[236:239], a[212:215], v[212:215]\n"
    "v_mfma_f32_16x16x32_bf16 v[244:247], a[232:235], a[216:219], 0\n"
    "ds_read_b64_tr_b16 a[254:255], v19 offset:12288\n"
    "v_mfma_f32_16x16x32_bf16 v[244:247], a[236:239], a[220:223], v[244:247]\n"
    "s_waitcnt lgkmcnt(4)\n"
    "v_mfma_f32_16x16x32_bf16 v[152:155], a[240:243], a[192:195], 0\n"
    "ds_read_b64_tr_b16 a[224:225], v12 offset:16384\n"
    "v_mfma_f32_16x16x32_bf16 v[152:155], a[244:247], a[196:199], v[152:155]\n"
    "buffer_load_dwordx4 a[184:187], v7, s[44:47], s72 offen sc1\n"
    "v_mfma_f32_16x16x32_bf16 v[184:187], a[240:243], a[200:203], 0\n"
    "s_add_u32 s75, s75, 1\n"
    "ds_read_b64_tr_b16 a[226:227], v12 offset:20480\n"
    "v_mfma_f32_16x16x32_bf16 v[184:187], a[244:247], a[204:207], v[184:187]\n"
    "buffer_store_dwordx4 v[44:47], v24, s[56:59], s89 offen offset:3072 sc1\n"
    "v_mfma_f32_16x16x32_bf16 v[216:219], a[240:243], a[208:211], 0\n"
    "ds_read_b64_tr_b16 a[228:229], v12 offset:24576\n"
    "v_mfma_f32_16x16x32_bf16 v[216:219], a[244:247], a[212:215], v[216:219]\n"
    "s_cmp_lt_u32 s75, 5\n"
    "s_cselect_b32 s75, s75, 0\n"
    "s_cselect_b32 s76, 0x8000, s77\n"
    "v_mfma_f32_16x16x32_bf16 v[248:251], a[240:243], a[216:219], 0\n"
    "ds_read_b64_tr_b16 a[230:231], v12 offset:28672\n"
    "v_add_u32 v12, s76, v12\n"
    "v_mfma_f32_16x16x32_bf16 v[248:251], a[244:247], a[220:223], v[248:251]\n"
    "s_waitcnt lgkmcnt(4)\n"
    "v_mfma_f32_16x16x32_bf16 v[156:159], a[248:251], a[192:195], 0\n"
    "ds_read_b64_tr_b16 a[232:233], v13 offset:16384\n"
    "v_mfma_f32_16x16x32_bf16 v[156:159], a[252:255], a[196:199], v[156:159]\n"
    "buffer_load_dwordx4 a[188:191], v7, s[44:47], s72 offen offset:1024 sc1\n"
    "v_mfma_f32_16x16x32_bf16 v[188:191], a[248:251], a[200:203], 0\n"
    "s_add_u32 s72, s72, 0x40000\n"
    "ds_read_b64_tr_b16 a[234:235], v13 offset:20480\n"
    "v_mfma_f32_16x16x32_bf16 v[188:191], a[252:255], a[204:207], v[188:191]\n"
    "buffer_load_dwordx4 v11, s[68:71], s73 offen offset:3072 lds\n"
    "v_mfma_f32_16x16x32_bf16 v[220:223], a[248:251], a[208:211], 0\n"
    "ds_read_b64_tr_b16 a[236:237], v13 offset:24576\n"
    "v_mfma_f32_16x16x32_bf16 v[220:223], a[252:255], a[212:215], v[220:223]\n"
    "v_mfma_f32_16x16x32_bf16 v[252:255], a[248:251], a[216:219], 0\n"
    "ds_read_b64_tr_b16 a[238:239], v13 offset:28672\n"
    "v_add_u32 v13, s76, v13\n"
    "v_mfma_f32_16x16x32_bf16 v[252:255], a[252:255], a[220:223], v[252:255]\n"
    "s_waitcnt vmcnt(18) lgkmcnt(4)\n"
    "v_mfma_f32_16x16x32_bf16 v[128:131], a[224:227], a[128:131], v[128:131]\n"
    "s_add_u32 s89, s89, 0x1000\n"
    "ds_read_b64_tr_b16 a[240:241], v14 offset:16384\n"
    "v_mfma_f32_16x16x32_bf16 v[128:131], a[228:231], a[132:135], v[128:131]\n"
    "buffer_load_dwordx4 a[192:195], v4, s[44:47], s72 offen sc1\n"
    "v_mfma_f32_16x16x32_bf16 v[160:163], a[224:227], a[136:139], v[160:163]\n"
    "ds_read_b64_tr_b16 a[242:243], v14 offset:20480\n"
    "v_mfma_f32_16x16x32_bf16 v[160:163], a[228:231], a[140:143], v[160:163]\n"
    "v_mfma_f32_16x16x32_bf16 v[192:195], a[224:227], a[144:147], v[192:195]\n"
    "ds_read_b64_tr_b16 a[244:245], v14 offset:24576\n"
    "v_mfma_f32_16x16x32_bf16 v[192:195], a[228:231], a[148:151], v[192:195]\n"
    "v_mfma_f32_16x16x32_bf16 v[224:227], a[224:227], a[152:155], v[224:227]\n"
    "ds_read_b64_tr_b16 a[246:247], v14 offset:28672\n"
    "v_add_u32 v14, s76, v14\n"
    "v_mfma_f32_16x16x32_bf16 v[224:227], a[228:231], a[156:159], v[224:227]\n"
    "s_waitcnt lgkmcnt(4)\n"
    "v_mfma_f32_16x16x32_bf16 v[132:135], a[232:235], a[128:131], v[132:135]\n"
    "ds_read_b64_tr_b16 a[248:249], v15 offset:16384\n"
    "v_mfma_f32_16x16x32_bf16 v[132:135], a[236:239], a[132:135], v[132:135]\n"
    "buffer_load_dwordx4 a[196:199], v4, s[44:47], s72 offen offset:1024 sc1\n"
    "v_mfma_f32_16x16x32_bf16 v[164:167], a[232:235], a[136:139], v[164:167]\n"
    "s_add_u32 m0, s74, s85\n"
    "ds_read_b64_tr_b16 a[250:251], v15 offset:20480\n"
    "v_mfma_f32_16x16x32_bf16 v[164:167], a[236:239], a[140:143], v[164:167]\n"
    "buffer_load_dwordx4 v8, s[68:71], s83 offen lds\n"
    "v_mfma_f32_16x16x32_bf16 v[196:199], a[232:235], a[144:147], v[196:199]\n"
    "ds_read_b64_tr_b16 a[252:253], v15 offset:24576\n"
    "v_mfma_f32_16x16x32_bf16 v[196:199], a[236:239], a[148:151], v[196:199]\n"
    "v_mfma_f32_16x16x32_bf16 v[228:231], a[232:235], a[152:155], v[228:231]\n"
    "ds_read_b64_tr_b16 a[254:255], v15 offset:28672\n"
    "v_add_u32 v15, s76, v15\n"
    "v_mfma_f32_16x16x32_bf16 v[228:231], a[236:239], a[156:159], v[228:231]\n"
    "s_waitcnt lgkmcnt(4)\n"
    "v_mfma_f32_16x16x32_bf16 v[136:139], a[240:243], a[128:131], v[136:139]\n"
    "ds_read_b64_tr_b16 a[224:225], v16 offset:16384\n"
    "v_mfma_f32_16x16x32_bf16 v[136:139], a[244:247], a[132:135], v[136:139]\n"
    "buffer_load_dwordx4 a[200:203], v5, s[44:47], s72 offen sc1\n"
    "v_mfma_f32_16x16x32_bf16 v[168:171], a[240:243], a[136:139], v[168:171]\n"
    "ds_read_b64_tr_b16 a[226:227], v16 offset:20480\n"
    "v_mfma_f32_16x16x32_bf16 v[168:171], a[244:247], a[140:143], v[168:171]\n"
    "buffer_store_dwordx4 v[48:51], v24, s[56:59], s89 offen sc1\n"
    "v_mfma_f32_16x16x32_bf16 v[200:203], a[240:243], a[144:147], v[200:203]\n"
    "ds_read_b64_tr_b16 a[228:229], v16 offset:24576\n"
    "v_mfma_f32_16x16x32_bf16 v[200:203], a[244:247], a[148:151], v[200:203]\n"
    "v_mfma_f32_16x16x32_bf16 v[232:235], a[240:243], a[152:155], v[232:235]\n"
    "ds_read_b64_tr_b16 a[230:231], v16 offset:28672\n"
    "v_add_u32 v16, s76, v16\n"
    "v_mfma_f32_16x16x32_bf16 v[232:235], a[244:247], a[156:159], v[232:235]\n"
    "s_waitcnt lgkmcnt(4)\n"
    "v_mfma_f32_16x16x32_bf16 v[140:143], a[248:251], a[128:131], v[140:143]\n"
    "ds_read_b64_tr_b16 a[232:233], v17 offset:16384\n"
    "v_mfma_f32_16x16x32_bf16 v[140:143], a[252:255], a[132:135], v[140:143]\n"
    "buffer_load_dwordx4 a[204:207], v5, s[44:47], s72 offen offset:1024 sc1\n"
    "v_mfma_f32_16x16x32_bf16 v[172:175], a[248:251], a[136:139], v[172:175]\n"
    "ds_read_b64_tr_b16 a[234:235], v17 offset:20480\n"
    "v_mfma_f32_16x16x32_bf16 v[172:175], a[252:255], a[140:143], v[172:175]\n"
    "buffer_load_dwordx4 v9, s[68:71], s83 offen offset:1024 lds\n"
    "v_mfma_f32_16x16x32_bf16 v[204:207], a[248:251], a[144:147], v[204:207]\n"
    "ds_read_b64_tr_b16 a[236:237], v17 offset:24576\n"
    "v_mfma_f32_16x16x32_bf16 v[204:207], a[252:255], a[148:151], v[204:207]\n"
    "v_mfma_f32_16x16x32_bf16 v[236:239], a[248:251], a[152:155], v[236:239]\n"
    "ds_read_b64_tr_b16 a[238:239], v17 offset:28672\n"
    "v_add_u32 v17, s76, v17\n"
    "v_mfma_f32_16x16x32_bf16 v[236:239], a[252:255], a[156:159], v[236:239]\n"
    "s_waitcnt lgkmcnt(4)\n"
    "v_mfma_f32_16x16x32_bf16 v[144:147], a[224:227], a[128:131], v[144:147]\n"
    "ds_read_b64_tr_b16 a[240:241], v18 offset:16384\n"
    "v_mfma_f32_16x16x32_bf16 v[144:147], a[228:231], a[132:135], v[144:147]\n"
    "buffer_load_dwordx4 a[208:211], v6, s[44:47], s72 offen sc1\n"
    "v_mfma_f32_16x16x32_bf16 v[176:179], a[224:227], a[136:139], v[176:179]\n"
    "ds_read_b64_tr_b16 a[242:243], v18 offset:20480\n"
    "v_mfma_f32_16x16x32_bf16 v[176:179], a[228:231], a[140:143], v[176:179]\n"
    "buffer_store_dwordx4 v[52:55], v24, s[56:59], s89 offen offset:1024 sc1\n"
    "v_mfma_f32_16x16x32_bf16 v[208:211], a[224:227], a[144:147], v[208:211]\n"
    "ds_read_b64_tr_b16 a[244:245], v18 offset:24576\n"
    "v_mfma_f32_16x16x32_bf16 v[208:211], a[228:231], a[148:151], v[208:211]\n"
    "buffer_load_dwordx4 a[212:215], v6, s[44:47], s72 offen offset:1024 sc1\n"
    "v_mfma_f32_16x16x32_bf16 v[240:243], a[224:227], a[152:155], v[240:243]\n"
    "ds_read_b64_tr_b16 a[246:247], v18 offset:28672\n"
    "v_add_u32 v18, s76, v18\n"
    "v_mfma_f32_16x16x32_bf16 v[240:243], a[228:231], a[156:159], v[240:243]\n"
    "s_waitcnt lgkmcnt(4)\n"
    "v_mfma_f32_16x16x32_bf16 v[148:151], a[232:235], a[128:131], v[148:151]\n"
    "ds_read_b64_tr_b16 a[248:249], v19 offset:16384\n"
    "v_mfma_f32_16x16x32_bf16 v[148:151], a[236:239], a[132:135], v[148:151]\n"
    "v_mfma_f32_16x16x32_bf16 v[180:183], a[232:235], a[136:139], v[180:183]\n"
    "ds_read_b64_tr_b16 a[250:251], v19 offset:20480\n"
    "v_mfma_f32_16x16x32_bf16 v[180:183], a[236:239], a[140:143], v[180:183]\n"
    "buffer_load_dwordx4 v10, s[68:71], s83 offen offset:2048 lds\n"
    "v_mfma_f32_16x16x32_bf16 v[212:215], a[232:235], a[144:147], v[212:215]\n"
    "ds_read_b64_tr_b16 a[252:253], v19 offset:24576\n"
    "v_mfma_f32_16x16x32_bf16 v[212:215], a[236:239], a[148:151], v[212:215]\n"
    "v_mfma_f32_16x16x32_bf16 v[244:247], a[232:235], a[152:155], v[244:247]\n"
    "ds_read_b64_tr_b16 a[254:255], v19 offset:28672\n"
    "v_add_u32 v19, s76, v19\n"
    "v_mfma_f32_16x16x32_bf16 v[244:247], a[236:239], a[156:159], v[244:247]\n"
    "s_waitcnt lgkmcnt(4)\n"
    "s_barrier\n"
    "v_mfma_f32_16x16x32_bf16 v[152:155], a[240:243], a[128:131], v[152:155]\n"
    "ds_read_b64_tr_b16 a[224:225], v12\n"
    "v_mfma_f32_16x16x32_bf16 v[152:155], a[244:247], a[132:135], v[152:155]\n"
    "buffer_load_dwordx4 a[216:219], v7, s[44:47], s72 offen sc1\n"
    "v_mfma_f32_16x16x32_bf16 v[184:187], a[240:243], a[136:139], v[184:187]\n"
    "ds_read_b64_tr_b16 a[226:227], v12 offset:4096\n"
    "v_mfma_f32_16x16x32_bf16 v[184:187], a[244:247], a[140:143], v[184:187]\n"
    "v_mfma_f32_16x16x32_bf16 v[216:219], a[240:243], a[144:147], v[216:219]\n"
    "ds_read_b64_tr_b16 a[228:229], v12 offset:8192\n"
    "v_mfma_f32_16x16x32_bf16 v[216:219], a[244:247], a[148:151], v[216:219]\n"
    "buffer_load_dwordx4 a[220:223], v7, s[44:47], s72 offen offset:1024 sc1\n"
    "v_mfma_f32_16x16x32_bf16 v[248:251], a[240:243], a[152:155], v[248:251]\n"
    "s_add_u32 s72, s72, 0x40000\n"
    "ds_read_b64_tr_b16 a[230:231], v12 offset:12288\n"
    "v_mfma_f32_16x16x32_bf16 v[248:251], a[244:247], a[156:159], v[248:251]\n"
    "s_waitcnt lgkmcnt(4)\n"
    "v_mfma_f32_16x16x32_bf16 v[156:159], a[248:251], a[128:131], v[156:159]\n"
    "ds_read_b64_tr_b16 a[232:233], v13\n"
    "v_mfma_f32_16x16x32_bf16 v[156:159], a[252:255], a[132:135], v[156:159]\n"
    "v_mfma_f32_16x16x32_bf16 v[188:191], a[248:251], a[136:139], v[188:191]\n"
    "ds_read_b64_tr_b16 a[234:235], v13 offset:4096\n"
    "v_mfma_f32_16x16x32_bf16 v[188:191], a[252:255], a[140:143], v[188:191]\n"
    "buffer_load_dwordx4 v11, s[68:71], s83 offen offset:3072 lds\n"
    "v_mfma_f32_16x16x32_bf16 v[220:223], a[248:251], a[144:147], v[220:223]\n"
    "s_add_u32 s73, s73, 0x100000\n"
    "ds_read_b64_tr_b16 a[236:237], v13 offset:8192\n"
    "v_mfma_f32_16x16x32_bf16 v[220:223], a[252:255], a[148:151], v[220:223]\n"
    "s_add_u32 s83, s83, 0x100000\n"
    "s_add_u32 s74, s74, 0x8000\n"
    "v_mfma_f32_16x16x32_bf16 v[252:255], a[248:251], a[152:155], v[252:255]\n"
    "ds_read_b64_tr_b16 a[238:239], v13 offset:12288\n"
    "v_mfma_f32_16x16x32_bf16 v[252:255], a[252:255], a[156:159], v[252:255]\n"
    "s_cmp_lt_u32 s74, 0x28000\n"
    "s_cselect_b32 s74, s74, 0\n"
    "s_waitcnt vmcnt(15) lgkmcnt(4)\n"
    "v_mfma_f32_16x16x32_bf16 v[128:131], a[224:227], a[160:163], v[128:131]\n"
    "ds_read_b64_tr_b16 a[240:241], v14\n"
    "v_mfma_f32_16x16x32_bf16 v[128:131], a[228:231], a[164:167], v[128:131]\n"
    "buffer_load_dwordx4 a[128:131], v4, s[44:47], s72 offen sc1\n"
    "v_mfma_f32_16x16x32_bf16 v[160:163], a[224:227], a[168:171], v[160:163]\n"
    "ds_read_b64_tr_b16 a[242:243], v14 offset:4096\n"
    "v_accvgpr_read_b32 v96, a0\n"
    "v_mfma_f32_16x16x32_bf16 v[160:163], a[228:231], a[172:175], v[160:163]\n"
    "buffer_store_dwordx4 v[56:59], v24, s[56:59], s89 offen offset:2048 sc1\n"
    "v_mfma_f32_16x16x32_bf16 v[192:195], a[224:227], a[176:179], v[192:195]\n"
    "ds_read_b64_tr_b16 a[244:245], v14 offset:8192\n"
    "v_accvgpr_read_b32 v97, a1\n"
    "v_mfma_f32_16x16x32_bf16 v[192:195], a[228:231], a[180:183], v[192:195]\n"
    "v_accvgpr_read_b32 v98, a2\n"
    "v_mfma_f32_16x16x32_bf16 v[224:227], a[224:227], a[184:187], v[224:227]\n"
    "ds_read_b64_tr_b16 a[246:247], v14 offset:12288\n"
    "v_accvgpr_read_b32 v99, a3\n"
    "v_mfma_f32_16x16x32_bf16 v[224:227], a[228:231], a[188:191], v[224:227]\n"
    "s_waitcnt lgkmcnt(4)\n"
    "v_mfma_f32_16x16x32_bf16 v[132:135], a[232:235], a[160:163], v[132:135]\n"
    "ds_read_b64_tr_b16 a[248:249], v15\n"
    "v_accvgpr_read_b32 v100, a4\n"
    "v_mfma_f32_16x16x32_bf16 v[132:135], a[236:239], a[164:167], v[132:135]\n"
    "buffer_load_dwordx4 a[132:135], v4, s[44:47], s72 offen offset:1024 sc1\n"
    "v_mfma_f32_16x16x32_bf16 v[164:167], a[232:235], a[168:171], v[164:167]\n"
    "s_add_u32 m0, s74, s84\n"
    "ds_read_b64_tr_b16 a[250:251], v15 offset:4096\n"
    "v_mfma_f32_16x16x32_bf16 v[164:167], a[236:239], a[172:175], v[164:167]\n"
    "buffer_load_dwordx4 v8, s[68:71], s73 offen lds\n"
    "v_mfma_f32_16x16x32_bf16 v[196:199], a[232:235], a[176:179], v[196:199]\n"
    "ds_read_b64_tr_b16 a[252:253], v15 offset:8192\n"
    "v_accvgpr_read_b32 v101, a5\n"
    "v_mfma_f32_16x16x32_bf16 v[196:199], a[236:239], a[180:183], v[196:199]\n"
    "v_accvgpr_read_b32 v102, a6\n"
    "v_accvgpr_read_b32 v103, a7\n"
    "v_mfma_f32_16x16x32_bf16 v[228:231], a[232:235], a[184:187], v[228:231]\n"
    "ds_read_b64_tr_b16 a[254:255], v15 offset:12288\n"
    "v_mfma_f32_16x16x32_bf16 v[228:231], a[236:239], a[188:191], v[228:231]\n"
    "s_waitcnt lgkmcnt(4)\n"
    "v_cvt_pk_bf16_f32 v32, v96, v97\n"
    "v_mfma_f32_16x16x32_bf16 v[136:139], a[240:243], a[160:163], v[136:139]\n"
    "ds_read_b64_tr_b16 a[224:225], v16\n"
    "v_cvt_pk_bf16_f32 v33, v98, v99\n"
    "v_mfma_f32_16x16x32_bf16 v[136:139], a[244:247], a[164:167], v[136:139]\n"
    "buffer_load_dwordx4 a[136:139], v5, s[44:47], s72 offen sc1\n"
    "v_mfma_f32_16x16x32_bf16 v[168:171], a[240:243], a[168:171], v[168:171]\n"
    "ds_read_b64_tr_b16 a[226:227], v16 offset:4096\n"
    "v_cvt_pk_bf16_f32 v34, v100, v101\n"
    "v_mfma_f32_16x16x32_bf16 v[168:171], a[244:247], a[172:175], v[168:171]\n"
    "buffer_store_dwordx4 v[60:63], v24, s[56:59], s89 offen offset:3072 sc1\n"
    "v_mfma_f32_16x16x32_bf16 v[200:203], a[240:243], a[176:179], v[200:203]\n"
    "ds_read_b64_tr_b16 a[228:229], v16 offset:8192\n"
    "v_cvt_pk_bf16_f32 v35, v102, v103\n"
    "v_mfma_f32_16x16x32_bf16 v[200:203], a[244:247], a[180:183], v[200:203]\n"
    "v_accvgpr_read_b32 v104, a8\n"
    "v_mfma_f32_16x16x32_bf16 v[232:235], a[240:243], a[184:187], v[232:235]\n"
    "ds_read_b64_tr_b16 a[230:231], v16 offset:12288\n"
    "v_mfma_f32_16x16x32_bf16 v[232:235], a[244:247], a[188:191], v[232:235]\n"
    "s_waitcnt lgkmcnt(4)\n"
    "v_accvgpr_read_b32 v105, a9\n"
    "v_mfma_f32_16x16x32_bf16 v[140:143], a[248:251], a[160:163], v[140:143]\n"
    "ds_read_b64_tr_b16 a[232:233], v17\n"
    "v_accvgpr_read_b32 v106, a10\n"
    "v_mfma_f32_16x16x32_bf16 v[140:143], a[252:255], a[164:167], v[140:143]\n"
    "buffer_load_dwordx4 a[140:143], v5, s[44:47], s72 offen offset:1024 sc1\n"
    "v_mfma_f32_16x16x32_bf16 v[172:175], a[248:251], a[168:171], v[172:175]\n"
    "ds_read_b64_tr_b16 a[234:235], v17 offset:4096\n"
    "v_accvgpr_read_b32 v107, a11\n"
    "v_mfma_f32_16x16x32_bf16 v[172:175], a[252:255], a[172:175], v[172:175]\n"
    "buffer_load_dwordx4 v9, s[68:71], s73 offen offset:1024 lds\n"
    "v_mfma_f32_16x16x32_bf16 v[204:207], a[248:251], a[176:179], v[204:207]\n"
    "ds_read_b64_tr_b16 a[236:237], v17 offset:8192\n"
    "v_accvgpr_read_b32 v108, a12\n"
    "v_mfma_f32_16x16x32_bf16 v[204:207], a[252:255], a[180:183], v[204:207]\n"
    "s_add_u32 s89, s89, 0x1000\n"
    "v_accvgpr_read_b32 v109, a13\n"
    "v_mfma_f32_16x16x32_bf16 v[236:239], a[248:251], a[184:187], v[236:239]\n"
    "ds_read_b64_tr_b16 a[238:239], v17 offset:12288\n"
    "v_mfma_f32_16x16x32_bf16 v[236:239], a[252:255], a[188:191], v[236:239]\n"
    "s_waitcnt lgkmcnt(4)\n"
    "v_accvgpr_read_b32 v110, a14\n"
    "v_mfma_f32_16x16x32_bf16 v[144:147], a[224:227], a[160:163], v[144:147]\n"
    "ds_read_b64_tr_b16 a[240:241], v18\n"
    "v_mfma_f32_16x16x32_bf16 v[144:147], a[228:231], a[164:167], v[144:147]\n"
    "buffer_load_dwordx4 a[144:147], v6, s[44:47], s72 offen sc1\n"
    "v_mfma_f32_16x16x32_bf16 v[176:179], a[224:227], a[168:171], v[176:179]\n"
    "ds_read_b64_tr_b16 a[242:243], v18 offset:4096\n"
    "v_accvgpr_read_b32 v111, a15\n"
    "v_mfma_f32_16x16x32_bf16 v[176:179], a[228:231], a[172:175], v[176:179]\n"
    "v_cvt_pk_bf16_f32 v36, v104, v105\n"
    "v_mfma_f32_16x16x32_bf16 v[208:211], a[224:227], a[176:179], v[208:211]\n"
    "ds_read_b64_tr_b16 a[244:245], v18 offset:8192\n"
    "v_cvt_pk_bf16_f32 v37, v106, v107\n"
    "v_mfma_f32_16x16x32_bf16 v[208:211], a[228:231], a[180:183], v[208:211]\n"
    "buffer_load_dwordx4 a[148:151], v6, s[44:47], s72 offen offset:1024 sc1\n"
    "v_mfma_f32_16x16x32_bf16 v[240:243], a[224:227], a[184:187], v[240:243]\n"
    "ds_read_b64_tr_b16 a[246:247], v18 offset:12288\n"
    "v_cvt_pk_bf16_f32 v38, v108, v109\n"
    "v_mfma_f32_16x16x32_bf16 v[240:243], a[228:231], a[188:191], v[240:243]\n"
    "s_waitcnt lgkmcnt(4)\n"
    "v_cvt_pk_bf16_f32 v39, v110, v111\n"
    "v_mfma_f32_16x16x32_bf16 v[148:151], a[232:235], a[160:163], v[148:151]\n"
    "ds_read_b64_tr_b16 a[248:249], v19\n"
    "v_mfma_f32_16x16x32_bf16 v[148:151], a[236:239], a[164:167], v[148:151]\n"
    "v_accvgpr_read_b32 v96, a16\n"
    "v_mfma_f32_16x16x32_bf16 v[180:183], a[232:235], a[168:171], v[180:183]\n"
    "ds_read_b64_tr_b16 a[250:251], v19 offset:4096\n"
    "v_accvgpr_read_b32 v97, a17\n"
    "v_mfma_f32_16x16x32_bf16 v[180:183], a[236:239], a[172:175], v[180:183]\n"
    "buffer_load_dwordx4 v10, s[68:71], s73 offen offset:2048 lds\n"
    "v_mfma_f32_16x16x32_bf16 v[212:215], a[232:235], a[176:179], v[212:215]\n"
    "ds_read_b64_tr_b16 a[252:253], v19 offset:8192\n"
    "v_accvgpr_read_b32 v98, a18\n"
    "v_mfma_f32_16x16x32_bf16 v[212:215], a[236:239], a[180:183], v[212:215]\n"
    "buffer_store_dwordx4 v[64:67], v24, s[56:59], s89 offen sc1\n"
    "v_mfma_f32_16x16x32_bf16 v[244:247], a[232:235], a[184:187], v[244:247]\n"
    "ds_read_b64_tr_b16 a[254:255], v19 offset:12288\n"
    "v_accvgpr_read_b32 v99, a19\n"
    "v_mfma_f32_16x16x32_bf16 v[244:247], a[236:239], a[188:191], v[244:247]\n"
    "s_waitcnt lgkmcnt(4)\n"
    "v_accvgpr_read_b32 v100, a20\n"
    "v_mfma_f32_16x16x32_bf16 v[152:155], a[240:243], a[160:163], v[152:155]\n"
    "ds_read_b64_tr_b16 a[224:225], v12 offset:16384\n"
    "v_mfma_f32_16x16x32_bf16 v[152:155], a[244:247], a[164:167], v[152:155]\n"
    "buffer_load_dwordx4 a[152:155], v7, s[44:47], s72 offen sc1\n"
    "v_mfma_f32_16x16x32_bf16 v[184:187], a[240:243], a[168:171], v[184:187]\n"
    "s_add_u32 s75, s75, 1\n"
    "ds_read_b64_tr_b16 a[226:227], v12 offset:20480\n"
    "v_mfma_f32_16x16x32_bf16 v[184:187], a[244:247], a[172:175], v[184:187]\n"
    "v_accvgpr_read_b32 v101, a21\n"
    "v_accvgpr_read_b32 v102, a22\n"
    "v_mfma_f32_16x16x32_bf16 v[216:219], a[240:243], a[176:179], v[216:219]\n"
    "ds_read_b64_tr_b16 a[228:229], v12 offset:24576\n"
    "v_accvgpr_read_b32 v103, a23\n"
    "v_mfma_f32_16x16x32_bf16 v[216:219], a[244:247], a[180:183], v[216:219]\n"
    "s_cmp_lt_u32 s75, 5\n"
    "s_cselect_b32 s75, s75, 0\n"
    "s_cselect_b32 s76, 0x8000, s77\n"
    "v_mfma_f32_16x16x32_bf16 v[248:251], a[240:243], a[184:187], v[248:251]\n"
    "ds_read_b64_tr_b16 a[230:231], v12 offset:28672\n"
    "v_add_u32 v12, s76, v12\n"
    "v_mfma_f32_16x16x32_bf16 v[248:251], a[244:247], a[188:191], v[248:251]\n"
    "s_waitcnt lgkmcnt(4)\n"
    "v_cvt_pk_bf16_f32 v40, v96, v97\n"
    "v_mfma_f32_16x16x32_bf16 v[156:159], a[248:251], a[160:163], v[156:159]\n"
    "ds_read_b64_tr_b16 a[232:233], v13 offset:16384\n"
    "v_cvt_pk_bf16_f32 v41, v98, v99\n"
    "v_mfma_f32_16x16x32_bf16 v[156:159], a[252:255], a[164:167], v[156:159]\n"
    "buffer_load_dwordx4 a[156:159], v7, s[44:47], s72 offen offset:1024 sc1\n"
    "v_mfma_f32_16x16x32_bf16 v[188:191], a[248:251], a[168:171], v[188:191]\n"
    "s_add_u32 s72, s72, 0x40000\n"
    "ds_read_b64_tr_b16 a[234:235], v13 offset:20480\n"
    "v_mfma_f32_16x16x32_bf16 v[188:191], a[252:255], a[172:175], v[188:191]\n"
    "buffer_load_dwordx4 v11, s[68:71], s73 offen offset:3072 lds\n"
    "v_mfma_f32_16x16x32_bf16 v[220:223], a[248:251], a[176:179], v[220:223]\n"
    "ds_read_b64_tr_b16 a[236:237], v13 offset:24576\n"
    "v_cvt_pk_bf16_f32 v42, v100, v101\n"
    "v_mfma_f32_16x16x32_bf16 v[220:223], a[252:255], a[180:183], v[220:223]\n"
    "buffer_store_dwordx4 v[68:71], v24, s[56:59], s89 offen offset:1024 sc1\n"
    "v_mfma_f32_16x16x32_bf16 v[252:255], a[248:251], a[184:187], v[252:255]\n"
    "ds_read_b64_tr_b16 a[238:239], v13 offset:28672\n"
    "v_add_u32 v13, s76, v13\n"
    "v_mfma_f32_16x16x32_bf16 v[252:255], a[252:255], a[188:191], v[252:255]\n"
    "s_waitcnt vmcnt(17) lgkmcnt(4)\n"
    "v_cvt_pk_bf16_f32 v43, v102, v103\n"
    "v_accvgpr_read_b32 v104, a24\n"
    "v_mfma_f32_16x16x32_bf16 v[128:131], a[224:227], a[192:195], v[128:131]\n"
    "ds_read_b64_tr_b16 a[240:241], v14 offset:16384\n"
    "v_accvgpr_read_b32 v105, a25\n"
    "v_mfma_f32_16x16x32_bf16 v[128:131], a[228:231], a[196:199], v[128:131]\n"
    "buffer_load_dwordx4 a[160:163], v4, s[44:47], s72 offen sc1\n"
    "v_mfma_f32_16x16x32_bf16 v[160:163], a[224:227], a[200:203], v[160:163]\n"
    "ds_read_b64_tr_b16 a[242:243], v14 offset:20480\n"
    "v_accvgpr_read_b32 v106, a26\n"
    "v_mfma_f32_16x16x32_bf16 v[160:163], a[228:231], a[204:207], v[160:163]\n"
    "v_accvgpr_read_b32 v107, a27\n"
    "v_accvgpr_read_b32 v108, a28\n"
    "v_mfma_f32_16x16x32_bf16 v[192:195], a[224:227], a[208:211], v[192:195]\n"
    "ds_read_b64_tr_b16 a[244:245], v14 offset:24576\n"
    "v_mfma_f32_16x16x32_bf16 v[192:195], a[228:231], a[212:215], v[192:195]\n"
    "buffer_store_dwordx4 v[72:75], v24, s[56:59], s89 offen offset:2048 sc1\n"
    "v_mfma_f32_16x16x32_bf16 v[224:227], a[224:227], a[216:219], v[224:227]\n"
    "ds_read_b64_tr_b16 a[246:247], v14 offset:28672\n"
    "v_add_u32 v14, s76, v14\n"
    "v_mfma_f32_16x16x32_bf16 v[224:227], a[228:231], a[220:223], v[224:227]\n"
    "s_waitcnt lgkmcnt(4)\n"
    "v_accvgpr_read_b32 v109, a29\n"
    "v_accvgpr_read_b32 v110, a30\n"
    "v_mfma_f32_16x16x32_bf16 v[132:135], a[232:235], a[192:195], v[132:135]\n"
    "ds_read_b64_tr_b16 a[248:249], v15 offset:16384\n"
    "v_accvgpr_read_b32 v111, a31\n"
    "v_mfma_f32_16x16x32_bf16 v[132:135], a[236:239], a[196:199], v[132:135]\n"
    "buffer_load_dwordx4 a[164:167], v4, s[44:47], s72 offen offset:1024 sc1\n"
    "v_mfma_f32_16x16x32_bf16 v[164:167], a[232:235], a[200:203], v[164:167]\n"
    "s_add_u32 m0, s74, s85\n"
    "ds_read_b64_tr_b16 a[250:251], v15 offset:20480\n"
    "v_mfma_f32_16x16x32_bf16 v[164:167], a[236:239], a[204:207], v[164:167]\n"
    "buffer_load_dwordx4 v8, s[68:71], s83 offen lds\n"
    "v_mfma_f32_16x16x32_bf16 v[196:199], a[232:235], a[208:211], v[196:199]\n"
    "ds_read_b64_tr_b16 a[252:253], v15 offset:24576\n"
    "v_cvt_pk_bf16_f32 v44, v104, v105\n"
    "v_mfma_f32_16x16x32_bf16 v[196:199], a[236:239], a[212:215], v[196:199]\n"
    "v_cvt_pk_bf16_f32 v45, v106, v107\n"
    "v_cvt_pk_bf16_f32 v46, v108, v109\n"
    "v_mfma_f32_16x16x32_bf16 v[228:231], a[232:235], a[216:219], v[228:231]\n"
    "ds_read_b64_tr_b16 a[254:255], v15 offset:28672\n"
    "v_add_u32 v15, s76, v15\n"
    "v_mfma_f32_16x16x32_bf16 v[228:231], a[236:239], a[220:223], v[228:231]\n"
    "s_waitcnt lgkmcnt(4)\n"
    "v_cvt_pk_bf16_f32 v47, v110, v111\n"
    "v_mfma_f32_16x16x32_bf16 v[136:139], a[240:243], a[192:195], v[136:139]\n"
    "ds_read_b64_tr_b16 a[224:225], v16 offset:16384\n"
    "v_accvgpr_read_b32 v96, a32\n"
    "v_mfma_f32_16x16x32_bf16 v[136:139], a[244:247], a[196:199], v[136:139]\n"
    "buffer_load_dwordx4 a[168:171], v5, s[44:47], s72 offen sc1\n"
    "v_mfma_f32_16x16x32_bf16 v[168:171], a[240:243], a[200:203], v[168:171]\n"
    "ds_read_b64_tr_b16 a[226:227], v16 offset:20480\n"
    "v_accvgpr_read_b32 v97, a33\n"
    "v_mfma_f32_16x16x32_bf16 v[168:171], a[244:247], a[204:207], v[168:171]\n"
    "v_accvgpr_read_b32 v98, a34\n"
    "v_mfma_f32_16x16x32_bf16 v[200:203], a[240:243], a[208:211], v[200:203]\n"
    "ds_read_b64_tr_b16 a[228:229], v16 offset:24576\n"
    "v_mfma_f32_16x16x32_bf16 v[200:203], a[244:247], a[212:215], v[200:203]\n"
    "buffer_store_dwordx4 v[76:79], v24, s[56:59], s89 offen offset:3072 sc1\n"
    "v_mfma_f32_16x16x32_bf16 v[232:235], a[240:243], a[216:219], v[232:235]\n"
    "ds_read_b64_tr_b16 a[230:231], v16 offset:28672\n"
    "v_add_u32 v16, s76, v16\n"
    "v_mfma_f32_16x16x32_bf16 v[232:235], a[244:247], a[220:223], v[232:235]\n"
    "s_waitcnt lgkmcnt(4)\n"
    "v_accvgpr_read_b32 v99, a35\n"
    "v_accvgpr_read_b32 v100, a36\n"
    "v_mfma_f32_16x16x32_bf16 v[140:143], a[248:251], a[192:195], v[140:143]\n"
    "ds_read_b64_tr_b16 a[232:233], v17 offset:16384\n"
    "v_accvgpr_read_b32 v101, a37\n"
    "v_mfma_f32_16x16x32_bf16 v[140:143], a[252:255], a[196:199], v[140:143]\n"
    "buffer_load_dwordx4 a[172:175], v5, s[44:47], s72 offen offset:1024 sc1\n"
    "v_mfma_f32_16x16x32_bf16 v[172:175], a[248:251], a[200:203], v[172:175]\n"
    "ds_read_b64_tr_b16 a[234:235], v17 offset:20480\n"
    "v_accvgpr_read_b32 v102, a38\n"
    "v_mfma_f32_16x16x32_bf16 v[172:175], a[252:255], a[204:207], v[172:175]\n"
    "buffer_load_dwordx4 v9, s[68:71], s83 offen offset:1024 lds\n"
    "v_mfma_f32_16x16x32_bf16 v[204:207], a[248:251], a[208:211], v[204:207]\n"
    "ds_read_b64_tr_b16 a[236:237], v17 offset:24576\n"
    "v_accvgpr_read_b32 v103, a39\n"
    "v_mfma_f32_16x16x32_bf16 v[204:207], a[252:255], a[212:215], v[204:207]\n"
    "v_cvt_pk_bf16_f32 v48, v96, v97\n"
    "v_mfma_f32_16x16x32_bf16 v[236:239], a[248:251], a[216:219], v[236:239]\n"
    "ds_read_b64_tr_b16 a[238:239], v17 offset:28672\n"
    "v_add_u32 v17, s76, v17\n"
    "v_mfma_f32_16x16x32_bf16 v[236:239], a[252:255], a[220:223], v[236:239]\n"
    "s_waitcnt lgkmcnt(4)\n"
    "v_cvt_pk_bf16_f32 v49, v98, v99\n"
    "v_mfma_f32_16x16x32_bf16 v[144:147], a[224:227], a[192:195], v[144:147]\n"
    "ds_read_b64_tr_b16 a[240:241], v18 offset:16384\n"
    "v_mfma_f32_16x16x32_bf16 v[144:147], a[228:231], a[196:199], v[144:147]\n"
    "buffer_load_dwordx4 a[176:179], v6, s[44:47], s72 offen sc1\n"
    "v_mfma_f32_16x16x32_bf16 v[176:179], a[224:227], a[200:203], v[176:179]\n"
    "s_add_u32 s89, s89, 0x1000\n"
    "ds_read_b64_tr_b16 a[242:243], v18 offset:20480\n"
    "v_mfma_f32_16x16x32_bf16 v[176:179], a[228:231], a[204:207], v[176:179]\n"
    "v_cvt_pk_bf16_f32 v50, v100, v101\n"
    "v_cvt_pk_bf16_f32 v51, v102, v103\n"
    "v_mfma_f32_16x16x32_bf16 v[208:211], a[224:227], a[208:211], v[208:211]\n"
    "ds_read_b64_tr_b16 a[244:245], v18 offset:24576\n"
    "v_accvgpr_read_b32 v104, a40\n"
    "v_mfma_f32_16x16x32_bf16 v[208:211], a[228:231], a[212:215], v[208:211]\n"
    "buffer_load_dwordx4 a[180:183], v6, s[44:47], s72 offen offset:1024 sc1\n"
    "v_mfma_f32_16x16x32_bf16 v[240:243], a[224:227], a[216:219], v[240:243]\n"
    "ds_read_b64_tr_b16 a[246:247], v18 offset:28672\n"
    "v_add_u32 v18, s76, v18\n"
    "v_mfma_f32_16x16x32_bf16 v[240:243], a[228:231], a[220:223], v[240:243]\n"
    "s_waitcnt lgkmcnt(4)\n"
    "v_accvgpr_read_b32 v105, a41\n"
    "v_accvgpr_read_b32 v106, a42\n"
    "v_mfma_f32_16x16x32_bf16 v[148:151], a[232:235], a[192:195], v[148:151]\n"
    "ds_read_b64_tr_b16 a[248:249], v19 offset:16384\n"
    "v_mfma_f32_16x16x32_bf16 v[148:151], a[236:239], a[196:199], v[148:151]\n"
    "v_accvgpr_read_b32 v107, a43\n"
    "v_mfma_f32_16x16x32_bf16 v[180:183], a[232:235], a[200:203], v[180:183]\n"
    "ds_read_b64_tr_b16 a[250:251], v19 offset:20480\n"
    "v_accvgpr_read_b32 v108, a44\n"
    "v_mfma_f32_16x16x32_bf16 v[180:183], a[236:239], a[204:207], v[180:183]\n"
    "buffer_load_dwordx4 v10, s[68:71], s83 offen offset:2048 lds\n"
    "v_mfma_f32_16x16x32_bf16 v[212:215], a[232:235], a[208:211], v[212:215]\n"
    "ds_read_b64_tr_b16 a[252:253], v19 offset:24576\n"
    "v_accvgpr_read_b32 v109, a45\n"
    "v_mfma_f32_16x16x32_bf16 v[212:215], a[236:239], a[212:215], v[212:215]\n"
    "v_mfma_f32_16x16x32_bf16 v[244:247], a[232:235], a[216:219], v[244:247]\n"
    "ds_read_b64_tr_b16 a[254:255], v19 offset:28672\n"
    "v_add_u32 v19, s76, v19\n"
    "v_mfma_f32_16x16x32_bf16 v[244:247], a[236:239], a[220:223], v[244:247]\n"
    "s_waitcnt lgkmcnt(4)\n"
    "s_barrier\n"
    "v_accvgpr_read_b32 v110, a46\n"
    "v_mfma_f32_16x16x32_bf16 v[152:155], a[240:243], a[192:195], v[152:155]\n"
    "ds_read_b64_tr_b16 a[224:225], v12\n"
    "v_accvgpr_read_b32 v111, a47\n"
    "v_mfma_f32_16x16x32_bf16 v[152:155], a[244:247], a[196:199], v[152:155]\n"
    "buffer_load_dwordx4 a[184:187], v7, s[44:47], s72 offen sc1\n"
    "v_mfma_f32_16x16x32_bf16 v[184:187], a[240:243], a[200:203], v[184:187]\n"
    "ds_read_b64_tr_b16 a[226:227], v12 offset:4096\n"
    "v_cvt_pk_bf16_f32 v52, v104, v105\n"
    "v_mfma_f32_16x16x32_bf16 v[184:187], a[244:247], a[204:207], v[184:187]\n"
    "v_cvt_pk_bf16_f32 v53, v106, v107\n"
    "v_mfma_f32_16x16x32_bf16 v[216:219], a[240:243], a[208:211], v[216:219]\n"
    "ds_read_b64_tr_b16 a[228:229], v12 offset:8192\n"
    "v_cvt_pk_bf16_f32 v54, v108, v109\n"
    "v_mfma_f32_16x16x32_bf16 v[216:219], a[244:247], a[212:215], v[216:219]\n"
    "buffer_load_dwordx4 a[188:191], v7, s[44:47], s72 offen offset:1024 sc1\n"
    "v_mfma_f32_16x16x32_bf16 v[248:251], a[240:243], a[216:219], v[248:251]\n"
    "s_add_u32 s72, s72, 0x40000\n"
    "ds_read_b64_tr_b16 a[230:231], v12 offset:12288\n"
    "v_mfma_f32_16x16x32_bf16 v[248:251], a[244:247], a[220:223], v[248:251]\n"
    "s_waitcnt lgkmcnt(4)\n"
    "v_cvt_pk_bf16_f32 v55, v110, v111\n"
    "v_mfma_f32_16x16x32_bf16 v[156:159], a[248:251], a[192:195], v[156:159]\n"
    "ds_read_b64_tr_b16 a[232:233], v13\n"
    "v_mfma_f32_16x16x32_bf16 v[156:159], a[252:255], a[196:199], v[156:159]\n"
    "v_mfma_f32_16x16x32_bf16 v[188:191], a[248:251], a[200:203], v[188:191]\n"
    "ds_read_b64_tr_b16 a[234:235], v13 offset:4096\n"
    "v_mfma_f32_16x16x32_bf16 v[188:191], a[252:255], a[204:207], v[188:191]\n"
    "buffer_load_dwordx4 v11, s[68:71], s83 offen offset:3072 lds\n"
    "v_mfma_f32_16x16x32_bf16 v[220:223], a[248:251], a[208:211], v[220:223]\n"
    "s_add_u32 s73, s73, 0x100000\n"
    "ds_read_b64_tr_b16 a[236:237], v13 offset:8192\n"
    "v_mfma_f32_16x16x32_bf16 v[220:223], a[252:255], a[212:215], v[220:223]\n"
    "s_add_u32 s83, s83, 0x100000\n"
    "s_add_u32 s74, s74, 0x8000\n"
    "v_mfma_f32_16x16x32_bf16 v[252:255], a[248:251], a[216:219], v[252:255]\n"
    "ds_read_b64_tr_b16 a[238:239], v13 offset:12288\n"
    "v_mfma_f32_16x16x32_bf16 v[252:255], a[252:255], a[220:223], v[252:255]\n"
    "s_cmp_lt_u32 s74, 0x28000\n"
    "s_cselect_b32 s74, s74, 0\n"
    "s_waitcnt vmcnt(16) lgkmcnt(4)\n"
    "v_mfma_f32_16x16x32_bf16 v[128:131], a[224:227], a[128:131], v[128:131]\n"
    "ds_read_b64_tr_b16 a[240:241], v14\n"
    "v_mfma_f32_16x16x32_bf16 v[128:131], a[228:231], a[132:135], v[128:131]\n"
    "buffer_load_dwordx4 a[192:195], v4, s[44:47], s72 offen sc1\n"
    "v_mfma_f32_16x16x32_bf16 v[160:163], a[224:227], a[136:139], v[160:163]\n"
    "ds_read_b64_tr_b16 a[242:243], v14 offset:4096\n"
    "v_accvgpr_read_b32 v96, a48\n"
    "v_mfma_f32_16x16x32_bf16 v[160:163], a[228:231], a[140:143], v[160:163]\n"
    "buffer_store_dwordx4 v[80:83], v24, s[56:59], s89 offen sc1\n"
    "v_mfma_f32_16x16x32_bf16 v[192:195], a[224:227], a[144:147], v[192:195]\n"
    "ds_read_b64_tr_b16 a[244:245], v14 offset:8192\n"
    "v_accvgpr_read_b32 v97, a49\n"
    "v_mfma_f32_16x16x32_bf16 v[192:195], a[228:231], a[148:151], v[192:195]\n"
    "v_accvgpr_read_b32 v98, a50\n"
    "v_mfma_f32_16x16x32_bf16 v[224:227], a[224:227], a[152:155], v[224:227]\n"
    "ds_read_b64_tr_b16 a[246:247], v14 offset:12288\n"
    "v_accvgpr_read_b32 v99, a51\n"
    "v_mfma_f32_16x16x32_bf16 v[224:227], a[228:231], a[156:159], v[224:227]\n"
    "s_waitcnt lgkmcnt(4)\n"
    "v_mfma_f32_16x16x32_bf16 v[132:135], a[232:235], a[128:131], v[132:135]\n"
    "ds_read_b64_tr_b16 a[248:249], v15\n"
    "v_accvgpr_read_b32 v100, a52\n"
    "v_mfma_f32_16x16x32_bf16 v[132:135], a[236:239], a[132:135], v[132:135]\n"
    "buffer_load_dwordx4 a[196:199], v4, s[44:47], s72 offen offset:1024 sc1\n"
    "v_mfma_f32_16x16x32_bf16 v[164:167], a[232:235], a[136:139], v[164:167]\n"
    "s_add_u32 m0, s74, s84\n"
    "ds_read_b64_tr_b16 a[250:251], v15 offset:4096\n"
    "v_mfma_f32_16x16x32_bf16 v[164:167], a[236:239], a[140:143], v[164:167]\n"
    "buffer_load_dwordx4 v8, s[68:71], s73 offen lds\n"
    "v_mfma_f32_16x16x32_bf16 v[196:199], a[232:235], a[144:147], v[196:199]\n"
    "ds_read_b64_tr_b16 a[252:253], v15 offset:8192\n"
    "v_accvgpr_read_b32 v101, a53\n"
    "v_mfma_f32_16x16x32_bf16 v[196:199], a[236:239], a[148:151], v[196:199]\n"
    "v_accvgpr_read_b32 v102, a54\n"
    "v_accvgpr_read_b32 v103, a55\n"
    "v_mfma_f32_16x16x32_bf16 v[228:231], a[232:235], a[152:155], v[228:231]\n"
    "ds_read_b64_tr_b16 a[254:255], v15 offset:12288\n"
    "v_mfma_f32_16x16x32_bf16 v[228:231], a[236:239], a[156:159], v[228:231]\n"
    "s_waitcnt lgkmcnt(4)\n"
    "v_cvt_pk_bf16_f32 v56, v96, v97\n"
    "v_mfma_f32_16x16x32_bf16 v[136:139], a[240:243], a[128:131], v[136:139]\n"
    "ds_read_b64_tr_b16 a[224:225], v16\n"
    "v_cvt_pk_bf16_f32 v57, v98, v99\n"
    "v_mfma_f32_16x16x32_bf16 v[136:139], a[244:247], a[132:135], v[136:139]\n"
    "buffer_load_dwordx4 a[200:203], v5, s[44:47], s72 offen sc1\n"
    "v_mfma_f32_16x16x32_bf16 v[168:171], a[240:243], a[136:139], v[168:171]\n"
    "ds_read_b64_tr_b16 a[226:227], v16 offset:4096\n"
    "v_cvt_pk_bf16_f32 v58, v100, v101\n"
    "v_mfma_f32_16x16x32_bf16 v[168:171], a[244:247], a[140:143], v[168:171]\n"
    "buffer_store_dwordx4 v[84:87], v24, s[56:59], s89 offen offset:1024 sc1\n"
    "v_mfma_f32_16x16x32_bf16 v[200:203], a[240:243], a[144:147], v[200:203]\n"
    "ds_read_b64_tr_b16 a[228:229], v16 offset:8192\n"
    "v_cvt_pk_bf16_f32 v59, v102, v103\n"
    "v_mfma_f32_16x16x32_bf16 v[200:203], a[244:247], a[148:151], v[200:203]\n"
    "v_accvgpr_read_b32 v104, a56\n"
    "v_mfma_f32_16x16x32_bf16 v[232:235], a[240:243], a[152:155], v[232:235]\n"
    "ds_read_b64_tr_b16 a[230:231], v16 offset:12288\n"
    "v_mfma_f32_16x16x32_bf16 v[232:235], a[244:247], a[156:159], v[232:235]\n"
    "s_waitcnt lgkmcnt(4)\n"
    "v_accvgpr_read_b32 v105, a57\n"
    "v_mfma_f32_16x16x32_bf16 v[140:143], a[248:251], a[128:131], v[140:143]\n"
    "ds_read_b64_tr_b16 a[232:233], v17\n"
    "v_accvgpr_read_b32 v106, a58\n"
    "v_mfma_f32_16x16x32_bf16 v[140:143], a[252:255], a[132:135], v[140:143]\n"
    "buffer_load_dwordx4 a[204:207], v5, s[44:47], s72 offen offset:1024 sc1\n"
    "v_mfma_f32_16x16x32_bf16 v[172:175], a[248:251], a[136:139], v[172:175]\n"
    "ds_read_b64_tr_b16 a[234:235], v17 offset:4096\n"
    "v_accvgpr_read_b32 v107, a59\n"
    "v_mfma_f32_16x16x32_bf16 v[172:175], a[252:255], a[140:143], v[172:175]\n"
    "buffer_load_dwordx4 v9, s[68:71], s73 offen offset:1024 lds\n"
    "v_mfma_f32_16x16x32_bf16 v[204:207], a[248:251], a[144:147], v[204:207]\n"
    "ds_read_b64_tr_b16 a[236:237], v17 offset:8192\n"
    "v_accvgpr_read_b32 v108, a60\n"
    "v_mfma_f32_16x16x32_bf16 v[204:207], a[252:255], a[148:151], v[204:207]\n"
    "buffer_store_dwordx4 v[88:91], v24, s[56:59], s89 offen offset:2048 sc1\n"
    "v_mfma_f32_16x16x32_bf16 v[236:239], a[248:251], a[152:155], v[236:239]\n"
    "ds_read_b64_tr_b16 a[238:239], v17 offset:12288\n"
    "v_accvgpr_read_b32 v109, a61\n"
    "v_mfma_f32_16x16x32_bf16 v[236:239], a[252:255], a[156:159], v[236:239]\n"
    "s_waitcnt lgkmcnt(4)\n"
    "v_accvgpr_read_b32 v110, a62\n"
    "v_mfma_f32_16x16x32_bf16 v[144:147], a[224:227], a[128:131], v[144:147]\n"
    "ds_read_b64_tr_b16 a[240:241], v18\n"
    "v_mfma_f32_16x16x32_bf16 v[144:147], a[228:231], a[132:135], v[144:147]\n"
    "buffer_load_dwordx4 a[208:211], v6, s[44:47], s72 offen sc1\n"
    "v_mfma_f32_16x16x32_bf16 v[176:179], a[224:227], a[136:139], v[176:179]\n"
    "ds_read_b64_tr_b16 a[242:243], v18 offset:4096\n"
    "v_accvgpr_read_b32 v111, a63\n"
    "v_mfma_f32_16x16x32_bf16 v[176:179], a[228:231], a[140:143], v[176:179]\n"
    "v_cvt_pk_bf16_f32 v60, v104, v105\n"
    "v_mfma_f32_16x16x32_bf16 v[208:211], a[224:227], a[144:147], v[208:211]\n"
    "ds_read_b64_tr_b16 a[244:245], v18 offset:8192\n"
    "v_cvt_pk_bf16_f32 v61, v106, v107\n"
    "v_mfma_f32_16x16x32_bf16 v[208:211], a[228:231], a[148:151], v[208:211]\n"
    "buffer_load_dwordx4 a[212:215], v6, s[44:47], s72 offen offset:1024 sc1\n"
    "v_mfma_f32_16x16x32_bf16 v[240:243], a[224:227], a[152:155], v[240:243]\n"
    "ds_read_b64_tr_b16 a[246:247], v18 offset:12288\n"
    "v_cvt_pk_bf16_f32 v62, v108, v109\n"
    "v_mfma_f32_16x16x32_bf16 v[240:243], a[228:231], a[156:159], v[240:243]\n"
    "s_waitcnt lgkmcnt(4)\n"
    "v_cvt_pk_bf16_f32 v63, v110, v111\n"
    "v_mfma_f32_16x16x32_bf16 v[148:151], a[232:235], a[128:131], v[148:151]\n"
    "ds_read_b64_tr_b16 a[248:249], v19\n"
    "v_mfma_f32_16x16x32_bf16 v[148:151], a[236:239], a[132:135], v[148:151]\n"
    "v_accvgpr_read_b32 v96, a64\n"
    "v_mfma_f32_16x16x32_bf16 v[180:183], a[232:235], a[136:139], v[180:183]\n"
    "ds_read_b64_tr_b16 a[250:251], v19 offset:4096\n"
    "v_accvgpr_read_b32 v97, a65\n"
    "v_mfma_f32_16x16x32_bf16 v[180:183], a[236:239], a[140:143], v[180:183]\n"
    "buffer_load_dwordx4 v10, s[68:71], s73 offen offset:2048 lds\n"
    "v_mfma_f32_16x16x32_bf16 v[212:215], a[232:235], a[144:147], v[212:215]\n"
    "ds_read_b64_tr_b16 a[252:253], v19 offset:8192\n"
    "v_accvgpr_read_b32 v98, a66\n"
    "v_mfma_f32_16x16x32_bf16 v[212:215], a[236:239], a[148:151], v[212:215]\n"
    "buffer_store_dwordx4 v[92:95], v24, s[56:59], s89 offen offset:3072 sc1\n"
    "v_mfma_f32_16x16x32_bf16 v[244:247], a[232:235], a[152:155], v[244:247]\n"
    "ds_read_b64_tr_b16 a[254:255], v19 offset:12288\n"
    "v_accvgpr_read_b32 v99, a67\n"
    "v_mfma_f32_16x16x32_bf16 v[244:247], a[236:239], a[156:159], v[244:247]\n"
    "s_waitcnt lgkmcnt(4)\n"
    "v_accvgpr_read_b32 v100, a68\n"
    "v_mfma_f32_16x16x32_bf16 v[152:155], a[240:243], a[128:131], v[152:155]\n"
    "ds_read_b64_tr_b16 a[224:225], v12 offset:16384\n"
    "v_mfma_f32_16x16x32_bf16 v[152:155], a[244:247], a[132:135], v[152:155]\n"
    "buffer_load_dwordx4 a[216:219], v7, s[44:47], s72 offen sc1\n"
    "v_mfma_f32_16x16x32_bf16 v[184:187], a[240:243], a[136:139], v[184:187]\n"
    "s_add_u32 s75, s75, 1\n"
    "ds_read_b64_tr_b16 a[226:227], v12 offset:20480\n"
    "v_mfma_f32_16x16x32_bf16 v[184:187], a[244:247], a[140:143], v[184:187]\n"
    "v_accvgpr_read_b32 v101, a69\n"
    "v_accvgpr_read_b32 v102, a70\n"
    "v_mfma_f32_16x16x32_bf16 v[216:219], a[240:243], a[144:147], v[216:219]\n"
    "ds_read_b64_tr_b16 a[228:229], v12 offset:24576\n"
    "v_accvgpr_read_b32 v103, a71\n"
    "v_mfma_f32_16x16x32_bf16 v[216:219], a[244:247], a[148:151], v[216:219]\n"
    "s_cmp_lt_u32 s75, 5\n"
    "s_cselect_b32 s75, s75, 0\n"
    "s_cselect_b32 s76, 0x8000, s77\n"
    "v_mfma_f32_16x16x32_bf16 v[248:251], a[240:243], a[152:155], v[248:251]\n"
    "ds_read_b64_tr_b16 a[230:231], v12 offset:28672\n"
    "v_add_u32 v12, s76, v12\n"
    "v_mfma_f32_16x16x32_bf16 v[248:251], a[244:247], a[156:159], v[248:251]\n"
    "s_waitcnt lgkmcnt(4)\n"
    "v_cvt_pk_bf16_f32 v64, v96, v97\n"
    "v_mfma_f32_16x16x32_bf16 v[156:159], a[248:251], a[128:131], v[156:159]\n"
    "ds_read_b64_tr_b16 a[232:233], v13 offset:16384\n"
    "v_cvt_pk_bf16_f32 v65, v98, v99\n"
    "v_mfma_f32_16x16x32_bf16 v[156:159], a[252:255], a[132:135], v[156:159]\n"
    "buffer_load_dwordx4 a[220:223], v7, s[44:47], s72 offen offset:1024 sc1\n"
    "v_mfma_f32_16x16x32_bf16 v[188:191], a[248:251], a[136:139], v[188:191]\n"
    "s_add_u32 s72, s72, 0x40000\n"
    "ds_read_b64_tr_b16 a[234:235], v13 offset:20480\n"
    "v_mfma_f32_16x16x32_bf16 v[188:191], a[252:255], a[140:143], v[188:191]\n"
    "buffer_load_dwordx4 v11, s[68:71], s73 offen offset:3072 lds\n"
    "v_mfma_f32_16x16x32_bf16 v[220:223], a[248:251], a[144:147], v[220:223]\n"
    "s_add_u32 s89, s89, 0x1000\n"
    "ds_read_b64_tr_b16 a[236:237], v13 offset:24576\n"
    "v_mfma_f32_16x16x32_bf16 v[220:223], a[252:255], a[148:151], v[220:223]\n"
    "v_cvt_pk_bf16_f32 v66, v100, v101\n"
    "v_cvt_pk_bf16_f32 v67, v102, v103\n"
    "v_mfma_f32_16x16x32_bf16 v[252:255], a[248:251], a[152:155], v[252:255]\n"
    "ds_read_b64_tr_b16 a[238:239], v13 offset:28672\n"
    "v_add_u32 v13, s76, v13\n"
    "v_mfma_f32_16x16x32_bf16 v[252:255], a[252:255], a[156:159], v[252:255]\n"
    "s_waitcnt vmcnt(17) lgkmcnt(4)\n"
    "v_accvgpr_read_b32 v104, a72\n"
    "v_accvgpr_read_b32 v105, a73\n"
    "v_mfma_f32_16x16x32_bf16 v[128:131], a[224:227], a[160:163], v[128:131]\n"
    "ds_read_b64_tr_b16 a[240:241], v14 offset:16384\n"
    "v_accvgpr_read_b32 v106, a74\n"
    "v_mfma_f32_16x16x32_bf16 v[128:131], a[228:231], a[164:167], v[128:131]\n"
    "buffer_load_dwordx4 a[128:131], v4, s[44:47], s72 offen sc1\n"
    "v_mfma_f32_16x16x32_bf16 v[160:163], a[224:227], a[168:171], v[160:163]\n"
    "ds_read_b64_tr_b16 a[242:243], v14 offset:20480\n"
    "v_accvgpr_read_b32 v107, a75\n"
    "v_mfma_f32_16x16x32_bf16 v[160:163], a[228:231], a[172:175], v[160:163]\n"
    "v_accvgpr_read_b32 v108, a76\n"
    "v_mfma_f32_16x16x32_bf16 v[192:195], a[224:227], a[176:179], v[192:195]\n"
    "ds_read_b64_tr_b16 a[244:245], v14 offset:24576\n"
    "v_mfma_f32_16x16x32_bf16 v[192:195], a[228:231], a[180:183], v[192:195]\n"
    "buffer_store_dwordx4 v[32:35], v24, s[56:59], s89 offen sc1\n"
    "v_mfma_f32_16x16x32_bf16 v[224:227], a[224:227], a[184:187], v[224:227]\n"
    "ds_read_b64_tr_b16 a[246:247], v14 offset:28672\n"
    "v_add_u32 v14, s76, v14\n"
    "v_mfma_f32_16x16x32_bf16 v[224:227], a[228:231], a[188:191], v[224:227]\n"
    "s_waitcnt lgkmcnt(4)\n"
    "v_accvgpr_read_b32 v109, a77\n"
    "v_accvgpr_read_b32 v110, a78\n"
    "v_mfma_f32_16x16x32_bf16 v[132:135], a[232:235], a[160:163], v[132:135]\n"
    "ds_read_b64_tr_b16 a[248:249], v15 offset:16384\n"
    "v_accvgpr_read_b32 v111, a79\n"
    "v_mfma_f32_16x16x32_bf16 v[132:135], a[236:239], a[164:167], v[132:135]\n"
    "buffer_load_dwordx4 a[132:135], v4, s[44:47], s72 offen offset:1024 sc1\n"
    "v_mfma_f32_16x16x32_bf16 v[164:167], a[232:235], a[168:171], v[164:167]\n"
    "s_add_u32 m0, s74, s85\n"
    "ds_read_b64_tr_b16 a[250:251], v15 offset:20480\n"
    "v_mfma_f32_16x16x32_bf16 v[164:167], a[236:239], a[172:175], v[164:167]\n"
    "buffer_load_dwordx4 v8, s[68:71], s83 offen lds\n"
    "v_mfma_f32_16x16x32_bf16 v[196:199], a[232:235], a[176:179], v[196:199]\n"
    "ds_read_b64_tr_b16 a[252:253], v15 offset:24576\n"
    "v_cvt_pk_bf16_f32 v68, v104, v105\n"
    "v_mfma_f32_16x16x32_bf16 v[196:199], a[236:239], a[180:183], v[196:199]\n"
    "v_cvt_pk_bf16_f32 v69, v106, v107\n"
    "v_cvt_pk_bf16_f32 v70, v108, v109\n"
    "v_mfma_f32_16x16x32_bf16 v[228:231], a[232:235], a[184:187], v[228:231]\n"
    "ds_read_b64_tr_b16 a[254:255], v15 offset:28672\n"
    "v_add_u32 v15, s76, v15\n"
    "v_mfma_f32_16x16x32_bf16 v[228:231], a[236:239], a[188:191], v[228:231]\n"
    "s_waitcnt lgkmcnt(4)\n"
    "v_cvt_pk_bf16_f32 v71, v110, v111\n"
    "v_mfma_f32_16x16x32_bf16 v[136:139], a[240:243], a[160:163], v[136:139]\n"
    "ds_read_b64_tr_b16 a[224:225], v16 offset:16384\n"
    "v_accvgpr_read_b32 v96, a80\n"
    "v_mfma_f32_16x16x32_bf16 v[136:139], a[244:247], a[164:167], v[136:139]\n"
    "buffer_load_dwordx4 a[136:139], v5, s[44:47], s72 offen sc1\n"
    "v_mfma_f32_16x16x32_bf16 v[168:171], a[240:243], a[168:171], v[168:171]\n"
    "ds_read_b64_tr_b16 a[226:227], v16 offset:20480\n"
    "v_accvgpr_read_b32 v97, a81\n"
    "v_mfma_f32_16x16x32_bf16 v[168:171], a[244:247], a[172:175], v[168:171]\n"
    "v_accvgpr_read_b32 v98, a82\n"
    "v_mfma_f32_16x16x32_bf16 v[200:203], a[240:243], a[176:179], v[200:203]\n"
    "ds_read_b64_tr_b16 a[228:229], v16 offset:24576\n"
    "v_mfma_f32_16x16x32_bf16 v[200:203], a[244:247], a[180:183], v[200:203]\n"
    "buffer_store_dwordx4 v[36:39], v24, s[56:59], s89 offen offset:1024 sc1\n"
    "v_mfma_f32_16x16x32_bf16 v[232:235], a[240:243], a[184:187], v[232:235]\n"
    "ds_read_b64_tr_b16 a[230:231], v16 offset:28672\n"
    "v_add_u32 v16, s76, v16\n"
    "v_mfma_f32_16x16x32_bf16 v[232:235], a[244:247], a[188:191], v[232:235]\n"
    "s_waitcnt lgkmcnt(4)\n"
    "v_accvgpr_read_b32 v99, a83\n"
    "v_accvgpr_read_b32 v100, a84\n"
    "v_mfma_f32_16x16x32_bf16 v[140:143], a[248:251], a[160:163], v[140:143]\n"
    "ds_read_b64_tr_b16 a[232:233], v17 offset:16384\n"
    "v_accvgpr_read_b32 v101, a85\n"
    "v_mfma_f32_16x16x32_bf16 v[140:143], a[252:255], a[164:167], v[140:143]\n"
    "buffer_load_dwordx4 a[140:143], v5, s[44:47], s72 offen offset:1024 sc1\n"
    "v_mfma_f32_16x16x32_bf16 v[172:175], a[248:251], a[168:171], v[172:175]\n"
    "ds_read_b64_tr_b16 a[234:235], v17 offset:20480\n"
    "v_accvgpr_read_b32 v102, a86\n"
    "v_mfma_f32_16x16x32_bf16 v[172:175], a[252:255], a[172:175], v[172:175]\n"
    "buffer_load_dwordx4 v9, s[68:71], s83 offen offset:1024 lds\n"
    "v_mfma_f32_16x16x32_bf16 v[204:207], a[248:251], a[176:179], v[204:207]\n"
    "ds_read_b64_tr_b16 a[236:237], v17 offset:24576\n"
    "v_accvgpr_read_b32 v103, a87\n"
    "v_mfma_f32_16x16x32_bf16 v[204:207], a[252:255], a[180:183], v[204:207]\n"
    "v_cvt_pk_bf16_f32 v72, v96, v97\n"
    "v_mfma_f32_16x16x32_bf16 v[236:239], a[248:251], a[184:187], v[236:239]\n"
    "ds_read_b64_tr_b16 a[238:239], v17 offset:28672\n"
    "v_add_u32 v17, s76, v17\n"
    "v_mfma_f32_16x16x32_bf16 v[236:239], a[252:255], a[188:191], v[236:239]\n"
    "s_waitcnt lgkmcnt(4)\n"
    "v_cvt_pk_bf16_f32 v73, v98, v99\n"
    "v_mfma_f32_16x16x32_bf16 v[144:147], a[224:227], a[160:163], v[144:147]\n"
    "ds_read_b64_tr_b16 a[240:241], v18 offset:16384\n"
    "v_mfma_f32_16x16x32_bf16 v[144:147], a[228:231], a[164:167], v[144:147]\n"
    "buffer_load_dwordx4 a[144:147], v6, s[44:47], s72 offen sc1\n"
    "v_mfma_f32_16x16x32_bf16 v[176:179], a[224:227], a[168:171], v[176:179]\n"
    "ds_read_b64_tr_b16 a[242:243], v18 offset:20480\n"
    "v_cvt_pk_bf16_f32 v74, v100, v101\n"
    "v_mfma_f32_16x16x32_bf16 v[176:179], a[228:231], a[172:175], v[176:179]\n"
    "buffer_store_dwordx4 v[40:43], v24, s[56:59], s89 offen offset:2048 sc1\n"
    "v_mfma_f32_16x16x32_bf16 v[208:211], a[224:227], a[176:179], v[208:211]\n"
    "ds_read_b64_tr_b16 a[244:245], v18 offset:24576\n"
    "v_cvt_pk_bf16_f32 v75, v102, v103\n"
    "v_mfma_f32_16x16x32_bf16 v[208:211], a[228:231], a[180:183], v[208:211]\n"
    "buffer_load_dwordx4 a[148:151], v6, s[44:47], s72 offen offset:1024 sc1\n"
    "v_mfma_f32_16x16x32_bf16 v[240:243], a[224:227], a[184:187], v[240:243]\n"
    "ds_read_b64_tr_b16 a[246:247], v18 offset:28672\n"
    "v_add_u32 v18, s76, v18\n"
    "v_mfma_f32_16x16x32_bf16 v[240:243], a[228:231], a[188:191], v[240:243]\n"
    "s_waitcnt lgkmcnt(4)\n"
    "v_accvgpr_read_b32 v104, a88\n"
    "v_accvgpr_read_b32 v105, a89\n"
    "v_mfma_f32_16x16x32_bf16 v[148:151], a[232:235], a[160:163], v[148:151]\n"
    "ds_read_b64_tr_b16 a[248:249], v19 offset:16384\n"
    "v_accvgpr_read_b32 v106, a90\n"
    "v_mfma_f32_16x16x32_bf16 v[148:151], a[236:239], a[164:167], v[148:151]\n"
    "v_accvgpr_read_b32 v107, a91\n"
    "v_mfma_f32_16x16x32_bf16 v[180:183], a[232:235], a[168:171], v[180:183]\n"
    "ds_read_b64_tr_b16 a[250:251], v19 offset:20480\n"
    "v_accvgpr_read_b32 v108, a92\n"
    "v_mfma_f32_16x16x32_bf16 v[180:183], a[236:239], a[172:175], v[180:183]\n"
    "buffer_load_dwordx4 v10, s[68:71], s83 offen offset:2048 lds\n"
    "v_mfma_f32_16x16x32_bf16 v[212:215], a[232:235], a[176:179], v[212:215]\n"
    "ds_read_b64_tr_b16 a[252:253], v19 offset:24576\n"
    "v_accvgpr_read_b32 v109, a93\n"
    "v_mfma_f32_16x16x32_bf16 v[212:215], a[236:239], a[180:183], v[212:215]\n"
    "v_mfma_f32_16x16x32_bf16 v[244:247], a[232:235], a[184:187], v[244:247]\n"
    "ds_read_b64_tr_b16 a[254:255], v19 offset:28672\n"
    "v_add_u32 v19, s76, v19\n"
    "v_mfma_f32_16x16x32_bf16 v[244:247], a[236:239], a[188:191], v[244:247]\n"
    "s_waitcnt lgkmcnt(4)\n"
    "s_barrier\n"
    "v_accvgpr_read_b32 v110, a94\n"
    "v_mfma_f32_16x16x32_bf16 v[152:155], a[240:243], a[160:163], v[152:155]\n"
    "ds_read_b64_tr_b16 a[224:225], v12\n"
    "v_accvgpr_read_b32 v111, a95\n"
    "v_mfma_f32_16x16x32_bf16 v[152:155], a[244:247], a[164:167], v[152:155]\n"
    "buffer_load_dwordx4 a[152:155], v7, s[44:47], s72 offen sc1\n"
    "v_mfma_f32_16x16x32_bf16 v[184:187], a[240:243], a[168:171], v[184:187]\n"
    "ds_read_b64_tr_b16 a[226:227], v12 offset:4096\n"
    "v_cvt_pk_bf16_f32 v76, v104, v105\n"
    "v_mfma_f32_16x16x32_bf16 v[184:187], a[244:247], a[172:175], v[184:187]\n"
    "v_cvt_pk_bf16_f32 v77, v106, v107\n"
    "v_mfma_f32_16x16x32_bf16 v[216:219], a[240:243], a[176:179], v[216:219]\n"
    "ds_read_b64_tr_b16 a[228:229], v12 offset:8192\n"
    "v_cvt_pk_bf16_f32 v78, v108, v109\n"
    "v_mfma_f32_16x16x32_bf16 v[216:219], a[244:247], a[180:183], v[216:219]\n"
    "buffer_load_dwordx4 a[156:159], v7, s[44:47], s72 offen offset:1024 sc1\n"
    "v_mfma_f32_16x16x32_bf16 v[248:251], a[240:243], a[184:187], v[248:251]\n"
    "s_add_u32 s72, s72, 0x40000\n"
    "ds_read_b64_tr_b16 a[230:231], v12 offset:12288\n"
    "v_mfma_f32_16x16x32_bf16 v[248:251], a[244:247], a[188:191], v[248:251]\n"
    "s_waitcnt lgkmcnt(4)\n"
    "v_cvt_pk_bf16_f32 v79, v110, v111\n"
    "v_mfma_f32_16x16x32_bf16 v[156:159], a[248:251], a[160:163], v[156:159]\n"
    "ds_read_b64_tr_b16 a[232:233], v13\n"
    "v_mfma_f32_16x16x32_bf16 v[156:159], a[252:255], a[164:167], v[156:159]\n"
    "v_mfma_f32_16x16x32_bf16 v[188:191], a[248:251], a[168:171], v[188:191]\n"
    "ds_read_b64_tr_b16 a[234:235], v13 offset:4096\n"
    "v_mfma_f32_16x16x32_bf16 v[188:191], a[252:255], a[172:175], v[188:191]\n"
    "buffer_load_dwordx4 v11, s[68:71], s83 offen offset:3072 lds\n"
    "v_mfma_f32_16x16x32_bf16 v[220:223], a[248:251], a[176:179], v[220:223]\n"
    "s_add_u32 s73, s73, 0x100000\n"
    "ds_read_b64_tr_b16 a[236:237], v13 offset:8192\n"
    "v_mfma_f32_16x16x32_bf16 v[220:223], a[252:255], a[180:183], v[220:223]\n"
    "s_add_u32 s83, s83, 0x100000\n"
    "s_add_u32 s74, s74, 0x8000\n"
    "v_mfma_f32_16x16x32_bf16 v[252:255], a[248:251], a[184:187], v[252:255]\n"
    "ds_read_b64_tr_b16 a[238:239], v13 offset:12288\n"
    "v_mfma_f32_16x16x32_bf16 v[252:255], a[252:255], a[188:191], v[252:255]\n"
    "s_cmp_lt_u32 s74, 0x28000\n"
    "s_cselect_b32 s74, s74, 0\n"
    "s_waitcnt vmcnt(16) lgkmcnt(4)\n"
    "v_mfma_f32_16x16x32_bf16 v[128:131], a[224:227], a[192:195], v[128:131]\n"
    "ds_read_b64_tr_b16 a[240:241], v14\n"
    "v_mfma_f32_16x16x32_bf16 v[128:131], a[228:231], a[196:199], v[128:131]\n"
    "buffer_load_dwordx4 a[160:163], v4, s[44:47], s72 offen sc1\n"
    "v_mfma_f32_16x16x32_bf16 v[160:163], a[224:227], a[200:203], v[160:163]\n"
    "ds_read_b64_tr_b16 a[242:243], v14 offset:4096\n"
    "v_accvgpr_read_b32 v96, a96\n"
    "v_mfma_f32_16x16x32_bf16 v[160:163], a[228:231], a[204:207], v[160:163]\n"
    "buffer_store_dwordx4 v[44:47], v24, s[56:59], s89 offen offset:3072 sc1\n"
    "v_mfma_f32_16x16x32_bf16 v[192:195], a[224:227], a[208:211], v[192:195]\n"
    "ds_read_b64_tr_b16 a[244:245], v14 offset:8192\n"
    "v_accvgpr_read_b32 v97, a97\n"
    "v_mfma_f32_16x16x32_bf16 v[192:195], a[228:231], a[212:215], v[192:195]\n"
    "v_mfma_f32_16x16x32_bf16 v[224:227], a[224:227], a[216:219], v[224:227]\n"
    "ds_read_b64_tr_b16 a[246:247], v14 offset:12288\n"
    "v_accvgpr_read_b32 v98, a98\n"
    "v_mfma_f32_16x16x32_bf16 v[224:227], a[228:231], a[220:223], v[224:227]\n"
    "s_waitcnt lgkmcnt(4)\n"
    "v_mfma_f32_16x16x32_bf16 v[132:135], a[232:235], a[192:195], v[132:135]\n"
    "ds_read_b64_tr_b16 a[248:249], v15\n"
    "v_mfma_f32_16x16x32_bf16 v[132:135], a[236:239], a[196:199], v[132:135]\n"
    "buffer_load_dwordx4 a[164:167], v4, s[44:47], s72 offen offset:1024 sc1\n"
    "v_mfma_f32_16x16x32_bf16 v[164:167], a[232:235], a[200:203], v[164:167]\n"
    "s_add_u32 m0, s74, s84\n"
    "ds_read_b64_tr_b16 a[250:251], v15 offset:4096\n"
    "v_mfma_f32_16x16x32_bf16 v[164:167], a[236:239], a[204:207], v[164:167]\n"
    "buffer_load_dwordx4 v8, s[68:71], s73 offen lds\n"
    "v_mfma_f32_16x16x32_bf16 v[196:199], a[232:235], a[208:211], v[196:199]\n"
    "ds_read_b64_tr_b16 a[252:253], v15 offset:8192\n"
    "v_accvgpr_read_b32 v99, a99\n"
    "v_mfma_f32_16x16x32_bf16 v[196:199], a[236:239], a[212:215], v[196:199]\n"
    "v_accvgpr_read_b32 v100, a100\n"
    "v_mfma_f32_16x16x32_bf16 v[228:231], a[232:235], a[216:219], v[228:231]\n"
    "s_add_u32 s89, s89, 0x1000\n"
    "ds_read_b64_tr_b16 a[254:255], v15 offset:12288\n"
    "v_mfma_f32_16x16x32_bf16 v[228:231], a[236:239], a[220:223], v[228:231]\n"
    "s_waitcnt lgkmcnt(4)\n"
    "v_accvgpr_read_b32 v101, a101\n"
    "v_mfma_f32_16x16x32_bf16 v[136:139], a[240:243], a[192:195], v[136:139]\n"
    "ds_read_b64_tr_b16 a[224:225], v16\n"
    "v_accvgpr_read_b32 v102, a102\n"
    "v_mfma_f32_16x16x32_bf16 v[136:139], a[244:247], a[196:199], v[136:139]\n"
    "buffer_load_dwordx4 a[168:171], v5, s[44:47], s72 offen sc1\n"
    "v_mfma_f32_16x16x32_bf16 v[168:171], a[240:243], a[200:203], v[168:171]\n"
    "ds_read_b64_tr_b16 a[226:227], v16 offset:4096\n"
    "v_mfma_f32_16x16x32_bf16 v[168:171], a[244:247], a[204:207], v[168:171]\n"
    "v_accvgpr_read_b32 v103, a103\n"
    "v_mfma_f32_16x16x32_bf16 v[200:203], a[240:243], a[208:211], v[200:203]\n"
    "ds_read_b64_tr_b16 a[228:229], v16 offset:8192\n"
    "v_mfma_f32_16x16x32_bf16 v[200:203], a[244:247], a[212:215], v[200:203]\n"
    "v_cvt_pk_bf16_f32 v80, v96, v97\n"
    "v_mfma_f32_16x16x32_bf16 v[232:235], a[240:243], a[216:219], v[232:235]\n"
    "ds_read_b64_tr_b16 a[230:231], v16 offset:12288\n"
    "v_mfma_f32_16x16x32_bf16 v[232:235], a[244:247], a[220:223], v[232:235]\n"
    "s_waitcnt lgkmcnt(4)\n"
    "v_mfma_f32_16x16x32_bf16 v[140:143], a[248:251], a[192:195], v[140:143]\n"
    "ds_read_b64_tr_b16 a[232:233], v17\n"
    "v_cvt_pk_bf16_f32 v81, v98, v99\n"
    "v_mfma_f32_16x16x32_bf16 v[140:143], a[252:255], a[196:199], v[140:143]\n"
    "buffer_load_dwordx4 a[172:175], v5, s[44:47], s72 offen offset:1024 sc1\n"
    "v_mfma_f32_16x16x32_bf16 v[172:175], a[248:251], a[200:203], v[172:175]\n"
    "ds_read_b64_tr_b16 a[234:235], v17 offset:4096\n"
    "v_cvt_pk_bf16_f32 v82, v100, v101\n"
    "v_mfma_f32_16x16x32_bf16 v[172:175], a[252:255], a[204:207], v[172:175]\n"
    "buffer_load_dwordx4 v9, s[68:71], s73 offen offset:1024 lds\n"
    "v_mfma_f32_16x16x32_bf16 v[204:207], a[248:251], a[208:211], v[204:207]\n"
    "ds_read_b64_tr_b16 a[236:237], v17 offset:8192\n"
    "v_mfma_f32_16x16x32_bf16 v[204:207], a[252:255], a[212:215], v[204:207]\n"
    "buffer_store_dwordx4 v[48:51], v24, s[56:59], s89 offen sc1\n"
    "v_mfma_f32_16x16x32_bf16 v[236:239], a[248:251], a[216:219], v[236:239]\n"
    "ds_read_b64_tr_b16 a[238:239], v17 offset:12288\n"
    "v_cvt_pk_bf16_f32 v83, v102, v103\n"
    "v_mfma_f32_16x16x32_bf16 v[236:239], a[252:255], a[220:223], v[236:239]\n"
    "s_waitcnt lgkmcnt(4)\n"
    "v_accvgpr_read_b32 v104, a104\n"
    "v_mfma_f32_16x16x32_bf16 v[144:147], a[224:227], a[192:195], v[144:147]\n"
    "ds_read_b64_tr_b16 a[240:241], v18\n"
    "v_mfma_f32_16x16x32_bf16 v[144:147], a[228:231], a[196:199], v[144:147]\n"
    "buffer_load_dwordx4 a[176:179], v6, s[44:47], s72 offen sc1\n"
    "v_mfma_f32_16x16x32_bf16 v[176:179], a[224:227], a[200:203], v[176:179]\n"
    "ds_read_b64_tr_b16 a[242:243], v18 offset:4096\n"
    "v_accvgpr_read_b32 v105, a105\n"
    "v_mfma_f32_16x16x32_bf16 v[176:179], a[228:231], a[204:207], v[176:179]\n"
    "v_mfma_f32_16x16x32_bf16 v[208:211], a[224:227], a[208:211], v[208:211]\n"
    "ds_read_b64_tr_b16 a[244:245], v18 offset:8192\n"
    "v_accvgpr_read_b32 v106, a106\n"
    "v_mfma_f32_16x16x32_bf16 v[208:211], a[228:231], a[212:215], v[208:211]\n"
    "buffer_load_dwordx4 a[180:183], v6, s[44:47], s72 offen offset:1024 sc1\n"
    "v_mfma_f32_16x16x32_bf16 v[240:243], a[224:227], a[216:219], v[240:243]\n"
    "ds_read_b64_tr_b16 a[246:247], v18 offset:12288\n"
    "v_accvgpr_read_b32 v107, a107\n"
    "v_mfma_f32_16x16x32_bf16 v[240:243], a[228:231], a[220:223], v[240:243]\n"
    "s_waitcnt lgkmcnt(4)\n"
    "v_mfma_f32_16x16x32_bf16 v[148:151], a[232:235], a[192:195], v[148:151]\n"
    "ds_read_b64_tr_b16 a[248:249], v19\n"
    "v_mfma_f32_16x16x32_bf16 v[148:151], a[236:239], a[196:199], v[148:151]\n"
    "buffer_store_dwordx4 v[52:55], v24, s[56:59], s89 offen offset:1024 sc1\n"
    "v_mfma_f32_16x16x32_bf16 v[180:183], a[232:235], a[200:203], v[180:183]\n"
    "ds_read_b64_tr_b16 a[250:251], v19 offset:4096\n"
    "v_accvgpr_read_b32 v108, a108\n"
    "v_mfma_f32_16x16x32_bf16 v[180:183], a[236:239], a[204:207], v[180:183]\n"
    "buffer_load_dwordx4 v10, s[68:71], s73 offen offset:2048 lds\n"
    "v_mfma_f32_16x16x32_bf16 v[212:215], a[232:235], a[208:211], v[212:215]\n"
    "ds_read_b64_tr_b16 a[252:253], v19 offset:8192\n"
    "v_accvgpr_read_b32 v109, a109\n"
    "v_mfma_f32_16x16x32_bf16 v[212:215], a[236:239], a[212:215], v[212:215]\n"
    "v_mfma_f32_16x16x32_bf16 v[244:247], a[232:235], a[216:219], v[244:247]\n"
    "ds_read_b64_tr_b16 a[254:255], v19 offset:12288\n"
    "v_accvgpr_read_b32 v110, a110\n"
    "v_mfma_f32_16x16x32_bf16 v[244:247], a[236:239], a[220:223], v[244:247]\n"
    "s_waitcnt lgkmcnt(4)\n"
    "v_mfma_f32_16x16x32_bf16 v[152:155], a[240:243], a[192:195], v[152:155]\n"
    "ds_read_b64_tr_b16 a[224:225], v12 offset:16384\n"
    "v_accvgpr_read_b32 v111, a111\n"
    "v_mfma_f32_16x16x32_bf16 v[152:155], a[244:247], a[196:199], v[152:155]\n"
    "buffer_load_dwordx4 a[184:187], v7, s[44:47], s72 offen sc1\n"
    "v_mfma_f32_16x16x32_bf16 v[184:187], a[240:243], a[200:203], v[184:187]\n"
    "s_add_u32 s75, s75, 1\n"
    "ds_read_b64_tr_b16 a[226:227], v12 offset:20480\n"
    "v_mfma_f32_16x16x32_bf16 v[184:187], a[244:247], a[204:207], v[184:187]\n"
    "buffer_store_dwordx4 v[56:59], v24, s[56:59], s89 offen offset:2048 sc1\n"
    "v_mfma_f32_16x16x32_bf16 v[216:219], a[240:243], a[208:211], v[216:219]\n"
    "ds_read_b64_tr_b16 a[228:229], v12 offset:24576\n"
    "v_cvt_pk_bf16_f32 v84, v104, v105\n"
    "v_mfma_f32_16x16x32_bf16 v[216:219], a[244:247], a[212:215], v[216:219]\n"
    "s_cmp_lt_u32 s75, 5\n"
    "s_cselect_b32 s75, s75, 0\n"
    "s_cselect_b32 s76, 0x8000, s77\n"
    "v_mfma_f32_16x16x32_bf16 v[248:251], a[240:243], a[216:219], v[248:251]\n"
    "ds_read_b64_tr_b16 a[230:231], v12 offset:28672\n"
    "v_add_u32 v12, s76, v12\n"
    "v_mfma_f32_16x16x32_bf16 v[248:251], a[244:247], a[220:223], v[248:251]\n"
    "s_waitcnt lgkmcnt(4)\n"
    "v_cvt_pk_bf16_f32 v85, v106, v107\n"
    "v_mfma_f32_16x16x32_bf16 v[156:159], a[248:251], a[192:195], v[156:159]\n"
    "ds_read_b64_tr_b16 a[232:233], v13 offset:16384\n"
    "v_cvt_pk_bf16_f32 v86, v108, v109\n"
    "v_mfma_f32_16x16x32_bf16 v[156:159], a[252:255], a[196:199], v[156:159]\n"
    "buffer_load_dwordx4 a[188:191], v7, s[44:47], s72 offen offset:1024 sc1\n"
    "v_mfma_f32_16x16x32_bf16 v[188:191], a[248:251], a[200:203], v[188:191]\n"
    "s_add_u32 s72, s72, 0x40000\n"
    "ds_read_b64_tr_b16 a[234:235], v13 offset:20480\n"
    "v_mfma_f32_16x16x32_bf16 v[188:191], a[252:255], a[204:207], v[188:191]\n"
    "buffer_load_dwordx4 v11, s[68:71], s73 offen offset:3072 lds\n"
    "v_mfma_f32_16x16x32_bf16 v[220:223], a[248:251], a[208:211], v[220:223]\n"
    "ds_read_b64_tr_b16 a[236:237], v13 offset:24576\n"
    "v_cvt_pk_bf16_f32 v87, v110, v111\n"
    "v_mfma_f32_16x16x32_bf16 v[220:223], a[252:255], a[212:215], v[220:223]\n"
    "v_accvgpr_read_b32 v96, a112\n"
    "v_mfma_f32_16x16x32_bf16 v[252:255], a[248:251], a[216:219], v[252:255]\n"
    "ds_read_b64_tr_b16 a[238:239], v13 offset:28672\n"
    "v_add_u32 v13, s76, v13\n"
    "v_mfma_f32_16x16x32_bf16 v[252:255], a[252:255], a[220:223], v[252:255]\n"
    "s_waitcnt vmcnt(17) lgkmcnt(4)\n"
    "v_accvgpr_read_b32 v97, a113\n"
    "v_mfma_f32_16x16x32_bf16 v[128:131], a[224:227], a[128:131], v[128:131]\n"
    "ds_read_b64_tr_b16 a[240:241], v14 offset:16384\n"
    "v_mfma_f32_16x16x32_bf16 v[128:131], a[228:231], a[132:135], v[128:131]\n"
    "buffer_load_dwordx4 a[192:195], v4, s[44:47], s72 offen sc1\n"
    "v_mfma_f32_16x16x32_bf16 v[160:163], a[224:227], a[136:139], v[160:163]\n"
    "ds_read_b64_tr_b16 a[242:243], v14 offset:20480\n"
    "v_accvgpr_read_b32 v98, a114\n"
    "v_mfma_f32_16x16x32_bf16 v[160:163], a[228:231], a[140:143], v[160:163]\n"
    "buffer_store_dwordx4 v[60:63], v24, s[56:59], s89 offen offset:3072 sc1\n"
    "v_mfma_f32_16x16x32_bf16 v[192:195], a[224:227], a[144:147], v[192:195]\n"
    "ds_read_b64_tr_b16 a[244:245], v14 offset:24576\n"
    "v_accvgpr_read_b32 v99, a115\n"
    "v_mfma_f32_16x16x32_bf16 v[192:195], a[228:231], a[148:151], v[192:195]\n"
    "v_mfma_f32_16x16x32_bf16 v[224:227], a[224:227], a[152:155], v[224:227]\n"
    "ds_read_b64_tr_b16 a[246:247], v14 offset:28672\n"
    "v_add_u32 v14, s76, v14\n"
    "v_mfma_f32_16x16x32_bf16 v[224:227], a[228:231], a[156:159], v[224:227]\n"
    "s_waitcnt lgkmcnt(4)\n"
    "v_accvgpr_read_b32 v100, a116\n"
    "v_mfma_f32_16x16x32_bf16 v[132:135], a[232:235], a[128:131], v[132:135]\n"
    "ds_read_b64_tr_b16 a[248:249], v15 offset:16384\n"
    "v_mfma_f32_16x16x32_bf16 v[132:135], a[236:239], a[132:135], v[132:135]\n"
    "buffer_load_dwordx4 a[196:199], v4, s[44:47], s72 offen offset:1024 sc1\n"
    "v_mfma_f32_16x16x32_bf16 v[164:167], a[232:235], a[136:139], v[164:167]\n"
    "s_add_u32 m0, s74, s85\n"
    "ds_read_b64_tr_b16 a[250:251], v15 offset:20480\n"
    "v_mfma_f32_16x16x32_bf16 v[164:167], a[236:239], a[140:143], v[164:167]\n"
    "buffer_load_dwordx4 v8, s[68:71], s83 offen lds\n"
    "v_mfma_f32_16x16x32_bf16 v[196:199], a[232:235], a[144:147], v[196:199]\n"
    "s_add_u32 s89, s89, 0x1000\n"
    "ds_read_b64_tr_b16 a[252:253], v15 offset:24576\n"
    "v_mfma_f32_16x16x32_bf16 v[196:199], a[236:239], a[148:151], v[196:199]\n"
    "v_accvgpr_read_b32 v101, a117\n"
    "v_accvgpr_read_b32 v102, a118\n"
    "v_mfma_f32_16x16x32_bf16 v[228:231], a[232:235], a[152:155], v[228:231]\n"
    "ds_read_b64_tr_b16 a[254:255], v15 offset:28672\n"
    "v_add_u32 v15, s76, v15\n"
    "v_mfma_f32_16x16x32_bf16 v[228:231], a[236:239], a[156:159], v[228:231]\n"
    "s_waitcnt lgkmcnt(4)\n"
    "v_accvgpr_read_b32 v103, a119\n"
    "v_mfma_f32_16x16x32_bf16 v[136:139], a[240:243], a[128:131], v[136:139]\n"
    "ds_read_b64_tr_b16 a[224:225], v16 offset:16384\n"
    "v_cvt_pk_bf16_f32 v88, v96, v97\n"
    "v_mfma_f32_16x16x32_bf16 v[136:139], a[244:247], a[132:135], v[136:139]\n"
    "buffer_load_dwordx4 a[200:203], v5, s[44:47], s72 offen sc1\n"
    "v_mfma_f32_16x16x32_bf16 v[168:171], a[240:243], a[136:139], v[168:171]\n"
    "ds_read_b64_tr_b16 a[226:227], v16 offset:20480\n"
    "v_mfma_f32_16x16x32_bf16 v[168:171], a[244:247], a[140:143], v[168:171]\n"
    "v_cvt_pk_bf16_f32 v89, v98, v99\n"
    "v_mfma_f32_16x16x32_bf16 v[200:203], a[240:243], a[144:147], v[200:203]\n"
    "ds_read_b64_tr_b16 a[228:229], v16 offset:24576\n"
    "v_mfma_f32_16x16x32_bf16 v[200:203], a[244:247], a[148:151], v[200:203]\n"
    "buffer_store_dwordx4 v[64:67], v24, s[56:59], s89 offen sc1\n"
    "v_mfma_f32_16x16x32_bf16 v[232:235], a[240:243], a[152:155], v[232:235]\n"
    "ds_read_b64_tr_b16 a[230:231], v16 offset:28672\n"
    "v_add_u32 v16, s76, v16\n"
    "v_mfma_f32_16x16x32_bf16 v[232:235], a[244:247], a[156:159], v[232:235]\n"
    "s_waitcnt lgkmcnt(4)\n"
    "v_cvt_pk_bf16_f32 v90, v100, v101\n"
    "v_mfma_f32_16x16x32_bf16 v[140:143], a[248:251], a[128:131], v[140:143]\n"
    "ds_read_b64_tr_b16 a[232:233], v17 offset:16384\n"
    "v_cvt_pk_bf16_f32 v91, v102, v103\n"
    "v_mfma_f32_16x16x32_bf16 v[140:143], a[252:255], a[132:135], v[140:143]\n"
    "buffer_load_dwordx4 a[204:207], v5, s[44:47], s72 offen offset:1024 sc1\n"
    "v_mfma_f32_16x16x32_bf16 v[172:175], a[248:251], a[136:139], v[172:175]\n"
    "ds_read_b64_tr_b16 a[234:235], v17 offset:20480\n"
    "v_accvgpr_read_b32 v104, a120\n"
    "v_mfma_f32_16x16x32_bf16 v[172:175], a[252:255], a[140:143], v[172:175]\n"
    "buffer_load_dwordx4 v9, s[68:71], s83 offen offset:1024 lds\n"
    "v_mfma_f32_16x16x32_bf16 v[204:207], a[248:251], a[144:147], v[204:207]\n"
    "ds_read_b64_tr_b16 a[236:237], v17 offset:24576\n"
    "v_accvgpr_read_b32 v105, a121\n"
    "v_mfma_f32_16x16x32_bf16 v[204:207], a[252:255], a[148:151], v[204:207]\n"
    "v_mfma_f32_16x16x32_bf16 v[236:239], a[248:251], a[152:155], v[236:239]\n"
    "ds_read_b64_tr_b16 a[238:239], v17 offset:28672\n"
    "v_add_u32 v17, s76, v17\n"
    "v_mfma_f32_16x16x32_bf16 v[236:239], a[252:255], a[156:159], v[236:239]\n"
    "s_waitcnt lgkmcnt(4)\n"
    "v_accvgpr_read_b32 v106, a122\n"
    "v_mfma_f32_16x16x32_bf16 v[144:147], a[224:227], a[128:131], v[144:147]\n"
    "ds_read_b64_tr_b16 a[240:241], v18 offset:16384\n"
    "v_mfma_f32_16x16x32_bf16 v[144:147], a[228:231], a[132:135], v[144:147]\n"
    "buffer_load_dwordx4 a[208:211], v6, s[44:47], s72 offen sc1\n"
    "v_mfma_f32_16x16x32_bf16 v[176:179], a[224:227], a[136:139], v[176:179]\n"
    "ds_read_b64_tr_b16 a[242:243], v18 offset:20480\n"
    "v_accvgpr_read_b32 v107, a123\n"
    "v_mfma_f32_16x16x32_bf16 v[176:179], a[228:231], a[140:143], v[176:179]\n"
    "buffer_store_dwordx4 v[68:71], v24, s[56:59], s89 offen offset:1024 sc1\n"
    "v_mfma_f32_16x16x32_bf16 v[208:211], a[224:227], a[144:147], v[208:211]\n"
    "ds_read_b64_tr_b16 a[244:245], v18 offset:24576\n"
    "v_accvgpr_read_b32 v108, a124\n"
    "v_mfma_f32_16x16x32_bf16 v[208:211], a[228:231], a[148:151], v[208:211]\n"
    "buffer_load_dwordx4 a[212:215], v6, s[44:47], s72 offen offset:1024 sc1\n"
    "v_mfma_f32_16x16x32_bf16 v[240:243], a[224:227], a[152:155], v[240:243]\n"
    "ds_read_b64_tr_b16 a[246:247], v18 offset:28672\n"
    "v_add_u32 v18, s76, v18\n"
    "v_mfma_f32_16x16x32_bf16 v[240:243], a[228:231], a[156:159], v[240:243]\n"
    "s_waitcnt lgkmcnt(4)\n"
    "v_accvgpr_read_b32 v109, a125\n"
    "v_mfma_f32_16x16x32_bf16 v[148:151], a[232:235], a[128:131], v[148:151]\n"
    "ds_read_b64_tr_b16 a[248:249], v19 offset:16384\n"
    "v_mfma_f32_16x16x32_bf16 v[148:151], a[236:239], a[132:135], v[148:151]\n"
    "v_accvgpr_read_b32 v110, a126\n"
    "v_mfma_f32_16x16x32_bf16 v[180:183], a[232:235], a[136:139], v[180:183]\n"
    "ds_read_b64_tr_b16 a[250:251], v19 offset:20480\n"
    "v_mfma_f32_16x16x32_bf16 v[180:183], a[236:239], a[140:143], v[180:183]\n"
    "buffer_load_dwordx4 v10, s[68:71], s83 offen offset:2048 lds\n"
    "v_mfma_f32_16x16x32_bf16 v[212:215], a[232:235], a[144:147], v[212:215]\n"
    "ds_read_b64_tr_b16 a[252:253], v19 offset:24576\n"
    "v_accvgpr_read_b32 v111, a127\n"
    "v_mfma_f32_16x16x32_bf16 v[212:215], a[236:239], a[148:151], v[212:215]\n"
    "v_mfma_f32_16x16x32_bf16 v[244:247], a[232:235], a[152:155], v[244:247]\n"
    "ds_read_b64_tr_b16 a[254:255], v19 offset:28672\n"
    "v_add_u32 v19, s76, v19\n"
    "v_mfma_f32_16x16x32_bf16 v[244:247], a[236:239], a[156:159], v[244:247]\n"
    "s_waitcnt lgkmcnt(4)\n"
    "s_barrier\n"
    "v_cvt_pk_bf16_f32 v92, v104, v105\n"
    "v_mfma_f32_16x16x32_bf16 v[152:155], a[240:243], a[128:131], v[152:155]\n"
    "ds_read_b64_tr_b16 a[224:225], v12\n"
    "v_cvt_pk_bf16_f32 v93, v106, v107\n"
    "v_mfma_f32_16x16x32_bf16 v[152:155], a[244:247], a[132:135], v[152:155]\n"
    "buffer_load_dwordx4 a[216:219], v7, s[44:47], s72 offen sc1\n"
    "v_mfma_f32_16x16x32_bf16 v[184:187], a[240:243], a[136:139], v[184:187]\n"
    "ds_read_b64_tr_b16 a[226:227], v12 offset:4096\n"
    "v_mfma_f32_16x16x32_bf16 v[184:187], a[244:247], a[140:143], v[184:187]\n"
    "v_cvt_pk_bf16_f32 v94, v108, v109\n"
    "v_mfma_f32_16x16x32_bf16 v[216:219], a[240:243], a[144:147], v[216:219]\n"
    "ds_read_b64_tr_b16 a[228:229], v12 offset:8192\n"
    "v_mfma_f32_16x16x32_bf16 v[216:219], a[244:247], a[148:151], v[216:219]\n"
    "buffer_load_dwordx4 a[220:223], v7, s[44:47], s72 offen offset:1024 sc1\n"
    "v_mfma_f32_16x16x32_bf16 v[248:251], a[240:243], a[152:155], v[248:251]\n"
    "s_add_u32 s72, s72, 0x40000\n"
    "ds_read_b64_tr_b16 a[230:231], v12 offset:12288\n"
    "v_mfma_f32_16x16x32_bf16 v[248:251], a[244:247], a[156:159], v[248:251]\n"
    "s_waitcnt lgkmcnt(4)\n"
    "v_cvt_pk_bf16_f32 v95, v110, v111\n"
    "v_mfma_f32_16x16x32_bf16 v[156:159], a[248:251], a[128:131], v[156:159]\n"
    "ds_read_b64_tr_b16 a[232:233], v13\n"
    "v_mfma_f32_16x16x32_bf16 v[156:159], a[252:255], a[132:135], v[156:159]\n"
    "v_mfma_f32_16x16x32_bf16 v[188:191], a[248:251], a[136:139], v[188:191]\n"
    "ds_read_b64_tr_b16 a[234:235], v13 offset:4096\n"
    "v_mfma_f32_16x16x32_bf16 v[188:191], a[252:255], a[140:143], v[188:191]\n"
    "buffer_load_dwordx4 v11, s[68:71], s83 offen offset:3072 lds\n"
    "v_mfma_f32_16x16x32_bf16 v[220:223], a[248:251], a[144:147], v[220:223]\n"
    "s_add_u32 s73, s73, 0x100000\n"
    "ds_read_b64_tr_b16 a[236:237], v13 offset:8192\n"
    "v_mfma_f32_16x16x32_bf16 v[220:223], a[252:255], a[148:151], v[220:223]\n"
    "s_add_u32 s83, s83, 0x100000\n"
    "s_add_u32 s74, s74, 0x8000\n"
    "v_mfma_f32_16x16x32_bf16 v[252:255], a[248:251], a[152:155], v[252:255]\n"
    "ds_read_b64_tr_b16 a[238:239], v13 offset:12288\n"
    "v_mfma_f32_16x16x32_bf16 v[252:255], a[252:255], a[156:159], v[252:255]\n"
    "s_cmp_lt_u32 s74, 0x28000\n"
    "s_cselect_b32 s74, s74, 0\n"
    "s_waitcnt vmcnt(16) lgkmcnt(4)\n"
    "v_mfma_f32_16x16x32_bf16 v[128:131], a[224:227], a[160:163], v[128:131]\n"
    "ds_read_b64_tr_b16 a[240:241], v14\n"
    "v_mfma_f32_16x16x32_bf16 v[128:131], a[228:231], a[164:167], v[128:131]\n"
    "buffer_load_dwordx4 a[128:131], v4, s[44:47], s72 offen sc1\n"
    "v_mfma_f32_16x16x32_bf16 v[160:163], a[224:227], a[168:171], v[160:163]\n"
    "ds_read_b64_tr_b16 a[242:243], v14 offset:4096\n"
    "v_accvgpr_write_b32 a0, 0\n"
    "v_mfma_f32_16x16x32_bf16 v[160:163], a[228:231], a[172:175], v[160:163]\n"
    "buffer_store_dwordx4 v[72:75], v24, s[56:59], s89 offen offset:2048 sc1\n"
    "v_mfma_f32_16x16x32_bf16 v[192:195], a[224:227], a[176:179], v[192:195]\n"
    "ds_read_b64_tr_b16 a[244:245], v14 offset:8192\n"
    "v_accvgpr_write_b32 a1, 0\n"
    "v_mfma_f32_16x16x32_bf16 v[192:195], a[228:231], a[180:183], v[192:195]\n"
    "v_accvgpr_write_b32 a2, 0\n"
    "v_mfma_f32_16x16x32_bf16 v[224:227], a[224:227], a[184:187], v[224:227]\n"
    "ds_read_b64_tr_b16 a[246:247], v14 offset:12288\n"
    "v_mfma_f32_16x16x32_bf16 v[224:227], a[228:231], a[188:191], v[224:227]\n"
    "s_waitcnt lgkmcnt(4)\n"
    "v_accvgpr_write_b32 a3, 0\n"
    "v_mfma_f32_16x16x32_bf16 v[132:135], a[232:235], a[160:163], v[132:135]\n"
    "ds_read_b64_tr_b16 a[248:249], v15\n"
    "v_mfma_f32_16x16x32_bf16 v[132:135], a[236:239], a[164:167], v[132:135]\n"
    "buffer_load_dwordx4 a[132:135], v4, s[44:47], s72 offen offset:1024 sc1\n"
    "v_mfma_f32_16x16x32_bf16 v[164:167], a[232:235], a[168:171], v[164:167]\n"
    "s_add_u32 m0, s74, s84\n"
    "ds_read_b64_tr_b16 a[250:251], v15 offset:4096\n"
    "v_mfma_f32_16x16x32_bf16 v[164:167], a[236:239], a[172:175], v[164:167]\n"
    "buffer_load_dwordx4 v8, s[68:71], s73 offen lds\n"
    "v_mfma_f32_16x16x32_bf16 v[196:199], a[232:235], a[176:179], v[196:199]\n"
    "ds_read_b64_tr_b16 a[252:253], v15 offset:8192\n"
    "v_accvgpr_write_b32 a4, 0\n"
    "v_mfma_f32_16x16x32_bf16 v[196:199], a[236:239], a[180:183], v[196:199]\n"
    "v_accvgpr_write_b32 a5, 0\n"
    "v_accvgpr_write_b32 a6, 0\n"
    "v_mfma_f32_16x16x32_bf16 v[228:231], a[232:235], a[184:187], v[228:231]\n"
    "ds_read_b64_tr_b16 a[254:255], v15 offset:12288\n"
    "v_mfma_f32_16x16x32_bf16 v[228:231], a[236:239], a[188:191], v[228:231]\n"
    "s_waitcnt lgkmcnt(4)\n"
    "v_accvgpr_write_b32 a7, 0\n"
    "v_mfma_f32_16x16x32_bf16 v[136:139], a[240:243], a[160:163], v[136:139]\n"
    "ds_read_b64_tr_b16 a[224:225], v16\n"
    "v_mfma_f32_16x16x32_bf16 v[136:139], a[244:247], a[164:167], v[136:139]\n"
    "buffer_load_dwordx4 a[136:139], v5, s[44:47], s72 offen sc1\n"
    "v_mfma_f32_16x16x32_bf16 v[168:171], a[240:243], a[168:171], v[168:171]\n"
    "ds_read_b64_tr_b16 a[226:227], v16 offset:4096\n"
    "v_accvgpr_write_b32 a8, 0\n"
    "v_mfma_f32_16x16x32_bf16 v[168:171], a[244:247], a[172:175], v[168:171]\n"
    "buffer_store_dwordx4 v[76:79], v24, s[56:59], s89 offen offset:3072 sc1\n"
    "v_mfma_f32_16x16x32_bf16 v[200:203], a[240:243], a[176:179], v[200:203]\n"
    "ds_read_b64_tr_b16 a[228:229], v16 offset:8192\n"
    "v_accvgpr_write_b32 a9, 0\n"
    "v_mfma_f32_16x16x32_bf16 v[200:203], a[244:247], a[180:183], v[200:203]\n"
    "v_accvgpr_write_b32 a10, 0\n"
    "v_mfma_f32_16x16x32_bf16 v[232:235], a[240:243], a[184:187], v[232:235]\n"
    "ds_read_b64_tr_b16 a[230:231], v16 offset:12288\n"
    "v_accvgpr_write_b32 a11, 0\n"
    "v_mfma_f32_16x16x32_bf16 v[232:235], a[244:247], a[188:191], v[232:235]\n"
    "s_waitcnt lgkmcnt(4)\n"
    "v_mfma_f32_16x16x32_bf16 v[140:143], a[248:251], a[160:163], v[140:143]\n"
    "ds_read_b64_tr_b16 a[232:233], v17\n"
    "v_accvgpr_write_b32 a12, 0\n"
    "v_mfma_f32_16x16x32_bf16 v[140:143], a[252:255], a[164:167], v[140:143]\n"
    "buffer_load_dwordx4 a[140:143], v5, s[44:47], s72 offen offset:1024 sc1\n"
    "v_mfma_f32_16x16x32_bf16 v[172:175], a[248:251], a[168:171], v[172:175]\n"
    "ds_read_b64_tr_b16 a[234:235], v17 offset:4096\n"
    "v_accvgpr_write_b32 a13, 0\n"
    "v_mfma_f32_16x16x32_bf16 v[172:175], a[252:255], a[172:175], v[172:175]\n"
    "buffer_load_dwordx4 v9, s[68:71], s73 offen offset:1024 lds\n"
    "v_mfma_f32_16x16x32_bf16 v[204:207], a[248:251], a[176:179], v[204:207]\n"
    "ds_read_b64_tr_b16 a[236:237], v17 offset:8192\n"
    "v_accvgpr_write_b32 a14, 0\n"
    "v_mfma_f32_16x16x32_bf16 v[204:207], a[252:255], a[180:183], v[204:207]\n"
    "s_add_u32 s89, s89, 0x1000\n"
    "v_mfma_f32_16x16x32_bf16 v[236:239], a[248:251], a[184:187], v[236:239]\n"
    "ds_read_b64_tr_b16 a[238:239], v17 offset:12288\n"
    "v_accvgpr_write_b32 a15, 0\n"
    "v_mfma_f32_16x16x32_bf16 v[236:239], a[252:255], a[188:191], v[236:239]\n"
    "s_waitcnt lgkmcnt(4)\n"
    "v_mfma_f32_16x16x32_bf16 v[144:147], a[224:227], a[160:163], v[144:147]\n"
    "ds_read_b64_tr_b16 a[240:241], v18\n"
    "v_accvgpr_write_b32 a16, 0\n"
    "v_mfma_f32_16x16x32_bf16 v[144:147], a[228:231], a[164:167], v[144:147]\n"
    "buffer_load_dwordx4 a[144:147], v6, s[44:47], s72 offen sc1\n"
    "v_mfma_f32_16x16x32_bf16 v[176:179], a[224:227], a[168:171], v[176:179]\n"
    "ds_read_b64_tr_b16 a[242:243], v18 offset:4096\n"
    "v_accvgpr_write_b32 a17, 0\n"
    "v_mfma_f32_16x16x32_bf16 v[176:179], a[228:231], a[172:175], v[176:179]\n"
    "v_mfma_f32_16x16x32_bf16 v[208:211], a[224:227], a[176:179], v[208:211]\n"
    "ds_read_b64_tr_b16 a[244:245], v18 offset:8192\n"
    "v_accvgpr_write_b32 a18, 0\n"
    "v_mfma_f32_16x16x32_bf16 v[208:211], a[228:231], a[180:183], v[208:211]\n"
    "buffer_load_dwordx4 a[148:151], v6, s[44:47], s72 offen offset:1024 sc1\n"
    "v_mfma_f32_16x16x32_bf16 v[240:243], a[224:227], a[184:187], v[240:243]\n"
    "ds_read_b64_tr_b16 a[246:247], v18 offset:12288\n"
    "v_accvgpr_write_b32 a19, 0\n"
    "v_mfma_f32_16x16x32_bf16 v[240:243], a[228:231], a[188:191], v[240:243]\n"
    "s_waitcnt lgkmcnt(4)\n"
    "v_mfma_f32_16x16x32_bf16 v[148:151], a[232:235], a[160:163], v[148:151]\n"
    "ds_read_b64_tr_b16 a[248:249], v19\n"
    "v_accvgpr_write_b32 a20, 0\n"
    "v_mfma_f32_16x16x32_bf16 v[148:151], a[236:239], a[164:167], v[148:151]\n"
    "v_mfma_f32_16x16x32_bf16 v[180:183], a[232:235], a[168:171], v[180:183]\n"
    "ds_read_b64_tr_b16 a[250:251], v19 offset:4096\n"
    "v_accvgpr_write_b32 a21, 0\n"
    "v_mfma_f32_16x16x32_bf16 v[180:183], a[236:239], a[172:175], v[180:183]\n"
    "buffer_load_dwordx4 v10, s[68:71], s73 offen offset:2048 lds\n"
    "v_mfma_f32_16x16x32_bf16 v[212:215], a[232:235], a[176:179], v[212:215]\n"
    "ds_read_b64_tr_b16 a[252:253], v19 offset:8192\n"
    "v_accvgpr_write_b32 a22, 0\n"
    "v_mfma_f32_16x16x32_bf16 v[212:215], a[236:239], a[180:183], v[212:215]\n"
    "buffer_store_dwordx4 v[80:83], v24, s[56:59], s89 offen sc1\n"
    "v_mfma_f32_16x16x32_bf16 v[244:247], a[232:235], a[184:187], v[244:247]\n"
    "ds_read_b64_tr_b16 a[254:255], v19 offset:12288\n"
    "v_accvgpr_write_b32 a23, 0\n"
    "v_mfma_f32_16x16x32_bf16 v[244:247], a[236:239], a[188:191], v[244:247]\n"
    "s_waitcnt lgkmcnt(4)\n"
    "v_accvgpr_write_b32 a24, 0\n"
    "v_mfma_f32_16x16x32_bf16 v[152:155], a[240:243], a[160:163], v[152:155]\n"
    "ds_read_b64_tr_b16 a[224:225], v12 offset:16384\n"
    "v_mfma_f32_16x16x32_bf16 v[152:155], a[244:247], a[164:167], v[152:155]\n"
    "buffer_load_dwordx4 a[152:155], v7, s[44:47], s72 offen sc1\n"
    "v_mfma_f32_16x16x32_bf16 v[184:187], a[240:243], a[168:171], v[184:187]\n"
    "s_add_u32 s75, s75, 1\n"
    "ds_read_b64_tr_b16 a[226:227], v12 offset:20480\n"
    "v_mfma_f32_16x16x32_bf16 v[184:187], a[244:247], a[172:175], v[184:187]\n"
    "v_accvgpr_write_b32 a25, 0\n"
    "v_accvgpr_write_b32 a26, 0\n"
    "v_mfma_f32_16x16x32_bf16 v[216:219], a[240:243], a[176:179], v[216:219]\n"
    "ds_read_b64_tr_b16 a[228:229], v12 offset:24576\n"
    "v_mfma_f32_16x16x32_bf16 v[216:219], a[244:247], a[180:183], v[216:219]\n"
    "s_cmp_lt_u32 s75, 5\n"
    "s_cselect_b32 s75, s75, 0\n"
    "s_cselect_b32 s76, 0x8000, s77\n"
    "v_mfma_f32_16x16x32_bf16 v[248:251], a[240:243], a[184:187], v[248:251]\n"
    "ds_read_b64_tr_b16 a[230:231], v12 offset:28672\n"
    "v_add_u32 v12, s76, v12\n"
    "v_mfma_f32_16x16x32_bf16 v[248:251], a[244:247], a[188:191], v[248:251]\n"
    "s_waitcnt lgkmcnt(4)\n"
    "v_accvgpr_write_b32 a27, 0\n"
    "v_accvgpr_write_b32 a28, 0\n"
    "v_mfma_f32_16x16x32_bf16 v[156:159], a[248:251], a[160:163], v[156:159]\n"
    "ds_read_b64_tr_b16 a[232:233], v13 offset:16384\n"
    "v_mfma_f32_16x16x32_bf16 v[156:159], a[252:255], a[164:167], v[156:159]\n"
    "buffer_load_dwordx4 a[156:159], v7, s[44:47], s72 offen offset:1024 sc1\n"
    "v_mfma_f32_16x16x32_bf16 v[188:191], a[248:251], a[168:171], v[188:191]\n"
    "s_add_u32 s72, s72, 0x40000\n"
    "ds_read_b64_tr_b16 a[234:235], v13 offset:20480\n"
    "v_mfma_f32_16x16x32_bf16 v[188:191], a[252:255], a[172:175], v[188:191]\n"
    "buffer_load_dwordx4 v11, s[68:71], s73 offen offset:3072 lds\n"
    "v_mfma_f32_16x16x32_bf16 v[220:223], a[248:251], a[176:179], v[220:223]\n"
    "ds_read_b64_tr_b16 a[236:237], v13 offset:24576\n"
    "v_accvgpr_write_b32 a29, 0\n"
    "v_mfma_f32_16x16x32_bf16 v[220:223], a[252:255], a[180:183], v[220:223]\n"
    "buffer_store_dwordx4 v[84:87], v24, s[56:59], s89 offen offset:1024 sc1\n"
    "v_mfma_f32_16x16x32_bf16 v[252:255], a[248:251], a[184:187], v[252:255]\n"
    "ds_read_b64_tr_b16 a[238:239], v13 offset:28672\n"
    "v_add_u32 v13, s76, v13\n"
    "v_mfma_f32_16x16x32_bf16 v[252:255], a[252:255], a[188:191], v[252:255]\n"
    "s_waitcnt vmcnt(17) lgkmcnt(4)\n"
    "v_accvgpr_write_b32 a30, 0\n"
    "v_accvgpr_write_b32 a31, 0\n"
    "v_mfma_f32_16x16x32_bf16 v[128:131], a[224:227], a[192:195], v[128:131]\n"
    "ds_read_b64_tr_b16 a[240:241], v14 offset:16384\n"
    "v_accvgpr_write_b32 a32, 0\n"
    "v_mfma_f32_16x16x32_bf16 v[128:131], a[228:231], a[196:199], v[128:131]\n"
    "buffer_load_dwordx4 a[160:163], v4, s[44:47], s72 offen sc1\n"
    "v_mfma_f32_16x16x32_bf16 v[160:163], a[224:227], a[200:203], v[160:163]\n"
    "ds_read_b64_tr_b16 a[242:243], v14 offset:20480\n"
    "v_accvgpr_write_b32 a33, 0\n"
    "v_mfma_f32_16x16x32_bf16 v[160:163], a[228:231], a[204:207], v[160:163]\n"
    "v_accvgpr_write_b32 a34, 0\n"
    "v_mfma_f32_16x16x32_bf16 v[192:195], a[224:227], a[208:211], v[192:195]\n"
    "ds_read_b64_tr_b16 a[244:245], v14 offset:24576\n"
    "v_accvgpr_write_b32 a35, 0\n"
    "v_mfma_f32_16x16x32_bf16 v[192:195], a[228:231], a[212:215], v[192:195]\n"
    "buffer_store_dwordx4 v[88:91], v24, s[56:59], s89 offen offset:2048 sc1\n"
    "v_mfma_f32_16x16x32_bf16 v[224:227], a[224:227], a[216:219], v[224:227]\n"
    "ds_read_b64_tr_b16 a[246:247], v14 offset:28672\n"
    "v_add_u32 v14, s76, v14\n"
    "v_mfma_f32_16x16x32_bf16 v[224:227], a[228:231], a[220:223], v[224:227]\n"
    "s_waitcnt lgkmcnt(4)\n"
    "v_accvgpr_write_b32 a36, 0\n"
    "v_mfma_f32_16x16x32_bf16 v[132:135], a[232:235], a[192:195], v[132:135]\n"
    "ds_read_b64_tr_b16 a[248:249], v15 offset:16384\n"
    "v_accvgpr_write_b32 a37, 0\n"
    "v_mfma_f32_16x16x32_bf16 v[132:135], a[236:239], a[196:199], v[132:135]\n"
    "buffer_load_dwordx4 a[164:167], v4, s[44:47], s72 offen offset:1024 sc1\n"
    "v_mfma_f32_16x16x32_bf16 v[164:167], a[232:235], a[200:203], v[164:167]\n"
    "s_add_u32 m0, s74, s85\n"
    "ds_read_b64_tr_b16 a[250:251], v15 offset:20480\n"
    "v_mfma_f32_16x16x32_bf16 v[164:167], a[236:239], a[204:207], v[164:167]\n"
    "buffer_load_dwordx4 v8, s[68:71], s83 offen lds\n"
    "v_mfma_f32_16x16x32_bf16 v[196:199], a[232:235], a[208:211], v[196:199]\n"
    "ds_read_b64_tr_b16 a[252:253], v15 offset:24576\n"
    "v_accvgpr_write_b32 a38, 0\n"
    "v_mfma_f32_16x16x32_bf16 v[196:199], a[236:239], a[212:215], v[196:199]\n"
    "v_accvgpr_write_b32 a39, 0\n"
    "v_mfma_f32_16x16x32_bf16 v[228:231], a[232:235], a[216:219], v[228:231]\n"
    "ds_read_b64_tr_b16 a[254:255], v15 offset:28672\n"
    "v_add_u32 v15, s76, v15\n"
    "v_mfma_f32_16x16x32_bf16 v[228:231], a[236:239], a[220:223], v[228:231]\n"
    "s_waitcnt lgkmcnt(4)\n"
    "v_accvgpr_write_b32 a40, 0\n"
    "v_mfma_f32_16x16x32_bf16 v[136:139], a[240:243], a[192:195], v[136:139]\n"
    "ds_read_b64_tr_b16 a[224:225], v16 offset:16384\n"
    "v_accvgpr_write_b32 a41, 0\n"
    "v_mfma_f32_16x16x32_bf16 v[136:139], a[244:247], a[196:199], v[136:139]\n"
    "buffer_load_dwordx4 a[168:171], v5, s[44:47], s72 offen sc1\n"
    "v_mfma_f32_16x16x32_bf16 v[168:171], a[240:243], a[200:203], v[168:171]\n"
    "ds_read_b64_tr_b16 a[226:227], v16 offset:20480\n"
    "v_accvgpr_write_b32 a42, 0\n"
    "v_mfma_f32_16x16x32_bf16 v[168:171], a[244:247], a[204:207], v[168:171]\n"
    "v_accvgpr_write_b32 a43, 0\n"
    "v_mfma_f32_16x16x32_bf16 v[200:203], a[240:243], a[208:211], v[200:203]\n"
    "ds_read_b64_tr_b16 a[228:229], v16 offset:24576\n"
    "v_mfma_f32_16x16x32_bf16 v[200:203], a[244:247], a[212:215], v[200:203]\n"
    "buffer_store_dwordx4 v[92:95], v24, s[56:59], s89 offen offset:3072 sc1\n"
    "v_mfma_f32_16x16x32_bf16 v[232:235], a[240:243], a[216:219], v[232:235]\n"
    "ds_read_b64_tr_b16 a[230:231], v16 offset:28672\n"
    "v_add_u32 v16, s76, v16\n"
    "v_mfma_f32_16x16x32_bf16 v[232:235], a[244:247], a[220:223], v[232:235]\n"
    "s_waitcnt lgkmcnt(4)\n"
    "v_accvgpr_write_b32 a44, 0\n"
    "v_accvgpr_write_b32 a45, 0\n"
    "v_mfma_f32_16x16x32_bf16 v[140:143], a[248:251], a[192:195], v[140:143]\n"
    "ds_read_b64_tr_b16 a[232:233], v17 offset:16384\n"
    "v_mfma_f32_16x16x32_bf16 v[140:143], a[252:255], a[196:199], v[140:143]\n"
    "buffer_load_dwordx4 a[172:175], v5, s[44:47], s72 offen offset:1024 sc1\n"
    "v_mfma_f32_16x16x32_bf16 v[172:175], a[248:251], a[200:203], v[172:175]\n"
    "ds_read_b64_tr_b16 a[234:235], v17 offset:20480\n"
    "v_accvgpr_write_b32 a46, 0\n"
    "v_mfma_f32_16x16x32_bf16 v[172:175], a[252:255], a[204:207], v[172:175]\n"
    "buffer_load_dwordx4 v9, s[68:71], s83 offen offset:1024 lds\n"
    "v_mfma_f32_16x16x32_bf16 v[204:207], a[248:251], a[208:211], v[204:207]\n"
    "ds_read_b64_tr_b16 a[236:237], v17 offset:24576\n"
    "v_accvgpr_write_b32 a47, 0\n"
    "v_mfma_f32_16x16x32_bf16 v[204:207], a[252:255], a[212:215], v[204:207]\n"
    "v_accvgpr_write_b32 a48, 0\n"
    "v_mfma_f32_16x16x32_bf16 v[236:239], a[248:251], a[216:219], v[236:239]\n"
    "ds_read_b64_tr_b16 a[238:239], v17 offset:28672\n"
    "v_add_u32 v17, s76, v17\n"
    "v_mfma_f32_16x16x32_bf16 v[236:239], a[252:255], a[220:223], v[236:239]\n"
    "s_waitcnt lgkmcnt(4)\n"
    "v_accvgpr_write_b32 a49, 0\n"
    "v_mfma_f32_16x16x32_bf16 v[144:147], a[224:227], a[192:195], v[144:147]\n"
    "ds_read_b64_tr_b16 a[240:241], v18 offset:16384\n"
    "v_mfma_f32_16x16x32_bf16 v[144:147], a[228:231], a[196:199], v[144:147]\n"
    "buffer_load_dwordx4 a[176:179], v6, s[44:47], s72 offen sc1\n"
    "v_mfma_f32_16x16x32_bf16 v[176:179], a[224:227], a[200:203], v[176:179]\n"
    "s_add_u32 s89, s89, 0x1000\n"
    "ds_read_b64_tr_b16 a[242:243], v18 offset:20480\n"
    "v_mfma_f32_16x16x32_bf16 v[176:179], a[228:231], a[204:207], v[176:179]\n"
    "v_accvgpr_write_b32 a50, 0\n"
    "v_accvgpr_write_b32 a51, 0\n"
    "v_mfma_f32_16x16x32_bf16 v[208:211], a[224:227], a[208:211], v[208:211]\n"
    "ds_read_b64_tr_b16 a[244:245], v18 offset:24576\n"
    "v_mfma_f32_16x16x32_bf16 v[208:211], a[228:231], a[212:215], v[208:211]\n"
    "buffer_load_dwordx4 a[180:183], v6, s[44:47], s72 offen offset:1024 sc1\n"
    "v_mfma_f32_16x16x32_bf16 v[240:243], a[224:227], a[216:219], v[240:243]\n"
    "ds_read_b64_tr_b16 a[246:247], v18 offset:28672\n"
    "v_add_u32 v18, s76, v18\n"
    "v_mfma_f32_16x16x32_bf16 v[240:243], a[228:231], a[220:223], v[240:243]\n"
    "s_waitcnt lgkmcnt(4)\n"
    "v_accvgpr_write_b32 a52, 0\n"
    "v_accvgpr_write_b32 a53, 0\n"
    "v_mfma_f32_16x16x32_bf16 v[148:151], a[232:235], a[192:195], v[148:151]\n"
    "ds_read_b64_tr_b16 a[248:249], v19 offset:16384\n"
    "v_accvgpr_write_b32 a54, 0\n"
    "v_mfma_f32_16x16x32_bf16 v[148:151], a[236:239], a[196:199], v[148:151]\n"
    "v_mfma_f32_16x16x32_bf16 v[180:183], a[232:235], a[200:203], v[180:183]\n"
    "ds_read_b64_tr_b16 a[250:251], v19 offset:20480\n"
    "v_accvgpr_write_b32 a55, 0\n"
    "v_mfma_f32_16x16x32_bf16 v[180:183], a[236:239], a[204:207], v[180:183]\n"
    "buffer_load_dwordx4 v10, s[68:71], s83 offen offset:2048 lds\n"
    "v_mfma_f32_16x16x32_bf16 v[212:215], a[232:235], a[208:211], v[212:215]\n"
    "ds_read_b64_tr_b16 a[252:253], v19 offset:24576\n"
    "v_accvgpr_write_b32 a56, 0\n"
    "v_mfma_f32_16x16x32_bf16 v[212:215], a[236:239], a[212:215], v[212:215]\n"
    "v_mfma_f32_16x16x32_bf16 v[244:247], a[232:235], a[216:219], v[244:247]\n"
    "ds_read_b64_tr_b16 a[254:255], v19 offset:28672\n"
    "v_add_u32 v19, s76, v19\n"
    "v_mfma_f32_16x16x32_bf16 v[244:247], a[236:239], a[220:223], v[244:247]\n"
    "s_waitcnt lgkmcnt(4)\n"
    "s_barrier\n"
    "v_accvgpr_write_b32 a57, 0\n"
    "v_mfma_f32_16x16x32_bf16 v[152:155], a[240:243], a[192:195], v[152:155]\n"
    "ds_read_b64_tr_b16 a[224:225], v12\n"
    "v_accvgpr_write_b32 a58, 0\n"
    "v_mfma_f32_16x16x32_bf16 v[152:155], a[244:247], a[196:199], v[152:155]\n"
    "buffer_load_dwordx4 a[184:187], v7, s[44:47], s72 offen sc1\n"
    "v_mfma_f32_16x16x32_bf16 v[184:187], a[240:243], a[200:203], v[184:187]\n"
    "ds_read_b64_tr_b16 a[226:227], v12 offset:4096\n"
    "v_accvgpr_write_b32 a59, 0\n"
    "v_mfma_f32_16x16x32_bf16 v[184:187], a[244:247], a[204:207], v[184:187]\n"
    "v_mfma_f32_16x16x32_bf16 v[216:219], a[240:243], a[208:211], v[216:219]\n"
    "ds_read_b64_tr_b16 a[228:229], v12 offset:8192\n"
    "v_accvgpr_write_b32 a60, 0\n"
    "v_mfma_f32_16x16x32_bf16 v[216:219], a[244:247], a[212:215], v[216:219]\n"
    "buffer_load_dwordx4 a[188:191], v7, s[44:47], s72 offen offset:1024 sc1\n"
    "v_mfma_f32_16x16x32_bf16 v[248:251], a[240:243], a[216:219], v[248:251]\n"
    "s_add_u32 s72, s72, 0x40000\n"
    "ds_read_b64_tr_b16 a[230:231], v12 offset:12288\n"
    "v_mfma_f32_16x16x32_bf16 v[248:251], a[244:247], a[220:223], v[248:251]\n"
    "s_waitcnt lgkmcnt(4)\n"
    "v_accvgpr_write_b32 a61, 0\n"
    "v_mfma_f32_16x16x32_bf16 v[156:159], a[248:251], a[192:195], v[156:159]\n"
    "ds_read_b64_tr_b16 a[232:233], v13\n"
    "v_accvgpr_write_b32 a62, 0\n"
    "v_mfma_f32_16x16x32_bf16 v[156:159], a[252:255], a[196:199], v[156:159]\n"
    "v_mfma_f32_16x16x32_bf16 v[188:191], a[248:251], a[200:203], v[188:191]\n"
    "ds_read_b64_tr_b16 a[234:235], v13 offset:4096\n"
    "v_accvgpr_write_b32 a63, 0\n"
    "v_mfma_f32_16x16x32_bf16 v[188:191], a[252:255], a[204:207], v[188:191]\n"
    "buffer_load_dwordx4 v11, s[68:71], s83 offen offset:3072 lds\n"
    "v_mfma_f32_16x16x32_bf16 v[220:223], a[248:251], a[208:211], v[220:223]\n"
    "s_add_u32 s73, s73, 0x100000\n"
    "ds_read_b64_tr_b16 a[236:237], v13 offset:8192\n"
    "v_mfma_f32_16x16x32_bf16 v[220:223], a[252:255], a[212:215], v[220:223]\n"
    "s_add_u32 s83, s83, 0x100000\n"
    "s_add_u32 s74, s74, 0x8000\n"
    "v_mfma_f32_16x16x32_bf16 v[252:255], a[248:251], a[216:219], v[252:255]\n"
    "ds_read_b64_tr_b16 a[238:239], v13 offset:12288\n"
    "v_mfma_f32_16x16x32_bf16 v[252:255], a[252:255], a[220:223], v[252:255]\n"
    "s_cmp_lt_u32 s74, 0x28000\n"
    "s_cselect_b32 s74, s74, 0\n"
    "s_waitcnt vmcnt(16) lgkmcnt(4)\n"
    "v_mfma_f32_16x16x32_bf16 v[128:131], a[224:227], a[128:131], v[128:131]\n"
    "ds_read_b64_tr_b16 a[240:241], v14\n"
    "v_mfma_f32_16x16x32_bf16 v[128:131], a[228:231], a[132:135], v[128:131]\n"
    "buffer_load_dwordx4 a[192:195], v4, s[44:47], s72 offen sc1\n"
    "v_mfma_f32_16x16x32_bf16 v[160:163], a[224:227], a[136:139], v[160:163]\n"
    "ds_read_b64_tr_b16 a[242:243], v14 offset:4096\n"
    "v_accvgpr_write_b32 a64, 0\n"
    "v_mfma_f32_16x16x32_bf16 v[160:163], a[228:231], a[140:143], v[160:163]\n"
    "v_accvgpr_write_b32 a65, 0\n"
    "v_mfma_f32_16x16x32_bf16 v[192:195], a[224:227], a[144:147], v[192:195]\n"
    "ds_read_b64_tr_b16 a[244:245], v14 offset:8192\n"
    "v_mfma_f32_16x16x32_bf16 v[192:195], a[228:231], a[148:151], v[192:195]\n"
    "v_accvgpr_write_b32 a66, 0\n"
    "v_mfma_f32_16x16x32_bf16 v[224:227], a[224:227], a[152:155], v[224:227]\n"
    "ds_read_b64_tr_b16 a[246:247], v14 offset:12288\n"
    "v_mfma_f32_16x16x32_bf16 v[224:227], a[228:231], a[156:159], v[224:227]\n"
    "s_waitcnt lgkmcnt(4)\n"
    "v_accvgpr_write_b32 a67, 0\n"
    "v_mfma_f32_16x16x32_bf16 v[132:135], a[232:235], a[128:131], v[132:135]\n"
    "ds_read_b64_tr_b16 a[248:249], v15\n"
    "v_mfma_f32_16x16x32_bf16 v[132:135], a[236:239], a[132:135], v[132:135]\n"
    "buffer_load_dwordx4 a[196:199], v4, s[44:47], s72 offen offset:1024 sc1\n"
    "v_mfma_f32_16x16x32_bf16 v[164:167], a[232:235], a[136:139], v[164:167]\n"
    "ds_read_b64_tr_b16 a[250:251], v15 offset:4096\n"
    "v_accvgpr_write_b32 a68, 0\n"
    "v_mfma_f32_16x16x32_bf16 v[164:167], a[236:239], a[140:143], v[164:167]\n"
    "v_accvgpr_write_b32 a69, 0\n"
    "v_mfma_f32_16x16x32_bf16 v[196:199], a[232:235], a[144:147], v[196:199]\n"
    "ds_read_b64_tr_b16 a[252:253], v15 offset:8192\n"
    "v_mfma_f32_16x16x32_bf16 v[196:199], a[236:239], a[148:151], v[196:199]\n"
    "v_accvgpr_write_b32 a70, 0\n"
    "v_mfma_f32_16x16x32_bf16 v[228:231], a[232:235], a[152:155], v[228:231]\n"
    "ds_read_b64_tr_b16 a[254:255], v15 offset:12288\n"
    "v_mfma_f32_16x16x32_bf16 v[228:231], a[236:239], a[156:159], v[228:231]\n"
    "s_waitcnt lgkmcnt(4)\n"
    "v_accvgpr_write_b32 a71, 0\n"
    "v_mfma_f32_16x16x32_bf16 v[136:139], a[240:243], a[128:131], v[136:139]\n"
    "ds_read_b64_tr_b16 a[224:225], v16\n"
    "v_mfma_f32_16x16x32_bf16 v[136:139], a[244:247], a[132:135], v[136:139]\n"
    "buffer_load_dwordx4 a[200:203], v5, s[44:47], s72 offen sc1\n"
    "v_mfma_f32_16x16x32_bf16 v[168:171], a[240:243], a[136:139], v[168:171]\n"
    "ds_read_b64_tr_b16 a[226:227], v16 offset:4096\n"
    "v_accvgpr_write_b32 a72, 0\n"
    "v_mfma_f32_16x16x32_bf16 v[168:171], a[244:247], a[140:143], v[168:171]\n"
    "v_accvgpr_write_b32 a73, 0\n"
    "v_mfma_f32_16x16x32_bf16 v[200:203], a[240:243], a[144:147], v[200:203]\n"
    "ds_read_b64_tr_b16 a[228:229], v16 offset:8192\n"
    "v_mfma_f32_16x16x32_bf16 v[200:203], a[244:247], a[148:151], v[200:203]\n"
    "v_accvgpr_write_b32 a74, 0\n"
    "v_mfma_f32_16x16x32_bf16 v[232:235], a[240:243], a[152:155], v[232:235]\n"
    "ds_read_b64_tr_b16 a[230:231], v16 offset:12288\n"
    "v_accvgpr_write_b32 a75, 0\n"
    "v_mfma_f32_16x16x32_bf16 v[232:235], a[244:247], a[156:159], v[232:235]\n"
    "s_waitcnt lgkmcnt(4)\n"
    "v_mfma_f32_16x16x32_bf16 v[140:143], a[248:251], a[128:131], v[140:143]\n"
    "ds_read_b64_tr_b16 a[232:233], v17\n"
    "v_accvgpr_write_b32 a76, 0\n"
    "v_mfma_f32_16x16x32_bf16 v[140:143], a[252:255], a[132:135], v[140:143]\n"
    "buffer_load_dwordx4 a[204:207], v5, s[44:47], s72 offen offset:1024 sc1\n"
    "v_mfma_f32_16x16x32_bf16 v[172:175], a[248:251], a[136:139], v[172:175]\n"
    "ds_read_b64_tr_b16 a[234:235], v17 offset:4096\n"
    "v_accvgpr_write_b32 a77, 0\n"
    "v_mfma_f32_16x16x32_bf16 v[172:175], a[252:255], a[140:143], v[172:175]\n"
    "v_mfma_f32_16x16x32_bf16 v[204:207], a[248:251], a[144:147], v[204:207]\n"
    "ds_read_b64_tr_b16 a[236:237], v17 offset:8192\n"
    "v_accvgpr_write_b32 a78, 0\n"
    "v_mfma_f32_16x16x32_bf16 v[204:207], a[252:255], a[148:151], v[204:207]\n"
    "v_mfma_f32_16x16x32_bf16 v[236:239], a[248:251], a[152:155], v[236:239]\n"
    "ds_read_b64_tr_b16 a[238:239], v17 offset:12288\n"
    "v_accvgpr_write_b32 a79, 0\n"
    "v_mfma_f32_16x16x32_bf16 v[236:239], a[252:255], a[156:159], v[236:239]\n"
    "s_waitcnt lgkmcnt(4)\n"
    "v_mfma_f32_16x16x32_bf16 v[144:147], a[224:227], a[128:131], v[144:147]\n"
    "ds_read_b64_tr_b16 a[240:241], v18\n"
    "v_accvgpr_write_b32 a80, 0\n"
    "v_mfma_f32_16x16x32_bf16 v[144:147], a[228:231], a[132:135], v[144:147]\n"
    "buffer_load_dwordx4 a[208:211], v6, s[44:47], s72 offen sc1\n"
    "v_mfma_f32_16x16x32_bf16 v[176:179], a[224:227], a[136:139], v[176:179]\n"
    "ds_read_b64_tr_b16 a[242:243], v18 offset:4096\n"
    "v_accvgpr_write_b32 a81, 0\n"
    "v_mfma_f32_16x16x32_bf16 v[176:179], a[228:231], a[140:143], v[176:179]\n"
    "v_mfma_f32_16x16x32_bf16 v[208:211], a[224:227], a[144:147], v[208:211]\n"
    "ds_read_b64_tr_b16 a[244:245], v18 offset:8192\n"
    "v_accvgpr_write_b32 a82, 0\n"
    "v_mfma_f32_16x16x32_bf16 v[208:211], a[228:231], a[148:151], v[208:211]\n"
    "v_mfma_f32_16x16x32_bf16 v[240:243], a[224:227], a[152:155], v[240:243]\n"
    "ds_read_b64_tr_b16 a[246:247], v18 offset:12288\n"
    "v_accvgpr_write_b32 a83, 0\n"
    "v_mfma_f32_16x16x32_bf16 v[240:243], a[228:231], a[156:159], v[240:243]\n"
    "s_waitcnt lgkmcnt(4)\n"
    "v_mfma_f32_16x16x32_bf16 v[148:151], a[232:235], a[128:131], v[148:151]\n"
    "ds_read_b64_tr_b16 a[248:249], v19\n"
    "v_accvgpr_write_b32 a84, 0\n"
    "v_mfma_f32_16x16x32_bf16 v[148:151], a[236:239], a[132:135], v[148:151]\n"
    "buffer_load_dwordx4 a[212:215], v6, s[44:47], s72 offen offset:1024 sc1\n"
    "v_mfma_f32_16x16x32_bf16 v[180:183], a[232:235], a[136:139], v[180:183]\n"
    "ds_read_b64_tr_b16 a[250:251], v19 offset:4096\n"
    "v_accvgpr_write_b32 a85, 0\n"
    "v_mfma_f32_16x16x32_bf16 v[180:183], a[236:239], a[140:143], v[180:183]\n"
    "v_accvgpr_write_b32 a86, 0\n"
    "v_mfma_f32_16x16x32_bf16 v[212:215], a[232:235], a[144:147], v[212:215]\n"
    "ds_read_b64_tr_b16 a[252:253], v19 offset:8192\n"
    "v_mfma_f32_16x16x32_bf16 v[212:215], a[236:239], a[148:151], v[212:215]\n"
    "v_accvgpr_write_b32 a87, 0\n"
    "v_mfma_f32_16x16x32_bf16 v[244:247], a[232:235], a[152:155], v[244:247]\n"
    "ds_read_b64_tr_b16 a[254:255], v19 offset:12288\n"
    "v_mfma_f32_16x16x32_bf16 v[244:247], a[236:239], a[156:159], v[244:247]\n"
    "s_waitcnt lgkmcnt(4)\n"
    "v_accvgpr_write_b32 a88, 0\n"
    "v_mfma_f32_16x16x32_bf16 v[152:155], a[240:243], a[128:131], v[152:155]\n"
    "ds_read_b64_tr_b16 a[224:225], v12 offset:16384\n"
    "v_mfma_f32_16x16x32_bf16 v[152:155], a[244:247], a[132:135], v[152:155]\n"
    "buffer_load_dwordx4 a[216:219], v7, s[44:47], s72 offen sc1\n"
    "v_mfma_f32_16x16x32_bf16 v[184:187], a[240:243], a[136:139], v[184:187]\n"
    "s_add_u32 s75, s75, 1\n"
    "ds_read_b64_tr_b16 a[226:227], v12 offset:20480\n"
    "v_mfma_f32_16x16x32_bf16 v[184:187], a[244:247], a[140:143], v[184:187]\n"
    "v_accvgpr_write_b32 a89, 0\n"
    "v_accvgpr_write_b32 a90, 0\n"
    "v_mfma_f32_16x16x32_bf16 v[216:219], a[240:243], a[144:147], v[216:219]\n"
    "ds_read_b64_tr_b16 a[228:229], v12 offset:24576\n"
    "v_mfma_f32_16x16x32_bf16 v[216:219], a[244:247], a[148:151], v[216:219]\n"
    "s_cmp_lt_u32 s75, 5\n"
    "s_cselect_b32 s75, s75, 0\n"
    "s_cselect_b32 s76, 0x8000, s77\n"
    "v_mfma_f32_16x16x32_bf16 v[248:251], a[240:243], a[152:155], v[248:251]\n"
    "ds_read_b64_tr_b16 a[230:231], v12 offset:28672\n"
    "v_add_u32 v12, s76, v12\n"
    "v_mfma_f32_16x16x32_bf16 v[248:251], a[244:247], a[156:159], v[248:251]\n"
    "s_waitcnt lgkmcnt(4)\n"
    "v_accvgpr_write_b32 a91, 0\n"
    "v_accvgpr_write_b32 a92, 0\n"
    "v_mfma_f32_16x16x32_bf16 v[156:159], a[248:251], a[128:131], v[156:159]\n"
    "ds_read_b64_tr_b16 a[232:233], v13 offset:16384\n"
    "v_mfma_f32_16x16x32_bf16 v[156:159], a[252:255], a[132:135], v[156:159]\n"
    "buffer_load_dwordx4 a[220:223], v7, s[44:47], s72 offen offset:1024 sc1\n"
    "v_mfma_f32_16x16x32_bf16 v[188:191], a[248:251], a[136:139], v[188:191]\n"
    "s_add_u32 s72, s72, 0x40000\n"
    "ds_read_b64_tr_b16 a[234:235], v13 offset:20480\n"
    "v_mfma_f32_16x16x32_bf16 v[188:191], a[252:255], a[140:143], v[188:191]\n"
    "v_accvgpr_write_b32 a93, 0\n"
    "v_accvgpr_write_b32 a94, 0\n"
    "v_mfma_f32_16x16x32_bf16 v[220:223], a[248:251], a[144:147], v[220:223]\n"
    "ds_read_b64_tr_b16 a[236:237], v13 offset:24576\n"
    "v_mfma_f32_16x16x32_bf16 v[220:223], a[252:255], a[148:151], v[220:223]\n"
    "v_accvgpr_write_b32 a95, 0\n"
    "v_mfma_f32_16x16x32_bf16 v[252:255], a[248:251], a[152:155], v[252:255]\n"
    "ds_read_b64_tr_b16 a[238:239], v13 offset:28672\n"
    "v_add_u32 v13, s76, v13\n"
    "v_mfma_f32_16x16x32_bf16 v[252:255], a[252:255], a[156:159], v[252:255]\n"
    "s_waitcnt vmcnt(9) lgkmcnt(4)\n"
    "v_accvgpr_write_b32 a96, 0\n"
    "v_mfma_f32_16x16x32_bf16 v[128:131], a[224:227], a[160:163], v[128:131]\n"
    "ds_read_b64_tr_b16 a[240:241], v14 offset:16384\n"
    "v_accvgpr_write_b32 a97, 0\n"
    "v_mfma_f32_16x16x32_bf16 v[128:131], a[228:231], a[164:167], v[128:131]\n"
    "buffer_load_dwordx4 a[128:131], v4, s[44:47], s72 offen sc1\n"
    "v_mfma_f32_16x16x32_bf16 v[160:163], a[224:227], a[168:171], v[160:163]\n"
    "ds_read_b64_tr_b16 a[242:243], v14 offset:20480\n"
    "v_accvgpr_write_b32 a98, 0\n"
    "v_mfma_f32_16x16x32_bf16 v[160:163], a[228:231], a[172:175], v[160:163]\n"
    "v_mfma_f32_16x16x32_bf16 v[192:195], a[224:227], a[176:179], v[192:195]\n"
    "ds_read_b64_tr_b16 a[244:245], v14 offset:24576\n"
    "v_accvgpr_write_b32 a99, 0\n"
    "v_mfma_f32_16x16x32_bf16 v[192:195], a[228:231], a[180:183], v[192:195]\n"
    "v_mfma_f32_16x16x32_bf16 v[224:227], a[224:227], a[184:187], v[224:227]\n"
    "ds_read_b64_tr_b16 a[246:247], v14 offset:28672\n"
    "v_add_u32 v14, s76, v14\n"
    "v_mfma_f32_16x16x32_bf16 v[224:227], a[228:231], a[188:191], v[224:227]\n"
    "s_waitcnt lgkmcnt(4)\n"
    "v_accvgpr_write_b32 a100, 0\n"
    "v_mfma_f32_16x16x32_bf16 v[132:135], a[232:235], a[160:163], v[132:135]\n"
    "ds_read_b64_tr_b16 a[248:249], v15 offset:16384\n"
    "v_accvgpr_write_b32 a101, 0\n"
    "v_mfma_f32_16x16x32_bf16 v[132:135], a[236:239], a[164:167], v[132:135]\n"
    "buffer_load_dwordx4 a[132:135], v4, s[44:47], s72 offen offset:1024 sc1\n"
    "v_mfma_f32_16x16x32_bf16 v[164:167], a[232:235], a[168:171], v[164:167]\n"
    "ds_read_b64_tr_b16 a[250:251], v15 offset:20480\n"
    "v_accvgpr_write_b32 a102, 0\n"
    "v_mfma_f32_16x16x32_bf16 v[164:167], a[236:239], a[172:175], v[164:167]\n"
    "v_mfma_f32_16x16x32_bf16 v[196:199], a[232:235], a[176:179], v[196:199]\n"
    "ds_read_b64_tr_b16 a[252:253], v15 offset:24576\n"
    "v_accvgpr_write_b32 a103, 0\n"
    "v_mfma_f32_16x16x32_bf16 v[196:199], a[236:239], a[180:183], v[196:199]\n"
    "v_mfma_f32_16x16x32_bf16 v[228:231], a[232:235], a[184:187], v[228:231]\n"
    "ds_read_b64_tr_b16 a[254:255], v15 offset:28672\n"
    "v_add_u32 v15, s76, v15\n"
    "v_mfma_f32_16x16x32_bf16 v[228:231], a[236:239], a[188:191], v[228:231]\n"
    "s_waitcnt lgkmcnt(4)\n"
    "v_accvgpr_write_b32 a104, 0\n"
    "v_mfma_f32_16x16x32_bf16 v[136:139], a[240:243], a[160:163], v[136:139]\n"
    "ds_read_b64_tr_b16 a[224:225], v16 offset:16384\n"
    "v_accvgpr_write_b32 a105, 0\n"
    "v_mfma_f32_16x16x32_bf16 v[136:139], a[244:247], a[164:167], v[136:139]\n"
    "buffer_load_dwordx4 a[136:139], v5, s[44:47], s72 offen sc1\n"
    "v_mfma_f32_16x16x32_bf16 v[168:171], a[240:243], a[168:171], v[168:171]\n"
    "ds_read_b64_tr_b16 a[226:227], v16 offset:20480\n"
    "v_accvgpr_write_b32 a106, 0\n"
    "v_mfma_f32_16x16x32_bf16 v[168:171], a[244:247], a[172:175], v[168:171]\n"
    "v_accvgpr_write_b32 a107, 0\n"
    "v_mfma_f32_16x16x32_bf16 v[200:203], a[240:243], a[176:179], v[200:203]\n"
    "ds_read_b64_tr_b16 a[228:229], v16 offset:24576\n"
    "v_mfma_f32_16x16x32_bf16 v[200:203], a[244:247], a[180:183], v[200:203]\n"
    "v_accvgpr_write_b32 a108, 0\n"
    "v_mfma_f32_16x16x32_bf16 v[232:235], a[240:243], a[184:187], v[232:235]\n"
    "ds_read_b64_tr_b16 a[230:231], v16 offset:28672\n"
    "v_add_u32 v16, s76, v16\n"
    "v_mfma_f32_16x16x32_bf16 v[232:235], a[244:247], a[188:191], v[232:235]\n"
    "s_waitcnt lgkmcnt(4)\n"
    "v_accvgpr_write_b32 a109, 0\n"
    "v_mfma_f32_16x16x32_bf16 v[140:143], a[248:251], a[160:163], v[140:143]\n"
    "ds_read_b64_tr_b16 a[232:233], v17 offset:16384\n"
    "v_accvgpr_write_b32 a110, 0\n"
    "v_mfma_f32_16x16x32_bf16 v[140:143], a[252:255], a[164:167], v[140:143]\n"
    "buffer_load_dwordx4 a[140:143], v5, s[44:47], s72 offen offset:1024 sc1\n"
    "v_mfma_f32_16x16x32_bf16 v[172:175], a[248:251], a[168:171], v[172:175]\n"
    "ds_read_b64_tr_b16 a[234:235], v17 offset:20480\n"
    "v_mfma_f32_16x16x32_bf16 v[172:175], a[252:255], a[172:175], v[172:175]\n"
    "s_lshl_b32 s82, s80, 5\n"
    "s_lshl_b32 s87, s78, 3\n"
    "v_mov_b32 v30, s94\n"
    "v_mov_b32 v31, s95\n"
    "v_mfma_f32_16x16x32_bf16 v[204:207], a[248:251], a[176:179], v[204:207]\n"
    "s_lshl_b32 s81, s79, 8\n"
    "s_add_u32 s82, s82, s87\n"
    "s_add_u32 s82, s82, s81\n"
    "ds_read_b64_tr_b16 a[236:237], v17 offset:24576\n"
    "v_mfma_f32_16x16x32_bf16 v[204:207], a[252:255], a[180:183], v[204:207]\n"
    "s_mov_b64 exec, 1\n buffer_store_dwordx2 v[30:31], off, s[64:67], s82 offset:0 sc1\n s_mov_b64 exec, -1\n"
    "v_mfma_f32_16x16x32_bf16 v[236:239], a[248:251], a[184:187], v[236:239]\n"
    "ds_read_b64_tr_b16 a[238:239], v17 offset:28672\n"
    "v_and_b32 v22, 31, v1\n"
    "v_lshlrev_b32 v22, 3, v22\n"
    "v_mfma_f32_16x16x32_bf16 v[236:239], a[252:255], a[188:191], v[236:239]\n"
    "s_waitcnt lgkmcnt(4)\n"
    "v_add_u32 v17, s76, v17\n"
    "v_accvgpr_write_b32 a111, 0\n"
    "v_mfma_f32_16x16x32_bf16 v[144:147], a[224:227], a[160:163], v[144:147]\n"
    "ds_read_b64_tr_b16 a[240:241], v18 offset:16384\n"
    "v_accvgpr_write_b32 a112, 0\n"
    "v_mfma_f32_16x16x32_bf16 v[144:147], a[228:231], a[164:167], v[144:147]\n"
    "buffer_load_dwordx4 a[144:147], v6, s[44:47], s72 offen sc1\n"
    "v_mfma_f32_16x16x32_bf16 v[176:179], a[224:227], a[168:171], v[176:179]\n"
    "ds_read_b64_tr_b16 a[242:243], v18 offset:20480\n"
    "v_accvgpr_write_b32 a113, 0\n"
    "v_mfma_f32_16x16x32_bf16 v[176:179], a[228:231], a[172:175], v[176:179]\n"
    "v_accvgpr_write_b32 a114, 0\n"
    "v_accvgpr_write_b32 a115, 0\n"
    "v_mfma_f32_16x16x32_bf16 v[208:211], a[224:227], a[176:179], v[208:211]\n"
    "ds_read_b64_tr_b16 a[244:245], v18 offset:24576\n"
    "v_mfma_f32_16x16x32_bf16 v[208:211], a[228:231], a[180:183], v[208:211]\n"
    "v_accvgpr_write_b32 a116, 0\n"
    "v_mfma_f32_16x16x32_bf16 v[240:243], a[224:227], a[184:187], v[240:243]\n"
    "ds_read_b64_tr_b16 a[246:247], v18 offset:28672\n"
    "v_add_u32 v18, s76, v18\n"
    "v_mfma_f32_16x16x32_bf16 v[240:243], a[228:231], a[188:191], v[240:243]\n"
    "s_waitcnt lgkmcnt(4)\n"
    "v_accvgpr_write_b32 a117, 0\n"
    "v_mfma_f32_16x16x32_bf16 v[148:151], a[232:235], a[160:163], v[148:151]\n"
    "ds_read_b64_tr_b16 a[248:249], v19 offset:16384\n"
    "v_accvgpr_write_b32 a118, 0\n"
    "v_mfma_f32_16x16x32_bf16 v[148:151], a[236:239], a[164:167], v[148:151]\n"
    "buffer_load_dwordx4 a[148:151], v6, s[44:47], s72 offen offset:1024 sc1\n"
    "v_mfma_f32_16x16x32_bf16 v[180:183], a[232:235], a[168:171], v[180:183]\n"
    "ds_read_b64_tr_b16 a[250:251], v19 offset:20480\n"
    "v_accvgpr_write_b32 a119, 0\n"
    "v_mfma_f32_16x16x32_bf16 v[180:183], a[236:239], a[172:175], v[180:183]\n"
    "v_mfma_f32_16x16x32_bf16 v[212:215], a[232:235], a[176:179], v[212:215]\n"
    "ds_read_b64_tr_b16 a[252:253], v19 offset:24576\n"
    "v_accvgpr_write_b32 a120, 0\n"
    "v_mfma_f32_16x16x32_bf16 v[212:215], a[236:239], a[180:183], v[212:215]\n"
    "v_mfma_f32_16x16x32_bf16 v[244:247], a[232:235], a[184:187], v[244:247]\n"
    "ds_read_b64_tr_b16 a[254:255], v19 offset:28672\n"
    "v_add_u32 v19, s76, v19\n"
    "v_mfma_f32_16x16x32_bf16 v[244:247], a[236:239], a[188:191], v[244:247]\n"
    "s_waitcnt lgkmcnt(4)\n"
    "s_barrier\n"
    "v_accvgpr_write_b32 a121, 0\n"
    "v_mfma_f32_16x16x32_bf16 v[152:155], a[240:243], a[160:163], v[152:155]\n"
    "ds_read_b64_tr_b16 a[224:225], v12\n"
    "v_accvgpr_write_b32 a122, 0\n"
    "v_mfma_f32_16x16x32_bf16 v[152:155], a[244:247], a[164:167], v[152:155]\n"
    "buffer_load_dwordx4 a[152:155], v7, s[44:47], s72 offen sc1\n"
    "v_mfma_f32_16x16x32_bf16 v[184:187], a[240:243], a[168:171], v[184:187]\n"
    "ds_read_b64_tr_b16 a[226:227], v12 offset:4096\n"
    "v_accvgpr_write_b32 a123, 0\n"
    "v_mfma_f32_16x16x32_bf16 v[184:187], a[244:247], a[172:175], v[184:187]\n"
    "v_mfma_f32_16x16x32_bf16 v[216:219], a[240:243], a[176:179], v[216:219]\n"
    "ds_read_b64_tr_b16 a[228:229], v12 offset:8192\n"
    "v_accvgpr_write_b32 a124, 0\n"
    "v_mfma_f32_16x16x32_bf16 v[216:219], a[244:247], a[180:183], v[216:219]\n"
    "v_mfma_f32_16x16x32_bf16 v[248:251], a[240:243], a[184:187], v[248:251]\n"
    "ds_read_b64_tr_b16 a[230:231], v12 offset:12288\n"
    "v_accvgpr_write_b32 a125, 0\n"
    "v_mfma_f32_16x16x32_bf16 v[248:251], a[244:247], a[188:191], v[248:251]\n"
    "s_waitcnt lgkmcnt(4)\n"
    "v_mfma_f32_16x16x32_bf16 v[156:159], a[248:251], a[160:163], v[156:159]\n"
    "ds_read_b64_tr_b16 a[232:233], v13\n"
    "v_accvgpr_write_b32 a126, 0\n"
    "v_mfma_f32_16x16x32_bf16 v[156:159], a[252:255], a[164:167], v[156:159]\n"
    "buffer_load_dwordx4 a[156:159], v7, s[44:47], s72 offen offset:1024 sc1\n"
    "v_mfma_f32_16x16x32_bf16 v[188:191], a[248:251], a[168:171], v[188:191]\n"
    "s_add_u32 s72, s72, 0x40000\n"
    "ds_read_b64_tr_b16 a[234:235], v13 offset:4096\n"
    "v_mfma_f32_16x16x32_bf16 v[188:191], a[252:255], a[172:175], v[188:191]\n"
    "v_accvgpr_write_b32 a127, 0\n"
    "v_mfma_f32_16x16x32_bf16 v[220:223], a[248:251], a[176:179], v[220:223]\n"
    "ds_read_b64_tr_b16 a[236:237], v13 offset:8192\n"
    "v_mfma_f32_16x16x32_bf16 v[220:223], a[252:255], a[180:183], v[220:223]\n"
    "v_mfma_f32_16x16x32_bf16 v[252:255], a[248:251], a[184:187], v[252:255]\n"
    "ds_read_b64_tr_b16 a[238:239], v13 offset:12288\n"
    "v_mfma_f32_16x16x32_bf16 v[252:255], a[252:255], a[188:191], v[252:255]\n"
    "s_waitcnt vmcnt(9) lgkmcnt(4)\n"
    "v_mfma_f32_16x16x32_bf16 v[128:131], a[224:227], a[192:195], v[128:131]\n"
    "ds_read_b64_tr_b16 a[240:241], v14\n"
    "v_mfma_f32_16x16x32_bf16 v[128:131], a[228:231], a[196:199], v[128:131]\n"
    "buffer_load_dwordx4 a[160:163], v4, s[44:47], s72 offen sc1\n"
    "v_mfma_f32_16x16x32_bf16 v[160:163], a[224:227], a[200:203], v[160:163]\n"
    "ds_read_b64_tr_b16 a[242:243], v14 offset:4096\n"
    "v_mfma_f32_16x16x32_bf16 v[160:163], a[228:231], a[204:207], v[160:163]\n"
    "v_mfma_f32_16x16x32_bf16 v[192:195], a[224:227], a[208:211], v[192:195]\n"
    "ds_read_b64_tr_b16 a[244:245], v14 offset:8192\n"
    "v_mfma_f32_16x16x32_bf16 v[192:195], a[228:231], a[212:215], v[192:195]\n"
    "v_mfma_f32_16x16x32_bf16 v[224:227], a[224:227], a[216:219], v[224:227]\n"
    "ds_read_b64_tr_b16 a[246:247], v14 offset:12288\n"
    "v_mfma_f32_16x16x32_bf16 v[224:227], a[228:231], a[220:223], v[224:227]\n"
    "s_waitcnt lgkmcnt(4)\n"
    "v_mfma_f32_16x16x32_bf16 v[132:135], a[232:235], a[192:195], v[132:135]\n"
    "s_mov_b64 s[48:49], s[16:17]\n"
    "ds_read_b64_tr_b16 a[248:249], v15\n"
    "v_mfma_f32_16x16x32_bf16 v[132:135], a[236:239], a[196:199], v[132:135]\n"
    "buffer_load_dwordx4 a[164:167], v4, s[44:47], s72 offen offset:1024 sc1\n"
    "v_mfma_f32_16x16x32_bf16 v[164:167], a[232:235], a[200:203], v[164:167]\n"
    "ds_read_b64_tr_b16 a[250:251], v15 offset:4096\n"
    "v_mfma_f32_16x16x32_bf16 v[164:167], a[236:239], a[204:207], v[164:167]\n"
    "v_mfma_f32_16x16x32_bf16 v[196:199], a[232:235], a[208:211], v[196:199]\n"
    "ds_read_b64_tr_b16 a[252:253], v15 offset:8192\n"
    "v_mfma_f32_16x16x32_bf16 v[196:199], a[236:239], a[212:215], v[196:199]\n"
    "s_cmp_eq_u32 s78, 2\n"
    "s_cselect_b64 s[48:49], s[20:21], s[48:49]\n"
    "v_mfma_f32_16x16x32_bf16 v[228:231], a[232:235], a[216:219], v[228:231]\n"
    "ds_read_b64_tr_b16 a[254:255], v15 offset:12288\n"
    "v_mfma_f32_16x16x32_bf16 v[228:231], a[236:239], a[220:223], v[228:231]\n"
    "s_waitcnt lgkmcnt(4)\n"
    "v_mfma_f32_16x16x32_bf16 v[136:139], a[240:243], a[192:195], v[136:139]\n"
    "ds_read_b64_tr_b16 a[224:225], v16\n"
    "v_mfma_f32_16x16x32_bf16 v[136:139], a[244:247], a[196:199], v[136:139]\n"
    "buffer_load_dwordx4 a[168:171], v5, s[44:47], s72 offen sc1\n"
    "v_mfma_f32_16x16x32_bf16 v[168:171], a[240:243], a[200:203], v[168:171]\n"
    "ds_read_b64_tr_b16 a[226:227], v16 offset:4096\n"
    "v_mfma_f32_16x16x32_bf16 v[168:171], a[244:247], a[204:207], v[168:171]\n"
    "s_cmp_eq_u32 s78, 3\n"
    "s_cselect_b64 s[48:49], s[18:19], s[48:49]\n"
    "v_mfma_f32_16x16x32_bf16 v[200:203], a[240:243], a[208:211], v[200:203]\n"
    "s_mov_b32 s52, 0x38000\n"
    "ds_read_b64_tr_b16 a[228:229], v16 offset:8192\n"
    "v_mfma_f32_16x16x32_bf16 v[200:203], a[244:247], a[212:215], v[200:203]\n"
    "v_mfma_f32_16x16x32_bf16 v[232:235], a[240:243], a[216:219], v[232:235]\n"
    "ds_read_b64_tr_b16 a[230:231], v16 offset:12288\n"
    "v_mfma_f32_16x16x32_bf16 v[232:235], a[244:247], a[220:223], v[232:235]\n"
    "s_waitcnt lgkmcnt(4)\n"
    "v_mfma_f32_16x16x32_bf16 v[140:143], a[248:251], a[192:195], v[140:143]\n"
    "s_lshl_b32 s53, s79, 1\n"
    "ds_read_b64_tr_b16 a[232:233], v17\n"
    "v_mfma_f32_16x16x32_bf16 v[140:143], a[252:255], a[196:199], v[140:143]\n"
    "buffer_load_dwordx4 a[172:175], v5, s[44:47], s72 offen offset:1024 sc1\n"
    "v_mfma_f32_16x16x32_bf16 v[172:175], a[248:251], a[200:203], v[172:175]\n"
    "ds_read_b64_tr_b16 a[234:235], v17 offset:4096\n"
    "v_mfma_f32_16x16x32_bf16 v[172:175], a[252:255], a[204:207], v[172:175]\n"
    "v_mfma_f32_16x16x32_bf16 v[204:207], a[248:251], a[208:211], v[204:207]\n"
    "s_add_u32 s53, s53, s78\n"
    "ds_read_b64_tr_b16 a[236:237], v17 offset:8192\n"
    "v_mfma_f32_16x16x32_bf16 v[204:207], a[252:255], a[212:215], v[204:207]\n"
    "v_mfma_f32_16x16x32_bf16 v[236:239], a[248:251], a[216:219], v[236:239]\n"
    "ds_read_b64_tr_b16 a[238:239], v17 offset:12288\n"
    "v_mfma_f32_16x16x32_bf16 v[236:239], a[252:255], a[220:223], v[236:239]\n"
    "s_waitcnt lgkmcnt(4)\n"
    "v_mfma_f32_16x16x32_bf16 v[144:147], a[224:227], a[192:195], v[144:147]\n"
    "ds_read_b64_tr_b16 a[240:241], v18\n"
    "v_mfma_f32_16x16x32_bf16 v[144:147], a[228:231], a[196:199], v[144:147]\n"
    "buffer_load_dwordx4 a[176:179], v6, s[44:47], s72 offen sc1\n"
    "v_mfma_f32_16x16x32_bf16 v[176:179], a[224:227], a[200:203], v[176:179]\n"
    "ds_read_b64_tr_b16 a[242:243], v18 offset:4096\n"
    "v_mfma_f32_16x16x32_bf16 v[176:179], a[228:231], a[204:207], v[176:179]\n"
    "s_cmp_lt_u32 s78, 2\n"
    "s_cselect_b32 s50, 0x20000, s52\n"
    "s_cselect_b32 s53, s53, s79\n"
    "s_cselect_b32 s40, 15, 25\n"
    "v_mfma_f32_16x16x32_bf16 v[208:211], a[224:227], a[208:211], v[208:211]\n"
    "s_mul_i32 s53, s53, s50\n"
    "ds_read_b64_tr_b16 a[244:245], v18 offset:8192\n"
    "v_mfma_f32_16x16x32_bf16 v[208:211], a[228:231], a[212:215], v[208:211]\n"
    "v_mfma_f32_16x16x32_bf16 v[240:243], a[224:227], a[216:219], v[240:243]\n"
    "ds_read_b64_tr_b16 a[246:247], v18 offset:12288\n"
    "v_mfma_f32_16x16x32_bf16 v[240:243], a[228:231], a[220:223], v[240:243]\n"
    "s_waitcnt lgkmcnt(4)\n"
    "v_mfma_f32_16x16x32_bf16 v[148:151], a[232:235], a[192:195], v[148:151]\n"
    "ds_read_b64_tr_b16 a[248:249], v19\n"
    "v_mfma_f32_16x16x32_bf16 v[148:151], a[236:239], a[196:199], v[148:151]\n"
    "buffer_load_dwordx4 a[180:183], v6, s[44:47], s72 offen offset:1024 sc1\n"
    "v_mfma_f32_16x16x32_bf16 v[180:183], a[232:235], a[200:203], v[180:183]\n"
    "ds_read_b64_tr_b16 a[250:251], v19 offset:4096\n"
    "v_mfma_f32_16x16x32_bf16 v[180:183], a[236:239], a[204:207], v[180:183]\n"
    "s_add_u32 s48, s48, s53\n"
    "s_addc_u32 s49, s49, 0\n"
    "v_mfma_f32_16x16x32_bf16 v[212:215], a[232:235], a[208:211], v[212:215]\n"
    "s_mov_b32 s51, 0x20000\n"
    "ds_read_b64_tr_b16 a[252:253], v19 offset:8192\n"
    "v_mfma_f32_16x16x32_bf16 v[212:215], a[236:239], a[212:215], v[212:215]\n"
    "v_mfma_f32_16x16x32_bf16 v[244:247], a[232:235], a[216:219], v[244:247]\n"
    "ds_read_b64_tr_b16 a[254:255], v19 offset:12288\n"
    "v_mfma_f32_16x16x32_bf16 v[244:247], a[236:239], a[220:223], v[244:247]\n"
    "s_waitcnt lgkmcnt(4)\n"
    "v_mfma_f32_16x16x32_bf16 v[152:155], a[240:243], a[192:195], v[152:155]\n"
    "s_mov_b32 s52, 0x130000\n"
    "ds_read_b64_tr_b16 a[224:225], v12 offset:16384\n"
    "v_mfma_f32_16x16x32_bf16 v[152:155], a[244:247], a[196:199], v[152:155]\n"
    "buffer_load_dwordx4 a[184:187], v7, s[44:47], s72 offen sc1\n"
    "v_mfma_f32_16x16x32_bf16 v[184:187], a[240:243], a[200:203], v[184:187]\n"
    "s_add_u32 s75, s75, 1\n"
    "ds_read_b64_tr_b16 a[226:227], v12 offset:20480\n"
    "v_mfma_f32_16x16x32_bf16 v[184:187], a[244:247], a[204:207], v[184:187]\n"
    "v_mfma_f32_16x16x32_bf16 v[216:219], a[240:243], a[208:211], v[216:219]\n"
    "s_mov_b32 s53, 0xaa0047\n"
    "ds_read_b64_tr_b16 a[228:229], v12 offset:24576\n"
    "v_mfma_f32_16x16x32_bf16 v[216:219], a[244:247], a[212:215], v[216:219]\n"
    "s_cmp_lt_u32 s75, 5\n"
    "s_cselect_b32 s75, s75, 0\n"
    "s_cselect_b32 s76, 0x8000, s77\n"
    "v_mfma_f32_16x16x32_bf16 v[248:251], a[240:243], a[216:219], v[248:251]\n"
    "ds_read_b64_tr_b16 a[230:231], v12 offset:28672\n"
    "v_add_u32 v12, s76, v12\n"
    "v_mfma_f32_16x16x32_bf16 v[248:251], a[244:247], a[220:223], v[248:251]\n"
    "s_waitcnt lgkmcnt(4)\n"
    "v_mfma_f32_16x16x32_bf16 v[156:159], a[248:251], a[192:195], v[156:159]\n"
    "s_mov_b32 s54, 0x235014a\n"
    "ds_read_b64_tr_b16 a[232:233], v13 offset:16384\n"
    "v_mfma_f32_16x16x32_bf16 v[156:159], a[252:255], a[196:199], v[156:159]\n"
    "buffer_load_dwordx4 a[188:191], v7, s[44:47], s72 offen offset:1024 sc1\n"
    "v_mfma_f32_16x16x32_bf16 v[188:191], a[248:251], a[200:203], v[188:191]\n"
    "s_add_u32 s72, s72, 0x40000\n"
    "ds_read_b64_tr_b16 a[234:235], v13 offset:20480\n"
    "v_mfma_f32_16x16x32_bf16 v[188:191], a[252:255], a[204:207], v[188:191]\n"
    "v_mfma_f32_16x16x32_bf16 v[220:223], a[248:251], a[208:211], v[220:223]\n"
    "s_mov_b32 s55, 0x5240379\n"
    "ds_read_b64_tr_b16 a[236:237], v13 offset:24576\n"
    "v_mfma_f32_16x16x32_bf16 v[220:223], a[252:255], a[212:215], v[220:223]\n"
    "v_mfma_f32_16x16x32_bf16 v[252:255], a[248:251], a[216:219], v[252:255]\n"
    "ds_read_b64_tr_b16 a[238:239], v13 offset:28672\n"
    "v_add_u32 v13, s76, v13\n"
    "v_mfma_f32_16x16x32_bf16 v[252:255], a[252:255], a[220:223], v[252:255]\n"
    "s_waitcnt vmcnt(8) lgkmcnt(4)\n"
    "v_mfma_f32_16x16x32_bf16 v[128:131], a[224:227], a[128:131], v[128:131]\n"
    "ds_read_b64_tr_b16 a[240:241], v14 offset:16384\n"
    "v_mfma_f32_16x16x32_bf16 v[128:131], a[228:231], a[132:135], v[128:131]\n"
    "buffer_load_dwordx4 a[192:195], v4, s[44:47], s72 offen sc1\n"
    "v_mfma_f32_16x16x32_bf16 v[160:163], a[224:227], a[136:139], v[160:163]\n"
    "ds_read_b64_tr_b16 a[242:243], v14 offset:20480\n"
    "v_mfma_f32_16x16x32_bf16 v[160:163], a[228:231], a[140:143], v[160:163]\n"
    "s_cmp_lt_u32 s78, 2\n"
    "s_cselect_b32 s52, 0xc0000, s52\n"
    "s_cselect_b32 s53, 0x68002c, s53\n"
    "s_cselect_b32 s54, 0x15400c8, s54\n"
    "s_cselect_b32 s55, 0x3100214, s55\n"
    "s_cselect_b32 s42, 20, 32\n"
    "s_cselect_b32 s43, 8, 14\n"
    "v_mfma_f32_16x16x32_bf16 v[192:195], a[224:227], a[144:147], v[192:195]\n"
    "ds_read_b64_tr_b16 a[244:245], v14 offset:24576\n"
    "v_mfma_f32_16x16x32_bf16 v[192:195], a[228:231], a[148:151], v[192:195]\n"
    "s_cmp_lt_u32 s80, 4\n"
    "s_cselect_b64 s[52:53], s[52:53], s[54:55]\n"
    "v_mfma_f32_16x16x32_bf16 v[224:227], a[224:227], a[152:155], v[224:227]\n"
    "ds_read_b64_tr_b16 a[246:247], v14 offset:28672\n"
    "v_add_u32 v14, s76, v14\n"
    "v_mfma_f32_16x16x32_bf16 v[224:227], a[228:231], a[156:159], v[224:227]\n"
    "s_waitcnt lgkmcnt(4)\n"
    "v_mfma_f32_16x16x32_bf16 v[132:135], a[232:235], a[128:131], v[132:135]\n"
    "s_and_b32 s73, s80, 3\n"
    "ds_read_b64_tr_b16 a[248:249], v15 offset:16384\n"
    "v_mfma_f32_16x16x32_bf16 v[132:135], a[236:239], a[132:135], v[132:135]\n"
    "buffer_load_dwordx4 a[196:199], v4, s[44:47], s72 offen offset:1024 sc1\n"
    "v_mfma_f32_16x16x32_bf16 v[164:167], a[232:235], a[136:139], v[164:167]\n"
    "ds_read_b64_tr_b16 a[250:251], v15 offset:20480\n"
    "v_mfma_f32_16x16x32_bf16 v[164:167], a[236:239], a[140:143], v[164:167]\n"
    "v_mfma_f32_16x16x32_bf16 v[196:199], a[232:235], a[144:147], v[196:199]\n"
    "s_lshl_b32 s73, s73, 4\n"
    "ds_read_b64_tr_b16 a[252:253], v15 offset:24576\n"
    "v_mfma_f32_16x16x32_bf16 v[196:199], a[236:239], a[148:151], v[196:199]\n"
    "v_mfma_f32_16x16x32_bf16 v[228:231], a[232:235], a[152:155], v[228:231]\n"
    "ds_read_b64_tr_b16 a[254:255], v15 offset:28672\n"
    "v_add_u32 v15, s76, v15\n"
    "v_mfma_f32_16x16x32_bf16 v[228:231], a[236:239], a[156:159], v[228:231]\n"
    "s_waitcnt lgkmcnt(4)\n"
    "v_mfma_f32_16x16x32_bf16 v[136:139], a[240:243], a[128:131], v[136:139]\n"
    "s_lshr_b64 s[52:53], s[52:53], s73\n"
    "ds_read_b64_tr_b16 a[224:225], v16 offset:16384\n"
    "v_mfma_f32_16x16x32_bf16 v[136:139], a[244:247], a[132:135], v[136:139]\n"
    "buffer_load_dwordx4 a[200:203], v5, s[44:47], s72 offen sc1\n"
    "v_mfma_f32_16x16x32_bf16 v[168:171], a[240:243], a[136:139], v[168:171]\n"
    "ds_read_b64_tr_b16 a[226:227], v16 offset:20480\n"
    "v_mfma_f32_16x16x32_bf16 v[168:171], a[244:247], a[140:143], v[168:171]\n"
    "v_mfma_f32_16x16x32_bf16 v[200:203], a[240:243], a[144:147], v[200:203]\n"
    "s_and_b32 s73, s52, 0xffff\n"
    "ds_read_b64_tr_b16 a[228:229], v16 offset:24576\n"
    "v_mfma_f32_16x16x32_bf16 v[200:203], a[244:247], a[148:151], v[200:203]\n"
    "v_mfma_f32_16x16x32_bf16 v[232:235], a[240:243], a[152:155], v[232:235]\n"
    "ds_read_b64_tr_b16 a[230:231], v16 offset:28672\n"
    "v_add_u32 v16, s76, v16\n"
    "v_mfma_f32_16x16x32_bf16 v[232:235], a[244:247], a[156:159], v[232:235]\n"
    "s_waitcnt lgkmcnt(4)\n"
    "v_mfma_f32_16x16x32_bf16 v[140:143], a[248:251], a[128:131], v[140:143]\n"
    "s_lshl_b32 s73, s73, 7\n"
    "ds_read_b64_tr_b16 a[232:233], v17 offset:16384\n"
    "v_mfma_f32_16x16x32_bf16 v[140:143], a[252:255], a[132:135], v[140:143]\n"
    "buffer_load_dwordx4 a[204:207], v5, s[44:47], s72 offen offset:1024 sc1\n"
    "v_mfma_f32_16x16x32_bf16 v[172:175], a[248:251], a[136:139], v[172:175]\n"
    "ds_read_b64_tr_b16 a[234:235], v17 offset:20480\n"
    "v_mfma_f32_16x16x32_bf16 v[172:175], a[252:255], a[140:143], v[172:175]\n"
    "v_mfma_f32_16x16x32_bf16 v[204:207], a[248:251], a[144:147], v[204:207]\n"
    "s_lshr_b32 s40, s42, 2\n"
    "ds_read_b64_tr_b16 a[236:237], v17 offset:24576\n"
    "v_mfma_f32_16x16x32_bf16 v[204:207], a[252:255], a[148:151], v[204:207]\n"
    "v_mfma_f32_16x16x32_bf16 v[236:239], a[248:251], a[152:155], v[236:239]\n"
    "ds_read_b64_tr_b16 a[238:239], v17 offset:28672\n"
    "v_add_u32 v17, s76, v17\n"
    "v_mfma_f32_16x16x32_bf16 v[236:239], a[252:255], a[156:159], v[236:239]\n"
    "s_waitcnt lgkmcnt(4)\n"
    "v_mfma_f32_16x16x32_bf16 v[144:147], a[224:227], a[128:131], v[144:147]\n"
    "ds_read_b64_tr_b16 a[240:241], v18 offset:16384\n"
    "v_lshlrev_b32 v2, 7, v1\n"
    "v_mfma_f32_16x16x32_bf16 v[144:147], a[228:231], a[132:135], v[144:147]\n"
    "buffer_load_dwordx4 a[208:211], v6, s[44:47], s72 offen sc1\n"
    "v_mfma_f32_16x16x32_bf16 v[176:179], a[224:227], a[136:139], v[176:179]\n"
    "ds_read_b64_tr_b16 a[242:243], v18 offset:20480\n"
    "v_mfma_f32_16x16x32_bf16 v[176:179], a[228:231], a[140:143], v[176:179]\n"
    "v_mfma_f32_16x16x32_bf16 v[208:211], a[224:227], a[144:147], v[208:211]\n"
    "buffer_load_dwordx2 v[30:31], v22, s[64:67], s81 offen sc1\n"
    "ds_read_b64_tr_b16 a[244:245], v18 offset:24576\n"
    "v_mfma_f32_16x16x32_bf16 v[208:211], a[228:231], a[148:151], v[208:211]\n"
    "v_mfma_f32_16x16x32_bf16 v[240:243], a[224:227], a[152:155], v[240:243]\n"
    "ds_read_b64_tr_b16 a[246:247], v18 offset:28672\n"
    "v_add_u32 v18, s76, v18\n"
    "v_mfma_f32_16x16x32_bf16 v[240:243], a[228:231], a[156:159], v[240:243]\n"
    "s_waitcnt lgkmcnt(4)\n"
    "v_mfma_f32_16x16x32_bf16 v[148:151], a[232:235], a[128:131], v[148:151]\n"
    "ds_read_b64_tr_b16 a[248:249], v19 offset:16384\n"
    "v_mfma_f32_16x16x32_bf16 v[148:151], a[236:239], a[132:135], v[148:151]\n"
    "buffer_load_dwordx4 a[212:215], v6, s[44:47], s72 offen offset:1024 sc1\n"
    "v_mfma_f32_16x16x32_bf16 v[180:183], a[232:235], a[136:139], v[180:183]\n"
    "ds_read_b64_tr_b16 a[250:251], v19 offset:20480\n"
    "v_mfma_f32_16x16x32_bf16 v[180:183], a[236:239], a[140:143], v[180:183]\n"
    "v_mfma_f32_16x16x32_bf16 v[212:215], a[232:235], a[144:147], v[212:215]\n"
    "ds_read_b64_tr_b16 a[252:253], v19 offset:24576\n"
    "v_mfma_f32_16x16x32_bf16 v[212:215], a[236:239], a[148:151], v[212:215]\n"
    "v_mfma_f32_16x16x32_bf16 v[244:247], a[232:235], a[152:155], v[244:247]\n"
    "ds_read_b64_tr_b16 a[254:255], v19 offset:28672\n"
    "v_add_u32 v19, s76, v19\n"
    "v_mfma_f32_16x16x32_bf16 v[244:247], a[236:239], a[156:159], v[244:247]\n"
    "s_waitcnt lgkmcnt(4)\n"
    "s_barrier\n"
    "v_mfma_f32_16x16x32_bf16 v[152:155], a[240:243], a[128:131], v[152:155]\n"
    "ds_read_b64_tr_b16 a[224:225], v12\n"
    "v_mfma_f32_16x16x32_bf16 v[152:155], a[244:247], a[132:135], v[152:155]\n"
    "buffer_load_dwordx4 a[216:219], v7, s[44:47], s72 offen sc1\n"
    "v_mfma_f32_16x16x32_bf16 v[184:187], a[240:243], a[136:139], v[184:187]\n"
    "ds_read_b64_tr_b16 a[226:227], v12 offset:4096\n"
    "v_mfma_f32_16x16x32_bf16 v[184:187], a[244:247], a[140:143], v[184:187]\n"
    "v_mfma_f32_16x16x32_bf16 v[216:219], a[240:243], a[144:147], v[216:219]\n"
    "ds_read_b64_tr_b16 a[228:229], v12 offset:8192\n"
    "v_mfma_f32_16x16x32_bf16 v[216:219], a[244:247], a[148:151], v[216:219]\n"
    "v_mfma_f32_16x16x32_bf16 v[248:251], a[240:243], a[152:155], v[248:251]\n"
    "ds_read_b64_tr_b16 a[230:231], v12 offset:12288\n"
    "v_mfma_f32_16x16x32_bf16 v[248:251], a[244:247], a[156:159], v[248:251]\n"
    "s_waitcnt lgkmcnt(4)\n"
    "v_mfma_f32_16x16x32_bf16 v[156:159], a[248:251], a[128:131], v[156:159]\n"
    "ds_read_b64_tr_b16 a[232:233], v13\n"
    "v_mfma_f32_16x16x32_bf16 v[156:159], a[252:255], a[132:135], v[156:159]\n"
    "buffer_load_dwordx4 a[220:223], v7, s[44:47], s72 offen offset:1024 sc1\n"
    "v_mfma_f32_16x16x32_bf16 v[188:191], a[248:251], a[136:139], v[188:191]\n"
    "s_add_u32 s72, s72, 0x40000\n"
    "ds_read_b64_tr_b16 a[234:235], v13 offset:4096\n"
    "v_mfma_f32_16x16x32_bf16 v[188:191], a[252:255], a[140:143], v[188:191]\n"
    "v_mfma_f32_16x16x32_bf16 v[220:223], a[248:251], a[144:147], v[220:223]\n"
    "ds_read_b64_tr_b16 a[236:237], v13 offset:8192\n"
    "v_mfma_f32_16x16x32_bf16 v[220:223], a[252:255], a[148:151], v[220:223]\n"
    "v_mfma_f32_16x16x32_bf16 v[252:255], a[248:251], a[152:155], v[252:255]\n"
    "ds_read_b64_tr_b16 a[238:239], v13 offset:12288\n"
    "v_mfma_f32_16x16x32_bf16 v[252:255], a[252:255], a[156:159], v[252:255]\n"
    "s_mov_b32 s87, 1048577\n"
    "s_waitcnt vmcnt(3)\n"
    "v_cmp_ne_u64 vcc, s[94:95], v[30:31]\n"
    "s_sub_u32 s87, s87, 1\n"
    "s_cmp_eq_u64 vcc, 0\n"
    "s_cbranch_scc1 at_go\n"
    "at_spin:\n"
    "buffer_load_dwordx2 v[30:31], v22, s[64:67], s81 offen sc1\n"
    "s_waitcnt vmcnt(0)\n"
    "v_cmp_ne_u64 vcc, s[94:95], v[30:31]\n"
    "s_sub_u32 s87, s87, 1\n"
    "s_cmp_eq_u64 vcc, 0\n"
    "s_cbranch_scc1 at_go\n"
    "s_sleep 4\n"
    "s_cmp_lg_u32 s87, 0\n"
    "s_cbranch_scc1 at_spin\n"
    "at_go:\n"
    "s_mov_b32 s31, s87\n"
    "s_mov_b32 s86, 0\n"
    "s_lshl_b32 s82, s80, 16\n"
    "s_sub_u32 s58, 0x100000, s82\n"
    "s_lshl_b32 s84, s78, 12\n"
    "s_add_u32 s85, s84, 0x4000\n"
    "v_subrev_u32 v31, 0x4000, v21\n"
    "s_waitcnt lgkmcnt(4)\n"
    "v_mfma_f32_16x16x32_bf16 v[128:131], a[224:227], a[160:163], v[128:131]\n"
    "s_add_u32 m0, s74, s84\n"
    "ds_read_b64_tr_b16 a[240:241], v14\n"
    "v_mfma_f32_16x16x32_bf16 v[128:131], a[228:231], a[164:167], v[128:131]\n"
    "buffer_load_dwordx4 v31, s[56:59], s86 offen sc1 lds\n"
    "v_mfma_f32_16x16x32_bf16 v[160:163], a[224:227], a[168:171], v[160:163]\n"
    "ds_read_b64_tr_b16 a[242:243], v14 offset:4096\n"
    "v_and_b32 v26, 15, v1\n"
    "v_mfma_f32_16x16x32_bf16 v[160:163], a[228:231], a[172:175], v[160:163]\n"
    "v_lshrrev_b32 v27, 4, v1\n"
    "v_mfma_f32_16x16x32_bf16 v[192:195], a[224:227], a[176:179], v[192:195]\n"
    "ds_read_b64_tr_b16 a[244:245], v14 offset:8192\n"
    "v_lshlrev_b32 v27, 2, v27\n"
    "v_mfma_f32_16x16x32_bf16 v[192:195], a[228:231], a[180:183], v[192:195]\n"
    "v_sub_u32 v26, v26, v27\n"
    "v_mfma_f32_16x16x32_bf16 v[224:227], a[224:227], a[184:187], v[224:227]\n"
    "s_lshl_b32 s87, s78, 4\n"
    "ds_read_b64_tr_b16 a[246:247], v14 offset:12288\n"
    "v_mfma_f32_16x16x32_bf16 v[224:227], a[228:231], a[188:191], v[224:227]\n"
    "s_waitcnt lgkmcnt(4)\n"
    "v_add_u32 v27, s87, v26\n"
    "v_mfma_f32_16x16x32_bf16 v[132:135], a[232:235], a[160:163], v[132:135]\n"
    "ds_read_b64_tr_b16 a[248:249], v15\n"
    "v_subrev_u32 v28, s87, v26\n"
    "v_mfma_f32_16x16x32_bf16 v[132:135], a[236:239], a[164:167], v[132:135]\n"
    "buffer_load_dwordx4 v31, s[56:59], s86 offen offset:1024 sc1 lds\n"
    "v_mfma_f32_16x16x32_bf16 v[164:167], a[232:235], a[168:171], v[164:167]\n"
    "ds_read_b64_tr_b16 a[250:251], v15 offset:4096\n"
    "v_add_u32 v28, 48, v28\n"
    "v_mfma_f32_16x16x32_bf16 v[164:167], a[236:239], a[172:175], v[164:167]\n"
    "v_mov_b32 v29, 0xf149f2ca\n"
    "v_mfma_f32_16x16x32_bf16 v[196:199], a[232:235], a[176:179], v[196:199]\n"
    "ds_read_b64_tr_b16 a[252:253], v15 offset:8192\n"
    "v_cmp_gt_i32 vcc, 0, v27\n"
    "v_mfma_f32_16x16x32_bf16 v[196:199], a[236:239], a[180:183], v[196:199]\n"
    "buffer_load_dwordx4 v31, s[56:59], s86 offen offset:2048 sc1 lds\n"
    "v_mfma_f32_16x16x32_bf16 v[228:231], a[232:235], a[184:187], v[228:231]\n"
    "ds_read_b64_tr_b16 a[254:255], v15 offset:12288\n"
    "v_cmp_gt_i32 s[92:93], 1, v27\n"
    "v_mfma_f32_16x16x32_bf16 v[228:231], a[236:239], a[188:191], v[228:231]\n"
    "s_waitcnt lgkmcnt(4)\n"
    "v_mov_b32 v48, 0\n"
    "v_mov_b32 v49, 0\n"
    "v_mfma_f32_16x16x32_bf16 v[136:139], a[240:243], a[160:163], v[136:139]\n"
    "ds_read_b64_tr_b16 a[224:225], v16\n"
    "v_mfma_f32_16x16x32_bf16 v[136:139], a[244:247], a[164:167], v[136:139]\n"
    "v_cndmask_b32 v64, 0, v29, vcc\n"
    "v_mfma_f32_16x16x32_bf16 v[168:171], a[240:243], a[168:171], v[168:171]\n"
    "ds_read_b64_tr_b16 a[226:227], v16 offset:4096\n"
    "v_cndmask_b32 v65, 0, v29, s[92:93]\n"
    "v_mfma_f32_16x16x32_bf16 v[168:171], a[244:247], a[172:175], v[168:171]\n"
    "buffer_load_dwordx4 v31, s[56:59], s86 offen offset:3072 sc1 lds\n"
    "v_mfma_f32_16x16x32_bf16 v[200:203], a[240:243], a[176:179], v[200:203]\n"
    "ds_read_b64_tr_b16 a[228:229], v16 offset:8192\n"
    "v_cmp_gt_i32 vcc, 2, v27\n"
    "v_mfma_f32_16x16x32_bf16 v[200:203], a[244:247], a[180:183], v[200:203]\n"
    "v_cmp_gt_i32 s[92:93], 3, v27\n"
    "v_mov_b32 v50, 0\n"
    "v_mfma_f32_16x16x32_bf16 v[232:235], a[240:243], a[184:187], v[232:235]\n"
    "ds_read_b64_tr_b16 a[230:231], v16 offset:12288\n"
    "v_mov_b32 v51, 0\n"
    "v_mfma_f32_16x16x32_bf16 v[232:235], a[244:247], a[188:191], v[232:235]\n"
    "s_waitcnt lgkmcnt(4)\n"
    "v_mfma_f32_16x16x32_bf16 v[140:143], a[248:251], a[160:163], v[140:143]\n"
    "ds_read_b64_tr_b16 a[232:233], v17\n"
    "v_cndmask_b32 v66, 0, v29, vcc\n"
    "v_mfma_f32_16x16x32_bf16 v[140:143], a[252:255], a[164:167], v[140:143]\n"
    "s_mov_b32 s76, s75\n"
    "s_mov_b32 s75, s74\n"
    "s_add_u32 s74, s74, 0x8000\n"
    "s_cmp_lt_u32 s74, 0x28000\n"
    "s_cselect_b32 s74, s74, 0\n"
    "s_add_u32 m0, s74, s84\n"
    "v_mfma_f32_16x16x32_bf16 v[172:175], a[248:251], a[168:171], v[172:175]\n"
    "ds_read_b64_tr_b16 a[234:235], v17 offset:4096\n"
    "v_cndmask_b32 v67, 0, v29, s[92:93]\n"
    "v_mfma_f32_16x16x32_bf16 v[172:175], a[252:255], a[172:175], v[172:175]\n"
    "buffer_load_dwordx4 v20, s[56:59], s86 offen sc1 lds\n"
    "v_mfma_f32_16x16x32_bf16 v[204:207], a[248:251], a[176:179], v[204:207]\n"
    "ds_read_b64_tr_b16 a[236:237], v17 offset:8192\n"
    "v_cmp_gt_i32 vcc, 16, v27\n"
    "v_mfma_f32_16x16x32_bf16 v[204:207], a[252:255], a[180:183], v[204:207]\n"
    "v_cmp_gt_i32 s[92:93], 17, v27\n"
    "v_mov_b32 v52, 0\n"
    "v_mfma_f32_16x16x32_bf16 v[236:239], a[248:251], a[184:187], v[236:239]\n"
    "ds_read_b64_tr_b16 a[238:239], v17 offset:12288\n"
    "v_mov_b32 v53, 0\n"
    "v_mfma_f32_16x16x32_bf16 v[236:239], a[252:255], a[188:191], v[236:239]\n"
    "s_waitcnt lgkmcnt(4)\n"
    "v_cndmask_b32 v68, 0, v29, vcc\n"
    "v_mfma_f32_16x16x32_bf16 v[144:147], a[224:227], a[160:163], v[144:147]\n"
    "ds_read_b64_tr_b16 a[240:241], v18\n"
    "v_cndmask_b32 v69, 0, v29, s[92:93]\n"
    "v_mfma_f32_16x16x32_bf16 v[144:147], a[228:231], a[164:167], v[144:147]\n"
    "buffer_load_dwordx4 v20, s[56:59], s86 offen offset:1024 sc1 lds\n"
    "v_mfma_f32_16x16x32_bf16 v[176:179], a[224:227], a[168:171], v[176:179]\n"
    "ds_read_b64_tr_b16 a[242:243], v18 offset:4096\n"
    "v_cmp_gt_i32 vcc, 18, v27\n"
    "v_mfma_f32_16x16x32_bf16 v[176:179], a[228:231], a[172:175], v[176:179]\n"
    "v_cmp_gt_i32 s[92:93], 19, v27\n"
    "v_mov_b32 v54, 0\n"
    "v_mfma_f32_16x16x32_bf16 v[208:211], a[224:227], a[176:179], v[208:211]\n"
    "ds_read_b64_tr_b16 a[244:245], v18 offset:8192\n"
    "v_mov_b32 v55, 0\n"
    "v_mfma_f32_16x16x32_bf16 v[208:211], a[228:231], a[180:183], v[208:211]\n"
    "v_mfma_f32_16x16x32_bf16 v[240:243], a[224:227], a[184:187], v[240:243]\n"
    "ds_read_b64_tr_b16 a[246:247], v18 offset:12288\n"
    "v_cndmask_b32 v70, 0, v29, vcc\n"
    "v_mfma_f32_16x16x32_bf16 v[240:243], a[228:231], a[188:191], v[240:243]\n"
    "s_waitcnt lgkmcnt(4)\n"
    "v_cndmask_b32 v71, 0, v29, s[92:93]\n"
    "v_mfma_f32_16x16x32_bf16 v[148:151], a[232:235], a[160:163], v[148:151]\n"
    "ds_read_b64_tr_b16 a[248:249], v19\n"
    "v_cmp_gt_i32 vcc, 32, v27\n"
    "v_mfma_f32_16x16x32_bf16 v[148:151], a[236:239], a[164:167], v[148:151]\n"
    "buffer_load_dwordx4 v20, s[56:59], s86 offen offset:2048 sc1 lds\n"
    "v_mfma_f32_16x16x32_bf16 v[180:183], a[232:235], a[168:171], v[180:183]\n"
    "ds_read_b64_tr_b16 a[250:251], v19 offset:4096\n"
    "v_cmp_gt_i32 s[92:93], 33, v27\n"
    "v_mfma_f32_16x16x32_bf16 v[180:183], a[236:239], a[172:175], v[180:183]\n"
    "v_mov_b32 v56, 0\n"
    "v_mov_b32 v57, 0\n"
    "v_mfma_f32_16x16x32_bf16 v[212:215], a[232:235], a[176:179], v[212:215]\n"
    "ds_read_b64_tr_b16 a[252:253], v19 offset:8192\n"
    "v_cndmask_b32 v72, 0, v29, vcc\n"
    "v_mfma_f32_16x16x32_bf16 v[212:215], a[236:239], a[180:183], v[212:215]\n"
    "buffer_load_dwordx4 v20, s[56:59], s86 offen offset:3072 sc1 lds\n"
    "v_mfma_f32_16x16x32_bf16 v[244:247], a[232:235], a[184:187], v[244:247]\n"
    "ds_read_b64_tr_b16 a[254:255], v19 offset:12288\n"
    "v_cndmask_b32 v73, 0, v29, s[92:93]\n"
    "v_mfma_f32_16x16x32_bf16 v[244:247], a[236:239], a[188:191], v[244:247]\n"
    "s_waitcnt lgkmcnt(4)\n"
    "v_cmp_gt_i32 vcc, 34, v27\n"
    "v_mfma_f32_16x16x32_bf16 v[152:155], a[240:243], a[160:163], v[152:155]\n"
    "ds_read_b64_tr_b16 a[224:225], v12 offset:16384\n"
    "v_cmp_gt_i32 s[92:93], 35, v27\n"
    "v_mfma_f32_16x16x32_bf16 v[152:155], a[244:247], a[164:167], v[152:155]\n"
    "v_mov_b32 v58, 0\n"
    "v_mfma_f32_16x16x32_bf16 v[184:187], a[240:243], a[168:171], v[184:187]\n"
    "s_add_u32 m0, s75, s85\n"
    "ds_read_b64_tr_b16 a[226:227], v12 offset:20480\n"
    "v_mfma_f32_16x16x32_bf16 v[184:187], a[244:247], a[172:175], v[184:187]\n"
    "buffer_load_dwordx4 v21, s[56:59], s86 offen sc1 lds\n"
    "v_mfma_f32_16x16x32_bf16 v[216:219], a[240:243], a[176:179], v[216:219]\n"
    "ds_read_b64_tr_b16 a[228:229], v12 offset:24576\n"
    "v_mov_b32 v59, 0\n"
    "v_mfma_f32_16x16x32_bf16 v[216:219], a[244:247], a[180:183], v[216:219]\n"
    "v_cndmask_b32 v74, 0, v29, vcc\n"
    "v_cndmask_b32 v75, 0, v29, s[92:93]\n"
    "v_mfma_f32_16x16x32_bf16 v[248:251], a[240:243], a[184:187], v[248:251]\n"
    "ds_read_b64_tr_b16 a[230:231], v12 offset:28672\n"
    "v_cmp_gt_i32 vcc, 48, v27\n"
    "v_mfma_f32_16x16x32_bf16 v[248:251], a[244:247], a[188:191], v[248:251]\n"
    "s_waitcnt lgkmcnt(4)\n"
    "v_cmp_gt_i32 s[92:93], 49, v27\n"
    "v_mfma_f32_16x16x32_bf16 v[156:159], a[248:251], a[160:163], v[156:159]\n"
    "ds_read_b64_tr_b16 a[232:233], v13 offset:16384\n"
    "v_mov_b32 v60, 0\n"
    "v_mfma_f32_16x16x32_bf16 v[156:159], a[252:255], a[164:167], v[156:159]\n"
    "buffer_load_dwordx4 v21, s[56:59], s86 offen offset:1024 sc1 lds\n"
    "v_mfma_f32_16x16x32_bf16 v[188:191], a[248:251], a[168:171], v[188:191]\n"
    "ds_read_b64_tr_b16 a[234:235], v13 offset:20480\n"
    "v_mov_b32 v61, 0\n"
    "v_mfma_f32_16x16x32_bf16 v[188:191], a[252:255], a[172:175], v[188:191]\n"
    "v_cndmask_b32 v76, 0, v29, vcc\n"
    "v_mfma_f32_16x16x32_bf16 v[220:223], a[248:251], a[176:179], v[220:223]\n"
    "ds_read_b64_tr_b16 a[236:237], v13 offset:24576\n"
    "v_cndmask_b32 v77, 0, v29, s[92:93]\n"
    "v_mfma_f32_16x16x32_bf16 v[220:223], a[252:255], a[180:183], v[220:223]\n"
    "v_cmp_gt_i32 vcc, 50, v27\n"
    "v_mfma_f32_16x16x32_bf16 v[252:255], a[248:251], a[184:187], v[252:255]\n"
    "ds_read_b64_tr_b16 a[238:239], v13 offset:28672\n"
    "v_cmp_gt_i32 s[92:93], 51, v27\n"
    "v_mfma_f32_16x16x32_bf16 v[252:255], a[252:255], a[188:191], v[252:255]\n"
    "s_waitcnt vmcnt(10) lgkmcnt(4)\n"
    "v_mov_b32 v62, 0\n"
    "v_mfma_f32_16x16x32_bf16 v[128:131], a[224:227], a[192:195], v[128:131]\n"
    "ds_read_b64_tr_b16 a[240:241], v14 offset:16384\n"
    "v_mov_b32 v63, 0\n"
    "v_mfma_f32_16x16x32_bf16 v[128:131], a[228:231], a[196:199], v[128:131]\n"
    "buffer_load_dwordx4 v21, s[56:59], s86 offen offset:2048 sc1 lds\n"
    "v_mfma_f32_16x16x32_bf16 v[160:163], a[224:227], a[200:203], v[160:163]\n"
    "ds_read_b64_tr_b16 a[242:243], v14 offset:20480\n"
    "v_cndmask_b32 v78, 0, v29, vcc\n"
    "v_mfma_f32_16x16x32_bf16 v[160:163], a[228:231], a[204:207], v[160:163]\n"
    "v_cndmask_b32 v79, 0, v29, s[92:93]\n"
    "v_mfma_f32_16x16x32_bf16 v[192:195], a[224:227], a[208:211], v[192:195]\n"
    "ds_read_b64_tr_b16 a[244:245], v14 offset:24576\n"
    "v_cmp_gt_i32 vcc, 0, v28\n"
    "v_mfma_f32_16x16x32_bf16 v[192:195], a[228:231], a[212:215], v[192:195]\n"
    "v_cmp_gt_i32 s[92:93], 1, v28\n"
    "v_mfma_f32_16x16x32_bf16 v[224:227], a[224:227], a[216:219], v[224:227]\n"
    "ds_read_b64_tr_b16 a[246:247], v14 offset:28672\n"
    "v_mov_b32 v80, 0\n"
    "v_mfma_f32_16x16x32_bf16 v[224:227], a[228:231], a[220:223], v[224:227]\n"
    "s_waitcnt lgkmcnt(4)\n"
    "v_mov_b32 v81, 0\n"
    "v_mfma_f32_16x16x32_bf16 v[132:135], a[232:235], a[192:195], v[132:135]\n"
    "ds_read_b64_tr_b16 a[248:249], v15 offset:16384\n"
    "v_cndmask_b32 v32, 0, v29, vcc\n"
    "v_mfma_f32_16x16x32_bf16 v[132:135], a[236:239], a[196:199], v[132:135]\n"
    "buffer_load_dwordx4 v21, s[56:59], s86 offen offset:3072 sc1 lds\n"
    "v_mfma_f32_16x16x32_bf16 v[164:167], a[232:235], a[200:203], v[164:167]\n"
    "ds_read_b64_tr_b16 a[250:251], v15 offset:20480\n"
    "v_cndmask_b32 v33, 0, v29, s[92:93]\n"
    "v_mfma_f32_16x16x32_bf16 v[164:167], a[236:239], a[204:207], v[164:167]\n"
    "v_cmp_gt_i32 vcc, 2, v28\n"
    "v_mfma_f32_16x16x32_bf16 v[196:199], a[232:235], a[208:211], v[196:199]\n"
    "ds_read_b64_tr_b16 a[252:253], v15 offset:24576\n"
    "v_cmp_gt_i32 s[92:93], 3, v28\n"
    "v_mfma_f32_16x16x32_bf16 v[196:199], a[236:239], a[212:215], v[196:199]\n"
    "s_add_u32 s86, s86, 0x8000\n"
    "s_mov_b32 s76, s75\n"
    "s_mov_b32 s75, s74\n"
    "s_add_u32 s74, s74, 0x8000\n"
    "s_cmp_lt_u32 s74, 0x28000\n"
    "s_cselect_b32 s74, s74, 0\n"
    "s_add_u32 m0, s74, s84\n"
    "v_mfma_f32_16x16x32_bf16 v[228:231], a[232:235], a[216:219], v[228:231]\n"
    "ds_read_b64_tr_b16 a[254:255], v15 offset:28672\n"
    "v_mov_b32 v82, 0\n"
    "v_mfma_f32_16x16x32_bf16 v[228:231], a[236:239], a[220:223], v[228:231]\n"
    "s_waitcnt lgkmcnt(4)\n"
    "v_mov_b32 v83, 0\n"
    "v_cndmask_b32 v34, 0, v29, vcc\n"
    "v_mfma_f32_16x16x32_bf16 v[136:139], a[240:243], a[192:195], v[136:139]\n"
    "ds_read_b64_tr_b16 a[224:225], v16 offset:16384\n"
    "v_mfma_f32_16x16x32_bf16 v[136:139], a[244:247], a[196:199], v[136:139]\n"
    "buffer_load_dwordx4 v20, s[56:59], s86 offen sc1 lds\n"
    "v_mfma_f32_16x16x32_bf16 v[168:171], a[240:243], a[200:203], v[168:171]\n"
    "ds_read_b64_tr_b16 a[226:227], v16 offset:20480\n"
    "v_cndmask_b32 v35, 0, v29, s[92:93]\n"
    "v_mfma_f32_16x16x32_bf16 v[168:171], a[244:247], a[204:207], v[168:171]\n"
    "buffer_load_dwordx4 v20, s[56:59], s86 offen offset:1024 sc1 lds\n"
    "v_mfma_f32_16x16x32_bf16 v[200:203], a[240:243], a[208:211], v[200:203]\n"
    "ds_read_b64_tr_b16 a[228:229], v16 offset:24576\n"
    "v_cmp_gt_i32 vcc, 16, v28\n"
    "v_mfma_f32_16x16x32_bf16 v[200:203], a[244:247], a[212:215], v[200:203]\n"
    "v_cmp_gt_i32 s[92:93], 17, v28\n"
    "v_mov_b32 v84, 0\n"
    "v_mfma_f32_16x16x32_bf16 v[232:235], a[240:243], a[216:219], v[232:235]\n"
    "ds_read_b64_tr_b16 a[230:231], v16 offset:28672\n"
    "v_mov_b32 v85, 0\n"
    "v_mfma_f32_16x16x32_bf16 v[232:235], a[244:247], a[220:223], v[232:235]\n"
    "s_waitcnt lgkmcnt(4)\n"
    "v_cndmask_b32 v36, 0, v29, vcc\n"
    "v_cndmask_b32 v37, 0, v29, s[92:93]\n"
    "v_mfma_f32_16x16x32_bf16 v[140:143], a[248:251], a[192:195], v[140:143]\n"
    "ds_read_b64_tr_b16 a[232:233], v17 offset:16384\n"
    "v_mfma_f32_16x16x32_bf16 v[140:143], a[252:255], a[196:199], v[140:143]\n"
    "v_cmp_gt_i32 vcc, 18, v28\n"
    "v_mfma_f32_16x16x32_bf16 v[172:175], a[248:251], a[200:203], v[172:175]\n"
    "ds_read_b64_tr_b16 a[234:235], v17 offset:20480\n"
    "v_cmp_gt_i32 s[92:93], 19, v28\n"
    "v_mfma_f32_16x16x32_bf16 v[172:175], a[252:255], a[204:207], v[172:175]\n"
    "buffer_load_dwordx4 v20, s[56:59], s86 offen offset:2048 sc1 lds\n"
    "v_mfma_f32_16x16x32_bf16 v[204:207], a[248:251], a[208:211], v[204:207]\n"
    "ds_read_b64_tr_b16 a[236:237], v17 offset:24576\n"
    "v_mov_b32 v86, 0\n"
    "v_mfma_f32_16x16x32_bf16 v[204:207], a[252:255], a[212:215], v[204:207]\n"
    "v_mov_b32 v87, 0\n"
    "v_cndmask_b32 v38, 0, v29, vcc\n"
    "v_mfma_f32_16x16x32_bf16 v[236:239], a[248:251], a[216:219], v[236:239]\n"
    "ds_read_b64_tr_b16 a[238:239], v17 offset:28672\n"
    "v_cndmask_b32 v39, 0, v29, s[92:93]\n"
    "v_mfma_f32_16x16x32_bf16 v[236:239], a[252:255], a[220:223], v[236:239]\n"
    "s_waitcnt lgkmcnt(4)\n"
    "v_mfma_f32_16x16x32_bf16 v[144:147], a[224:227], a[192:195], v[144:147]\n"
    "ds_read_b64_tr_b16 a[240:241], v18 offset:16384\n"
    "v_cmp_gt_i32 vcc, 32, v28\n"
    "v_mfma_f32_16x16x32_bf16 v[144:147], a[228:231], a[196:199], v[144:147]\n"
    "buffer_load_dwordx4 v20, s[56:59], s86 offen offset:3072 sc1 lds\n"
    "v_mfma_f32_16x16x32_bf16 v[176:179], a[224:227], a[200:203], v[176:179]\n"
    "ds_read_b64_tr_b16 a[242:243], v18 offset:20480\n"
    "v_cmp_gt_i32 s[92:93], 33, v28\n"
    "v_mfma_f32_16x16x32_bf16 v[176:179], a[228:231], a[204:207], v[176:179]\n"
    "v_mov_b32 v88, 0\n"
    "v_mov_b32 v89, 0\n"
    "v_mfma_f32_16x16x32_bf16 v[208:211], a[224:227], a[208:211], v[208:211]\n"
    "ds_read_b64_tr_b16 a[244:245], v18 offset:24576\n"
    "v_cndmask_b32 v40, 0, v29, vcc\n"
    "v_mfma_f32_16x16x32_bf16 v[208:211], a[228:231], a[212:215], v[208:211]\n"
    "s_add_u32 m0, s75, s85\n"
    "v_cndmask_b32 v41, 0, v29, s[92:93]\n"
    "v_mfma_f32_16x16x32_bf16 v[240:243], a[224:227], a[216:219], v[240:243]\n"
    "ds_read_b64_tr_b16 a[246:247], v18 offset:28672\n"
    "v_mfma_f32_16x16x32_bf16 v[240:243], a[228:231], a[220:223], v[240:243]\n"
    "s_waitcnt lgkmcnt(4)\n"
    "v_cmp_gt_i32 vcc, 34, v28\n"
    "v_mfma_f32_16x16x32_bf16 v[148:151], a[232:235], a[192:195], v[148:151]\n"
    "ds_read_b64_tr_b16 a[248:249], v19 offset:16384\n"
    "v_cmp_gt_i32 s[92:93], 35, v28\n"
    "v_mfma_f32_16x16x32_bf16 v[148:151], a[236:239], a[196:199], v[148:151]\n"
    "buffer_load_dwordx4 v21, s[56:59], s86 offen sc1 lds\n"
    "v_mfma_f32_16x16x32_bf16 v[180:183], a[232:235], a[200:203], v[180:183]\n"
    "ds_read_b64_tr_b16 a[250:251], v19 offset:20480\n"
    "v_mov_b32 v90, 0\n"
    "v_mfma_f32_16x16x32_bf16 v[180:183], a[236:239], a[204:207], v[180:183]\n"
    "v_mov_b32 v91, 0\n"
    "v_cndmask_b32 v42, 0, v29, vcc\n"
    "v_mfma_f32_16x16x32_bf16 v[212:215], a[232:235], a[208:211], v[212:215]\n"
    "ds_read_b64_tr_b16 a[252:253], v19 offset:24576\n"
    "v_cndmask_b32 v43, 0, v29, s[92:93]\n"
    "v_mfma_f32_16x16x32_bf16 v[212:215], a[236:239], a[212:215], v[212:215]\n"
    "buffer_load_dwordx4 v21, s[56:59], s86 offen offset:1024 sc1 lds\n"
    "v_mfma_f32_16x16x32_bf16 v[244:247], a[232:235], a[216:219], v[244:247]\n"
    "ds_read_b64_tr_b16 a[254:255], v19 offset:28672\n"
    "v_cmp_gt_i32 vcc, 48, v28\n"
    "v_mfma_f32_16x16x32_bf16 v[244:247], a[236:239], a[220:223], v[244:247]\n"
    "s_waitcnt lgkmcnt(4)\n"
    "v_cmp_gt_i32 s[92:93], 49, v28\n"
    "v_mfma_f32_16x16x32_bf16 v[152:155], a[240:243], a[192:195], v[152:155]\n"
    "v_mov_b32 v92, 0\n"
    "v_mfma_f32_16x16x32_bf16 v[152:155], a[244:247], a[196:199], v[152:155]\n"
    "v_mov_b32 v93, 0\n"
    "v_mfma_f32_16x16x32_bf16 v[184:187], a[240:243], a[200:203], v[184:187]\n"
    "v_cndmask_b32 v44, 0, v29, vcc\n"
    "v_mfma_f32_16x16x32_bf16 v[184:187], a[244:247], a[204:207], v[184:187]\n"
    "buffer_load_dwordx4 v21, s[56:59], s86 offen offset:2048 sc1 lds\n"
    "v_mfma_f32_16x16x32_bf16 v[216:219], a[240:243], a[208:211], v[216:219]\n"
    "v_cndmask_b32 v45, 0, v29, s[92:93]\n"
    "v_mfma_f32_16x16x32_bf16 v[216:219], a[244:247], a[212:215], v[216:219]\n"
    "v_cmp_gt_i32 vcc, 50, v28\n"
    "v_mfma_f32_16x16x32_bf16 v[248:251], a[240:243], a[216:219], v[248:251]\n"
    "v_cmp_gt_i32 s[92:93], 51, v28\n"
    "v_mfma_f32_16x16x32_bf16 v[248:251], a[244:247], a[220:223], v[248:251]\n"
    "s_waitcnt lgkmcnt(0)\n"
    "v_mov_b32 v94, 0\n"
    "v_mfma_f32_16x16x32_bf16 v[156:159], a[248:251], a[192:195], v[156:159]\n"
    "v_mov_b32 v95, 0\n"
    "v_mfma_f32_16x16x32_bf16 v[156:159], a[252:255], a[196:199], v[156:159]\n"
    "buffer_load_dwordx4 v21, s[56:59], s86 offen offset:3072 sc1 lds\n"
    "v_mfma_f32_16x16x32_bf16 v[188:191], a[248:251], a[200:203], v[188:191]\n"
    "s_add_u32 s86, s86, 0x8000\n"
    "s_mov_b32 s76, s75\n"
    "s_mov_b32 s75, s74\n"
    "v_mfma_f32_16x16x32_bf16 v[188:191], a[252:255], a[204:207], v[188:191]\n"
    "s_add_u32 s74, s74, 0x8000\n"
    "s_cmp_lt_u32 s74, 0x28000\n"
    "s_cselect_b32 s74, s74, 0\n"
    "v_mfma_f32_16x16x32_bf16 v[220:223], a[248:251], a[208:211], v[220:223]\n"
    "v_cndmask_b32 v46, 0, v29, vcc\n"
    "v_cndmask_b32 v47, 0, v29, s[92:93]\n"
    "v_mfma_f32_16x16x32_bf16 v[220:223], a[252:255], a[212:215], v[220:223]\n"
    "v_mfma_f32_16x16x32_bf16 v[252:255], a[248:251], a[216:219], v[252:255]\n"
    "v_mfma_f32_16x16x32_bf16 v[252:255], a[252:255], a[220:223], v[252:255]\n"
    "v_add_u32 v22, 0x8000, v24\n"
    "s_waitcnt vmcnt(16)\n"
    "s_barrier\n"
    "ds_read_b128 a[128:131], v22\n"
    "ds_read_b128 a[132:135], v22 offset:1024\n"
    "ds_read_b128 a[136:139], v22 offset:2048\n"
    "ds_read_b128 a[140:143], v22 offset:3072\n"
    "ds_read_b128 a[144:147], v22 offset:4096\n"
    "ds_read_b128 a[148:151], v22 offset:5120\n"
    "ds_read_b128 a[152:155], v22 offset:6144\n"
    "ds_read_b128 a[156:159], v22 offset:7168\n"
    "ds_read_b128 a[160:163], v22 offset:8192\n"
    "ds_read_b128 a[164:167], v22 offset:9216\n"
    "ds_read_b128 a[168:171], v22 offset:10240\n"
    "ds_read_b128 a[172:175], v22 offset:11264\n"
    "v_mov_b32 v8, 0x3f803f80\n"
    "v_mov_b32 v9, 0x3f803f80\n"
    "v_mov_b32 v10, 0x3f803f80\n"
    "v_mov_b32 v11, 0x3f803f80\n"
    "v_cvt_pk_bf16_f32 v255, v254, v255\n"
    "v_cvt_pk_bf16_f32 v254, v252, v253\n"
    "v_cvt_pk_bf16_f32 v253, v250, v251\n"
    "v_cvt_pk_bf16_f32 v252, v248, v249\n"
    "v_cvt_pk_bf16_f32 v251, v246, v247\n"
    "v_cvt_pk_bf16_f32 v250, v244, v245\n"
    "v_cvt_pk_bf16_f32 v249, v242, v243\n"
    "v_cvt_pk_bf16_f32 v248, v240, v241\n"
    "v_cvt_pk_bf16_f32 v247, v238, v239\n"
    "v_cvt_pk_bf16_f32 v246, v236, v237\n"
    "v_cvt_pk_bf16_f32 v245, v234, v235\n"
    "v_cvt_pk_bf16_f32 v244, v232, v233\n"
    "v_cvt_pk_bf16_f32 v243, v230, v231\n"
    "v_cvt_pk_bf16_f32 v242, v228, v229\n"
    "v_cvt_pk_bf16_f32 v241, v226, v227\n"
    "v_cvt_pk_bf16_f32 v240, v224, v225\n"
    "v_cvt_pk_bf16_f32 v239, v222, v223\n"
    "v_cvt_pk_bf16_f32 v238, v220, v221\n"
    "v_cvt_pk_bf16_f32 v237, v218, v219\n"
    "v_cvt_pk_bf16_f32 v236, v216, v217\n"
    "v_cvt_pk_bf16_f32 v235, v214, v215\n"
    "v_cvt_pk_bf16_f32 v234, v212, v213\n"
    "v_cvt_pk_bf16_f32 v233, v210, v211\n"
    "v_cvt_pk_bf16_f32 v232, v208, v209\n"
    "v_cvt_pk_bf16_f32 v231, v206, v207\n"
    "v_cvt_pk_bf16_f32 v230, v204, v205\n"
    "v_cvt_pk_bf16_f32 v229, v202, v203\n"
    "v_cvt_pk_bf16_f32 v228, v200, v201\n"
    "v_cvt_pk_bf16_f32 v227, v198, v199\n"
    "v_cvt_pk_bf16_f32 v226, v196, v197\n"
    "v_cvt_pk_bf16_f32 v225, v194, v195\n"
    "v_cvt_pk_bf16_f32 v224, v192, v193\n"
    "v_cvt_pk_bf16_f32 v223, v190, v191\n"
    "v_cvt_pk_bf16_f32 v222, v188, v189\n"
    "v_cvt_pk_bf16_f32 v221, v186, v187\n"
    "v_cvt_pk_bf16_f32 v220, v184, v185\n"
    "v_cvt_pk_bf16_f32 v219, v182, v183\n"
    "v_cvt_pk_bf16_f32 v218, v180, v181\n"
    "v_cvt_pk_bf16_f32 v217, v178, v179\n"
    "v_cvt_pk_bf16_f32 v216, v176, v177\n"
    "v_cvt_pk_bf16_f32 v215, v174, v175\n"
    "v_cvt_pk_bf16_f32 v214, v172, v173\n"
    "v_cvt_pk_bf16_f32 v213, v170, v171\n"
    "v_cvt_pk_bf16_f32 v212, v168, v169\n"
    "v_cvt_pk_bf16_f32 v211, v166, v167\n"
    "v_cvt_pk_bf16_f32 v210, v164, v165\n"
    "v_cvt_pk_bf16_f32 v209, v162, v163\n"
    "v_cvt_pk_bf16_f32 v208, v160, v161\n"
    "v_cvt_pk_bf16_f32 v207, v158, v159\n"
    "v_cvt_pk_bf16_f32 v206, v156, v157\n"
    "v_cvt_pk_bf16_f32 v205, v154, v155\n"
    "v_cvt_pk_bf16_f32 v204, v152, v153\n"
    "v_cvt_pk_bf16_f32 v203, v150, v151\n"
    "v_cvt_pk_bf16_f32 v202, v148, v149\n"
    "v_cvt_pk_bf16_f32 v201, v146, v147\n"
    "v_cvt_pk_bf16_f32 v200, v144, v145\n"
    "v_cvt_pk_bf16_f32 v199, v142, v143\n"
    "v_cvt_pk_bf16_f32 v198, v140, v141\n"
    "v_cvt_pk_bf16_f32 v197, v138, v139\n"
    "v_cvt_pk_bf16_f32 v196, v136, v137\n"
    "v_cvt_pk_bf16_f32 v195, v134, v135\n"
    "v_cvt_pk_bf16_f32 v194, v132, v133\n"
    "v_cvt_pk_bf16_f32 v193, v130, v131\n"
    "v_cvt_pk_bf16_f32 v192, v128, v129\n"
    "s_lshl_b32 s87, s80, 1\n"
    "s_cmp_lg_u32 s80, 0\n"
    "s_cbranch_scc1 at_s0\n"
    "v_mov_b32 v48, v64\n"
    "v_mov_b32 v49, v65\n"
    "v_mov_b32 v50, v66\n"
    "v_mov_b32 v51, v67\n"
    "v_mov_b32 v52, v68\n"
    "v_mov_b32 v53, v69\n"
    "v_mov_b32 v54, v70\n"
    "v_mov_b32 v55, v71\n"
    "v_mov_b32 v56, v72\n"
    "v_mov_b32 v57, v73\n"
    "v_mov_b32 v58, v74\n"
    "v_mov_b32 v59, v75\n"
    "v_mov_b32 v60, v76\n"
    "v_mov_b32 v61, v77\n"
    "v_mov_b32 v62, v78\n"
    "v_mov_b32 v63, v79\n"
    "at_s0:\n"
    "s_waitcnt lgkmcnt(8)\n"
    "v_mfma_f32_16x16x32_bf16 v[176:179], a[128:131], v[240:243], 0\n"
    "s_add_u32 m0, s74, s84\n"
    "ds_read_b128 a[176:179], v22 offset:12288\n"
    "v_mfma_f32_16x16x32_bf16 v[176:179], a[132:135], v[244:247], v[176:179]\n"
    "buffer_load_dwordx4 v20, s[56:59], s86 offen sc1 lds\n"
    "v_mfma_f32_16x16x32_bf16 v[176:179], a[136:139], v[248:251], v[176:179]\n"
    "v_mfma_f32_16x16x32_bf16 v[176:179], a[140:143], v[252:255], v[176:179]\n"
    "ds_read_b128 a[180:183], v22 offset:13312\n"
    "v_mfma_f32_16x16x32_bf16 v[160:163], a[128:131], v[224:227], 0\n"
    "s_bfm_b64 exec, s40, 0\n buffer_load_dword v0, v2, s[48:51], s73 offen\n s_mov_b64 exec, -1\n"
    "v_mfma_f32_16x16x32_bf16 v[160:163], a[132:135], v[228:231], v[160:163]\n"
    "s_lshl_b32 s41, s40, 7\n"
    "v_mfma_f32_16x16x32_bf16 v[160:163], a[136:139], v[232:235], v[160:163]\n"
    "buffer_load_dwordx4 v20, s[56:59], s86 offen offset:1024 sc1 lds\n"
    "v_mfma_f32_16x16x32_bf16 v[160:163], a[140:143], v[236:239], v[160:163]\n"
    "s_add_u32 s73, s73, s41\n"
    "ds_read_b128 a[184:187], v22 offset:14336\n"
    "v_mfma_f32_16x16x32_bf16 v[144:147], a[128:131], v[208:211], 0\n"
    "s_add_u32 s42, s42, s43\n"
    "v_exp_f32 v176, v176\n"
    "v_mfma_f32_16x16x32_bf16 v[144:147], a[132:135], v[212:215], v[144:147]\n"
    "s_lshr_b32 s40, s42, 2\n"
    "v_exp_f32 v177, v177\n"
    "v_mfma_f32_16x16x32_bf16 v[144:147], a[136:139], v[216:219], v[144:147]\n"
    "v_exp_f32 v178, v178\n"
    "v_mfma_f32_16x16x32_bf16 v[144:147], a[140:143], v[220:223], v[144:147]\n"
    "ds_read_b128 a[188:191], v22 offset:15360\n"
    "v_exp_f32 v179, v179\n"
    "v_mfma_f32_16x16x32_bf16 v[128:131], a[128:131], v[192:195], v[48:51]\n"
    "buffer_load_dwordx4 v20, s[56:59], s86 offen offset:2048 sc1 lds\n"
    "v_mov_b32 v23, v22\n"
    "v_mfma_f32_16x16x32_bf16 v[128:131], a[132:135], v[196:199], v[128:131]\n"
    "v_exp_f32 v160, v160\n"
    "v_add_u32 v22, s76, v24\n"
    "v_mfma_f32_16x16x32_bf16 v[128:131], a[136:139], v[200:203], v[128:131]\n"
    "v_exp_f32 v161, v161\n"
    "v_mfma_f32_16x16x32_bf16 v[128:131], a[140:143], v[204:207], v[128:131]\n"
    "s_waitcnt lgkmcnt(8)\n"
    "v_exp_f32 v162, v162\n"
    "v_mfma_f32_16x16x32_bf16 v[180:183], a[144:147], v[240:243], 0\n"
    "v_exp_f32 v163, v163\n"
    "v_mfma_f32_16x16x32_bf16 v[180:183], a[148:151], v[244:247], v[180:183]\n"
    "v_exp_f32 v144, v144\n"
    "v_mfma_f32_16x16x32_bf16 v[180:183], a[152:155], v[248:251], v[180:183]\n"
    "buffer_load_dwordx4 v20, s[56:59], s86 offen offset:3072 sc1 lds\n"
    "v_mfma_f32_16x16x32_bf16 v[180:183], a[156:159], v[252:255], v[180:183]\n"
    "s_add_u32 m0, s75, s85\n"
    "v_exp_f32 v145, v145\n"
    "v_mfma_f32_16x16x32_bf16 v[164:167], a[144:147], v[224:227], 0\n"
    "v_exp_f32 v146, v146\n"
    "v_mfma_f32_16x16x32_bf16 v[164:167], a[148:151], v[228:231], v[164:167]\n"
    "v_exp_f32 v147, v147\n"
    "v_mfma_f32_16x16x32_bf16 v[164:167], a[152:155], v[232:235], v[164:167]\n"
    "v_exp_f32 v128, v128\n"
    "v_mfma_f32_16x16x32_bf16 v[164:167], a[156:159], v[236:239], v[164:167]\n"
    "v_exp_f32 v129, v129\n"
    "v_mfma_f32_16x16x32_bf16 v[148:151], a[144:147], v[208:211], 0\n"
    "buffer_load_dwordx4 v21, s[56:59], s86 offen sc1 lds\n"
    "v_mfma_f32_16x16x32_bf16 v[148:151], a[148:151], v[212:215], v[148:151]\n"
    "v_exp_f32 v130, v130\n"
    "v_mfma_f32_16x16x32_bf16 v[148:151], a[152:155], v[216:219], v[148:151]\n"
    "v_exp_f32 v131, v131\n"
    "v_mfma_f32_16x16x32_bf16 v[148:151], a[156:159], v[220:223], v[148:151]\n"
    "v_exp_f32 v180, v180\n"
    "v_mfma_f32_16x16x32_bf16 v[132:135], a[144:147], v[192:195], v[52:55]\n"
    "v_exp_f32 v181, v181\n"
    "v_mfma_f32_16x16x32_bf16 v[132:135], a[148:151], v[196:199], v[132:135]\n"
    "v_exp_f32 v182, v182\n"
    "v_mfma_f32_16x16x32_bf16 v[132:135], a[152:155], v[200:203], v[132:135]\n"
    "buffer_load_dwordx4 v21, s[56:59], s86 offen offset:1024 sc1 lds\n"
    "v_mfma_f32_16x16x32_bf16 v[132:135], a[156:159], v[204:207], v[132:135]\n"
    "s_waitcnt lgkmcnt(4)\n"
    "v_exp_f32 v183, v183\n"
    "v_mfma_f32_16x16x32_bf16 v[184:187], a[160:163], v[240:243], 0\n"
    "v_exp_f32 v164, v164\n"
    "v_mfma_f32_16x16x32_bf16 v[184:187], a[164:167], v[244:247], v[184:187]\n"
    "v_exp_f32 v165, v165\n"
    "v_mfma_f32_16x16x32_bf16 v[184:187], a[168:171], v[248:251], v[184:187]\n"
    "v_exp_f32 v166, v166\n"
    "v_mfma_f32_16x16x32_bf16 v[184:187], a[172:175], v[252:255], v[184:187]\n"
    "v_exp_f32 v167, v167\n"
    "v_mfma_f32_16x16x32_bf16 v[168:171], a[160:163], v[224:227], 0\n"
    "buffer_load_dwordx4 v21, s[56:59], s86 offen offset:2048 sc1 lds\n"
    "v_mfma_f32_16x16x32_bf16 v[168:171], a[164:167], v[228:231], v[168:171]\n"
    "v_exp_f32 v148, v148\n"
    "v_mfma_f32_16x16x32_bf16 v[168:171], a[168:171], v[232:235], v[168:171]\n"
    "v_exp_f32 v149, v149\n"
    "v_mfma_f32_16x16x32_bf16 v[168:171], a[172:175], v[236:239], v[168:171]\n"
    "v_exp_f32 v150, v150\n"
    "v_mfma_f32_16x16x32_bf16 v[152:155], a[160:163], v[208:211], 0\n"
    "v_exp_f32 v151, v151\n"
    "v_mfma_f32_16x16x32_bf16 v[152:155], a[164:167], v[212:215], v[152:155]\n"
    "v_exp_f32 v132, v132\n"
    "v_mfma_f32_16x16x32_bf16 v[152:155], a[168:171], v[216:219], v[152:155]\n"
    "buffer_load_dwordx4 v21, s[56:59], s86 offen offset:3072 sc1 lds\n"
    "v_mfma_f32_16x16x32_bf16 v[152:155], a[172:175], v[220:223], v[152:155]\n"
    "v_exp_f32 v133, v133\n"
    "v_mfma_f32_16x16x32_bf16 v[136:139], a[160:163], v[192:195], v[56:59]\n"
    "v_exp_f32 v134, v134\n"
    "v_mfma_f32_16x16x32_bf16 v[136:139], a[164:167], v[196:199], v[136:139]\n"
    "v_exp_f32 v135, v135\n"
    "v_mfma_f32_16x16x32_bf16 v[136:139], a[168:171], v[200:203], v[136:139]\n"
    "v_exp_f32 v184, v184\n"
    "v_mfma_f32_16x16x32_bf16 v[136:139], a[172:175], v[204:207], v[136:139]\n"
    "s_waitcnt lgkmcnt(0)\n"
    "v_exp_f32 v185, v185\n"
    "v_mfma_f32_16x16x32_bf16 v[188:191], a[176:179], v[240:243], 0\n"
    "v_exp_f32 v186, v186\n"
    "v_mfma_f32_16x16x32_bf16 v[188:191], a[180:183], v[244:247], v[188:191]\n"
    "v_exp_f32 v187, v187\n"
    "v_mfma_f32_16x16x32_bf16 v[188:191], a[184:187], v[248:251], v[188:191]\n"
    "s_waitcnt vmcnt(17)\n"
    "s_barrier\n"
    "v_exp_f32 v168, v168\n"
    "v_exp_f32 v169, v169\n"
    "v_exp_f32 v170, v170\n"
    "v_mfma_f32_16x16x32_bf16 v[188:191], a[188:191], v[252:255], v[188:191]\n"
    "ds_read_b128 a[128:131], v22\n"
    "v_exp_f32 v171, v171\n"
    "v_exp_f32 v152, v152\n"
    "v_mfma_f32_16x16x32_bf16 v[172:175], a[176:179], v[224:227], 0\n"
    "ds_read_b128 a[132:135], v22 offset:1024\n"
    "v_exp_f32 v153, v153\n"
    "v_exp_f32 v154, v154\n"
    "v_mfma_f32_16x16x32_bf16 v[172:175], a[180:183], v[228:231], v[172:175]\n"
    "ds_read_b128 a[136:139], v22 offset:2048\n"
    "v_exp_f32 v155, v155\n"
    "v_exp_f32 v136, v136\n"
    "v_exp_f32 v137, v137\n"
    "v_mfma_f32_16x16x32_bf16 v[172:175], a[184:187], v[232:235], v[172:175]\n"
    "s_add_u32 s86, s86, 0x8000\n"
    "ds_read_b128 a[140:143], v22 offset:3072\n"
    "v_mfma_f32_16x16x32_bf16 v[172:175], a[188:191], v[236:239], v[172:175]\n"
    "s_mov_b32 s76, s75\n"
    "ds_read_b128 a[144:147], v22 offset:4096\n"
    "v_mfma_f32_16x16x32_bf16 v[156:159], a[176:179], v[208:211], 0\n"
    "s_mov_b32 s75, s74\n"
    "ds_read_b128 a[148:151], v22 offset:5120\n"
    "v_mfma_f32_16x16x32_bf16 v[156:159], a[180:183], v[212:215], v[156:159]\n"
    "s_add_u32 s74, s74, 0x8000\n"
    "ds_read_b128 a[152:155], v22 offset:6144\n"
    "v_mfma_f32_16x16x32_bf16 v[156:159], a[184:187], v[216:219], v[156:159]\n"
    "s_cmp_lt_u32 s74, 0x28000\n"
    "s_cselect_b32 s74, s74, 0\n"
    "ds_read_b128 a[156:159], v22 offset:7168\n"
    "v_mfma_f32_16x16x32_bf16 v[156:159], a[188:191], v[220:223], v[156:159]\n"
    "ds_read_b128 a[160:163], v22 offset:8192\n"
    "v_mfma_f32_16x16x32_bf16 v[140:143], a[176:179], v[192:195], v[60:63]\n"
    "ds_read_b128 a[164:167], v22 offset:9216\n"
    "v_mfma_f32_16x16x32_bf16 v[140:143], a[180:183], v[196:199], v[140:143]\n"
    "ds_read_b128 a[168:171], v22 offset:10240\n"
    "v_cvt_pk_bf16_f32 v120, v176, v177\n"
    "v_mfma_f32_16x16x32_bf16 v[140:143], a[184:187], v[200:203], v[140:143]\n"
    "ds_read_b128 a[172:175], v22 offset:11264\n"
    "v_cvt_pk_bf16_f32 v121, v178, v179\n"
    "v_mfma_f32_16x16x32_bf16 v[140:143], a[188:191], v[204:207], v[140:143]\n"
    "v_exp_f32 v138, v138\n"
    "s_nop 7\n"
    "v_exp_f32 v139, v139\n"
    "v_exp_f32 v188, v188\n"
    "v_exp_f32 v189, v189\n"
    "v_exp_f32 v190, v190\n"
    "v_exp_f32 v191, v191\n"
    "v_exp_f32 v172, v172\n"
    "v_exp_f32 v173, v173\n"
    "v_exp_f32 v174, v174\n"
    "v_exp_f32 v175, v175\n"
    "v_exp_f32 v156, v156\n"
    "v_exp_f32 v157, v157\n"
    "v_exp_f32 v158, v158\n"
    "v_exp_f32 v159, v159\n"
    "v_exp_f32 v140, v140\n"
    "v_exp_f32 v141, v141\n"
    "v_exp_f32 v142, v142\n"
    "v_exp_f32 v143, v143\n"
    "s_cmp_eq_u32 s87, 0\n"
    "s_cbranch_scc1 at_a1\n"
    "at_aa:\n"
    "s_waitcnt lgkmcnt(8)\n"
    "v_mfma_f32_16x16x32_bf16 v[176:179], a[128:131], v[240:243], 0\n"
    "s_add_u32 m0, s74, s84\n"
    "ds_read_b128 a[176:179], v22 offset:12288\n"
    "v_cvt_pk_bf16_f32 v112, v160, v161\n"
    "v_mfma_f32_16x16x32_bf16 v[176:179], a[132:135], v[244:247], v[176:179]\n"
    "buffer_load_dwordx4 v20, s[56:59], s86 offen sc1 lds\n"
    "v_mfma_f32_16x16x32_bf16 v[176:179], a[136:139], v[248:251], v[176:179]\n"
    "v_cvt_pk_bf16_f32 v113, v162, v163\n"
    "v_mfma_f32_16x16x32_bf16 v[176:179], a[140:143], v[252:255], v[176:179]\n"
    "s_bfm_b64 exec, s40, 0\n buffer_load_dword v0, v2, s[48:51], s73 offen\n s_mov_b64 exec, -1\n"
    "v_mfma_f32_16x16x32_bf16 v[160:163], a[128:131], v[224:227], 0\n"
    "ds_read_b128 a[180:183], v22 offset:13312\n"
    "v_mfma_f32_16x16x32_bf16 v[160:163], a[132:135], v[228:231], v[160:163]\n"
    "s_lshl_b32 s41, s40, 7\n"
    "v_mfma_f32_16x16x32_bf16 v[160:163], a[136:139], v[232:235], v[160:163]\n"
    "s_add_u32 s73, s73, s41\n"
    "v_mfma_f32_16x16x32_bf16 v[160:163], a[140:143], v[236:239], v[160:163]\n"
    "s_add_u32 s42, s42, s43\n"
    "v_cvt_pk_bf16_f32 v104, v144, v145\n"
    "v_cvt_pk_bf16_f32 v105, v146, v147\n"
    "v_mfma_f32_16x16x32_bf16 v[144:147], a[128:131], v[208:211], 0\n"
    "ds_read_b128 a[184:187], v22 offset:14336\n"
    "v_cvt_pk_bf16_f32 v96, v128, v129\n"
    "v_mfma_f32_16x16x32_bf16 v[144:147], a[132:135], v[212:215], v[144:147]\n"
    "s_lshr_b32 s40, s42, 2\n"
    "v_exp_f32 v176, v176\n"
    "v_mfma_f32_16x16x32_bf16 v[144:147], a[136:139], v[216:219], v[144:147]\n"
    "v_exp_f32 v177, v177\n"
    "v_mfma_f32_16x16x32_bf16 v[144:147], a[140:143], v[220:223], v[144:147]\n"
    "v_cvt_pk_bf16_f32 v97, v130, v131\n"
    "v_mfma_f32_16x16x32_bf16 v[128:131], a[128:131], v[192:195], v[48:51]\n"
    "ds_read_b128 a[188:191], v22 offset:15360\n"
    "v_cvt_pk_bf16_f32 v122, v180, v181\n"
    "v_mfma_f32_16x16x32_bf16 v[128:131], a[132:135], v[196:199], v[128:131]\n"
    "v_exp_f32 v178, v178\n"
    "v_mfma_f32_16x16x32_bf16 v[128:131], a[136:139], v[200:203], v[128:131]\n"
    "v_exp_f32 v179, v179\n"
    "v_mfma_f32_16x16x32_bf16 v[128:131], a[140:143], v[204:207], v[128:131]\n"
    "s_waitcnt lgkmcnt(8)\n"
    "v_cvt_pk_bf16_f32 v123, v182, v183\n"
    "v_mfma_f32_16x16x32_bf16 v[180:183], a[144:147], v[240:243], 0\n"
    "ds_read_b128 a[192:195], v23 offset:16384\n"
    "v_cvt_pk_bf16_f32 v114, v164, v165\n"
    "v_mfma_f32_16x16x32_bf16 v[180:183], a[148:151], v[244:247], v[180:183]\n"
    "buffer_load_dwordx4 v20, s[56:59], s86 offen offset:1024 sc1 lds\n"
    "v_mfma_f32_16x16x32_bf16 v[180:183], a[152:155], v[248:251], v[180:183]\n"
    "v_exp_f32 v160, v160\n"
    "v_mfma_f32_16x16x32_bf16 v[180:183], a[156:159], v[252:255], v[180:183]\n"
    "v_cvt_pk_bf16_f32 v115, v166, v167\n"
    "v_mfma_f32_16x16x32_bf16 v[164:167], a[144:147], v[224:227], 0\n"
    "ds_read_b128 a[196:199], v23 offset:17408\n"
    "v_cvt_pk_bf16_f32 v106, v148, v149\n"
    "v_mfma_f32_16x16x32_bf16 v[164:167], a[148:151], v[228:231], v[164:167]\n"
    "v_exp_f32 v161, v161\n"
    "v_mfma_f32_16x16x32_bf16 v[164:167], a[152:155], v[232:235], v[164:167]\n"
    "v_mfma_f32_16x16x32_bf16 v[164:167], a[156:159], v[236:239], v[164:167]\n"
    "v_cvt_pk_bf16_f32 v107, v150, v151\n"
    "v_mfma_f32_16x16x32_bf16 v[148:151], a[144:147], v[208:211], 0\n"
    "ds_read_b128 a[200:203], v23 offset:18432\n"
    "v_cvt_pk_bf16_f32 v98, v132, v133\n"
    "v_mfma_f32_16x16x32_bf16 v[148:151], a[148:151], v[212:215], v[148:151]\n"
    "v_exp_f32 v162, v162\n"
    "v_mfma_f32_16x16x32_bf16 v[148:151], a[152:155], v[216:219], v[148:151]\n"
    "v_exp_f32 v163, v163\n"
    "v_mfma_f32_16x16x32_bf16 v[148:151], a[156:159], v[220:223], v[148:151]\n"
    "v_cvt_pk_bf16_f32 v99, v134, v135\n"
    "v_mfma_f32_16x16x32_bf16 v[132:135], a[144:147], v[192:195], v[52:55]\n"
    "ds_read_b128 a[204:207], v23 offset:19456\n"
    "v_cvt_pk_bf16_f32 v124, v184, v185\n"
    "v_mfma_f32_16x16x32_bf16 v[132:135], a[148:151], v[196:199], v[132:135]\n"
    "v_exp_f32 v144, v144\n"
    "v_mfma_f32_16x16x32_bf16 v[132:135], a[152:155], v[200:203], v[132:135]\n"
    "v_exp_f32 v145, v145\n"
    "v_mfma_f32_16x16x32_bf16 v[132:135], a[156:159], v[204:207], v[132:135]\n"
    "s_waitcnt lgkmcnt(8)\n"
    "v_cvt_pk_bf16_f32 v125, v186, v187\n"
    "v_mfma_f32_16x16x32_bf16 v[184:187], a[160:163], v[240:243], 0\n"
    "buffer_load_dwordx4 v20, s[56:59], s86 offen offset:2048 sc1 lds\n"
    "v_mfma_f32_16x16x32_bf16 v[184:187], a[164:167], v[244:247], v[184:187]\n"
    "ds_read_b128 a[208:211], v23 offset:20480\n"
    "v_cvt_pk_bf16_f32 v116, v168, v169\n"
    "v_mfma_f32_16x16x32_bf16 v[184:187], a[168:171], v[248:251], v[184:187]\n"
    "v_exp_f32 v146, v146\n"
    "v_mfma_f32_16x16x32_bf16 v[184:187], a[172:175], v[252:255], v[184:187]\n"
    "v_cvt_pk_bf16_f32 v117, v170, v171\n"
    "v_mfma_f32_16x16x32_bf16 v[168:171], a[160:163], v[224:227], 0\n"
    "v_mfma_f32_16x16x32_bf16 v[168:171], a[164:167], v[228:231], v[168:171]\n"
    "ds_read_b128 a[212:215], v23 offset:21504\n"
    "v_cvt_pk_bf16_f32 v108, v152, v153\n"
    "v_mfma_f32_16x16x32_bf16 v[168:171], a[168:171], v[232:235], v[168:171]\n"
    "v_exp_f32 v147, v147\n"
    "v_mfma_f32_16x16x32_bf16 v[168:171], a[172:175], v[236:239], v[168:171]\n"
    "v_cvt_pk_bf16_f32 v109, v154, v155\n"
    "v_mfma_f32_16x16x32_bf16 v[152:155], a[160:163], v[208:211], 0\n"
    "v_exp_f32 v128, v128\n"
    "v_mfma_f32_16x16x32_bf16 v[152:155], a[164:167], v[212:215], v[152:155]\n"
    "ds_read_b128 a[216:219], v23 offset:22528\n"
    "v_cvt_pk_bf16_f32 v100, v136, v137\n"
    "v_mfma_f32_16x16x32_bf16 v[152:155], a[168:171], v[216:219], v[152:155]\n"
    "v_exp_f32 v129, v129\n"
    "v_mfma_f32_16x16x32_bf16 v[152:155], a[172:175], v[220:223], v[152:155]\n"
    "v_cvt_pk_bf16_f32 v101, v138, v139\n"
    "v_mfma_f32_16x16x32_bf16 v[136:139], a[160:163], v[192:195], v[56:59]\n"
    "v_exp_f32 v130, v130\n"
    "v_mfma_f32_16x16x32_bf16 v[136:139], a[164:167], v[196:199], v[136:139]\n"
    "ds_read_b128 a[220:223], v23 offset:23552\n"
    "v_cvt_pk_bf16_f32 v126, v188, v189\n"
    "v_mfma_f32_16x16x32_bf16 v[136:139], a[168:171], v[200:203], v[136:139]\n"
    "s_waitcnt lgkmcnt(8)\n"
    "v_cvt_pk_bf16_f32 v127, v190, v191\n"
    "v_mfma_f32_16x16x32_bf16 v[136:139], a[172:175], v[204:207], v[136:139]\n"
    "buffer_load_dwordx4 v20, s[56:59], s86 offen offset:3072 sc1 lds\n"
    "v_mfma_f32_16x16x32_bf16 v[188:191], a[176:179], v[240:243], 0\n"
    "s_add_u32 m0, s75, s85\n"
    "v_exp_f32 v131, v131\n"
    "v_mfma_f32_16x16x32_bf16 v[188:191], a[180:183], v[244:247], v[188:191]\n"
    "ds_read_b128 a[224:227], v23 offset:24576\n"
    "v_cvt_pk_bf16_f32 v118, v172, v173\n"
    "v_mfma_f32_16x16x32_bf16 v[188:191], a[184:187], v[248:251], v[188:191]\n"
    "v_mfma_f32_16x16x32_bf16 v[188:191], a[188:191], v[252:255], v[188:191]\n"
    "v_cvt_pk_bf16_f32 v119, v174, v175\n"
    "v_mfma_f32_16x16x32_bf16 v[172:175], a[176:179], v[224:227], 0\n"
    "v_exp_f32 v180, v180\n"
    "v_mfma_f32_16x16x32_bf16 v[172:175], a[180:183], v[228:231], v[172:175]\n"
    "ds_read_b128 a[228:231], v23 offset:25600\n"
    "v_cvt_pk_bf16_f32 v110, v156, v157\n"
    "v_mfma_f32_16x16x32_bf16 v[172:175], a[184:187], v[232:235], v[172:175]\n"
    "v_exp_f32 v181, v181\n"
    "v_mfma_f32_16x16x32_bf16 v[172:175], a[188:191], v[236:239], v[172:175]\n"
    "v_cvt_pk_bf16_f32 v111, v158, v159\n"
    "v_mfma_f32_16x16x32_bf16 v[156:159], a[176:179], v[208:211], 0\n"
    "v_exp_f32 v182, v182\n"
    "v_mfma_f32_16x16x32_bf16 v[156:159], a[180:183], v[212:215], v[156:159]\n"
    "ds_read_b128 a[232:235], v23 offset:26624\n"
    "v_cvt_pk_bf16_f32 v102, v140, v141\n"
    "v_mfma_f32_16x16x32_bf16 v[156:159], a[184:187], v[216:219], v[156:159]\n"
    "v_exp_f32 v183, v183\n"
    "v_mfma_f32_16x16x32_bf16 v[156:159], a[188:191], v[220:223], v[156:159]\n"
    "v_cvt_pk_bf16_f32 v103, v142, v143\n"
    "v_mfma_f32_16x16x32_bf16 v[140:143], a[176:179], v[192:195], v[60:63]\n"
    "v_exp_f32 v164, v164\n"
    "v_mfma_f32_16x16x32_bf16 v[140:143], a[180:183], v[196:199], v[140:143]\n"
    "ds_read_b128 a[236:239], v23 offset:27648\n"
    "v_mfma_f32_16x16x32_bf16 v[140:143], a[184:187], v[200:203], v[140:143]\n"
    "v_exp_f32 v165, v165\n"
    "v_mfma_f32_16x16x32_bf16 v[140:143], a[188:191], v[204:207], v[140:143]\n"
    "buffer_load_dwordx4 v21, s[56:59], s86 offen sc1 lds\n"
    "v_mfma_f32_16x16x32_bf16 v[92:95], v[8:11], v[120:123], v[92:95]\n"
    "v_mfma_f32_16x16x32_bf16 v[92:95], v[8:11], v[124:127], v[92:95]\n"
    "v_exp_f32 v166, v166\n"
    "v_mfma_f32_16x16x32_bf16 v[88:91], v[8:11], v[112:115], v[88:91]\n"
    "ds_read_b128 a[240:243], v23 offset:28672\n"
    "v_mfma_f32_16x16x32_bf16 v[88:91], v[8:11], v[116:119], v[88:91]\n"
    "v_exp_f32 v167, v167\n"
    "v_mfma_f32_16x16x32_bf16 v[84:87], v[8:11], v[104:107], v[84:87]\n"
    "v_exp_f32 v148, v148\n"
    "v_mfma_f32_16x16x32_bf16 v[84:87], v[8:11], v[108:111], v[84:87]\n"
    "v_exp_f32 v149, v149\n"
    "v_mfma_f32_16x16x32_bf16 v[80:83], v[8:11], v[96:99], v[80:83]\n"
    "ds_read_b128 a[244:247], v23 offset:29696\n"
    "v_mfma_f32_16x16x32_bf16 v[80:83], v[8:11], v[100:103], v[80:83]\n"
    "s_waitcnt lgkmcnt(10)\n"
    "v_exp_f32 v150, v150\n"
    "v_mfma_f32_16x16x32_bf16 a[96:99], a[192:195], v[120:123], a[96:99]\n"
    "v_mfma_f32_16x16x32_bf16 a[96:99], a[196:199], v[124:127], a[96:99]\n"
    "v_exp_f32 v151, v151\n"
    "v_mfma_f32_16x16x32_bf16 a[64:67], a[192:195], v[112:115], a[64:67]\n"
    "ds_read_b128 a[248:251], v23 offset:30720\n"
    "v_mfma_f32_16x16x32_bf16 a[64:67], a[196:199], v[116:119], a[64:67]\n"
    "v_exp_f32 v132, v132\n"
    "v_mfma_f32_16x16x32_bf16 a[32:35], a[192:195], v[104:107], a[32:35]\n"
    "v_exp_f32 v133, v133\n"
    "v_mfma_f32_16x16x32_bf16 a[32:35], a[196:199], v[108:111], a[32:35]\n"
    "v_exp_f32 v134, v134\n"
    "v_mfma_f32_16x16x32_bf16 a[0:3], a[192:195], v[96:99], a[0:3]\n"
    "ds_read_b128 a[252:255], v23 offset:31744\n"
    "v_mov_b32 v23, v22\n"
    "v_mfma_f32_16x16x32_bf16 a[0:3], a[196:199], v[100:103], a[0:3]\n"
    "buffer_load_dwordx4 v21, s[56:59], s86 offen offset:1024 sc1 lds\n"
    "v_mfma_f32_16x16x32_bf16 a[100:103], a[200:203], v[120:123], a[100:103]\n"
    "v_exp_f32 v135, v135\n"
    "v_mfma_f32_16x16x32_bf16 a[100:103], a[204:207], v[124:127], a[100:103]\n"
    "s_waitcnt vmcnt(16)\n"
    "s_barrier\n"
    "v_add_u32 v22, s76, v24\n"
    "v_mfma_f32_16x16x32_bf16 a[68:71], a[200:203], v[112:115], a[68:71]\n"
    "ds_read_b128 a[128:131], v22\n"
    "v_mfma_f32_16x16x32_bf16 a[68:71], a[204:207], v[116:119], a[68:71]\n"
    "v_mfma_f32_16x16x32_bf16 a[36:39], a[200:203], v[104:107], a[36:39]\n"
    "v_exp_f32 v184, v184\n"
    "v_mfma_f32_16x16x32_bf16 a[36:39], a[204:207], v[108:111], a[36:39]\n"
    "v_exp_f32 v185, v185\n"
    "v_mfma_f32_16x16x32_bf16 a[4:7], a[200:203], v[96:99], a[4:7]\n"
    "ds_read_b128 a[132:135], v22 offset:1024\n"
    "v_mfma_f32_16x16x32_bf16 a[4:7], a[204:207], v[100:103], a[4:7]\n"
    "s_waitcnt lgkmcnt(10)\n"
    "v_exp_f32 v186, v186\n"
    "v_mfma_f32_16x16x32_bf16 a[104:107], a[208:211], v[120:123], a[104:107]\n"
    "v_exp_f32 v187, v187\n"
    "v_mfma_f32_16x16x32_bf16 a[104:107], a[212:215], v[124:127], a[104:107]\n"
    "v_exp_f32 v168, v168\n"
    "v_mfma_f32_16x16x32_bf16 a[72:75], a[208:211], v[112:115], a[72:75]\n"
    "ds_read_b128 a[136:139], v22 offset:2048\n"
    "v_mfma_f32_16x16x32_bf16 a[72:75], a[212:215], v[116:119], a[72:75]\n"
    "v_exp_f32 v169, v169\n"
    "v_mfma_f32_16x16x32_bf16 a[40:43], a[208:211], v[104:107], a[40:43]\n"
    "v_mfma_f32_16x16x32_bf16 a[40:43], a[212:215], v[108:111], a[40:43]\n"
    "v_exp_f32 v170, v170\n"
    "v_mfma_f32_16x16x32_bf16 a[8:11], a[208:211], v[96:99], a[8:11]\n"
    "ds_read_b128 a[140:143], v22 offset:3072\n"
    "v_mfma_f32_16x16x32_bf16 a[8:11], a[212:215], v[100:103], a[8:11]\n"
    "buffer_load_dwordx4 v21, s[56:59], s86 offen offset:2048 sc1 lds\n"
    "v_mfma_f32_16x16x32_bf16 a[108:111], a[216:219], v[120:123], a[108:111]\n"
    "v_exp_f32 v171, v171\n"
    "v_mfma_f32_16x16x32_bf16 a[108:111], a[220:223], v[124:127], a[108:111]\n"
    "v_exp_f32 v152, v152\n"
    "v_mfma_f32_16x16x32_bf16 a[76:79], a[216:219], v[112:115], a[76:79]\n"
    "ds_read_b128 a[144:147], v22 offset:4096\n"
    "v_mfma_f32_16x16x32_bf16 a[76:79], a[220:223], v[116:119], a[76:79]\n"
    "v_exp_f32 v153, v153\n"
    "v_mfma_f32_16x16x32_bf16 a[44:47], a[216:219], v[104:107], a[44:47]\n"
    "v_exp_f32 v154, v154\n"
    "v_mfma_f32_16x16x32_bf16 a[44:47], a[220:223], v[108:111], a[44:47]\n"
    "v_mfma_f32_16x16x32_bf16 a[12:15], a[216:219], v[96:99], a[12:15]\n"
    "ds_read_b128 a[148:151], v22 offset:5120\n"
    "v_mfma_f32_16x16x32_bf16 a[12:15], a[220:223], v[100:103], a[12:15]\n"
    "s_waitcnt lgkmcnt(10)\n"
    "v_exp_f32 v155, v155\n"
    "v_mfma_f32_16x16x32_bf16 a[112:115], a[224:227], v[120:123], a[112:115]\n"
    "v_exp_f32 v136, v136\n"
    "v_mfma_f32_16x16x32_bf16 a[112:115], a[228:231], v[124:127], a[112:115]\n"
    "v_exp_f32 v137, v137\n"
    "v_mfma_f32_16x16x32_bf16 a[80:83], a[224:227], v[112:115], a[80:83]\n"
    "ds_read_b128 a[152:155], v22 offset:6144\n"
    "v_mfma_f32_16x16x32_bf16 a[80:83], a[228:231], v[116:119], a[80:83]\n"
    "v_exp_f32 v138, v138\n"
    "v_mfma_f32_16x16x32_bf16 a[48:51], a[224:227], v[104:107], a[48:51]\n"
    "v_exp_f32 v139, v139\n"
    "v_mfma_f32_16x16x32_bf16 a[48:51], a[228:231], v[108:111], a[48:51]\n"
    "v_mfma_f32_16x16x32_bf16 a[16:19], a[224:227], v[96:99], a[16:19]\n"
    "ds_read_b128 a[156:159], v22 offset:7168\n"
    "v_mfma_f32_16x16x32_bf16 a[16:19], a[228:231], v[100:103], a[16:19]\n"
    "buffer_load_dwordx4 v21, s[56:59], s86 offen offset:3072 sc1 lds\n"
    "v_mfma_f32_16x16x32_bf16 a[116:119], a[232:235], v[120:123], a[116:119]\n"
    "s_add_u32 s86, s86, 0x8000\n"
    "v_exp_f32 v188, v188\n"
    "v_mfma_f32_16x16x32_bf16 a[116:119], a[236:239], v[124:127], a[116:119]\n"
    "s_mov_b32 s76, s75\n"
    "v_exp_f32 v189, v189\n"
    "v_mfma_f32_16x16x32_bf16 a[84:87], a[232:235], v[112:115], a[84:87]\n"
    "s_mov_b32 s75, s74\n"
    "v_exp_f32 v190, v190\n"
    "v_mfma_f32_16x16x32_bf16 a[84:87], a[236:239], v[116:119], a[84:87]\n"
    "s_add_u32 s74, s74, 0x8000\n"
    "ds_read_b128 a[160:163], v22 offset:8192\n"
    "v_mfma_f32_16x16x32_bf16 a[52:55], a[232:235], v[104:107], a[52:55]\n"
    "v_exp_f32 v191, v191\n"
    "v_mfma_f32_16x16x32_bf16 a[52:55], a[236:239], v[108:111], a[52:55]\n"
    "v_exp_f32 v172, v172\n"
    "v_mfma_f32_16x16x32_bf16 a[20:23], a[232:235], v[96:99], a[20:23]\n"
    "v_exp_f32 v173, v173\n"
    "v_mfma_f32_16x16x32_bf16 a[20:23], a[236:239], v[100:103], a[20:23]\n"
    "s_waitcnt lgkmcnt(9)\n"
    "ds_read_b128 a[164:167], v22 offset:9216\n"
    "v_mfma_f32_16x16x32_bf16 a[120:123], a[240:243], v[120:123], a[120:123]\n"
    "s_cmp_lt_u32 s74, 0x28000\n"
    "s_cselect_b32 s74, s74, 0\n"
    "v_mfma_f32_16x16x32_bf16 a[120:123], a[244:247], v[124:127], a[120:123]\n"
    "s_sub_u32 s87, s87, 1\n"
    "v_exp_f32 v174, v174\n"
    "v_mfma_f32_16x16x32_bf16 a[88:91], a[240:243], v[112:115], a[88:91]\n"
    "s_cmp_gt_u32 s87, 1\n"
    "v_exp_f32 v175, v175\n"
    "v_mfma_f32_16x16x32_bf16 a[88:91], a[244:247], v[116:119], a[88:91]\n"
    "ds_read_b128 a[168:171], v22 offset:10240\n"
    "v_mfma_f32_16x16x32_bf16 a[56:59], a[240:243], v[104:107], a[56:59]\n"
    "v_exp_f32 v156, v156\n"
    "v_mfma_f32_16x16x32_bf16 a[56:59], a[244:247], v[108:111], a[56:59]\n"
    "v_exp_f32 v157, v157\n"
    "v_mfma_f32_16x16x32_bf16 a[24:27], a[240:243], v[96:99], a[24:27]\n"
    "v_exp_f32 v158, v158\n"
    "v_mfma_f32_16x16x32_bf16 a[24:27], a[244:247], v[100:103], a[24:27]\n"
    "ds_read_b128 a[172:175], v22 offset:11264\n"
    "v_mfma_f32_16x16x32_bf16 a[124:127], a[248:251], v[120:123], a[124:127]\n"
    "v_mfma_f32_16x16x32_bf16 a[124:127], a[252:255], v[124:127], a[124:127]\n"
    "v_exp_f32 v159, v159\n"
    "v_mfma_f32_16x16x32_bf16 a[92:95], a[248:251], v[112:115], a[92:95]\n"
    "v_exp_f32 v140, v140\n"
    "v_mfma_f32_16x16x32_bf16 a[92:95], a[252:255], v[116:119], a[92:95]\n"
    "v_exp_f32 v141, v141\n"
    "v_mfma_f32_16x16x32_bf16 a[60:63], a[248:251], v[104:107], a[60:63]\n"
    "v_exp_f32 v142, v142\n"
    "v_mfma_f32_16x16x32_bf16 a[60:63], a[252:255], v[108:111], a[60:63]\n"
    "v_cvt_pk_bf16_f32 v120, v176, v177\n"
    "v_cvt_pk_bf16_f32 v121, v178, v179\n"
    "v_mfma_f32_16x16x32_bf16 a[28:31], a[248:251], v[96:99], a[28:31]\n"
    "v_exp_f32 v143, v143\n"
    "v_mfma_f32_16x16x32_bf16 a[28:31], a[252:255], v[100:103], a[28:31]\n"
    "s_cbranch_scc1 at_aa\n"
    "s_cmp_eq_u32 s87, 0\n"
    "s_cbranch_scc1 at_a1\n"
    "v_mov_b32 v48, v64\n"
    "v_mov_b32 v49, v65\n"
    "v_mov_b32 v50, v66\n"
    "v_mov_b32 v51, v67\n"
    "v_mov_b32 v52, v68\n"
    "v_mov_b32 v53, v69\n"
    "v_mov_b32 v54, v70\n"
    "v_mov_b32 v55, v71\n"
    "v_mov_b32 v56, v72\n"
    "v_mov_b32 v57, v73\n"
    "v_mov_b32 v58, v74\n"
    "v_mov_b32 v59, v75\n"
    "v_mov_b32 v60, v76\n"
    "v_mov_b32 v61, v77\n"
    "v_mov_b32 v62, v78\n"
    "v_mov_b32 v63, v79\n"
    "s_branch at_aa\n"
    "at_a1:\n"
    "s_waitcnt lgkmcnt(8)\n"
    "v_mfma_f32_16x16x32_bf16 v[176:179], a[128:131], v[240:243], 0\n"
    "s_add_u32 m0, s74, s84\n"
    "ds_read_b128 a[176:179], v22 offset:12288\n"
    "v_cvt_pk_bf16_f32 v113, v162, v163\n"
    "v_mfma_f32_16x16x32_bf16 v[176:179], a[132:135], v[244:247], v[176:179]\n"
    "buffer_load_dwordx4 v20, s[56:59], s86 offen sc1 lds\n"
    "v_mfma_f32_16x16x32_bf16 v[176:179], a[136:139], v[248:251], v[176:179]\n"
    "v_mfma_f32_16x16x32_bf16 v[176:179], a[140:143], v[252:255], v[176:179]\n"
    "ds_read_b128 a[180:183], v22 offset:13312\n"
    "v_cvt_pk_bf16_f32 v112, v160, v161\n"
    "v_mfma_f32_16x16x32_bf16 v[160:163], a[128:131], v[224:227], 0\n"
    "s_bfm_b64 exec, s40, 0\n buffer_load_dword v0, v2, s[48:51], s73 offen\n s_mov_b64 exec, -1\n"
    "v_mfma_f32_16x16x32_bf16 v[160:163], a[132:135], v[228:231], v[160:163]\n"
    "s_lshl_b32 s41, s40, 7\n"
    "v_mfma_f32_16x16x32_bf16 v[160:163], a[136:139], v[232:235], v[160:163]\n"
    "s_add_u32 s73, s73, s41\n"
    "v_cvt_pk_bf16_f32 v105, v146, v147\n"
    "v_mfma_f32_16x16x32_bf16 v[160:163], a[140:143], v[236:239], v[160:163]\n"
    "ds_read_b128 a[184:187], v22 offset:14336\n"
    "v_cvt_pk_bf16_f32 v104, v144, v145\n"
    "v_mfma_f32_16x16x32_bf16 v[144:147], a[128:131], v[208:211], v[32:35]\n"
    "s_add_u32 s42, s42, s43\n"
    "v_exp_f32 v176, v176\n"
    "v_mfma_f32_16x16x32_bf16 v[144:147], a[132:135], v[212:215], v[144:147]\n"
    "s_lshr_b32 s40, s42, 2\n"
    "v_exp_f32 v177, v177\n"
    "v_mfma_f32_16x16x32_bf16 v[144:147], a[136:139], v[216:219], v[144:147]\n"
    "ds_read_b128 a[188:191], v22 offset:15360\n"
    "v_cvt_pk_bf16_f32 v122, v180, v181\n"
    "v_mfma_f32_16x16x32_bf16 v[144:147], a[140:143], v[220:223], v[144:147]\n"
    "s_waitcnt lgkmcnt(8)\n"
    "v_cvt_pk_bf16_f32 v123, v182, v183\n"
    "v_mfma_f32_16x16x32_bf16 v[180:183], a[144:147], v[240:243], 0\n"
    "v_exp_f32 v178, v178\n"
    "v_mfma_f32_16x16x32_bf16 v[180:183], a[148:151], v[244:247], v[180:183]\n"
    "v_cvt_pk_bf16_f32 v115, v166, v167\n"
    "v_mfma_f32_16x16x32_bf16 v[180:183], a[152:155], v[248:251], v[180:183]\n"
    "ds_read_b128 a[192:195], v23 offset:16384\n"
    "v_cvt_pk_bf16_f32 v114, v164, v165\n"
    "v_mfma_f32_16x16x32_bf16 v[180:183], a[156:159], v[252:255], v[180:183]\n"
    "buffer_load_dwordx4 v20, s[56:59], s86 offen offset:1024 sc1 lds\n"
    "v_mfma_f32_16x16x32_bf16 v[164:167], a[144:147], v[224:227], 0\n"
    "v_exp_f32 v179, v179\n"
    "v_mfma_f32_16x16x32_bf16 v[164:167], a[148:151], v[228:231], v[164:167]\n"
    "v_mfma_f32_16x16x32_bf16 v[164:167], a[152:155], v[232:235], v[164:167]\n"
    "ds_read_b128 a[196:199], v23 offset:17408\n"
    "v_cvt_pk_bf16_f32 v106, v148, v149\n"
    "v_mfma_f32_16x16x32_bf16 v[164:167], a[156:159], v[236:239], v[164:167]\n"
    "v_cvt_pk_bf16_f32 v107, v150, v151\n"
    "v_mfma_f32_16x16x32_bf16 v[148:151], a[144:147], v[208:211], v[36:39]\n"
    "v_exp_f32 v160, v160\n"
    "v_mfma_f32_16x16x32_bf16 v[148:151], a[148:151], v[212:215], v[148:151]\n"
    "ds_read_b128 a[200:203], v23 offset:18432\n"
    "v_cvt_pk_bf16_f32 v124, v184, v185\n"
    "v_mfma_f32_16x16x32_bf16 v[148:151], a[152:155], v[216:219], v[148:151]\n"
    "v_exp_f32 v161, v161\n"
    "v_mfma_f32_16x16x32_bf16 v[148:151], a[156:159], v[220:223], v[148:151]\n"
    "s_waitcnt lgkmcnt(7)\n"
    "v_cvt_pk_bf16_f32 v125, v186, v187\n"
    "v_mfma_f32_16x16x32_bf16 v[184:187], a[160:163], v[240:243], 0\n"
    "v_exp_f32 v162, v162\n"
    "v_mfma_f32_16x16x32_bf16 v[184:187], a[164:167], v[244:247], v[184:187]\n"
    "ds_read_b128 a[204:207], v23 offset:19456\n"
    "v_cvt_pk_bf16_f32 v116, v168, v169\n"
    "v_mfma_f32_16x16x32_bf16 v[184:187], a[168:171], v[248:251], v[184:187]\n"
    "v_mfma_f32_16x16x32_bf16 v[184:187], a[172:175], v[252:255], v[184:187]\n"
    "v_cvt_pk_bf16_f32 v117, v170, v171\n"
    "v_mfma_f32_16x16x32_bf16 v[168:171], a[160:163], v[224:227], 0\n"
    "buffer_load_dwordx4 v20, s[56:59], s86 offen offset:2048 sc1 lds\n"
    "v_mfma_f32_16x16x32_bf16 v[168:171], a[164:167], v[228:231], v[168:171]\n"
    "ds_read_b128 a[208:211], v23 offset:20480\n"
    "v_cvt_pk_bf16_f32 v108, v152, v153\n"
    "v_mfma_f32_16x16x32_bf16 v[168:171], a[168:171], v[232:235], v[168:171]\n"
    "v_exp_f32 v163, v163\n"
    "v_mfma_f32_16x16x32_bf16 v[168:171], a[172:175], v[236:239], v[168:171]\n"
    "v_cvt_pk_bf16_f32 v109, v154, v155\n"
    "v_mfma_f32_16x16x32_bf16 v[152:155], a[160:163], v[208:211], v[40:43]\n"
    "ds_read_b128 a[212:215], v23 offset:21504\n"
    "v_cvt_pk_bf16_f32 v126, v188, v189\n"
    "v_mfma_f32_16x16x32_bf16 v[152:155], a[164:167], v[212:215], v[152:155]\n"
    "v_exp_f32 v144, v144\n"
    "v_mfma_f32_16x16x32_bf16 v[152:155], a[168:171], v[216:219], v[152:155]\n"
    "v_exp_f32 v145, v145\n"
    "v_mfma_f32_16x16x32_bf16 v[152:155], a[172:175], v[220:223], v[152:155]\n"
    "s_waitcnt lgkmcnt(6)\n"
    "v_cvt_pk_bf16_f32 v127, v190, v191\n"
    "v_mfma_f32_16x16x32_bf16 v[188:191], a[176:179], v[240:243], 0\n"
    "ds_read_b128 a[216:219], v23 offset:22528\n"
    "v_cvt_pk_bf16_f32 v118, v172, v173\n"
    "v_mfma_f32_16x16x32_bf16 v[188:191], a[180:183], v[244:247], v[188:191]\n"
    "v_exp_f32 v146, v146\n"
    "v_mfma_f32_16x16x32_bf16 v[188:191], a[184:187], v[248:251], v[188:191]\n"
    "v_cvt_pk_bf16_f32 v119, v174, v175\n"
    "v_mfma_f32_16x16x32_bf16 v[188:191], a[188:191], v[252:255], v[188:191]\n"
    "ds_read_b128 a[220:223], v23 offset:23552\n"
    "v_cvt_pk_bf16_f32 v97, v130, v131\n"
    "v_mfma_f32_16x16x32_bf16 v[172:175], a[176:179], v[224:227], 0\n"
    "v_cvt_pk_bf16_f32 v102, v140, v141\n"
    "v_cvt_pk_bf16_f32 v103, v142, v143\n"
    "v_mfma_f32_16x16x32_bf16 v[172:175], a[180:183], v[228:231], v[172:175]\n"
    "buffer_load_dwordx4 v20, s[56:59], s86 offen offset:3072 sc1 lds\n"
    "v_mfma_f32_16x16x32_bf16 v[172:175], a[184:187], v[232:235], v[172:175]\n"
    "s_add_u32 m0, s75, s85\n"
    "v_cvt_pk_bf16_f32 v110, v156, v157\n"
    "v_cvt_pk_bf16_f32 v111, v158, v159\n"
    "v_mfma_f32_16x16x32_bf16 v[172:175], a[188:191], v[236:239], v[172:175]\n"
    "ds_read_b128 a[224:227], v23 offset:24576\n"
    "v_cvt_pk_bf16_f32 v96, v128, v129\n"
    "v_mfma_f32_16x16x32_bf16 v[156:159], a[176:179], v[208:211], v[44:47]\n"
    "v_cvt_pk_bf16_f32 v100, v136, v137\n"
    "v_cvt_pk_bf16_f32 v101, v138, v139\n"
    "v_mfma_f32_16x16x32_bf16 v[156:159], a[180:183], v[212:215], v[156:159]\n"
    "v_cvt_pk_bf16_f32 v98, v132, v133\n"
    "v_cvt_pk_bf16_f32 v99, v134, v135\n"
    "v_mfma_f32_16x16x32_bf16 v[156:159], a[184:187], v[216:219], v[156:159]\n"
    "v_mfma_f32_16x16x32_bf16 v[156:159], a[188:191], v[220:223], v[156:159]\n"
    "ds_read_b128 a[228:231], v23 offset:25600\n"
    "v_mfma_f32_16x16x32_bf16 v[92:95], v[8:11], v[120:123], v[92:95]\n"
    "v_exp_f32 v147, v147\n"
    "v_mfma_f32_16x16x32_bf16 v[92:95], v[8:11], v[124:127], v[92:95]\n"
    "v_exp_f32 v180, v180\n"
    "v_mfma_f32_16x16x32_bf16 v[88:91], v[8:11], v[112:115], v[88:91]\n"
    "ds_read_b128 a[232:235], v23 offset:26624\n"
    "v_mfma_f32_16x16x32_bf16 v[88:91], v[8:11], v[116:119], v[88:91]\n"
    "v_exp_f32 v181, v181\n"
    "v_mfma_f32_16x16x32_bf16 v[84:87], v[8:11], v[104:107], v[84:87]\n"
    "v_mfma_f32_16x16x32_bf16 v[84:87], v[8:11], v[108:111], v[84:87]\n"
    "v_exp_f32 v182, v182\n"
    "v_mfma_f32_16x16x32_bf16 v[80:83], v[8:11], v[96:99], v[80:83]\n"
    "s_waitcnt lgkmcnt(7)\n"
    "ds_read_b128 a[236:239], v23 offset:27648\n"
    "v_mfma_f32_16x16x32_bf16 v[80:83], v[8:11], v[100:103], v[80:83]\n"
    "buffer_load_dwordx4 v21, s[56:59], s86 offen sc1 lds\n"
    "v_mfma_f32_16x16x32_bf16 a[96:99], a[192:195], v[120:123], a[96:99]\n"
    "v_exp_f32 v183, v183\n"
    "v_mfma_f32_16x16x32_bf16 a[96:99], a[196:199], v[124:127], a[96:99]\n"
    "v_exp_f32 v164, v164\n"
    "v_mfma_f32_16x16x32_bf16 a[64:67], a[192:195], v[112:115], a[64:67]\n"
    "ds_read_b128 a[240:243], v23 offset:28672\n"
    "v_mfma_f32_16x16x32_bf16 a[64:67], a[196:199], v[116:119], a[64:67]\n"
    "v_exp_f32 v165, v165\n"
    "v_mfma_f32_16x16x32_bf16 a[32:35], a[192:195], v[104:107], a[32:35]\n"
    "v_mfma_f32_16x16x32_bf16 a[32:35], a[196:199], v[108:111], a[32:35]\n"
    "ds_read_b128 a[244:247], v23 offset:29696\n"
    "v_mfma_f32_16x16x32_bf16 a[0:3], a[192:195], v[96:99], a[0:3]\n"
    "v_exp_f32 v166, v166\n"
    "v_mfma_f32_16x16x32_bf16 a[0:3], a[196:199], v[100:103], a[0:3]\n"
    "v_exp_f32 v167, v167\n"
    "v_mfma_f32_16x16x32_bf16 a[100:103], a[200:203], v[120:123], a[100:103]\n"
    "v_exp_f32 v148, v148\n"
    "v_mfma_f32_16x16x32_bf16 a[100:103], a[204:207], v[124:127], a[100:103]\n"
    "ds_read_b128 a[248:251], v23 offset:30720\n"
    "v_mfma_f32_16x16x32_bf16 a[68:71], a[200:203], v[112:115], a[68:71]\n"
    "v_mfma_f32_16x16x32_bf16 a[68:71], a[204:207], v[116:119], a[68:71]\n"
    "v_exp_f32 v149, v149\n"
    "v_mfma_f32_16x16x32_bf16 a[36:39], a[200:203], v[104:107], a[36:39]\n"
    "ds_read_b128 a[252:255], v23 offset:31744\n"
    "v_mov_b32 v23, v22\n"
    "v_add_u32 v22, s76, v24\n"
    "v_mfma_f32_16x16x32_bf16 a[36:39], a[204:207], v[108:111], a[36:39]\n"
    "buffer_load_dwordx4 v21, s[56:59], s86 offen offset:1024 sc1 lds\n"
    "v_mfma_f32_16x16x32_bf16 a[4:7], a[200:203], v[96:99], a[4:7]\n"
    "v_exp_f32 v150, v150\n"
    "v_mfma_f32_16x16x32_bf16 a[4:7], a[204:207], v[100:103], a[4:7]\n"
    "s_waitcnt vmcnt(16)\n"
    "s_barrier\n"
    "s_waitcnt lgkmcnt(8)\n"
    "v_mfma_f32_16x16x32_bf16 a[104:107], a[208:211], v[120:123], a[104:107]\n"
    "ds_read_b128 a[128:131], v22\n"
    "v_mfma_f32_16x16x32_bf16 a[104:107], a[212:215], v[124:127], a[104:107]\n"
    "v_exp_f32 v151, v151\n"
    "v_mfma_f32_16x16x32_bf16 a[72:75], a[208:211], v[112:115], a[72:75]\n"
    "v_mfma_f32_16x16x32_bf16 a[72:75], a[212:215], v[116:119], a[72:75]\n"
    "ds_read_b128 a[132:135], v22 offset:1024\n"
    "v_mfma_f32_16x16x32_bf16 a[40:43], a[208:211], v[104:107], a[40:43]\n"
    "v_exp_f32 v184, v184\n"
    "v_mfma_f32_16x16x32_bf16 a[40:43], a[212:215], v[108:111], a[40:43]\n"
    "v_exp_f32 v185, v185\n"
    "v_mfma_f32_16x16x32_bf16 a[8:11], a[208:211], v[96:99], a[8:11]\n"
    "v_exp_f32 v186, v186\n"
    "v_mfma_f32_16x16x32_bf16 a[8:11], a[212:215], v[100:103], a[8:11]\n"
    "ds_read_b128 a[136:139], v22 offset:2048\n"
    "v_mfma_f32_16x16x32_bf16 a[108:111], a[216:219], v[120:123], a[108:111]\n"
    "v_exp_f32 v187, v187\n"
    "v_mfma_f32_16x16x32_bf16 a[108:111], a[220:223], v[124:127], a[108:111]\n"
    "v_mfma_f32_16x16x32_bf16 a[76:79], a[216:219], v[112:115], a[76:79]\n"
    "ds_read_b128 a[140:143], v22 offset:3072\n"
    "v_mfma_f32_16x16x32_bf16 a[76:79], a[220:223], v[116:119], a[76:79]\n"
    "buffer_load_dwordx4 v21, s[56:59], s86 offen offset:2048 sc1 lds\n"
    "v_mfma_f32_16x16x32_bf16 a[44:47], a[216:219], v[104:107], a[44:47]\n"
    "v_exp_f32 v168, v168\n"
    "v_mfma_f32_16x16x32_bf16 a[44:47], a[220:223], v[108:111], a[44:47]\n"
    "v_exp_f32 v169, v169\n"
    "v_mfma_f32_16x16x32_bf16 a[12:15], a[216:219], v[96:99], a[12:15]\n"
    "ds_read_b128 a[144:147], v22 offset:4096\n"
    "v_mfma_f32_16x16x32_bf16 a[12:15], a[220:223], v[100:103], a[12:15]\n"
    "s_waitcnt lgkmcnt(9)\n"
    "v_exp_f32 v170, v170\n"
    "v_mfma_f32_16x16x32_bf16 a[112:115], a[224:227], v[120:123], a[112:115]\n"
    "v_mfma_f32_16x16x32_bf16 a[112:115], a[228:231], v[124:127], a[112:115]\n"
    "v_exp_f32 v171, v171\n"
    "v_mfma_f32_16x16x32_bf16 a[80:83], a[224:227], v[112:115], a[80:83]\n"
    "ds_read_b128 a[148:151], v22 offset:5120\n"
    "v_mfma_f32_16x16x32_bf16 a[80:83], a[228:231], v[116:119], a[80:83]\n"
    "v_exp_f32 v152, v152\n"
    "v_mfma_f32_16x16x32_bf16 a[48:51], a[224:227], v[104:107], a[48:51]\n"
    "v_exp_f32 v153, v153\n"
    "v_mfma_f32_16x16x32_bf16 a[48:51], a[228:231], v[108:111], a[48:51]\n"
    "ds_read_b128 a[152:155], v22 offset:6144\n"
    "v_mfma_f32_16x16x32_bf16 a[16:19], a[224:227], v[96:99], a[16:19]\n"
    "v_exp_f32 v154, v154\n"
    "v_mfma_f32_16x16x32_bf16 a[16:19], a[228:231], v[100:103], a[16:19]\n"
    "v_mfma_f32_16x16x32_bf16 a[116:119], a[232:235], v[120:123], a[116:119]\n"
    "buffer_load_dwordx4 v21, s[56:59], s86 offen offset:3072 sc1 lds\n"
    "v_mfma_f32_16x16x32_bf16 a[116:119], a[236:239], v[124:127], a[116:119]\n"
    "s_add_u32 s86, s86, 0x8000\n"
    "ds_read_b128 a[156:159], v22 offset:7168\n"
    "v_mfma_f32_16x16x32_bf16 a[84:87], a[232:235], v[112:115], a[84:87]\n"
    "s_mov_b32 s76, s75\n"
    "v_exp_f32 v155, v155\n"
    "v_mfma_f32_16x16x32_bf16 a[84:87], a[236:239], v[116:119], a[84:87]\n"
    "s_mov_b32 s75, s74\n"
    "v_exp_f32 v188, v188\n"
    "v_mfma_f32_16x16x32_bf16 a[52:55], a[232:235], v[104:107], a[52:55]\n"
    "s_add_u32 s74, s74, 0x8000\n"
    "v_exp_f32 v189, v189\n"
    "v_mfma_f32_16x16x32_bf16 a[52:55], a[236:239], v[108:111], a[52:55]\n"
    "ds_read_b128 a[160:163], v22 offset:8192\n"
    "v_mfma_f32_16x16x32_bf16 a[20:23], a[232:235], v[96:99], a[20:23]\n"
    "s_cmp_lt_u32 s74, 0x28000\n"
    "s_cselect_b32 s74, s74, 0\n"
    "v_mfma_f32_16x16x32_bf16 a[20:23], a[236:239], v[100:103], a[20:23]\n"
    "s_waitcnt lgkmcnt(9)\n"
    "v_exp_f32 v190, v190\n"
    "v_mfma_f32_16x16x32_bf16 a[120:123], a[240:243], v[120:123], a[120:123]\n"
    "ds_read_b128 a[164:167], v22 offset:9216\n"
    "v_mfma_f32_16x16x32_bf16 a[120:123], a[244:247], v[124:127], a[120:123]\n"
    "v_exp_f32 v191, v191\n"
    "v_mfma_f32_16x16x32_bf16 a[88:91], a[240:243], v[112:115], a[88:91]\n"
    "v_exp_f32 v172, v172\n"
    "v_mfma_f32_16x16x32_bf16 a[88:91], a[244:247], v[116:119], a[88:91]\n"
    "v_exp_f32 v173, v173\n"
    "v_mfma_f32_16x16x32_bf16 a[56:59], a[240:243], v[104:107], a[56:59]\n"
    "ds_read_b128 a[168:171], v22 offset:10240\n"
    "v_mfma_f32_16x16x32_bf16 a[56:59], a[244:247], v[108:111], a[56:59]\n"
    "v_mfma_f32_16x16x32_bf16 a[24:27], a[240:243], v[96:99], a[24:27]\n"
    "v_exp_f32 v174, v174\n"
    "v_mfma_f32_16x16x32_bf16 a[24:27], a[244:247], v[100:103], a[24:27]\n"
    "ds_read_b128 a[172:175], v22 offset:11264\n"
    "v_mfma_f32_16x16x32_bf16 a[124:127], a[248:251], v[120:123], a[124:127]\n"
    "v_exp_f32 v175, v175\n"
    "v_mfma_f32_16x16x32_bf16 a[124:127], a[252:255], v[124:127], a[124:127]\n"
    "v_exp_f32 v156, v156\n"
    "v_mfma_f32_16x16x32_bf16 a[92:95], a[248:251], v[112:115], a[92:95]\n"
    "v_mfma_f32_16x16x32_bf16 a[92:95], a[252:255], v[116:119], a[92:95]\n"
    "v_exp_f32 v157, v157\n"
    "v_mfma_f32_16x16x32_bf16 a[60:63], a[248:251], v[104:107], a[60:63]\n"
    "v_exp_f32 v158, v158\n"
    "v_mfma_f32_16x16x32_bf16 a[60:63], a[252:255], v[108:111], a[60:63]\n"
    "v_cvt_pk_bf16_f32 v120, v176, v177\n"
    "v_cvt_pk_bf16_f32 v121, v178, v179\n"
    "v_mfma_f32_16x16x32_bf16 a[28:31], a[248:251], v[96:99], a[28:31]\n"
    "v_exp_f32 v159, v159\n"
    "v_mfma_f32_16x16x32_bf16 a[28:31], a[252:255], v[100:103], a[28:31]\n"
    "s_lshl_b32 s87, s80, 2\n"
    "s_sub_u32 s87, 28, s87\n"
    "s_cmp_eq_u32 s87, 0\n"
    "s_cbranch_scc1 at_ba\n"
    "v_mov_b32 v48, 0\n"
    "v_mov_b32 v49, 0\n"
    "v_mov_b32 v50, 0\n"
    "v_mov_b32 v51, 0\n"
    "v_mov_b32 v52, 0\n"
    "v_mov_b32 v53, 0\n"
    "v_mov_b32 v54, 0\n"
    "v_mov_b32 v55, 0\n"
    "v_mov_b32 v56, 0\n"
    "v_mov_b32 v57, 0\n"
    "v_mov_b32 v58, 0\n"
    "v_mov_b32 v59, 0\n"
    "v_mov_b32 v60, 0\n"
    "v_mov_b32 v61, 0\n"
    "v_mov_b32 v62, 0\n"
    "v_mov_b32 v63, 0\n"
    "at_ba:\n"
    "s_waitcnt lgkmcnt(8)\n"
    "v_mfma_f32_16x16x32_bf16 v[176:179], a[128:131], v[240:243], 0\n"
    "s_add_u32 m0, s74, s84\n"
    "ds_read_b128 a[176:179], v22 offset:12288\n"
    "v_cvt_pk_bf16_f32 v113, v162, v163\n"
    "v_mfma_f32_16x16x32_bf16 v[176:179], a[132:135], v[244:247], v[176:179]\n"
    "buffer_load_dwordx4 v20, s[56:59], s86 offen sc1 lds\n"
    "v_mfma_f32_16x16x32_bf16 v[176:179], a[136:139], v[248:251], v[176:179]\n"
    "ds_read_b128 a[180:183], v22 offset:13312\n"
    "v_cvt_pk_bf16_f32 v112, v160, v161\n"
    "v_mfma_f32_16x16x32_bf16 v[176:179], a[140:143], v[252:255], v[176:179]\n"
    "v_mfma_f32_16x16x32_bf16 v[160:163], a[128:131], v[224:227], v[48:51]\n"
    "v_mfma_f32_16x16x32_bf16 v[160:163], a[132:135], v[228:231], v[160:163]\n"
    "ds_read_b128 a[184:187], v22 offset:14336\n"
    "v_cvt_pk_bf16_f32 v123, v182, v183\n"
    "v_mfma_f32_16x16x32_bf16 v[160:163], a[136:139], v[232:235], v[160:163]\n"
    "s_waitcnt lgkmcnt(7)\n"
    "v_mfma_f32_16x16x32_bf16 v[160:163], a[140:143], v[236:239], v[160:163]\n"
    "ds_read_b128 a[188:191], v22 offset:15360\n"
    "v_cvt_pk_bf16_f32 v122, v180, v181\n"
    "v_mfma_f32_16x16x32_bf16 v[180:183], a[144:147], v[240:243], 0\n"
    "v_exp_f32 v176, v176\n"
    "v_mfma_f32_16x16x32_bf16 v[180:183], a[148:151], v[244:247], v[180:183]\n"
    "v_cvt_pk_bf16_f32 v115, v166, v167\n"
    "v_mfma_f32_16x16x32_bf16 v[180:183], a[152:155], v[248:251], v[180:183]\n"
    "ds_read_b128 a[192:195], v23 offset:16384\n"
    "v_cvt_pk_bf16_f32 v114, v164, v165\n"
    "v_mfma_f32_16x16x32_bf16 v[180:183], a[156:159], v[252:255], v[180:183]\n"
    "buffer_load_dwordx4 v20, s[56:59], s86 offen offset:1024 sc1 lds\n"
    "v_mfma_f32_16x16x32_bf16 v[164:167], a[144:147], v[224:227], v[52:55]\n"
    "ds_read_b128 a[196:199], v23 offset:17408\n"
    "v_cvt_pk_bf16_f32 v125, v186, v187\n"
    "v_mfma_f32_16x16x32_bf16 v[164:167], a[148:151], v[228:231], v[164:167]\n"
    "v_exp_f32 v177, v177\n"
    "v_mfma_f32_16x16x32_bf16 v[164:167], a[152:155], v[232:235], v[164:167]\n"
    "s_waitcnt lgkmcnt(6)\n"
    "v_exp_f32 v178, v178\n"
    "v_mfma_f32_16x16x32_bf16 v[164:167], a[156:159], v[236:239], v[164:167]\n"
    "ds_read_b128 a[200:203], v23 offset:18432\n"
    "v_cvt_pk_bf16_f32 v124, v184, v185\n"
    "v_mfma_f32_16x16x32_bf16 v[184:187], a[160:163], v[240:243], 0\n"
    "v_exp_f32 v179, v179\n"
    "v_mfma_f32_16x16x32_bf16 v[184:187], a[164:167], v[244:247], v[184:187]\n"
    "ds_read_b128 a[204:207], v23 offset:19456\n"
    "v_cvt_pk_bf16_f32 v116, v168, v169\n"
    "v_mfma_f32_16x16x32_bf16 v[184:187], a[168:171], v[248:251], v[184:187]\n"
    "v_cvt_pk_bf16_f32 v117, v170, v171\n"
    "v_mfma_f32_16x16x32_bf16 v[184:187], a[172:175], v[252:255], v[184:187]\n"
    "buffer_load_dwordx4 v20, s[56:59], s86 offen offset:2048 sc1 lds\n"
    "v_mfma_f32_16x16x32_bf16 v[168:171], a[160:163], v[224:227], v[56:59]\n"
    "ds_read_b128 a[208:211], v23 offset:20480\n"
    "v_cvt_pk_bf16_f32 v126, v188, v189\n"
    "v_mfma_f32_16x16x32_bf16 v[168:171], a[164:167], v[228:231], v[168:171]\n"
    "v_exp_f32 v160, v160\n"
    "v_mfma_f32_16x16x32_bf16 v[168:171], a[168:171], v[232:235], v[168:171]\n"
    "s_waitcnt lgkmcnt(5)\n"
    "v_cvt_pk_bf16_f32 v127, v190, v191\n"
    "v_cvt_pk_bf16_f32 v111, v158, v159\n"
    "v_mfma_f32_16x16x32_bf16 v[168:171], a[172:175], v[236:239], v[168:171]\n"
    "ds_read_b128 a[212:215], v23 offset:21504\n"
    "v_cvt_pk_bf16_f32 v106, v148, v149\n"
    "v_mfma_f32_16x16x32_bf16 v[188:191], a[176:179], v[240:243], 0\n"
    "v_cvt_pk_bf16_f32 v119, v174, v175\n"
    "v_cvt_pk_bf16_f32 v110, v156, v157\n"
    "v_mfma_f32_16x16x32_bf16 v[188:191], a[180:183], v[244:247], v[188:191]\n"
    "ds_read_b128 a[216:219], v23 offset:22528\n"
    "v_cvt_pk_bf16_f32 v105, v146, v147\n"
    "v_mfma_f32_16x16x32_bf16 v[188:191], a[184:187], v[248:251], v[188:191]\n"
    "v_cvt_pk_bf16_f32 v109, v154, v155\n"
    "v_cvt_pk_bf16_f32 v118, v172, v173\n"
    "v_mfma_f32_16x16x32_bf16 v[188:191], a[188:191], v[252:255], v[188:191]\n"
    "v_cvt_pk_bf16_f32 v107, v150, v151\n"
    "v_cvt_pk_bf16_f32 v108, v152, v153\n"
    "v_mfma_f32_16x16x32_bf16 v[172:175], a[176:179], v[224:227], v[60:63]\n"
    "ds_read_b128 a[220:223], v23 offset:23552\n"
    "v_cvt_pk_bf16_f32 v104, v144, v145\n"
    "v_mfma_f32_16x16x32_bf16 v[172:175], a[180:183], v[228:231], v[172:175]\n"
    "buffer_load_dwordx4 v20, s[56:59], s86 offen offset:3072 sc1 lds\n"
    "v_mfma_f32_16x16x32_bf16 v[172:175], a[184:187], v[232:235], v[172:175]\n"
    "s_add_u32 m0, s75, s85\n"
    "ds_read_b128 a[224:227], v23 offset:24576\n"
    "v_mfma_f32_16x16x32_bf16 v[172:175], a[188:191], v[236:239], v[172:175]\n"
    "v_exp_f32 v161, v161\n"
    "v_mfma_f32_16x16x32_bf16 v[92:95], v[8:11], v[120:123], v[92:95]\n"
    "v_exp_f32 v162, v162\n"
    "v_mfma_f32_16x16x32_bf16 v[92:95], v[8:11], v[124:127], v[92:95]\n"
    "ds_read_b128 a[228:231], v23 offset:25600\n"
    "v_mfma_f32_16x16x32_bf16 v[88:91], v[8:11], v[112:115], v[88:91]\n"
    "v_exp_f32 v163, v163\n"
    "v_mfma_f32_16x16x32_bf16 v[88:91], v[8:11], v[116:119], v[88:91]\n"
    "ds_read_b128 a[232:235], v23 offset:26624\n"
    "v_mfma_f32_16x16x32_bf16 v[84:87], v[8:11], v[104:107], v[84:87]\n"
    "v_exp_f32 v180, v180\n"
    "v_mfma_f32_16x16x32_bf16 v[84:87], v[8:11], v[108:111], v[84:87]\n"
    "s_waitcnt lgkmcnt(7)\n"
    "v_exp_f32 v181, v181\n"
    "v_mfma_f32_16x16x32_bf16 a[96:99], a[192:195], v[120:123], a[96:99]\n"
    "ds_read_b128 a[236:239], v23 offset:27648\n"
    "v_mfma_f32_16x16x32_bf16 a[96:99], a[196:199], v[124:127], a[96:99]\n"
    "buffer_load_dwordx4 v21, s[56:59], s86 offen sc1 lds\n"
    "v_mfma_f32_16x16x32_bf16 a[64:67], a[192:195], v[112:115], a[64:67]\n"
    "v_exp_f32 v182, v182\n"
    "v_mfma_f32_16x16x32_bf16 a[64:67], a[196:199], v[116:119], a[64:67]\n"
    "ds_read_b128 a[240:243], v23 offset:28672\n"
    "v_mfma_f32_16x16x32_bf16 a[32:35], a[192:195], v[104:107], a[32:35]\n"
    "v_mfma_f32_16x16x32_bf16 a[32:35], a[196:199], v[108:111], a[32:35]\n"
    "ds_read_b128 a[244:247], v23 offset:29696\n"
    "v_mfma_f32_16x16x32_bf16 a[100:103], a[200:203], v[120:123], a[100:103]\n"
    "v_exp_f32 v183, v183\n"
    "v_mfma_f32_16x16x32_bf16 a[100:103], a[204:207], v[124:127], a[100:103]\n"
    "v_exp_f32 v164, v164\n"
    "v_mfma_f32_16x16x32_bf16 a[68:71], a[200:203], v[112:115], a[68:71]\n"
    "ds_read_b128 a[248:251], v23 offset:30720\n"
    "v_mfma_f32_16x16x32_bf16 a[68:71], a[204:207], v[116:119], a[68:71]\n"
    "buffer_load_dwordx4 v21, s[56:59], s86 offen offset:1024 sc1 lds\n"
    "v_mfma_f32_16x16x32_bf16 a[36:39], a[200:203], v[104:107], a[36:39]\n"
    "ds_read_b128 a[252:255], v23 offset:31744\n"
    "v_mov_b32 v23, v22\n"
    "v_mfma_f32_16x16x32_bf16 a[36:39], a[204:207], v[108:111], a[36:39]\n"
    "s_waitcnt lgkmcnt(8)\n"
    "v_exp_f32 v165, v165\n"
    "v_mfma_f32_16x16x32_bf16 a[104:107], a[208:211], v[120:123], a[104:107]\n"
    "s_waitcnt vmcnt(15)\n"
    "s_barrier\n"
    "v_add_u32 v22, s76, v24\n"
    "v_mfma_f32_16x16x32_bf16 a[104:107], a[212:215], v[124:127], a[104:107]\n"
    "ds_read_b128 a[128:131], v22\n"
    "v_mfma_f32_16x16x32_bf16 a[72:75], a[208:211], v[112:115], a[72:75]\n"
    "v_exp_f32 v166, v166\n"
    "v_mfma_f32_16x16x32_bf16 a[72:75], a[212:215], v[116:119], a[72:75]\n"
    "ds_read_b128 a[132:135], v22 offset:1024\n"
    "v_mfma_f32_16x16x32_bf16 a[40:43], a[208:211], v[104:107], a[40:43]\n"
    "v_exp_f32 v167, v167\n"
    "v_mfma_f32_16x16x32_bf16 a[40:43], a[212:215], v[108:111], a[40:43]\n"
    "v_exp_f32 v184, v184\n"
    "v_mfma_f32_16x16x32_bf16 a[108:111], a[216:219], v[120:123], a[108:111]\n"
    "ds_read_b128 a[136:139], v22 offset:2048\n"
    "v_mfma_f32_16x16x32_bf16 a[108:111], a[220:223], v[124:127], a[108:111]\n"
    "buffer_load_dwordx4 v21, s[56:59], s86 offen offset:2048 sc1 lds\n"
    "v_mfma_f32_16x16x32_bf16 a[76:79], a[216:219], v[112:115], a[76:79]\n"
    "ds_read_b128 a[140:143], v22 offset:3072\n"
    "v_mfma_f32_16x16x32_bf16 a[76:79], a[220:223], v[116:119], a[76:79]\n"
    "v_exp_f32 v185, v185\n"
    "v_mfma_f32_16x16x32_bf16 a[44:47], a[216:219], v[104:107], a[44:47]\n"
    "v_exp_f32 v186, v186\n"
    "v_mfma_f32_16x16x32_bf16 a[44:47], a[220:223], v[108:111], a[44:47]\n"
    "s_waitcnt lgkmcnt(8)\n"
    "ds_read_b128 a[144:147], v22 offset:4096\n"
    "v_mfma_f32_16x16x32_bf16 a[112:115], a[224:227], v[120:123], a[112:115]\n"
    "v_exp_f32 v187, v187\n"
    "v_mfma_f32_16x16x32_bf16 a[112:115], a[228:231], v[124:127], a[112:115]\n"
    "ds_read_b128 a[148:151], v22 offset:5120\n"
    "v_mfma_f32_16x16x32_bf16 a[80:83], a[224:227], v[112:115], a[80:83]\n"
    "v_exp_f32 v168, v168\n"
    "v_mfma_f32_16x16x32_bf16 a[80:83], a[228:231], v[116:119], a[80:83]\n"
    "v_exp_f32 v169, v169\n"
    "v_mfma_f32_16x16x32_bf16 a[48:51], a[224:227], v[104:107], a[48:51]\n"
    "ds_read_b128 a[152:155], v22 offset:6144\n"
    "v_mfma_f32_16x16x32_bf16 a[48:51], a[228:231], v[108:111], a[48:51]\n"
    "buffer_load_dwordx4 v21, s[56:59], s86 offen offset:3072 sc1 lds\n"
    "v_mfma_f32_16x16x32_bf16 a[116:119], a[232:235], v[120:123], a[116:119]\n"
    "s_add_u32 s86, s86, 0x8000\n"
    "ds_read_b128 a[156:159], v22 offset:7168\n"
    "v_mfma_f32_16x16x32_bf16 a[116:119], a[236:239], v[124:127], a[116:119]\n"
    "s_mov_b32 s76, s75\n"
    "v_mfma_f32_16x16x32_bf16 a[84:87], a[232:235], v[112:115], a[84:87]\n"
    "s_mov_b32 s75, s74\n"
    "v_exp_f32 v170, v170\n"
    "v_mfma_f32_16x16x32_bf16 a[84:87], a[236:239], v[116:119], a[84:87]\n"
    "s_add_u32 s74, s74, 0x8000\n"
    "ds_read_b128 a[160:163], v22 offset:8192\n"
    "v_mfma_f32_16x16x32_bf16 a[52:55], a[232:235], v[104:107], a[52:55]\n"
    "v_exp_f32 v171, v171\n"
    "v_mfma_f32_16x16x32_bf16 a[52:55], a[236:239], v[108:111], a[52:55]\n"
    "s_waitcnt lgkmcnt(9)\n"
    "v_exp_f32 v188, v188\n"
    "v_mfma_f32_16x16x32_bf16 a[120:123], a[240:243], v[120:123], a[120:123]\n"
    "ds_read_b128 a[164:167], v22 offset:9216\n"
    "v_mfma_f32_16x16x32_bf16 a[120:123], a[244:247], v[124:127], a[120:123]\n"
    "v_exp_f32 v189, v189\n"
    "v_mfma_f32_16x16x32_bf16 a[88:91], a[240:243], v[112:115], a[88:91]\n"
    "ds_read_b128 a[168:171], v22 offset:10240\n"
    "v_mfma_f32_16x16x32_bf16 a[88:91], a[244:247], v[116:119], a[88:91]\n"
    "v_exp_f32 v190, v190\n"
    "v_mfma_f32_16x16x32_bf16 a[56:59], a[240:243], v[104:107], a[56:59]\n"
    "v_exp_f32 v191, v191\n"
    "v_mfma_f32_16x16x32_bf16 a[56:59], a[244:247], v[108:111], a[56:59]\n"
    "ds_read_b128 a[172:175], v22 offset:11264\n"
    "v_mfma_f32_16x16x32_bf16 a[124:127], a[248:251], v[120:123], a[124:127]\n"
    "v_exp_f32 v172, v172\n"
    "v_mfma_f32_16x16x32_bf16 a[124:127], a[252:255], v[124:127], a[124:127]\n"
    "v_exp_f32 v173, v173\n"
    "v_mfma_f32_16x16x32_bf16 a[92:95], a[248:251], v[112:115], a[92:95]\n"
    "v_exp_f32 v174, v174\n"
    "v_mfma_f32_16x16x32_bf16 a[92:95], a[252:255], v[116:119], a[92:95]\n"
    "v_cvt_pk_bf16_f32 v120, v176, v177\n"
    "v_cvt_pk_bf16_f32 v121, v178, v179\n"
    "v_mfma_f32_16x16x32_bf16 a[60:63], a[248:251], v[104:107], a[60:63]\n"
    "v_exp_f32 v175, v175\n"
    "v_mfma_f32_16x16x32_bf16 a[60:63], a[252:255], v[108:111], a[60:63]\n"
    "s_cmp_lt_u32 s74, 0x28000\n"
    "s_cselect_b32 s74, s74, 0\n"
    "s_getpc_b64 s[52:53]\n s_add_u32 s52, s52, k_oproj@rel32@lo+4\n s_addc_u32 s53, s53, k_oproj@rel32@hi+12\n"
    "s_mov_b32 s54, k_oproj_code_end-k_oproj\n"
    "s_mov_b32 s55, 0x20000\n"
    "s_lshr_b32 s81, s30, 3\n"
    "s_lshl_b32 s81, s81, 2\n"
    "s_add_u32 s81, s81, s78\n"
    "s_lshl_b32 s81, s81, 7\n"
    "v_lshlrev_b32 v26, 14, v1\n"
    "s_mov_b64 exec, 3\n buffer_load_dword v0, v26, s[52:55], s81 offen\n s_mov_b64 exec, -1\n"
    "s_getpc_b64 s[52:53]\n s_add_u32 s52, s52, k_ffn1@rel32@lo+4\n s_addc_u32 s53, s53, k_ffn1@rel32@hi+12\n"
    "s_mov_b32 s54, k_ffn2_code_end-k_ffn1\n"
    "s_mov_b32 s55, 0x20000\n"
    "s_lshl_b32 s81, s30, 2\n"
    "s_add_u32 s81, s81, s78\n"
    "s_lshl_b32 s81, s81, 7\n"
    "buffer_load_dword v0, off, s[52:55], s81\n"
    "s_nop 6\n"
    "v_mov_b32 v28, 0\n"
    "v_mov_b32 v29, 0\n"
    "v_mov_b32 v26, 0\n"
    "v_mov_b32 v27, 0\n"
    "s_lshl_b32 s81, s79, 19\n"
    "s_add_u32 s82, s81, 0x40000\n"
    "v_rcp_f32 v30, v80\n"
    "v_accvgpr_read_b32 v128, a0\n"
    "v_accvgpr_read_b32 v129, a1\n"
    "v_accvgpr_read_b32 v130, a2\n"
    "v_accvgpr_read_b32 v131, a3\n"
    "v_accvgpr_read_b32 v132, a4\n"
    "v_accvgpr_read_b32 v133, a5\n"
    "v_accvgpr_read_b32 v134, a6\n"
    "v_accvgpr_read_b32 v135, a7\n"
    "v_pk_fma_f32 v[28:29], v[128:129], v[26:27], v[28:29]\n"
    "v_pk_fma_f32 v[28:29], v[130:131], v[26:27], v[28:29]\n"
    "v_pk_fma_f32 v[28:29], v[132:133], v[26:27], v[28:29]\n"
    "v_pk_fma_f32 v[28:29], v[134:135], v[26:27], v[28:29]\n"
    "v_pk_mul_f32 v[128:129], v[128:129], v[30:31] op_sel_hi:[1,0]\n"
    "v_pk_mul_f32 v[130:131], v[130:131], v[30:31] op_sel_hi:[1,0]\n"
    "v_pk_mul_f32 v[132:133], v[132:133], v[30:31] op_sel_hi:[1,0]\n"
    "v_pk_mul_f32 v[134:135], v[134:135], v[30:31] op_sel_hi:[1,0]\n"
    "v_cvt_pk_bf16_f32 v144, v128, v129\n"
    "v_cvt_pk_bf16_f32 v145, v130, v131\n"
    "v_cvt_pk_bf16_f32 v146, v132, v133\n"
    "v_cvt_pk_bf16_f32 v147, v134, v135\n"
    "buffer_store_dwordx4 v[144:147], v4, s[60:63], s81 offen sc1\n"
    "v_accvgpr_read_b32 v136, a8\n"
    "v_accvgpr_read_b32 v137, a9\n"
    "v_accvgpr_read_b32 v138, a10\n"
    "v_accvgpr_read_b32 v139, a11\n"
    "v_accvgpr_read_b32 v140, a12\n"
    "v_accvgpr_read_b32 v141, a13\n"
    "v_accvgpr_read_b32 v142, a14\n"
    "v_accvgpr_read_b32 v143, a15\n"
    "v_pk_fma_f32 v[28:29], v[136:137], v[26:27], v[28:29]\n"
    "v_pk_fma_f32 v[28:29], v[138:139], v[26:27], v[28:29]\n"
    "v_pk_fma_f32 v[28:29], v[140:141], v[26:27], v[28:29]\n"
    "v_pk_fma_f32 v[28:29], v[142:143], v[26:27], v[28:29]\n"
    "v_pk_mul_f32 v[136:137], v[136:137], v[30:31] op_sel_hi:[1,0]\n"
    "v_pk_mul_f32 v[138:139], v[138:139], v[30:31] op_sel_hi:[1,0]\n"
    "v_pk_mul_f32 v[140:141], v[140:141], v[30:31] op_sel_hi:[1,0]\n"
    "v_pk_mul_f32 v[142:143], v[142:143], v[30:31] op_sel_hi:[1,0]\n"
    "v_cvt_pk_bf16_f32 v148, v136, v137\n"
    "v_cvt_pk_bf16_f32 v149, v138, v139\n"
    "v_cvt_pk_bf16_f32 v150, v140, v141\n"
    "v_cvt_pk_bf16_f32 v151, v142, v143\n"
    "buffer_store_dwordx4 v[148:151], v4, s[60:63], s81 offen offset:1024 sc1\n"
    "v_accvgpr_read_b32 v128, a16\n"
    "v_accvgpr_read_b32 v129, a17\n"
    "v_accvgpr_read_b32 v130, a18\n"
    "v_accvgpr_read_b32 v131, a19\n"
    "v_accvgpr_read_b32 v132, a20\n"
    "v_accvgpr_read_b32 v133, a21\n"
    "v_accvgpr_read_b32 v134, a22\n"
    "v_accvgpr_read_b32 v135, a23\n"
    "v_pk_fma_f32 v[28:29], v[128:129], v[26:27], v[28:29]\n"
    "v_pk_fma_f32 v[28:29], v[130:131], v[26:27], v[28:29]\n"
    "v_pk_fma_f32 v[28:29], v[132:133], v[26:27], v[28:29]\n"
    "v_pk_fma_f32 v[28:29], v[134:135], v[26:27], v[28:29]\n"
    "v_pk_mul_f32 v[128:129], v[128:129], v[30:31] op_sel_hi:[1,0]\n"
    "v_pk_mul_f32 v[130:131], v[130:131], v[30:31] op_sel_hi:[1,0]\n"
    "v_pk_mul_f32 v[132:133], v[132:133], v[30:31] op_sel_hi:[1,0]\n"
    "v_pk_mul_f32 v[134:135], v[134:135], v[30:31] op_sel_hi:[1,0]\n"
    "v_cvt_pk_bf16_f32 v152, v128, v129\n"
    "v_cvt_pk_bf16_f32 v153, v130, v131\n"
    "v_cvt_pk_bf16_f32 v154, v132, v133\n"
    "v_cvt_pk_bf16_f32 v155, v134, v135\n"
    "buffer_store_dwordx4 v[152:155], v4, s[60:63], s82 offen sc1\n"
    "v_accvgpr_read_b32 v136, a24\n"
    "v_accvgpr_read_b32 v137, a25\n"
    "v_accvgpr_read_b32 v138, a26\n"
    "v_accvgpr_read_b32 v139, a27\n"
    "v_accvgpr_read_b32 v140, a28\n"
    "v_accvgpr_read_b32 v141, a29\n"
    "v_accvgpr_read_b32 v142, a30\n"
    "v_accvgpr_read_b32 v143, a31\n"
    "v_pk_fma_f32 v[28:29], v[136:137], v[26:27], v[28:29]\n"
    "v_pk_fma_f32 v[28:29], v[138:139], v[26:27], v[28:29]\n"
    "v_pk_fma_f32 v[28:29], v[140:141], v[26:27], v[28:29]\n"
    "v_pk_fma_f32 v[28:29], v[142:143], v[26:27], v[28:29]\n"
    "v_pk_mul_f32 v[136:137], v[136:137], v[30:31] op_sel_hi:[1,0]\n"
    "v_pk_mul_f32 v[138:139], v[138:139], v[30:31] op_sel_hi:[1,0]\n"
    "v_pk_mul_f32 v[140:141], v[140:141], v[30:31] op_sel_hi:[1,0]\n"
    "v_pk_mul_f32 v[142:143], v[142:143], v[30:31] op_sel_hi:[1,0]\n"
    "v_cvt_pk_bf16_f32 v156, v136, v137\n"
    "v_cvt_pk_bf16_f32 v157, v138, v139\n"
    "v_cvt_pk_bf16_f32 v158, v140, v141\n"
    "v_cvt_pk_bf16_f32 v159, v142, v143\n"
    "buffer_store_dwordx4 v[156:159], v4, s[60:63], s82 offen offset:1024 sc1\n"
    "v_rcp_f32 v30, v84\n"
    "v_accvgpr_read_b32 v128, a32\n"
    "v_accvgpr_read_b32 v129, a33\n"
    "v_accvgpr_read_b32 v130, a34\n"
    "v_accvgpr_read_b32 v131, a35\n"
    "v_accvgpr_read_b32 v132, a36\n"
    "v_accvgpr_read_b32 v133, a37\n"
    "v_accvgpr_read_b32 v134, a38\n"
    "v_accvgpr_read_b32 v135, a39\n"
    "v_pk_fma_f32 v[28:29], v[128:129], v[26:27], v[28:29]\n"
    "v_pk_fma_f32 v[28:29], v[130:131], v[26:27], v[28:29]\n"
    "v_pk_fma_f32 v[28:29], v[132:133], v[26:27], v[28:29]\n"
    "v_pk_fma_f32 v[28:29], v[134:135], v[26:27], v[28:29]\n"
    "v_pk_mul_f32 v[128:129], v[128:129], v[30:31] op_sel_hi:[1,0]\n"
    "v_pk_mul_f32 v[130:131], v[130:131], v[30:31] op_sel_hi:[1,0]\n"
    "v_pk_mul_f32 v[132:133], v[132:133], v[30:31] op_sel_hi:[1,0]\n"
    "v_pk_mul_f32 v[134:135], v[134:135], v[30:31] op_sel_hi:[1,0]\n"
    "v_cvt_pk_bf16_f32 v144, v128, v129\n"
    "v_cvt_pk_bf16_f32 v145, v130, v131\n"
    "v_cvt_pk_bf16_f32 v146, v132, v133\n"
    "v_cvt_pk_bf16_f32 v147, v134, v135\n"
    "buffer_store_dwordx4 v[144:147], v5, s[60:63], s81 offen sc1\n"
    "v_accvgpr_read_b32 v136, a40\n"
    "v_accvgpr_read_b32 v137, a41\n"
    "v_accvgpr_read_b32 v138, a42\n"
    "v_accvgpr_read_b32 v139, a43\n"
    "v_accvgpr_read_b32 v140, a44\n"
    "v_accvgpr_read_b32 v141, a45\n"
    "v_accvgpr_read_b32 v142, a46\n"
    "v_accvgpr_read_b32 v143, a47\n"
    "v_pk_fma_f32 v[28:29], v[136:137], v[26:27], v[28:29]\n"
    "v_pk_fma_f32 v[28:29], v[138:139], v[26:27], v[28:29]\n"
    "v_pk_fma_f32 v[28:29], v[140:141], v[26:27], v[28:29]\n"
    "v_pk_fma_f32 v[28:29], v[142:143], v[26:27], v[28:29]\n"
    "v_pk_mul_f32 v[136:137], v[136:137], v[30:31] op_sel_hi:[1,0]\n"
    "v_pk_mul_f32 v[138:139], v[138:139], v[30:31] op_sel_hi:[1,0]\n"
    "v_pk_mul_f32 v[140:141], v[140:141], v[30:31] op_sel_hi:[1,0]\n"
    "v_pk_mul_f32 v[142:143], v[142:143], v[30:31] op_sel_hi:[1,0]\n"
    "v_cvt_pk_bf16_f32 v148, v136, v137\n"
    "v_cvt_pk_bf16_f32 v149, v138, v139\n"
    "v_cvt_pk_bf16_f32 v150, v140, v141\n"
    "v_cvt_pk_bf16_f32 v151, v142, v143\n"
    "buffer_store_dwordx4 v[148:151], v5, s[60:63], s81 offen offset:1024 sc1\n"
    "v_accvgpr_read_b32 v128, a48\n"
    "v_accvgpr_read_b32 v129, a49\n"
    "v_accvgpr_read_b32 v130, a50\n"
    "v_accvgpr_read_b32 v131, a51\n"
    "v_accvgpr_read_b32 v132, a52\n"
    "v_accvgpr_read_b32 v133, a53\n"
    "v_accvgpr_read_b32 v134, a54\n"
    "v_accvgpr_read_b32 v135, a55\n"
    "v_pk_fma_f32 v[28:29], v[128:129], v[26:27], v[28:29]\n"
    "v_pk_fma_f32 v[28:29], v[130:131], v[26:27], v[28:29]\n"
    "v_pk_fma_f32 v[28:29], v[132:133], v[26:27], v[28:29]\n"
    "v_pk_fma_f32 v[28:29], v[134:135], v[26:27], v[28:29]\n"
    "v_pk_mul_f32 v[128:129], v[128:129], v[30:31] op_sel_hi:[1,0]\n"
    "v_pk_mul_f32 v[130:131], v[130:131], v[30:31] op_sel_hi:[1,0]\n"
    "v_pk_mul_f32 v[132:133], v[132:133], v[30:31] op_sel_hi:[1,0]\n"
    "v_pk_mul_f32 v[134:135], v[134:135], v[30:31] op_sel_hi:[1,0]\n"
    "v_cvt_pk_bf16_f32 v152, v128, v129\n"
    "v_cvt_pk_bf16_f32 v153, v130, v131\n"
    "v_cvt_pk_bf16_f32 v154, v132, v133\n"
    "v_cvt_pk_bf16_f32 v155, v134, v135\n"
    "buffer_store_dwordx4 v[152:155], v5, s[60:63], s82 offen sc1\n"
    "v_accvgpr_read_b32 v136, a56\n"
    "v_accvgpr_read_b32 v137, a57\n"
    "v_accvgpr_read_b32 v138, a58\n"
    "v_accvgpr_read_b32 v139, a59\n"
    "v_accvgpr_read_b32 v140, a60\n"
    "v_accvgpr_read_b32 v141, a61\n"
    "v_accvgpr_read_b32 v142, a62\n"
    "v_accvgpr_read_b32 v143, a63\n"
    "v_pk_fma_f32 v[28:29], v[136:137], v[26:27], v[28:29]\n"
    "v_pk_fma_f32 v[28:29], v[138:139], v[26:27], v[28:29]\n"
    "v_pk_fma_f32 v[28:29], v[140:141], v[26:27], v[28:29]\n"
    "v_pk_fma_f32 v[28:29], v[142:143], v[26:27], v[28:29]\n"
    "v_pk_mul_f32 v[136:137], v[136:137], v[30:31] op_sel_hi:[1,0]\n"
    "v_pk_mul_f32 v[138:139], v[138:139], v[30:31] op_sel_hi:[1,0]\n"
    "v_pk_mul_f32 v[140:141], v[140:141], v[30:31] op_sel_hi:[1,0]\n"
    "v_pk_mul_f32 v[142:143], v[142:143], v[30:31] op_sel_hi:[1,0]\n"
    "v_cvt_pk_bf16_f32 v156, v136, v137\n"
    "v_cvt_pk_bf16_f32 v157, v138, v139\n"
    "v_cvt_pk_bf16_f32 v158, v140, v141\n"
    "v_cvt_pk_bf16_f32 v159, v142, v143\n"
    "buffer_store_dwordx4 v[156:159], v5, s[60:63], s82 offen offset:1024 sc1\n"
    "v_add_f32 v28, v28, v29\n"
    "v_min_f32 v30, v80, v84\n"
    "v_max_f32 v31, v80, v84\n"
    "v_fma_f32 v28, v80, v26, v28\n"
    "v_fma_f32 v28, v84, v26, v28\n"
    "v_add_f32 v30, v30, v28\n"
    "s_mov_b32 s81, 0x21800000\n"
    "s_mov_b32 s82, 0x5d800000\n"
    "v_cmp_nle_f32 vcc, s81, v30\n"
    "v_cmp_nge_f32 s[92:93], s82, v31\n"
    "s_nop 0\n"
    "s_or_b64 vcc, vcc, s[92:93]\n"
    "s_cselect_b32 s81, 1, 0\n"
    "s_or_b32 s88, s88, s81\n"
    "s_cmp_eq_u32 s87, 0\n"
    "s_cbranch_scc1 at_b1\n"
    "at_bb:\n"
    "s_waitcnt lgkmcnt(8)\n"
    "v_mfma_f32_16x16x32_bf16 v[176:179], a[128:131], v[240:243], 0\n"
    "s_add_u32 m0, s74, s84\n"
    "ds_read_b128 a[176:179], v22 offset:12288\n"
    "v_cvt_pk_bf16_f32 v113, v162, v163\n"
    "v_mfma_f32_16x16x32_bf16 v[176:179], a[132:135], v[244:247], v[176:179]\n"
    "buffer_load_dwordx4 v20, s[56:59], s86 offen sc1 lds\n"
    "v_mfma_f32_16x16x32_bf16 v[176:179], a[136:139], v[248:251], v[176:179]\n"
    "ds_read_b128 a[180:183], v22 offset:13312\n"
    "v_cvt_pk_bf16_f32 v112, v160, v161\n"
    "v_mfma_f32_16x16x32_bf16 v[176:179], a[140:143], v[252:255], v[176:179]\n"
    "v_mfma_f32_16x16x32_bf16 v[160:163], a[128:131], v[224:227], v[48:51]\n"
    "ds_read_b128 a[184:187], v22 offset:14336\n"
    "v_cvt_pk_bf16_f32 v123, v182, v183\n"
    "v_mfma_f32_16x16x32_bf16 v[160:163], a[132:135], v[228:231], v[160:163]\n"
    "v_mfma_f32_16x16x32_bf16 v[160:163], a[136:139], v[232:235], v[160:163]\n"
    "ds_read_b128 a[188:191], v22 offset:15360\n"
    "v_cvt_pk_bf16_f32 v122, v180, v181\n"
    "v_mfma_f32_16x16x32_bf16 v[160:163], a[140:143], v[236:239], v[160:163]\n"
    "s_waitcnt lgkmcnt(8)\n"
    "v_mfma_f32_16x16x32_bf16 v[180:183], a[144:147], v[240:243], 0\n"
    "ds_read_b128 a[192:195], v23 offset:16384\n"
    "v_cvt_pk_bf16_f32 v115, v166, v167\n"
    "v_mfma_f32_16x16x32_bf16 v[180:183], a[148:151], v[244:247], v[180:183]\n"
    "buffer_load_dwordx4 v20, s[56:59], s86 offen offset:1024 sc1 lds\n"
    "v_mfma_f32_16x16x32_bf16 v[180:183], a[152:155], v[248:251], v[180:183]\n"
    "ds_read_b128 a[196:199], v23 offset:17408\n"
    "v_cvt_pk_bf16_f32 v114, v164, v165\n"
    "v_mfma_f32_16x16x32_bf16 v[180:183], a[156:159], v[252:255], v[180:183]\n"
    "v_exp_f32 v176, v176\n"
    "v_mfma_f32_16x16x32_bf16 v[164:167], a[144:147], v[224:227], v[52:55]\n"
    "ds_read_b128 a[200:203], v23 offset:18432\n"
    "v_cvt_pk_bf16_f32 v125, v186, v187\n"
    "v_mfma_f32_16x16x32_bf16 v[164:167], a[148:151], v[228:231], v[164:167]\n"
    "s_waitcnt lgkmcnt(7)\n"
    "v_exp_f32 v177, v177\n"
    "v_mfma_f32_16x16x32_bf16 v[164:167], a[152:155], v[232:235], v[164:167]\n"
    "ds_read_b128 a[204:207], v23 offset:19456\n"
    "v_cvt_pk_bf16_f32 v124, v184, v185\n"
    "v_mfma_f32_16x16x32_bf16 v[164:167], a[156:159], v[236:239], v[164:167]\n"
    "buffer_load_dwordx4 v20, s[56:59], s86 offen offset:2048 sc1 lds\n"
    "v_mfma_f32_16x16x32_bf16 v[184:187], a[160:163], v[240:243], 0\n"
    "ds_read_b128 a[208:211], v23 offset:20480\n"
    "v_cvt_pk_bf16_f32 v117, v170, v171\n"
    "v_mfma_f32_16x16x32_bf16 v[184:187], a[164:167], v[244:247], v[184:187]\n"
    "v_exp_f32 v178, v178\n"
    "v_mfma_f32_16x16x32_bf16 v[184:187], a[168:171], v[248:251], v[184:187]\n"
    "ds_read_b128 a[212:215], v23 offset:21504\n"
    "v_cvt_pk_bf16_f32 v116, v168, v169\n"
    "v_mfma_f32_16x16x32_bf16 v[184:187], a[172:175], v[252:255], v[184:187]\n"
    "v_exp_f32 v179, v179\n"
    "v_mfma_f32_16x16x32_bf16 v[168:171], a[160:163], v[224:227], v[56:59]\n"
    "ds_read_b128 a[216:219], v23 offset:22528\n"
    "v_cvt_pk_bf16_f32 v127, v190, v191\n"
    "v_mfma_f32_16x16x32_bf16 v[168:171], a[164:167], v[228:231], v[168:171]\n"
    "s_waitcnt lgkmcnt(7)\n"
    "v_exp_f32 v160, v160\n"
    "v_mfma_f32_16x16x32_bf16 v[168:171], a[168:171], v[232:235], v[168:171]\n"
    "ds_read_b128 a[220:223], v23 offset:23552\n"
    "v_cvt_pk_bf16_f32 v126, v188, v189\n"
    "v_mfma_f32_16x16x32_bf16 v[168:171], a[172:175], v[236:239], v[168:171]\n"
    "buffer_load_dwordx4 v20, s[56:59], s86 offen offset:3072 sc1 lds\n"
    "v_mfma_f32_16x16x32_bf16 v[188:191], a[176:179], v[240:243], 0\n"
    "ds_read_b128 a[224:227], v23 offset:24576\n"
    "v_cvt_pk_bf16_f32 v119, v174, v175\n"
    "v_mfma_f32_16x16x32_bf16 v[188:191], a[180:183], v[244:247], v[188:191]\n"
    "s_add_u32 m0, s75, s85\n"
    "v_exp_f32 v161, v161\n"
    "v_mfma_f32_16x16x32_bf16 v[188:191], a[184:187], v[248:251], v[188:191]\n"
    "ds_read_b128 a[228:231], v23 offset:25600\n"
    "v_cvt_pk_bf16_f32 v118, v172, v173\n"
    "v_mfma_f32_16x16x32_bf16 v[188:191], a[188:191], v[252:255], v[188:191]\n"
    "v_exp_f32 v162, v162\n"
    "v_mfma_f32_16x16x32_bf16 v[172:175], a[176:179], v[224:227], v[60:63]\n"
    "ds_read_b128 a[232:235], v23 offset:26624\n"
    "v_mfma_f32_16x16x32_bf16 v[172:175], a[180:183], v[228:231], v[172:175]\n"
    "buffer_load_dwordx4 v21, s[56:59], s86 offen sc1 lds\n"
    "v_mfma_f32_16x16x32_bf16 v[172:175], a[184:187], v[232:235], v[172:175]\n"
    "ds_read_b128 a[236:239], v23 offset:27648\n"
    "v_exp_f32 v163, v163\n"
    "v_mfma_f32_16x16x32_bf16 v[172:175], a[188:191], v[236:239], v[172:175]\n"
    "v_exp_f32 v180, v180\n"
    "v_mfma_f32_16x16x32_bf16 v[92:95], v[8:11], v[120:123], v[92:95]\n"
    "ds_read_b128 a[240:243], v23 offset:28672\n"
    "v_mfma_f32_16x16x32_bf16 v[92:95], v[8:11], v[124:127], v[92:95]\n"
    "v_exp_f32 v181, v181\n"
    "v_mfma_f32_16x16x32_bf16 v[88:91], v[8:11], v[112:115], v[88:91]\n"
    "ds_read_b128 a[244:247], v23 offset:29696\n"
    "v_exp_f32 v182, v182\n"
    "v_mfma_f32_16x16x32_bf16 v[88:91], v[8:11], v[116:119], v[88:91]\n"
    "s_waitcnt lgkmcnt(10)\n"
    "v_exp_f32 v183, v183\n"
    "v_mfma_f32_16x16x32_bf16 a[96:99], a[192:195], v[120:123], a[96:99]\n"
    "ds_read_b128 a[248:251], v23 offset:30720\n"
    "v_mfma_f32_16x16x32_bf16 a[96:99], a[196:199], v[124:127], a[96:99]\n"
    "buffer_load_dwordx4 v21, s[56:59], s86 offen offset:1024 sc1 lds\n"
    "v_mfma_f32_16x16x32_bf16 a[64:67], a[192:195], v[112:115], a[64:67]\n"
    "ds_read_b128 a[252:255], v23 offset:31744\n"
    "v_exp_f32 v164, v164\n"
    "v_mfma_f32_16x16x32_bf16 a[64:67], a[196:199], v[116:119], a[64:67]\n"
    "s_waitcnt vmcnt(14)\n"
    "s_barrier\n"
    "v_mov_b32 v23, v22\n"
    "v_add_u32 v22, s76, v24\n"
    "v_mfma_f32_16x16x32_bf16 a[100:103], a[200:203], v[120:123], a[100:103]\n"
    "ds_read_b128 a[128:131], v22\n"
    "v_exp_f32 v165, v165\n"
    "v_mfma_f32_16x16x32_bf16 a[100:103], a[204:207], v[124:127], a[100:103]\n"
    "v_exp_f32 v166, v166\n"
    "v_mfma_f32_16x16x32_bf16 a[68:71], a[200:203], v[112:115], a[68:71]\n"
    "s_waitcnt lgkmcnt(9)\n"
    "ds_read_b128 a[132:135], v22 offset:1024\n"
    "v_mfma_f32_16x16x32_bf16 a[68:71], a[204:207], v[116:119], a[68:71]\n"
    "buffer_load_dwordx4 v21, s[56:59], s86 offen offset:2048 sc1 lds\n"
    "v_mfma_f32_16x16x32_bf16 a[104:107], a[208:211], v[120:123], a[104:107]\n"
    "ds_read_b128 a[136:139], v22 offset:2048\n"
    "v_exp_f32 v167, v167\n"
    "v_mfma_f32_16x16x32_bf16 a[104:107], a[212:215], v[124:127], a[104:107]\n"
    "v_exp_f32 v184, v184\n"
    "v_mfma_f32_16x16x32_bf16 a[72:75], a[208:211], v[112:115], a[72:75]\n"
    "ds_read_b128 a[140:143], v22 offset:3072\n"
    "v_mfma_f32_16x16x32_bf16 a[72:75], a[212:215], v[116:119], a[72:75]\n"
    "v_exp_f32 v185, v185\n"
    "v_mfma_f32_16x16x32_bf16 a[108:111], a[216:219], v[120:123], a[108:111]\n"
    "ds_read_b128 a[144:147], v22 offset:4096\n"
    "v_exp_f32 v186, v186\n"
    "v_mfma_f32_16x16x32_bf16 a[108:111], a[220:223], v[124:127], a[108:111]\n"
    "v_exp_f32 v187, v187\n"
    "v_mfma_f32_16x16x32_bf16 a[76:79], a[216:219], v[112:115], a[76:79]\n"
    "s_waitcnt lgkmcnt(9)\n"
    "ds_read_b128 a[148:151], v22 offset:5120\n"
    "v_mfma_f32_16x16x32_bf16 a[76:79], a[220:223], v[116:119], a[76:79]\n"
    "buffer_load_dwordx4 v21, s[56:59], s86 offen offset:3072 sc1 lds\n"
    "v_mfma_f32_16x16x32_bf16 a[112:115], a[224:227], v[120:123], a[112:115]\n"
    "ds_read_b128 a[152:155], v22 offset:6144\n"
    "v_exp_f32 v168, v168\n"
    "v_mfma_f32_16x16x32_bf16 a[112:115], a[228:231], v[124:127], a[112:115]\n"
    "s_add_u32 s86, s86, 0x8000\n"
    "v_exp_f32 v169, v169\n"
    "v_mfma_f32_16x16x32_bf16 a[80:83], a[224:227], v[112:115], a[80:83]\n"
    "s_mov_b32 s76, s75\n"
    "ds_read_b128 a[156:159], v22 offset:7168\n"
    "v_mfma_f32_16x16x32_bf16 a[80:83], a[228:231], v[116:119], a[80:83]\n"
    "s_mov_b32 s75, s74\n"
    "v_exp_f32 v170, v170\n"
    "v_mfma_f32_16x16x32_bf16 a[116:119], a[232:235], v[120:123], a[116:119]\n"
    "ds_read_b128 a[160:163], v22 offset:8192\n"
    "v_exp_f32 v171, v171\n"
    "v_mfma_f32_16x16x32_bf16 a[116:119], a[236:239], v[124:127], a[116:119]\n"
    "s_add_u32 s74, s74, 0x8000\n"
    "v_exp_f32 v188, v188\n"
    "v_mfma_f32_16x16x32_bf16 a[84:87], a[232:235], v[112:115], a[84:87]\n"
    "s_cmp_lt_u32 s74, 0x28000\n"
    "s_cselect_b32 s74, s74, 0\n"
    "ds_read_b128 a[164:167], v22 offset:9216\n"
    "v_mfma_f32_16x16x32_bf16 a[84:87], a[236:239], v[116:119], a[84:87]\n"
    "s_waitcnt lgkmcnt(10)\n"
    "v_exp_f32 v189, v189\n"
    "v_mfma_f32_16x16x32_bf16 a[120:123], a[240:243], v[120:123], a[120:123]\n"
    "ds_read_b128 a[168:171], v22 offset:10240\n"
    "v_exp_f32 v190, v190\n"
    "v_mfma_f32_16x16x32_bf16 a[120:123], a[244:247], v[124:127], a[120:123]\n"
    "s_sub_u32 s87, s87, 1\n"
    "v_exp_f32 v191, v191\n"
    "v_mfma_f32_16x16x32_bf16 a[88:91], a[240:243], v[112:115], a[88:91]\n"
    "s_cmp_gt_u32 s87, 1\n"
    "ds_read_b128 a[172:175], v22 offset:11264\n"
    "v_mfma_f32_16x16x32_bf16 a[88:91], a[244:247], v[116:119], a[88:91]\n"
    "v_exp_f32 v172, v172\n"
    "v_mfma_f32_16x16x32_bf16 a[124:127], a[248:251], v[120:123], a[124:127]\n"
    "v_exp_f32 v173, v173\n"
    "v_mfma_f32_16x16x32_bf16 a[124:127], a[252:255], v[124:127], a[124:127]\n"
    "v_cvt_pk_bf16_f32 v120, v176, v177\n"
    "v_cvt_pk_bf16_f32 v121, v178, v179\n"
    "v_mfma_f32_16x16x32_bf16 a[92:95], a[248:251], v[112:115], a[92:95]\n"
    "v_exp_f32 v174, v174\n"
    "v_mfma_f32_16x16x32_bf16 a[92:95], a[252:255], v[116:119], a[92:95]\n"
    "v_exp_f32 v175, v175\n"
    "s_cbranch_scc1 at_bb\n"
    "s_cmp_eq_u32 s87, 0\n"
    "s_cbranch_scc1 at_b1\n"
    "v_mov_b32 v48, v64\n"
    "v_mov_b32 v49, v65\n"
    "v_mov_b32 v50, v66\n"
    "v_mov_b32 v51, v67\n"
    "v_mov_b32 v52, v68\n"
    "v_mov_b32 v53, v69\n"
    "v_mov_b32 v54, v70\n"
    "v_mov_b32 v55, v71\n"
    "v_mov_b32 v56, v72\n"
    "v_mov_b32 v57, v73\n"
    "v_mov_b32 v58, v74\n"
    "v_mov_b32 v59, v75\n"
    "v_mov_b32 v60, v76\n"
    "v_mov_b32 v61, v77\n"
    "v_mov_b32 v62, v78\n"
    "v_mov_b32 v63, v79\n"
    "s_branch at_bb\n"
    "at_b1:\n"
    "s_mov_b64 s[52:53], s[18:19]\n"
    "s_mov_b32 s54, 0x700000\n"
    "s_mov_b32 s55, 0x20000\n"
    "s_lshl_b32 s81, s30, 2\n"
    "s_add_u32 s81, s81, s78\n"
    "s_mul_i32 s84, s81, 0x493\n"
    "s_lshr_b32 s84, s84, 16\n"
    "s_mul_i32 s82, s84, 56\n"
    "s_sub_u32 s85, s81, s82\n"
    "s_mul_i32 s81, s84, 0x70000\n"
    "s_lshl_b32 s82, s85, 7\n"
    "s_add_u32 s81, s81, s82\n"
    "v_lshrrev_b32 v26, 3, v1\n"
    "v_mul_u32_u24 v26, 0x1c00, v26\n"
    "v_and_b32 v27, 7, v1\n"
    "v_lshl_add_u32 v26, v27, 4, v26\n"
    "buffer_load_dwordx4 v[128:131], v26, s[52:55], s81 offen\n"
    "s_add_u32 s81, s81, 0xe000\n"
    "buffer_load_dwordx4 v[132:135], v26, s[52:55], s81 offen\n"
    "s_add_u32 s81, s81, 0xe000\n"
    "buffer_load_dwordx4 v[136:139], v26, s[52:55], s81 offen\n"
    "s_add_u32 s81, s81, 0xe000\n"
    "buffer_load_dwordx4 v[140:143], v26, s[52:55], s81 offen\n"
    "s_add_u32 s81, s81, 0xe000\n"
    "buffer_load_dwordx4 v[144:147], v26, s[52:55], s81 offen\n"
    "s_add_u32 s81, s81, 0xe000\n"
    "buffer_load_dwordx4 v[148:151], v26, s[52:55], s81 offen\n"
    "s_add_u32 s81, s81, 0xe000\n"
    "buffer_load_dwordx4 v[152:155], v26, s[52:55], s81 offen\n"
    "s_add_u32 s81, s81, 0xe000\n"
    "buffer_load_dwordx4 v[156:159], v26, s[52:55], s81 offen\n"
    "s_add_u32 s81, s81, 0xe000\n"
    "s_waitcnt lgkmcnt(8)\n"
    "v_mfma_f32_16x16x32_bf16 v[176:179], a[128:131], v[240:243], v[32:35]\n"
    "ds_read_b128 a[176:179], v22 offset:12288\n"
    "v_cvt_pk_bf16_f32 v123, v182, v183\n"
    "v_mfma_f32_16x16x32_bf16 v[176:179], a[132:135], v[244:247], v[176:179]\n"
    "v_mfma_f32_16x16x32_bf16 v[176:179], a[136:139], v[248:251], v[176:179]\n"
    "ds_read_b128 a[180:183], v22 offset:13312\n"
    "v_cvt_pk_bf16_f32 v122, v180, v181\n"
    "v_mfma_f32_16x16x32_bf16 v[176:179], a[140:143], v[252:255], v[176:179]\n"
    "s_waitcnt lgkmcnt(6)\n"
    "v_mfma_f32_16x16x32_bf16 v[180:183], a[144:147], v[240:243], v[36:39]\n"
    "ds_read_b128 a[184:187], v22 offset:14336\n"
    "v_cvt_pk_bf16_f32 v125, v186, v187\n"
    "v_mfma_f32_16x16x32_bf16 v[180:183], a[148:151], v[244:247], v[180:183]\n"
    "ds_read_b128 a[188:191], v22 offset:15360\n"
    "v_cvt_pk_bf16_f32 v124, v184, v185\n"
    "v_mfma_f32_16x16x32_bf16 v[180:183], a[152:155], v[248:251], v[180:183]\n"
    "v_mfma_f32_16x16x32_bf16 v[180:183], a[156:159], v[252:255], v[180:183]\n"
    "s_waitcnt lgkmcnt(4)\n"
    "v_cvt_pk_bf16_f32 v119, v174, v175\n"
    "v_mfma_f32_16x16x32_bf16 v[184:187], a[160:163], v[240:243], v[40:43]\n"
    "v_cvt_pk_bf16_f32 v127, v190, v191\n"
    "v_cvt_pk_bf16_f32 v118, v172, v173\n"
    "v_mfma_f32_16x16x32_bf16 v[184:187], a[164:167], v[244:247], v[184:187]\n"
    "ds_read_b128 a[192:195], v23 offset:16384\n"
    "v_cvt_pk_bf16_f32 v114, v164, v165\n"
    "v_mfma_f32_16x16x32_bf16 v[184:187], a[168:171], v[248:251], v[184:187]\n"
    "v_cvt_pk_bf16_f32 v117, v170, v171\n"
    "v_cvt_pk_bf16_f32 v126, v188, v189\n"
    "v_mfma_f32_16x16x32_bf16 v[184:187], a[172:175], v[252:255], v[184:187]\n"
    "s_waitcnt lgkmcnt(1)\n"
    "v_cvt_pk_bf16_f32 v115, v166, v167\n"
    "v_cvt_pk_bf16_f32 v116, v168, v169\n"
    "v_mfma_f32_16x16x32_bf16 v[188:191], a[176:179], v[240:243], v[44:47]\n"
    "ds_read_b128 a[196:199], v23 offset:17408\n"
    "v_cvt_pk_bf16_f32 v113, v162, v163\n"
    "v_mfma_f32_16x16x32_bf16 v[188:191], a[180:183], v[244:247], v[188:191]\n"
    "ds_read_b128 a[200:203], v23 offset:18432\n"
    "v_cvt_pk_bf16_f32 v112, v160, v161\n"
    "v_mfma_f32_16x16x32_bf16 v[188:191], a[184:187], v[248:251], v[188:191]\n"
    "ds_read_b128 a[204:207], v23 offset:19456\n"
    "v_mfma_f32_16x16x32_bf16 v[188:191], a[188:191], v[252:255], v[188:191]\n"
    "v_exp_f32 v176, v176\n"
    "v_mfma_f32_16x16x32_bf16 v[92:95], v[8:11], v[120:123], v[92:95]\n"
    "v_exp_f32 v177, v177\n"
    "v_mfma_f32_16x16x32_bf16 v[92:95], v[8:11], v[124:127], v[92:95]\n"
    "v_mfma_f32_16x16x32_bf16 v[88:91], v[8:11], v[112:115], v[88:91]\n"
    "v_exp_f32 v178, v178\n"
    "v_mfma_f32_16x16x32_bf16 v[88:91], v[8:11], v[116:119], v[88:91]\n"
    "s_waitcnt lgkmcnt(0)\n"
    "ds_read_b128 a[208:211], v23 offset:20480\n"
    "v_mfma_f32_16x16x32_bf16 a[96:99], a[192:195], v[120:123], a[96:99]\n"
    "ds_read_b128 a[212:215], v23 offset:21504\n"
    "v_mfma_f32_16x16x32_bf16 a[96:99], a[196:199], v[124:127], a[96:99]\n"
    "ds_read_b128 a[216:219], v23 offset:22528\n"
    "v_mfma_f32_16x16x32_bf16 a[64:67], a[192:195], v[112:115], a[64:67]\n"
    "ds_read_b128 a[220:223], v23 offset:23552\n"
    "v_mfma_f32_16x16x32_bf16 a[64:67], a[196:199], v[116:119], a[64:67]\n"
    "v_exp_f32 v179, v179\n"
    "v_mfma_f32_16x16x32_bf16 a[100:103], a[200:203], v[120:123], a[100:103]\n"
    "v_mfma_f32_16x16x32_bf16 a[100:103], a[204:207], v[124:127], a[100:103]\n"
    "v_exp_f32 v180, v180\n"
    "v_mfma_f32_16x16x32_bf16 a[68:71], a[200:203], v[112:115], a[68:71]\n"
    "v_exp_f32 v181, v181\n"
    "v_mfma_f32_16x16x32_bf16 a[68:71], a[204:207], v[116:119], a[68:71]\n"
    "s_waitcnt lgkmcnt(0)\n"
    "v_mfma_f32_16x16x32_bf16 a[104:107], a[208:211], v[120:123], a[104:107]\n"
    "ds_read_b128 a[224:227], v23 offset:24576\n"
    "v_mfma_f32_16x16x32_bf16 a[104:107], a[212:215], v[124:127], a[104:107]\n"
    "ds_read_b128 a[228:231], v23 offset:25600\n"
    "v_mfma_f32_16x16x32_bf16 a[72:75], a[208:211], v[112:115], a[72:75]\n"
    "ds_read_b128 a[232:235], v23 offset:26624\n"
    "v_mfma_f32_16x16x32_bf16 a[72:75], a[212:215], v[116:119], a[72:75]\n"
    "ds_read_b128 a[236:239], v23 offset:27648\n"
    "v_mfma_f32_16x16x32_bf16 a[108:111], a[216:219], v[120:123], a[108:111]\n"
    "v_exp_f32 v182, v182\n"
    "v_mfma_f32_16x16x32_bf16 a[108:111], a[220:223], v[124:127], a[108:111]\n"
    "v_exp_f32 v183, v183\n"
    "v_mfma_f32_16x16x32_bf16 a[76:79], a[216:219], v[112:115], a[76:79]\n"
    "v_mfma_f32_16x16x32_bf16 a[76:79], a[220:223], v[116:119], a[76:79]\n"
    "s_waitcnt lgkmcnt(0)\n"
    "v_exp_f32 v184, v184\n"
    "v_mfma_f32_16x16x32_bf16 a[112:115], a[224:227], v[120:123], a[112:115]\n"
    "ds_read_b128 a[240:243], v23 offset:28672\n"
    "v_mfma_f32_16x16x32_bf16 a[112:115], a[228:231], v[124:127], a[112:115]\n"
    "ds_read_b128 a[244:247], v23 offset:29696\n"
    "v_mfma_f32_16x16x32_bf16 a[80:83], a[224:227], v[112:115], a[80:83]\n"
    "ds_read_b128 a[248:251], v23 offset:30720\n"
    "v_mfma_f32_16x16x32_bf16 a[80:83], a[228:231], v[116:119], a[80:83]\n"
    "ds_read_b128 a[252:255], v23 offset:31744\n"
    "v_mfma_f32_16x16x32_bf16 a[116:119], a[232:235], v[120:123], a[116:119]\n"
    "v_exp_f32 v185, v185\n"
    "v_mfma_f32_16x16x32_bf16 a[116:119], a[236:239], v[124:127], a[116:119]\n"
    "v_mfma_f32_16x16x32_bf16 a[84:87], a[232:235], v[112:115], a[84:87]\n"
    "v_exp_f32 v186, v186\n"
    "v_mfma_f32_16x16x32_bf16 a[84:87], a[236:239], v[116:119], a[84:87]\n"
    "s_waitcnt lgkmcnt(0)\n"
    "v_exp_f32 v187, v187\n"
    "v_mfma_f32_16x16x32_bf16 a[120:123], a[240:243], v[120:123], a[120:123]\n"
    "v_mfma_f32_16x16x32_bf16 a[120:123], a[244:247], v[124:127], a[120:123]\n"
    "v_exp_f32 v188, v188\n"
    "v_mfma_f32_16x16x32_bf16 a[88:91], a[240:243], v[112:115], a[88:91]\n"
    "v_exp_f32 v189, v189\n"
    "v_mfma_f32_16x16x32_bf16 a[88:91], a[244:247], v[116:119], a[88:91]\n"
    "v_mfma_f32_16x16x32_bf16 a[124:127], a[248:251], v[120:123], a[124:127]\n"
    "v_exp_f32 v190, v190\n"
    "v_mfma_f32_16x16x32_bf16 a[124:127], a[252:255], v[124:127], a[124:127]\n"
    "v_cvt_pk_bf16_f32 v120, v176, v177\n"
    "v_cvt_pk_bf16_f32 v121, v178, v179\n"
    "v_mfma_f32_16x16x32_bf16 a[92:95], a[248:251], v[112:115], a[92:95]\n"
    "v_exp_f32 v191, v191\n"
    "v_mfma_f32_16x16x32_bf16 a[92:95], a[252:255], v[116:119], a[92:95]\n"
    "v_mov_b32 v23, v22\n"
    "s_waitcnt vmcnt(16)\n"
    "s_barrier\n"
    "ds_read_b128 a[192:195], v23 offset:16384\n"
    "ds_read_b128 a[196:199], v23 offset:17408\n"
    "ds_read_b128 a[200:203], v23 offset:18432\n"
    "ds_read_b128 a[204:207], v23 offset:19456\n"
    "ds_read_b128 a[208:211], v23 offset:20480\n"
    "ds_read_b128 a[212:215], v23 offset:21504\n"
    "ds_read_b128 a[216:219], v23 offset:22528\n"
    "ds_read_b128 a[220:223], v23 offset:23552\n"
    "v_cvt_pk_bf16_f32 v122, v180, v181\n"
    "v_cvt_pk_bf16_f32 v123, v182, v183\n"
    "v_cvt_pk_bf16_f32 v124, v184, v185\n"
    "v_cvt_pk_bf16_f32 v125, v186, v187\n"
    "v_cvt_pk_bf16_f32 v126, v188, v189\n"
    "v_cvt_pk_bf16_f32 v127, v190, v191\n"
    "s_nop 1\n"
    "v_mfma_f32_16x16x32_bf16 v[92:95], v[8:11], v[120:123], v[92:95]\n"
    "v_mfma_f32_16x16x32_bf16 v[92:95], v[8:11], v[124:127], v[92:95]\n"
    "s_waitcnt lgkmcnt(4)\n"
    "v_mfma_f32_16x16x32_bf16 a[96:99], a[192:195], v[120:123], a[96:99]\n"
    "ds_read_b128 a[224:227], v23 offset:24576\n"
    "v_mfma_f32_16x16x32_bf16 a[96:99], a[196:199], v[124:127], a[96:99]\n"
    "ds_read_b128 a[228:231], v23 offset:25600\n"
    "v_mfma_f32_16x16x32_bf16 a[100:103], a[200:203], v[120:123], a[100:103]\n"
    "ds_read_b128 a[232:235], v23 offset:26624\n"
    "v_mfma_f32_16x16x32_bf16 a[100:103], a[204:207], v[124:127], a[100:103]\n"
    "s_waitcnt lgkmcnt(3)\n"
    "ds_read_b128 a[236:239], v23 offset:27648\n"
    "v_mfma_f32_16x16x32_bf16 a[104:107], a[208:211], v[120:123], a[104:107]\n"
    "ds_read_b128 a[240:243], v23 offset:28672\n"
    "v_mfma_f32_16x16x32_bf16 a[104:107], a[212:215], v[124:127], a[104:107]\n"
    "ds_read_b128 a[244:247], v23 offset:29696\n"
    "v_mfma_f32_16x16x32_bf16 a[108:111], a[216:219], v[120:123], a[108:111]\n"
    "ds_read_b128 a[248:251], v23 offset:30720\n"
    "v_mfma_f32_16x16x32_bf16 a[108:111], a[220:223], v[124:127], a[108:111]\n"
    "s_waitcnt lgkmcnt(3)\n"
    "ds_read_b128 a[252:255], v23 offset:31744\n"
    "v_mfma_f32_16x16x32_bf16 a[112:115], a[224:227], v[120:123], a[112:115]\n"
    "v_mfma_f32_16x16x32_bf16 a[112:115], a[228:231], v[124:127], a[112:115]\n"
    "v_mfma_f32_16x16x32_bf16 a[116:119], a[232:235], v[120:123], a[116:119]\n"
    "v_mfma_f32_16x16x32_bf16 a[116:119], a[236:239], v[124:127], a[116:119]\n"
    "s_waitcnt lgkmcnt(0)\n"
    "v_mfma_f32_16x16x32_bf16 a[120:123], a[240:243], v[120:123], a[120:123]\n"
    "v_mfma_f32_16x16x32_bf16 a[120:123], a[244:247], v[124:127], a[120:123]\n"
    "v_mfma_f32_16x16x32_bf16 a[124:127], a[248:251], v[120:123], a[124:127]\n"
    "v_mfma_f32_16x16x32_bf16 a[124:127], a[252:255], v[124:127], a[124:127]\n"
    "s_nop 7\n"
    "v_mov_b32 v28, 0\n"
    "v_mov_b32 v29, 0\n"
    "v_mov_b32 v26, 0\n"
    "v_mov_b32 v27, 0\n"
    "s_lshl_b32 s81, s79, 19\n"
    "s_add_u32 s82, s81, 0x40000\n"
    "v_rcp_f32 v30, v88\n"
    "v_accvgpr_read_b32 v160, a64\n"
    "v_accvgpr_read_b32 v161, a65\n"
    "v_accvgpr_read_b32 v162, a66\n"
    "v_accvgpr_read_b32 v163, a67\n"
    "v_accvgpr_read_b32 v164, a68\n"
    "v_accvgpr_read_b32 v165, a69\n"
    "v_accvgpr_read_b32 v166, a70\n"
    "v_accvgpr_read_b32 v167, a71\n"
    "v_pk_fma_f32 v[28:29], v[160:161], v[26:27], v[28:29]\n"
    "v_pk_fma_f32 v[28:29], v[162:163], v[26:27], v[28:29]\n"
    "v_pk_fma_f32 v[28:29], v[164:165], v[26:27], v[28:29]\n"
    "v_pk_fma_f32 v[28:29], v[166:167], v[26:27], v[28:29]\n"
    "v_pk_mul_f32 v[160:161], v[160:161], v[30:31] op_sel_hi:[1,0]\n"
    "v_pk_mul_f32 v[162:163], v[162:163], v[30:31] op_sel_hi:[1,0]\n"
    "v_pk_mul_f32 v[164:165], v[164:165], v[30:31] op_sel_hi:[1,0]\n"
    "v_pk_mul_f32 v[166:167], v[166:167], v[30:31] op_sel_hi:[1,0]\n"
    "v_cvt_pk_bf16_f32 v176, v160, v161\n"
    "v_cvt_pk_bf16_f32 v177, v162, v163\n"
    "v_cvt_pk_bf16_f32 v178, v164, v165\n"
    "v_cvt_pk_bf16_f32 v179, v166, v167\n"
    "buffer_store_dwordx4 v[176:179], v6, s[60:63], s81 offen sc1\n"
    "v_accvgpr_read_b32 v168, a72\n"
    "v_accvgpr_read_b32 v169, a73\n"
    "v_accvgpr_read_b32 v170, a74\n"
    "v_accvgpr_read_b32 v171, a75\n"
    "v_accvgpr_read_b32 v172, a76\n"
    "v_accvgpr_read_b32 v173, a77\n"
    "v_accvgpr_read_b32 v174, a78\n"
    "v_accvgpr_read_b32 v175, a79\n"
    "v_pk_fma_f32 v[28:29], v[168:169], v[26:27], v[28:29]\n"
    "v_pk_fma_f32 v[28:29], v[170:171], v[26:27], v[28:29]\n"
    "v_pk_fma_f32 v[28:29], v[172:173], v[26:27], v[28:29]\n"
    "v_pk_fma_f32 v[28:29], v[174:175], v[26:27], v[28:29]\n"
    "v_pk_mul_f32 v[168:169], v[168:169], v[30:31] op_sel_hi:[1,0]\n"
    "v_pk_mul_f32 v[170:171], v[170:171], v[30:31] op_sel_hi:[1,0]\n"
    "v_pk_mul_f32 v[172:173], v[172:173], v[30:31] op_sel_hi:[1,0]\n"
    "v_pk_mul_f32 v[174:175], v[174:175], v[30:31] op_sel_hi:[1,0]\n"
    "v_cvt_pk_bf16_f32 v180, v168, v169\n"
    "v_cvt_pk_bf16_f32 v181, v170, v171\n"
    "v_cvt_pk_bf16_f32 v182, v172, v173\n"
    "v_cvt_pk_bf16_f32 v183, v174, v175\n"
    "buffer_store_dwordx4 v[180:183], v6, s[60:63], s81 offen offset:1024 sc1\n"
    "v_accvgpr_read_b32 v160, a80\n"
    "v_accvgpr_read_b32 v161, a81\n"
    "v_accvgpr_read_b32 v162, a82\n"
    "v_accvgpr_read_b32 v163, a83\n"
    "v_accvgpr_read_b32 v164, a84\n"
    "v_accvgpr_read_b32 v165, a85\n"
    "v_accvgpr_read_b32 v166, a86\n"
    "v_accvgpr_read_b32 v167, a87\n"
    "v_pk_fma_f32 v[28:29], v[160:161], v[26:27], v[28:29]\n"
    "v_pk_fma_f32 v[28:29], v[162:163], v[26:27], v[28:29]\n"
    "v_pk_fma_f32 v[28:29], v[164:165], v[26:27], v[28:29]\n"
    "v_pk_fma_f32 v[28:29], v[166:167], v[26:27], v[28:29]\n"
    "v_pk_mul_f32 v[160:161], v[160:161], v[30:31] op_sel_hi:[1,0]\n"
    "v_pk_mul_f32 v[162:163], v[162:163], v[30:31] op_sel_hi:[1,0]\n"
    "v_pk_mul_f32 v[164:165], v[164:165], v[30:31] op_sel_hi:[1,0]\n"
    "v_pk_mul_f32 v[166:167], v[166:167], v[30:31] op_sel_hi:[1,0]\n"
    "v_cvt_pk_bf16_f32 v184, v160, v161\n"
    "v_cvt_pk_bf16_f32 v185, v162, v163\n"
    "v_cvt_pk_bf16_f32 v186, v164, v165\n"
    "v_cvt_pk_bf16_f32 v187, v166, v167\n"
    "buffer_store_dwordx4 v[184:187], v6, s[60:63], s82 offen sc1\n"
    "v_accvgpr_read_b32 v168, a88\n"
    "v_accvgpr_read_b32 v169, a89\n"
    "v_accvgpr_read_b32 v170, a90\n"
    "v_accvgpr_read_b32 v171, a91\n"
    "v_accvgpr_read_b32 v172, a92\n"
    "v_accvgpr_read_b32 v173, a93\n"
    "v_accvgpr_read_b32 v174, a94\n"
    "v_accvgpr_read_b32 v175, a95\n"
    "v_pk_fma_f32 v[28:29], v[168:169], v[26:27], v[28:29]\n"
    "v_pk_fma_f32 v[28:29], v[170:171], v[26:27], v[28:29]\n"
    "v_pk_fma_f32 v[28:29], v[172:173], v[26:27], v[28:29]\n"
    "v_pk_fma_f32 v[28:29], v[174:175], v[26:27], v[28:29]\n"
    "v_pk_mul_f32 v[168:169], v[168:169], v[30:31] op_sel_hi:[1,0]\n"
    "v_pk_mul_f32 v[170:171], v[170:171], v[30:31] op_sel_hi:[1,0]\n"
    "v_pk_mul_f32 v[172:173], v[172:173], v[30:31] op_sel_hi:[1,0]\n"
    "v_pk_mul_f32 v[174:175], v[174:175], v[30:31] op_sel_hi:[1,0]\n"
    "v_cvt_pk_bf16_f32 v188, v168, v169\n"
    "v_cvt_pk_bf16_f32 v189, v170, v171\n"
    "v_cvt_pk_bf16_f32 v190, v172, v173\n"
    "v_cvt_pk_bf16_f32 v191, v174, v175\n"
    "buffer_store_dwordx4 v[188:191], v6, s[60:63], s82 offen offset:1024 sc1\n"
    "v_rcp_f32 v30, v92\n"
    "v_accvgpr_read_b32 v160, a96\n"
    "v_accvgpr_read_b32 v161, a97\n"
    "v_accvgpr_read_b32 v162, a98\n"
    "v_accvgpr_read_b32 v163, a99\n"
    "v_accvgpr_read_b32 v164, a100\n"
    "v_accvgpr_read_b32 v165, a101\n"
    "v_accvgpr_read_b32 v166, a102\n"
    "v_accvgpr_read_b32 v167, a103\n"
    "v_pk_fma_f32 v[28:29], v[160:161], v[26:27], v[28:29]\n"
    "v_pk_fma_f32 v[28:29], v[162:163], v[26:27], v[28:29]\n"
    "v_pk_fma_f32 v[28:29], v[164:165], v[26:27], v[28:29]\n"
    "v_pk_fma_f32 v[28:29], v[166:167], v[26:27], v[28:29]\n"
    "v_pk_mul_f32 v[160:161], v[160:161], v[30:31] op_sel_hi:[1,0]\n"
    "v_pk_mul_f32 v[162:163], v[162:163], v[30:31] op_sel_hi:[1,0]\n"
    "v_pk_mul_f32 v[164:165], v[164:165], v[30:31] op_sel_hi:[1,0]\n"
    "v_pk_mul_f32 v[166:167], v[166:167], v[30:31] op_sel_hi:[1,0]\n"
    "v_cvt_pk_bf16_f32 v176, v160, v161\n"
    "v_cvt_pk_bf16_f32 v177, v162, v163\n"
    "v_cvt_pk_bf16_f32 v178, v164, v165\n"
    "v_cvt_pk_bf16_f32 v179, v166, v167\n"
    "buffer_store_dwordx4 v[176:179], v7, s[60:63], s81 offen sc1\n"
    "v_accvgpr_read_b32 v168, a104\n"
    "v_accvgpr_read_b32 v169, a105\n"
    "v_accvgpr_read_b32 v170, a106\n"
    "v_accvgpr_read_b32 v171, a107\n"
    "v_accvgpr_read_b32 v172, a108\n"
    "v_accvgpr_read_b32 v173, a109\n"
    "v_accvgpr_read_b32 v174, a110\n"
    "v_accvgpr_read_b32 v175, a111\n"
    "v_pk_fma_f32 v[28:29], v[168:169], v[26:27], v[28:29]\n"
    "v_pk_fma_f32 v[28:29], v[170:171], v[26:27], v[28:29]\n"
    "v_pk_fma_f32 v[28:29], v[172:173], v[26:27], v[28:29]\n"
    "v_pk_fma_f32 v[28:29], v[174:175], v[26:27], v[28:29]\n"
    "v_pk_mul_f32 v[168:169], v[168:169], v[30:31] op_sel_hi:[1,0]\n"
    "v_pk_mul_f32 v[170:171], v[170:171], v[30:31] op_sel_hi:[1,0]\n"
    "v_pk_mul_f32 v[172:173], v[172:173], v[30:31] op_sel_hi:[1,0]\n"
    "v_pk_mul_f32 v[174:175], v[174:175], v[30:31] op_sel_hi:[1,0]\n"
    "v_cvt_pk_bf16_f32 v180, v168, v169\n"
    "v_cvt_pk_bf16_f32 v181, v170, v171\n"
    "v_cvt_pk_bf16_f32 v182, v172, v173\n"
    "v_cvt_pk_bf16_f32 v183, v174, v175\n"
    "buffer_store_dwordx4 v[180:183], v7, s[60:63], s81 offen offset:1024 sc1\n"
    "v_accvgpr_read_b32 v160, a112\n"
    "v_accvgpr_read_b32 v161, a113\n"
    "v_accvgpr_read_b32 v162, a114\n"
    "v_accvgpr_read_b32 v163, a115\n"
    "v_accvgpr_read_b32 v164, a116\n"
    "v_accvgpr_read_b32 v165, a117\n"
    "v_accvgpr_read_b32 v166, a118\n"
    "v_accvgpr_read_b32 v167, a119\n"
    "v_pk_fma_f32 v[28:29], v[160:161], v[26:27], v[28:29]\n"
    "v_pk_fma_f32 v[28:29], v[162:163], v[26:27], v[28:29]\n"
    "v_pk_fma_f32 v[28:29], v[164:165], v[26:27], v[28:29]\n"
    "v_pk_fma_f32 v[28:29], v[166:167], v[26:27], v[28:29]\n"
    "v_pk_mul_f32 v[160:161], v[160:161], v[30:31] op_sel_hi:[1,0]\n"
    "v_pk_mul_f32 v[162:163], v[162:163], v[30:31] op_sel_hi:[1,0]\n"
    "v_pk_mul_f32 v[164:165], v[164:165], v[30:31] op_sel_hi:[1,0]\n"
    "v_pk_mul_f32 v[166:167], v[166:167], v[30:31] op_sel_hi:[1,0]\n"
    "v_cvt_pk_bf16_f32 v184, v160, v161\n"
    "v_cvt_pk_bf16_f32 v185, v162, v163\n"
    "v_cvt_pk_bf16_f32 v186, v164, v165\n"
    "v_cvt_pk_bf16_f32 v187, v166, v167\n"
    "buffer_store_dwordx4 v[184:187], v7, s[60:63], s82 offen sc1\n"
    "v_accvgpr_read_b32 v168, a120\n"
    "v_accvgpr_read_b32 v169, a121\n"
    "v_accvgpr_read_b32 v170, a122\n"
    "v_accvgpr_read_b32 v171, a123\n"
    "v_accvgpr_read_b32 v172, a124\n"
    "v_accvgpr_read_b32 v173, a125\n"
    "v_accvgpr_read_b32 v174, a126\n"
    "v_accvgpr_read_b32 v175, a127\n"
    "v_pk_fma_f32 v[28:29], v[168:169], v[26:27], v[28:29]\n"
    "v_pk_fma_f32 v[28:29], v[170:171], v[26:27], v[28:29]\n"
    "v_pk_fma_f32 v[28:29], v[172:173], v[26:27], v[28:29]\n"
    "v_pk_fma_f32 v[28:29], v[174:175], v[26:27], v[28:29]\n"
    "v_pk_mul_f32 v[168:169], v[168:169], v[30:31] op_sel_hi:[1,0]\n"
    "v_pk_mul_f32 v[170:171], v[170:171], v[30:31] op_sel_hi:[1,0]\n"
    "v_pk_mul_f32 v[172:173], v[172:173], v[30:31] op_sel_hi:[1,0]\n"
    "v_pk_mul_f32 v[174:175], v[174:175], v[30:31] op_sel_hi:[1,0]\n"
    "v_cvt_pk_bf16_f32 v188, v168, v169\n"
    "v_cvt_pk_bf16_f32 v189, v170, v171\n"
    "v_cvt_pk_bf16_f32 v190, v172, v173\n"
    "v_cvt_pk_bf16_f32 v191, v174, v175\n"
    "buffer_store_dwordx4 v[188:191], v7, s[60:63], s82 offen offset:1024 sc1\n"
    "v_add_f32 v28, v28, v29\n"
    "v_min_f32 v30, v88, v92\n"
    "v_max_f32 v31, v88, v92\n"
    "v_fma_f32 v28, v88, v26, v28\n"
    "v_fma_f32 v28, v92, v26, v28\n"
    "v_add_f32 v30, v30, v28\n"
    "s_mov_b32 s81, 0x21800000\n"
    "s_mov_b32 s82, 0x5d800000\n"
    "v_cmp_nle_f32 vcc, s81, v30\n"
    "v_cmp_nge_f32 s[92:93], s82, v31\n"
    "s_nop 0\n"
    "s_or_b64 vcc, vcc, s[92:93]\n"
    "s_cselect_b32 s81, 2, 0\n"
    "s_or_b32 s88, s88, s81\n"
    "s_add_u32 s52, s32, 0x80000\n"
    "s_addc_u32 s53, s33, 0\n"
    "s_mov_b32 s54, 0x38000\n"
    "s_mov_b32 s55, 0x20000\n"
    "s_waitcnt vmcnt(8)\n"
    "v_lshlrev_b32 v96, 16, v128\n"
    "v_and_b32 v97, 0xffff0000, v128\n"
    "v_lshlrev_b32 v98, 16, v129\n"
    "v_and_b32 v99, 0xffff0000, v129\n"
    "v_lshlrev_b32 v100, 16, v130\n"
    "v_and_b32 v101, 0xffff0000, v130\n"
    "v_lshlrev_b32 v102, 16, v131\n"
    "v_and_b32 v103, 0xffff0000, v131\n"
    "v_lshlrev_b32 v104, 16, v132\n"
    "v_and_b32 v105, 0xffff0000, v132\n"
    "v_lshlrev_b32 v106, 16, v133\n"
    "v_and_b32 v107, 0xffff0000, v133\n"
    "v_lshlrev_b32 v108, 16, v134\n"
    "v_and_b32 v109, 0xffff0000, v134\n"
    "v_lshlrev_b32 v110, 16, v135\n"
    "v_and_b32 v111, 0xffff0000, v135\n"
    "v_pk_add_f32 v[96:97], v[96:97], v[104:105]\n"
    "v_pk_add_f32 v[98:99], v[98:99], v[106:107]\n"
    "v_pk_add_f32 v[100:101], v[100:101], v[108:109]\n"
    "v_pk_add_f32 v[102:103], v[102:103], v[110:111]\n"
    "v_lshlrev_b32 v104, 16, v136\n"
    "v_and_b32 v105, 0xffff0000, v136\n"
    "v_lshlrev_b32 v106, 16, v137\n"
    "v_and_b32 v107, 0xffff0000, v137\n"
    "v_lshlrev_b32 v108, 16, v138\n"
    "v_and_b32 v109, 0xffff0000, v138\n"
    "v_lshlrev_b32 v110, 16, v139\n"
    "v_and_b32 v111, 0xffff0000, v139\n"
    "v_pk_add_f32 v[96:97], v[96:97], v[104:105]\n"
    "v_pk_add_f32 v[98:99], v[98:99], v[106:107]\n"
    "v_pk_add_f32 v[100:101], v[100:101], v[108:109]\n"
    "v_pk_add_f32 v[102:103], v[102:103], v[110:111]\n"
    "v_lshlrev_b32 v104, 16, v140\n"
    "v_and_b32 v105, 0xffff0000, v140\n"
    "v_lshlrev_b32 v106, 16, v141\n"
    "v_and_b32 v107, 0xffff0000, v141\n"
    "v_lshlrev_b32 v108, 16, v142\n"
    "v_and_b32 v109, 0xffff0000, v142\n"
    "v_lshlrev_b32 v110, 16, v143\n"
    "v_and_b32 v111, 0xffff0000, v143\n"
    "v_pk_add_f32 v[96:97], v[96:97], v[104:105]\n"
    "v_pk_add_f32 v[98:99], v[98:99], v[106:107]\n"
    "v_pk_add_f32 v[100:101], v[100:101], v[108:109]\n"
    "v_pk_add_f32 v[102:103], v[102:103], v[110:111]\n"
    "v_lshlrev_b32 v104, 16, v144\n"
    "v_and_b32 v105, 0xffff0000, v144\n"
    "v_lshlrev_b32 v106, 16, v145\n"
    "v_and_b32 v107, 0xffff0000, v145\n"
    "v_lshlrev_b32 v108, 16, v146\n"
    "v_and_b32 v109, 0xffff0000, v146\n"
    "v_lshlrev_b32 v110, 16, v147\n"
    "v_and_b32 v111, 0xffff0000, v147\n"
    "v_pk_add_f32 v[96:97], v[96:97], v[104:105]\n"
    "v_pk_add_f32 v[98:99], v[98:99], v[106:107]\n"
    "v_pk_add_f32 v[100:101], v[100:101], v[108:109]\n"
    "v_pk_add_f32 v[102:103], v[102:103], v[110:111]\n"
    "v_lshlrev_b32 v104, 16, v148\n"
    "v_and_b32 v105, 0xffff0000, v148\n"
    "v_lshlrev_b32 v106, 16, v149\n"
    "v_and_b32 v107, 0xffff0000, v149\n"
    "v_lshlrev_b32 v108, 16, v150\n"
    "v_and_b32 v109, 0xffff0000, v150\n"
    "v_lshlrev_b32 v110, 16, v151\n"
    "v_and_b32 v111, 0xffff0000, v151\n"
    "v_pk_add_f32 v[96:97], v[96:97], v[104:105]\n"
    "v_pk_add_f32 v[98:99], v[98:99], v[106:107]\n"
    "v_pk_add_f32 v[100:101], v[100:101], v[108:109]\n"
    "v_pk_add_f32 v[102:103], v[102:103], v[110:111]\n"
    "v_lshlrev_b32 v104, 16, v152\n"
    "v_and_b32 v105, 0xffff0000, v152\n"
    "v_lshlrev_b32 v106, 16, v153\n"
    "v_and_b32 v107, 0xffff0000, v153\n"
    "v_lshlrev_b32 v108, 16, v154\n"
    "v_and_b32 v109, 0xffff0000, v154\n"
    "v_lshlrev_b32 v110, 16, v155\n"
    "v_and_b32 v111, 0xffff0000, v155\n"
    "v_pk_add_f32 v[96:97], v[96:97], v[104:105]\n"
    "v_pk_add_f32 v[98:99], v[98:99], v[106:107]\n"
    "v_pk_add_f32 v[100:101], v[100:101], v[108:109]\n"
    "v_pk_add_f32 v[102:103], v[102:103], v[110:111]\n"
    "v_lshlrev_b32 v104, 16, v156\n"
    "v_and_b32 v105, 0xffff0000, v156\n"
    "v_lshlrev_b32 v106, 16, v157\n"
    "v_and_b32 v107, 0xffff0000, v157\n"
    "v_lshlrev_b32 v108, 16, v158\n"
    "v_and_b32 v109, 0xffff0000, v158\n"
    "v_lshlrev_b32 v110, 16, v159\n"
    "v_and_b32 v111, 0xffff0000, v159\n"
    "v_pk_add_f32 v[96:97], v[96:97], v[104:105]\n"
    "v_pk_add_f32 v[98:99], v[98:99], v[106:107]\n"
    "v_pk_add_f32 v[100:101], v[100:101], v[108:109]\n"
    "v_pk_add_f32 v[102:103], v[102:103], v[110:111]\n"
    "v_add_f32_dpp v96, v96, v96 row_ror:8 row_mask:0xf bank_mask:0xf\n"
    "v_add_f32_dpp v97, v97, v97 row_ror:8 row_mask:0xf bank_mask:0xf\n"
    "v_add_f32_dpp v98, v98, v98 row_ror:8 row_mask:0xf bank_mask:0xf\n"
    "v_add_f32_dpp v99, v99, v99 row_ror:8 row_mask:0xf bank_mask:0xf\n"
    "v_add_f32_dpp v100, v100, v100 row_ror:8 row_mask:0xf bank_mask:0xf\n"
    "v_add_f32_dpp v101, v101, v101 row_ror:8 row_mask:0xf bank_mask:0xf\n"
    "v_add_f32_dpp v102, v102, v102 row_ror:8 row_mask:0xf bank_mask:0xf\n"
    "v_add_f32_dpp v103, v103, v103 row_ror:8 row_mask:0xf bank_mask:0xf\n"
    "v_mov_b32 v104, v96\n"
    "v_mov_b32 v105, v97\n"
    "v_mov_b32 v106, v98\n"
    "v_mov_b32 v107, v99\n"
    "v_mov_b32 v108, v100\n"
    "v_mov_b32 v109, v101\n"
    "v_mov_b32 v110, v102\n"
    "v_mov_b32 v111, v103\n"
    "s_nop 1\n"
    "v_permlane32_swap_b32 v96, v104\n"
    "v_permlane32_swap_b32 v97, v105\n"
    "v_permlane32_swap_b32 v98, v106\n"
    "v_permlane32_swap_b32 v99, v107\n"
    "v_permlane32_swap_b32 v100, v108\n"
    "v_permlane32_swap_b32 v101, v109\n"
    "v_permlane32_swap_b32 v102, v110\n"
    "v_permlane32_swap_b32 v103, v111\n"
    "s_nop 0\n"
    "v_add_f32 v96, v96, v104\n"
    "v_add_f32 v97, v97, v105\n"
    "v_add_f32 v98, v98, v106\n"
    "v_add_f32 v99, v99, v107\n"
    "v_add_f32 v100, v100, v108\n"
    "v_add_f32 v101, v101, v109\n"
    "v_add_f32 v102, v102, v110\n"
    "v_add_f32 v103, v103, v111\n"
    "v_mov_b32 v104, v96\n"
    "v_mov_b32 v105, v97\n"
    "v_mov_b32 v106, v98\n"
    "v_mov_b32 v107, v99\n"
    "v_mov_b32 v108, v100\n"
    "v_mov_b32 v109, v101\n"
    "v_mov_b32 v110, v102\n"
    "v_mov_b32 v111, v103\n"
    "s_nop 1\n"
    "v_permlane16_swap_b32 v96, v104\n"
    "v_permlane16_swap_b32 v97, v105\n"
    "v_permlane16_swap_b32 v98, v106\n"
    "v_permlane16_swap_b32 v99, v107\n"
    "v_permlane16_swap_b32 v100, v108\n"
    "v_permlane16_swap_b32 v101, v109\n"
    "v_permlane16_swap_b32 v102, v110\n"
    "v_permlane16_swap_b32 v103, v111\n"
    "s_nop 0\n"
    "v_add_f32 v96, v96, v104\n"
    "v_add_f32 v97, v97, v105\n"
    "v_add_f32 v98, v98, v106\n"
    "v_add_f32 v99, v99, v107\n"
    "v_add_f32 v100, v100, v108\n"
    "v_add_f32 v101, v101, v109\n"
    "v_add_f32 v102, v102, v110\n"
    "v_add_f32 v103, v103, v111\n"
    "s_mul_i32 s81, s84, 0x3800\n"
    "s_lshl_b32 s82, s85, 8\n"
    "s_add_u32 s81, s81, s82\n"
    "v_lshlrev_b32 v26, 5, v1\n"
    "v_cmp_gt_u32 vcc, 8, v1\n"
    "v_mov_b32 v27, 0x7ffffff0\n"
    "s_nop 0\n"
    "v_cndmask_b32 v26, v27, v26, vcc\n"
    "buffer_store_dwordx4 v[96:99], v26, s[52:55], s81 offen sc1\n"
    "buffer_store_dwordx4 v[100:103], v26, s[52:55], s81 offen offset:16 sc1\n"
    "s_cmp_eq_u32 s88, 0\n"
    "s_cbranch_scc1 fb_end\n"
    "s_waitcnt vmcnt(0) lgkmcnt(0)\n"
    "v_mov_b32 v67, 0xf149f2ca\n"
    "v_mov_b32 v32, 0x3f803f80\n"
    "v_mov_b32 v33, 0x3f803f80\n"
    "v_mov_b32 v34, 0x3f803f80\n"
    "v_mov_b32 v35, 0x3f803f80\n"
    "s_and_b32 s81, s88, 1\n"
    "s_cbranch_scc0 fb_skip0\n"
    "v_mov_b32 v96, v192\n"
    "v_mov_b32 v97, v193\n"
    "v_mov_b32 v98, v194\n"
    "v_mov_b32 v99, v195\n"
    "v_mov_b32 v100, v196\n"
    "v_mov_b32 v101, v197\n"
    "v_mov_b32 v102, v198\n"
    "v_mov_b32 v103, v199\n"
    "v_mov_b32 v104, v200\n"
    "v_mov_b32 v105, v201\n"
    "v_mov_b32 v106, v202\n"
    "v_mov_b32 v107, v203\n"
    "v_mov_b32 v108, v204\n"
    "v_mov_b32 v109, v205\n"
    "v_mov_b32 v110, v206\n"
    "v_mov_b32 v111, v207\n"
    "v_mov_b32 v66, v4\n"
    "v_lshrrev_b32 v64, 11, v66\n"
    "v_lshlrev_b32 v64, 4, v64\n"
    "s_nop 0\n"
    "v_readfirstlane_b32 s84, v64\n"
    "v_and_b32 v68, 15, v1\n"
    "v_add_u32 v64, v64, v68\n"
    "v_lshrrev_b32 v68, 4, v1\n"
    "v_lshlrev_b32 v68, 2, v68\n"
    "v_sub_u32 v64, v64, v68\n"
    "s_lshr_b32 s84, s84, 6\n"
    "s_add_u32 s84, s84, 1\n"
    "s_getpc_b64 s[90:91]\n"
    "s_add_u32 s90, s90, 12\n"
    "s_addc_u32 s91, s91, 0\n"
    "s_branch fb_sub\n"
    "fb_skip0:\n"
    "s_and_b32 s81, s88, 1\n"
    "s_cbranch_scc0 fb_skip1\n"
    "v_mov_b32 v96, v208\n"
    "v_mov_b32 v97, v209\n"
    "v_mov_b32 v98, v210\n"
    "v_mov_b32 v99, v211\n"
    "v_mov_b32 v100, v212\n"
    "v_mov_b32 v101, v213\n"
    "v_mov_b32 v102, v214\n"
    "v_mov_b32 v103, v215\n"
    "v_mov_b32 v104, v216\n"
    "v_mov_b32 v105, v217\n"
    "v_mov_b32 v106, v218\n"
    "v_mov_b32 v107, v219\n"
    "v_mov_b32 v108, v220\n"
    "v_mov_b32 v109, v221\n"
    "v_mov_b32 v110, v222\n"
    "v_mov_b32 v111, v223\n"
    "v_mov_b32 v66, v5\n"
    "v_lshrrev_b32 v64, 11, v66\n"
    "v_lshlrev_b32 v64, 4, v64\n"
    "s_nop 0\n"
    "v_readfirstlane_b32 s84, v64\n"
    "v_and_b32 v68, 15, v1\n"
    "v_add_u32 v64, v64, v68\n"
    "v_lshrrev_b32 v68, 4, v1\n"
    "v_lshlrev_b32 v68, 2, v68\n"
    "v_sub_u32 v64, v64, v68\n"
    "s_lshr_b32 s84, s84, 6\n"
    "s_add_u32 s84, s84, 1\n"
    "s_getpc_b64 s[90:91]\n"
    "s_add_u32 s90, s90, 12\n"
    "s_addc_u32 s91, s91, 0\n"
    "s_branch fb_sub\n"
    "fb_skip1:\n"
    "s_and_b32 s81, s88, 2\n"
    "s_cbranch_scc0 fb_skip2\n"
    "v_mov_b32 v96, v224\n"
    "v_mov_b32 v97, v225\n"
    "v_mov_b32 v98, v226\n"
    "v_mov_b32 v99, v227\n"
    "v_mov_b32 v100, v228\n"
    "v_mov_b32 v101, v229\n"
    "v_mov_b32 v102, v230\n"
    "v_mov_b32 v103, v231\n"
    "v_mov_b32 v104, v232\n"
    "v_mov_b32 v105, v233\n"
    "v_mov_b32 v106, v234\n"
    "v_mov_b32 v107, v235\n"
    "v_mov_b32 v108, v236\n"
    "v_mov_b32 v109, v237\n"
    "v_mov_b32 v110, v238\n"
    "v_mov_b32 v111, v239\n"
    "v_mov_b32 v66, v6\n"
    "v_lshrrev_b32 v64, 11, v66\n"
    "v_lshlrev_b32 v64, 4, v64\n"
    "s_nop 0\n"
    "v_readfirstlane_b32 s84, v64\n"
    "v_and_b32 v68, 15, v1\n"
    "v_add_u32 v64, v64, v68\n"
    "v_lshrrev_b32 v68, 4, v1\n"
    "v_lshlrev_b32 v68, 2, v68\n"
    "v_sub_u32 v64, v64, v68\n"
    "s_lshr_b32 s84, s84, 6\n"
    "s_add_u32 s84, s84, 1\n"
    "s_getpc_b64 s[90:91]\n"
    "s_add_u32 s90, s90, 12\n"
    "s_addc_u32 s91, s91, 0\n"
    "s_branch fb_sub\n"
    "fb_skip2:\n"
    "s_and_b32 s81, s88, 2\n"
    "s_cbranch_scc0 fb_skip3\n"
    "v_mov_b32 v96, v240\n"
    "v_mov_b32 v97, v241\n"
    "v_mov_b32 v98, v242\n"
    "v_mov_b32 v99, v243\n"
    "v_mov_b32 v100, v244\n"
    "v_mov_b32 v101, v245\n"
    "v_mov_b32 v102, v246\n"
    "v_mov_b32 v103, v247\n"
    "v_mov_b32 v104, v248\n"
    "v_mov_b32 v105, v249\n"
    "v_mov_b32 v106, v250\n"
    "v_mov_b32 v107, v251\n"
    "v_mov_b32 v108, v252\n"
    "v_mov_b32 v109, v253\n"
    "v_mov_b32 v110, v254\n"
    "v_mov_b32 v111, v255\n"
    "v_mov_b32 v66, v7\n"
    "v_lshrrev_b32 v64, 11, v66\n"
    "v_lshlrev_b32 v64, 4, v64\n"
    "s_nop 0\n"
    "v_readfirstlane_b32 s84, v64\n"
    "v_and_b32 v68, 15, v1\n"
    "v_add_u32 v64, v64, v68\n"
    "v_lshrrev_b32 v68, 4, v1\n"
    "v_lshlrev_b32 v68, 2, v68\n"
    "v_sub_u32 v64, v64, v68\n"
    "s_lshr_b32 s84, s84, 6\n"
    "s_add_u32 s84, s84, 1\n"
    "s_getpc_b64 s[90:91]\n"
    "s_add_u32 s90, s90, 12\n"
    "s_addc_u32 s91, s91, 0\n"
    "s_branch fb_sub\n"
    "fb_skip3:\n"
    "s_branch fb_end\n"
    "fb_sub:\n"
    "s_mov_b32 s86, 0\n"
    "s_mov_b32 s85, 0\n"
    "s_mov_b32 s87, s84\n"
    "v_mov_b32 v65, v67\n"
    "fb_p1:\n"
    "s_add_u32 s81, s86, 0\n"
    "buffer_load_dwordx4 a[128:131], v24, s[56:59], s81 offen sc1\n"
    "buffer_load_dwordx4 a[132:135], v24, s[56:59], s81 offen offset:1024 sc1\n"
    "buffer_load_dwordx4 a[136:139], v24, s[56:59], s81 offen offset:2048 sc1\n"
    "buffer_load_dwordx4 a[140:143], v24, s[56:59], s81 offen offset:3072 sc1\n"
    "s_add_u32 s81, s86, 0x1000\n"
    "buffer_load_dwordx4 a[144:147], v24, s[56:59], s81 offen sc1\n"
    "buffer_load_dwordx4 a[148:151], v24, s[56:59], s81 offen offset:1024 sc1\n"
    "buffer_load_dwordx4 a[152:155], v24, s[56:59], s81 offen offset:2048 sc1\n"
    "buffer_load_dwordx4 a[156:159], v24, s[56:59], s81 offen offset:3072 sc1\n"
    "s_add_u32 s81, s86, 0x2000\n"
    "buffer_load_dwordx4 a[160:163], v24, s[56:59], s81 offen sc1\n"
    "buffer_load_dwordx4 a[164:167], v24, s[56:59], s81 offen offset:1024 sc1\n"
    "buffer_load_dwordx4 a[168:171], v24, s[56:59], s81 offen offset:2048 sc1\n"
    "buffer_load_dwordx4 a[172:175], v24, s[56:59], s81 offen offset:3072 sc1\n"
    "s_add_u32 s81, s86, 0x3000\n"
    "buffer_load_dwordx4 a[176:179], v24, s[56:59], s81 offen sc1\n"
    "buffer_load_dwordx4 a[180:183], v24, s[56:59], s81 offen offset:1024 sc1\n"
    "buffer_load_dwordx4 a[184:187], v24, s[56:59], s81 offen offset:2048 sc1\n"
    "buffer_load_dwordx4 a[188:191], v24, s[56:59], s81 offen offset:3072 sc1\n"
    "s_waitcnt vmcnt(0)\n"
    "v_mfma_f32_16x16x32_bf16 v[128:131], a[128:131], v[96:99], 0\n"
    "v_mfma_f32_16x16x32_bf16 v[128:131], a[132:135], v[100:103], v[128:131]\n"
    "v_mfma_f32_16x16x32_bf16 v[128:131], a[136:139], v[104:107], v[128:131]\n"
    "v_mfma_f32_16x16x32_bf16 v[128:131], a[140:143], v[108:111], v[128:131]\n"
    "v_mfma_f32_16x16x32_bf16 v[132:135], a[144:147], v[96:99], 0\n"
    "v_mfma_f32_16x16x32_bf16 v[132:135], a[148:151], v[100:103], v[132:135]\n"
    "v_mfma_f32_16x16x32_bf16 v[132:135], a[152:155], v[104:107], v[132:135]\n"
    "v_mfma_f32_16x16x32_bf16 v[132:135], a[156:159], v[108:111], v[132:135]\n"
    "v_mfma_f32_16x16x32_bf16 v[136:139], a[160:163], v[96:99], 0\n"
    "v_mfma_f32_16x16x32_bf16 v[136:139], a[164:167], v[100:103], v[136:139]\n"
    "v_mfma_f32_16x16x32_bf16 v[136:139], a[168:171], v[104:107], v[136:139]\n"
    "v_mfma_f32_16x16x32_bf16 v[136:139], a[172:175], v[108:111], v[136:139]\n"
    "v_mfma_f32_16x16x32_bf16 v[140:143], a[176:179], v[96:99], 0\n"
    "v_mfma_f32_16x16x32_bf16 v[140:143], a[180:183], v[100:103], v[140:143]\n"
    "v_mfma_f32_16x16x32_bf16 v[140:143], a[184:187], v[104:107], v[140:143]\n"
    "v_mfma_f32_16x16x32_bf16 v[140:143], a[188:191], v[108:111], v[140:143]\n"
    "v_subrev_u32 v69, s85, v64\n"
    "s_nop 7\n"
    "v_cmp_gt_i32 vcc, 0, v69\n"
    "s_nop 1\n"
    "v_cndmask_b32 v128, v128, v67, vcc\n"
    "v_max_f32 v65, v65, v128\n"
    "v_cmp_gt_i32 vcc, 1, v69\n"
    "s_nop 1\n"
    "v_cndmask_b32 v129, v129, v67, vcc\n"
    "v_max_f32 v65, v65, v129\n"
    "v_cmp_gt_i32 vcc, 2, v69\n"
    "s_nop 1\n"
    "v_cndmask_b32 v130, v130, v67, vcc\n"
    "v_max_f32 v65, v65, v130\n"
    "v_cmp_gt_i32 vcc, 3, v69\n"
    "s_nop 1\n"
    "v_cndmask_b32 v131, v131, v67, vcc\n"
    "v_max_f32 v65, v65, v131\n"
    "v_cmp_gt_i32 vcc, 16, v69\n"
    "s_nop 1\n"
    "v_cndmask_b32 v132, v132, v67, vcc\n"
    "v_max_f32 v65, v65, v132\n"
    "v_cmp_gt_i32 vcc, 17, v69\n"
    "s_nop 1\n"
    "v_cndmask_b32 v133, v133, v67, vcc\n"
    "v_max_f32 v65, v65, v133\n"
    "v_cmp_gt_i32 vcc, 18, v69\n"
    "s_nop 1\n"
    "v_cndmask_b32 v134, v134, v67, vcc\n"
    "v_max_f32 v65, v65, v134\n"
    "v_cmp_gt_i32 vcc, 19, v69\n"
    "s_nop 1\n"
    "v_cndmask_b32 v135, v135, v67, vcc\n"
    "v_max_f32 v65, v65, v135\n"
    "v_cmp_gt_i32 vcc, 32, v69\n"
    "s_nop 1\n"
    "v_cndmask_b32 v136, v136, v67, vcc\n"
    "v_max_f32 v65, v65, v136\n"
    "v_cmp_gt_i32 vcc, 33, v69\n"
    "s_nop 1\n"
    "v_cndmask_b32 v137, v137, v67, vcc\n"
    "v_max_f32 v65, v65, v137\n"
    "v_cmp_gt_i32 vcc, 34, v69\n"
    "s_nop 1\n"
    "v_cndmask_b32 v138, v138, v67, vcc\n"
    "v_max_f32 v65, v65, v138\n"
    "v_cmp_gt_i32 vcc, 35, v69\n"
    "s_nop 1\n"
    "v_cndmask_b32 v139, v139, v67, vcc\n"
    "v_max_f32 v65, v65, v139\n"
    "v_cmp_gt_i32 vcc, 48, v69\n"
    "s_nop 1\n"
    "v_cndmask_b32 v140, v140, v67, vcc\n"
    "v_max_f32 v65, v65, v140\n"
    "v_cmp_gt_i32 vcc, 49, v69\n"
    "s_nop 1\n"
    "v_cndmask_b32 v141, v141, v67, vcc\n"
    "v_max_f32 v65, v65, v141\n"
    "v_cmp_gt_i32 vcc, 50, v69\n"
    "s_nop 1\n"
    "v_cndmask_b32 v142, v142, v67, vcc\n"
    "v_max_f32 v65, v65, v142\n"
    "v_cmp_gt_i32 vcc, 51, v69\n"
    "s_nop 1\n"
    "v_cndmask_b32 v143, v143, v67, vcc\n"
    "v_max_f32 v65, v65, v143\n"
    "s_add_u32 s86, s86, 0x8000\n"
    "s_add_u32 s85, s85, 64\n"
    "s_sub_u32 s87, s87, 1\n"
    "s_cmp_lg_u32 s87, 0\n"
    "s_cbranch_scc1 fb_p1\n"
    "v_mov_b32 v68, v65\n"
    "s_nop 1\n"
    "v_permlane32_swap_b32 v65, v68\n"
    "s_nop 0\n"
    "v_max_f32 v65, v65, v68\n"
    "v_mov_b32 v68, v65\n"
    "s_nop 1\n"
    "v_permlane16_swap_b32 v65, v68\n"
    "s_nop 0\n"
    "v_max_f32 v65, v65, v68\n"
    "s_mov_b32 s86, 0\n"
    "s_mov_b32 s85, 0\n"
    "s_mov_b32 s87, s84\n"
    "v_accvgpr_write_b32 a0, 0\n"
    "v_accvgpr_write_b32 a1, 0\n"
    "v_accvgpr_write_b32 a2, 0\n"
    "v_accvgpr_write_b32 a3, 0\n"
    "v_accvgpr_write_b32 a4, 0\n"
    "v_accvgpr_write_b32 a5, 0\n"
    "v_accvgpr_write_b32 a6, 0\n"
    "v_accvgpr_write_b32 a7, 0\n"
    "v_accvgpr_write_b32 a8, 0\n"
    "v_accvgpr_write_b32 a9, 0\n"
    "v_accvgpr_write_b32 a10, 0\n"
    "v_accvgpr_write_b32 a11, 0\n"
    "v_accvgpr_write_b32 a12, 0\n"
    "v_accvgpr_write_b32 a13, 0\n"
    "v_accvgpr_write_b32 a14, 0\n"
    "v_accvgpr_write_b32 a15, 0\n"
    "v_accvgpr_write_b32 a16, 0\n"
    "v_accvgpr_write_b32 a17, 0\n"
    "v_accvgpr_write_b32 a18, 0\n"
    "v_accvgpr_write_b32 a19, 0\n"
    "v_accvgpr_write_b32 a20, 0\n"
    "v_accvgpr_write_b32 a21, 0\n"
    "v_accvgpr_write_b32 a22, 0\n"
    "v_accvgpr_write_b32 a23, 0\n"
    "v_accvgpr_write_b32 a24, 0\n"
    "v_accvgpr_write_b32 a25, 0\n"
    "v_accvgpr_write_b32 a26, 0\n"
    "v_accvgpr_write_b32 a27, 0\n"
    "v_accvgpr_write_b32 a28, 0\n"
    "v_accvgpr_write_b32 a29, 0\n"
    "v_accvgpr_write_b32 a30, 0\n"
    "v_accvgpr_write_b32 a31, 0\n"
    "v_mov_b32 v176, 0\n"
    "v_mov_b32 v177, 0\n"
    "v_mov_b32 v178, 0\n"
    "v_mov_b32 v179, 0\n"
    "fb_p2:\n"
    "s_add_u32 s81, s86, 0\n"
    "buffer_load_dwordx4 a[128:131], v24, s[56:59], s81 offen sc1\n"
    "buffer_load_dwordx4 a[132:135], v24, s[56:59], s81 offen offset:1024 sc1\n"
    "buffer_load_dwordx4 a[136:139], v24, s[56:59], s81 offen offset:2048 sc1\n"
    "buffer_load_dwordx4 a[140:143], v24, s[56:59], s81 offen offset:3072 sc1\n"
    "s_add_u32 s81, s86, 0x1000\n"
    "buffer_load_dwordx4 a[144:147], v24, s[56:59], s81 offen sc1\n"
    "buffer_load_dwordx4 a[148:151], v24, s[56:59], s81 offen offset:1024 sc1\n"
    "buffer_load_dwordx4 a[152:155], v24, s[56:59], s81 offen offset:2048 sc1\n"
    "buffer_load_dwordx4 a[156:159], v24, s[56:59], s81 offen offset:3072 sc1\n"
    "s_add_u32 s81, s86, 0x2000\n"
    "buffer_load_dwordx4 a[160:163], v24, s[56:59], s81 offen sc1\n"
    "buffer_load_dwordx4 a[164:167], v24, s[56:59], s81 offen offset:1024 sc1\n"
    "buffer_load_dwordx4 a[168:171], v24, s[56:59], s81 offen offset:2048 sc1\n"
    "buffer_load_dwordx4 a[172:175], v24, s[56:59], s81 offen offset:3072 sc1\n"
    "s_add_u32 s81, s86, 0x3000\n"
    "buffer_load_dwordx4 a[176:179], v24, s[56:59], s81 offen sc1\n"
    "buffer_load_dwordx4 a[180:183], v24, s[56:59], s81 offen offset:1024 sc1\n"
    "buffer_load_dwordx4 a[184:187], v24, s[56:59], s81 offen offset:2048 sc1\n"
    "buffer_load_dwordx4 a[188:191], v24, s[56:59], s81 offen offset:3072 sc1\n"
    "s_add_u32 s81, s86, 0x4000\n"
    "buffer_load_dwordx4 a[192:195], v24, s[56:59], s81 offen sc1\n"
    "buffer_load_dwordx4 a[196:199], v24, s[56:59], s81 offen offset:1024 sc1\n"
    "buffer_load_dwordx4 a[200:203], v24, s[56:59], s81 offen offset:2048 sc1\n"
    "buffer_load_dwordx4 a[204:207], v24, s[56:59], s81 offen offset:3072 sc1\n"
    "s_add_u32 s81, s86, 0x5000\n"
    "buffer_load_dwordx4 a[208:211], v24, s[56:59], s81 offen sc1\n"
    "buffer_load_dwordx4 a[212:215], v24, s[56:59], s81 offen offset:1024 sc1\n"
    "buffer_load_dwordx4 a[216:219], v24, s[56:59], s81 offen offset:2048 sc1\n"
    "buffer_load_dwordx4 a[220:223], v24, s[56:59], s81 offen offset:3072 sc1\n"
    "s_add_u32 s81, s86, 0x6000\n"
    "buffer_load_dwordx4 a[224:227], v24, s[56:59], s81 offen sc1\n"
    "buffer_load_dwordx4 a[228:231], v24, s[56:59], s81 offen offset:1024 sc1\n"
    "buffer_load_dwordx4 a[232:235], v24, s[56:59], s81 offen offset:2048 sc1\n"
    "buffer_load_dwordx4 a[236:239], v24, s[56:59], s81 offen offset:3072 sc1\n"
    "s_add_u32 s81, s86, 0x7000\n"
    "buffer_load_dwordx4 a[240:243], v24, s[56:59], s81 offen sc1\n"
    "buffer_load_dwordx4 a[244:247], v24, s[56:59], s81 offen offset:1024 sc1\n"
    "buffer_load_dwordx4 a[248:251], v24, s[56:59], s81 offen offset:2048 sc1\n"
    "buffer_load_dwordx4 a[252:255], v24, s[56:59], s81 offen offset:3072 sc1\n"
    "s_waitcnt vmcnt(0)\n"
    "v_mfma_f32_16x16x32_bf16 v[128:131], a[128:131], v[96:99], 0\n"
    "v_mfma_f32_16x16x32_bf16 v[128:131], a[132:135], v[100:103], v[128:131]\n"
    "v_mfma_f32_16x16x32_bf16 v[128:131], a[136:139], v[104:107], v[128:131]\n"
    "v_mfma_f32_16x16x32_bf16 v[128:131], a[140:143], v[108:111], v[128:131]\n"
    "v_mfma_f32_16x16x32_bf16 v[132:135], a[144:147], v[96:99], 0\n"
    "v_mfma_f32_16x16x32_bf16 v[132:135], a[148:151], v[100:103], v[132:135]\n"
    "v_mfma_f32_16x16x32_bf16 v[132:135], a[152:155], v[104:107], v[132:135]\n"
    "v_mfma_f32_16x16x32_bf16 v[132:135], a[156:159], v[108:111], v[132:135]\n"
    "v_mfma_f32_16x16x32_bf16 v[136:139], a[160:163], v[96:99], 0\n"
    "v_mfma_f32_16x16x32_bf16 v[136:139], a[164:167], v[100:103], v[136:139]\n"
    "v_mfma_f32_16x16x32_bf16 v[136:139], a[168:171], v[104:107], v[136:139]\n"
    "v_mfma_f32_16x16x32_bf16 v[136:139], a[172:175], v[108:111], v[136:139]\n"
    "v_mfma_f32_16x16x32_bf16 v[140:143], a[176:179], v[96:99], 0\n"
    "v_mfma_f32_16x16x32_bf16 v[140:143], a[180:183], v[100:103], v[140:143]\n"
    "v_mfma_f32_16x16x32_bf16 v[140:143], a[184:187], v[104:107], v[140:143]\n"
    "v_mfma_f32_16x16x32_bf16 v[140:143], a[188:191], v[108:111], v[140:143]\n"
    "v_subrev_u32 v69, s85, v64\n"
    "s_nop 7\n"
    "v_sub_f32 v128, v128, v65\n"
    "v_cmp_gt_i32 vcc, 0, v69\n"
    "s_nop 1\n"
    "v_cndmask_b32 v128, v128, v67, vcc\n"
    "v_exp_f32 v128, v128\n"
    "v_sub_f32 v129, v129, v65\n"
    "v_cmp_gt_i32 vcc, 1, v69\n"
    "s_nop 1\n"
    "v_cndmask_b32 v129, v129, v67, vcc\n"
    "v_exp_f32 v129, v129\n"
    "v_sub_f32 v130, v130, v65\n"
    "v_cmp_gt_i32 vcc, 2, v69\n"
    "s_nop 1\n"
    "v_cndmask_b32 v130, v130, v67, vcc\n"
    "v_exp_f32 v130, v130\n"
    "v_sub_f32 v131, v131, v65\n"
    "v_cmp_gt_i32 vcc, 3, v69\n"
    "s_nop 1\n"
    "v_cndmask_b32 v131, v131, v67, vcc\n"
    "v_exp_f32 v131, v131\n"
    "v_sub_f32 v132, v132, v65\n"
    "v_cmp_gt_i32 vcc, 16, v69\n"
    "s_nop 1\n"
    "v_cndmask_b32 v132, v132, v67, vcc\n"
    "v_exp_f32 v132, v132\n"
    "v_sub_f32 v133, v133, v65\n"
    "v_cmp_gt_i32 vcc, 17, v69\n"
    "s_nop 1\n"
    "v_cndmask_b32 v133, v133, v67, vcc\n"
    "v_exp_f32 v133, v133\n"
    "v_sub_f32 v134, v134, v65\n"
    "v_cmp_gt_i32 vcc, 18, v69\n"
    "s_nop 1\n"
    "v_cndmask_b32 v134, v134, v67, vcc\n"
    "v_exp_f32 v134, v134\n"
    "v_sub_f32 v135, v135, v65\n"
    "v_cmp_gt_i32 vcc, 19, v69\n"
    "s_nop 1\n"
    "v_cndmask_b32 v135, v135, v67, vcc\n"
    "v_exp_f32 v135, v135\n"
    "v_sub_f32 v136, v136, v65\n"
    "v_cmp_gt_i32 vcc, 32, v69\n"
    "s_nop 1\n"
    "v_cndmask_b32 v136, v136, v67, vcc\n"
    "v_exp_f32 v136, v136\n"
    "v_sub_f32 v137, v137, v65\n"
    "v_cmp_gt_i32 vcc, 33, v69\n"
    "s_nop 1\n"
    "v_cndmask_b32 v137, v137, v67, vcc\n"
    "v_exp_f32 v137, v137\n"
    "v_sub_f32 v138, v138, v65\n"
    "v_cmp_gt_i32 vcc, 34, v69\n"
    "s_nop 1\n"
    "v_cndmask_b32 v138, v138, v67, vcc\n"
    "v_exp_f32 v138, v138\n"
    "v_sub_f32 v139, v139, v65\n"
    "v_cmp_gt_i32 vcc, 35, v69\n"
    "s_nop 1\n"
    "v_cndmask_b32 v139, v139, v67, vcc\n"
    "v_exp_f32 v139, v139\n"
    "v_sub_f32 v140, v140, v65\n"
    "v_cmp_gt_i32 vcc, 48, v69\n"
    "s_nop 1\n"
    "v_cndmask_b32 v140, v140, v67, vcc\n"
    "v_exp_f32 v140, v140\n"
    "v_sub_f32 v141, v141, v65\n"
    "v_cmp_gt_i32 vcc, 49, v69\n"
    "s_nop 1\n"
    "v_cndmask_b32 v141, v141, v67, vcc\n"
    "v_exp_f32 v141, v141\n"
    "v_sub_f32 v142, v142, v65\n"
    "v_cmp_gt_i32 vcc, 50, v69\n"
    "s_nop 1\n"
    "v_cndmask_b32 v142, v142, v67, vcc\n"
    "v_exp_f32 v142, v142\n"
    "v_sub_f32 v143, v143, v65\n"
    "v_cmp_gt_i32 vcc, 51, v69\n"
    "s_nop 1\n"
    "v_cndmask_b32 v143, v143, v67, vcc\n"
    "v_exp_f32 v143, v143\n"
    "s_nop 0\n"
    "v_cvt_pk_bf16_f32 v112, v128, v129\n"
    "v_cvt_pk_bf16_f32 v113, v130, v131\n"
    "v_cvt_pk_bf16_f32 v114, v132, v133\n"
    "v_cvt_pk_bf16_f32 v115, v134, v135\n"
    "v_cvt_pk_bf16_f32 v116, v136, v137\n"
    "v_cvt_pk_bf16_f32 v117, v138, v139\n"
    "v_cvt_pk_bf16_f32 v118, v140, v141\n"
    "v_cvt_pk_bf16_f32 v119, v142, v143\n"
    "s_nop 1\n"
    "v_mfma_f32_16x16x32_bf16 v[176:179], v[32:35], v[112:115], v[176:179]\n"
    "v_mfma_f32_16x16x32_bf16 v[176:179], v[32:35], v[116:119], v[176:179]\n"
    "v_mfma_f32_16x16x32_bf16 a[0:3], a[192:195], v[112:115], a[0:3]\n"
    "v_mfma_f32_16x16x32_bf16 a[0:3], a[196:199], v[116:119], a[0:3]\n"
    "v_mfma_f32_16x16x32_bf16 a[4:7], a[200:203], v[112:115], a[4:7]\n"
    "v_mfma_f32_16x16x32_bf16 a[4:7], a[204:207], v[116:119], a[4:7]\n"
    "v_mfma_f32_16x16x32_bf16 a[8:11], a[208:211], v[112:115], a[8:11]\n"
    "v_mfma_f32_16x16x32_bf16 a[8:11], a[212:215], v[116:119], a[8:11]\n"
    "v_mfma_f32_16x16x32_bf16 a[12:15], a[216:219], v[112:115], a[12:15]\n"
    "v_mfma_f32_16x16x32_bf16 a[12:15], a[220:223], v[116:119], a[12:15]\n"
    "v_mfma_f32_16x16x32_bf16 a[16:19], a[224:227], v[112:115], a[16:19]\n"
    "v_mfma_f32_16x16x32_bf16 a[16:19], a[228:231], v[116:119], a[16:19]\n"
    "v_mfma_f32_16x16x32_bf16 a[20:23], a[232:235], v[112:115], a[20:23]\n"
    "v_mfma_f32_16x16x32_bf16 a[20:23], a[236:239], v[116:119], a[20:23]\n"
    "v_mfma_f32_16x16x32_bf16 a[24:27], a[240:243], v[112:115], a[24:27]\n"
    "v_mfma_f32_16x16x32_bf16 a[24:27], a[244:247], v[116:119], a[24:27]\n"
    "v_mfma_f32_16x16x32_bf16 a[28:31], a[248:251], v[112:115], a[28:31]\n"
    "v_mfma_f32_16x16x32_bf16 a[28:31], a[252:255], v[116:119], a[28:31]\n"
    "s_add_u32 s86, s86, 0x8000\n"
    "s_add_u32 s85, s85, 64\n"
    "s_sub_u32 s87, s87, 1\n"
    "s_cmp_lg_u32 s87, 0\n"
    "s_cbranch_scc1 fb_p2\n"
    "s_nop 7\n"
    "v_rcp_f32 v68, v176\n"
    "s_lshl_b32 s81, s79, 19\n"
    "s_add_u32 s82, s81, 0x40000\n"
    "v_accvgpr_read_b32 v144, a0\n"
    "v_accvgpr_read_b32 v145, a1\n"
    "v_accvgpr_read_b32 v146, a2\n"
    "v_accvgpr_read_b32 v147, a3\n"
    "v_accvgpr_read_b32 v148, a4\n"
    "v_accvgpr_read_b32 v149, a5\n"
    "v_accvgpr_read_b32 v150, a6\n"
    "v_accvgpr_read_b32 v151, a7\n"
    "v_accvgpr_read_b32 v152, a8\n"
    "v_accvgpr_read_b32 v153, a9\n"
    "v_accvgpr_read_b32 v154, a10\n"
    "v_accvgpr_read_b32 v155, a11\n"
    "v_accvgpr_read_b32 v156, a12\n"
    "v_accvgpr_read_b32 v157, a13\n"
    "v_accvgpr_read_b32 v158, a14\n"
    "v_accvgpr_read_b32 v159, a15\n"
    "v_accvgpr_read_b32 v160, a16\n"
    "v_accvgpr_read_b32 v161, a17\n"
    "v_accvgpr_read_b32 v162, a18\n"
    "v_accvgpr_read_b32 v163, a19\n"
    "v_accvgpr_read_b32 v164, a20\n"
    "v_accvgpr_read_b32 v165, a21\n"
    "v_accvgpr_read_b32 v166, a22\n"
    "v_accvgpr_read_b32 v167, a23\n"
    "v_accvgpr_read_b32 v168, a24\n"
    "v_accvgpr_read_b32 v169, a25\n"
    "v_accvgpr_read_b32 v170, a26\n"
    "v_accvgpr_read_b32 v171, a27\n"
    "v_accvgpr_read_b32 v172, a28\n"
    "v_accvgpr_read_b32 v173, a29\n"
    "v_accvgpr_read_b32 v174, a30\n"
    "v_accvgpr_read_b32 v175, a31\n"
    "v_mul_f32 v144, v144, v68\n"
    "v_mul_f32 v145, v145, v68\n"
    "v_mul_f32 v146, v146, v68\n"
    "v_mul_f32 v147, v147, v68\n"
    "v_mul_f32 v148, v148, v68\n"
    "v_mul_f32 v149, v149, v68\n"
    "v_mul_f32 v150, v150, v68\n"
    "v_mul_f32 v151, v151, v68\n"
    "v_mul_f32 v152, v152, v68\n"
    "v_mul_f32 v153, v153, v68\n"
    "v_mul_f32 v154, v154, v68\n"
    "v_mul_f32 v155, v155, v68\n"
    "v_mul_f32 v156, v156, v68\n"
    "v_mul_f32 v157, v157, v68\n"
    "v_mul_f32 v158, v158, v68\n"
    "v_mul_f32 v159, v159, v68\n"
    "v_mul_f32 v160, v160, v68\n"
    "v_mul_f32 v161, v161, v68\n"
    "v_mul_f32 v162, v162, v68\n"
    "v_mul_f32 v163, v163, v68\n"
    "v_mul_f32 v164, v164, v68\n"
    "v_mul_f32 v165, v165, v68\n"
    "v_mul_f32 v166, v166, v68\n"
    "v_mul_f32 v167, v167, v68\n"
    "v_mul_f32 v168, v168, v68\n"
    "v_mul_f32 v169, v169, v68\n"
    "v_mul_f32 v170, v170, v68\n"
    "v_mul_f32 v171, v171, v68\n"
    "v_mul_f32 v172, v172, v68\n"
    "v_mul_f32 v173, v173, v68\n"
    "v_mul_f32 v174, v174, v68\n"
    "v_mul_f32 v175, v175, v68\n"
    "v_cvt_pk_bf16_f32 v128, v144, v145\n"
    "v_cvt_pk_bf16_f32 v129, v146, v147\n"
    "v_cvt_pk_bf16_f32 v130, v148, v149\n"
    "v_cvt_pk_bf16_f32 v131, v150, v151\n"
    "v_cvt_pk_bf16_f32 v132, v152, v153\n"
    "v_cvt_pk_bf16_f32 v133, v154, v155\n"
    "v_cvt_pk_bf16_f32 v134, v156, v157\n"
    "v_cvt_pk_bf16_f32 v135, v158, v159\n"
    "v_cvt_pk_bf16_f32 v136, v160, v161\n"
    "v_cvt_pk_bf16_f32 v137, v162, v163\n"
    "v_cvt_pk_bf16_f32 v138, v164, v165\n"
    "v_cvt_pk_bf16_f32 v139, v166, v167\n"
    "v_cvt_pk_bf16_f32 v140, v168, v169\n"
    "v_cvt_pk_bf16_f32 v141, v170, v171\n"
    "v_cvt_pk_bf16_f32 v142, v172, v173\n"
    "v_cvt_pk_bf16_f32 v143, v174, v175\n"
    "buffer_store_dwordx4 v[128:131], v66, s[60:63], s81 offen sc1\n"
    "buffer_store_dwordx4 v[132:135], v66, s[60:63], s81 offen offset:1024 sc1\n"
    "buffer_store_dwordx4 v[136:139], v66, s[60:63], s82 offen sc1\n"
    "buffer_store_dwordx4 v[140:143], v66, s[60:63], s82 offen offset:1024 sc1\n"
    "s_waitcnt vmcnt(0)\n"
    "s_setpc_b64 s[90:91]\n"
    "fb_end:\n"
    "k_attn_code_end:\n"
    "k_oproj:\n"
    "s_mov_b32 s80, s100\n"
    "s_mov_b64 s[34:35], s[16:17]\n"
    "s_mov_b64 s[36:37], s[22:23]\n"
    "s_and_b32 s83, s30, 1\n"
    "s_lshr_b32 s86, s30, 3\n"
    "s_bfe_u32 s81, s30, 0x10002\n"
    "s_lshl_b32 s81, s81, 3\n"
    "s_lshr_b32 s75, s86, 2\n"
    "s_add_u32 s81, s81, s75\n"
    "s_and_b32 s82, s30, 2\n"
    "s_lshl_b32 s82, s82, 1\n"
    "s_and_b32 s75, s86, 3\n"
    "s_add_u32 s82, s82, s75\n"
    "s_mov_b32 s44, s34\n"
    "s_mov_b32 s45, s35\n"
    "s_mov_b32 s46, 0x800000\n"
    "s_mov_b32 s47, 0x20000\n"
    "s_add_u32 s40, s32, 0x2c00000\n"
    "s_addc_u32 s41, s33, 0\n"
    "s_mov_b32 s42, 0x1000000\n"
    "s_mov_b32 s43, 0x20000\n"
    "s_cmp_eq_u32 s83, 0\n"
    "s_cselect_b32 s46, 0x3c0000, s46\n"
    "s_cselect_b32 s42, 0x780000, s42\n"
    "s_mul_i32 s72, s83, 0x780000\n"
    "s_mul_i32 s73, s83, 0x3c0000\n"
    "v_lshrrev_b32 v18, 4, v1\n"
    "v_bfe_u32 v19, v1, 1, 3\n"
    "v_and_b32 v20, 1, v1\n"
    "v_lshlrev_b32 v20, 4, v20\n"
    "v_lshl_add_u32 v20, v18, 11, v20\n"
    "v_xor_b32 v19, v19, v18\n"
    "v_xor_b32 v21, 4, v19\n"
    "s_lshl_b32 s85, s82, 8\n"
    "v_add_u32 v20, s85, v20\n"
    "v_lshl_add_u32 v4, v19, 5, v20\n"
    "v_lshl_add_u32 v5, v21, 5, v20\n"
    "v_add_u32 v6, 0x3800, v4\n"
    "v_add_u32 v7, 0x5400, v5\n"
    "v_add_u32 v5, 0x1c00, v5\n"
    "s_lshl_b32 s85, s80, 16\n"
    "s_add_u32 s73, s73, s85\n"
    "s_add_u32 s74, s73, 0x8000\n"
    "s_lshl_b32 s87, s80, 13\n"
    "s_add_u32 s88, s87, 0x1000\n"
    "s_mov_b32 s76, 0\n"
    "s_lshl_b32 s84, s81, 3\n"
    "s_lshl_b32 s86, s80, 1\n"
    "s_add_u32 s84, s84, s86\n"
    "s_lshl_b32 s86, s84, 11\n"
    "v_lshlrev_b32 v3, 4, v1\n"
    "v_add_u32 v2, s86, v3\n"
    "s_barrier\n"
    "s_add_u32 m0, s76, s87\n"
    "s_nop 0\n"
    "buffer_load_dwordx4 v4, s[44:47], s73 offen lds\n"
    "buffer_load_dwordx4 v5, s[44:47], s73 offen offset:1024 lds\n"
    "buffer_load_dwordx4 v6, s[44:47], s73 offen offset:2048 lds\n"
    "buffer_load_dwordx4 v7, s[44:47], s73 offen offset:3072 lds\n"
    "s_add_u32 m0, s76, s88\n"
    "s_nop 0\n"
    "buffer_load_dwordx4 v4, s[44:47], s74 offen lds\n"
    "buffer_load_dwordx4 v5, s[44:47], s74 offen offset:1024 lds\n"
    "buffer_load_dwordx4 v6, s[44:47], s74 offen offset:2048 lds\n"
    "buffer_load_dwordx4 v7, s[44:47], s74 offen offset:3072 lds\n"
    "s_add_u32 s73, s73, 0x40000\n"
    "s_add_u32 s74, s74, 0x40000\n"
    "s_add_u32 s76, s76, 0x8000\n"
    "s_cmp_lt_u32 s76, 0x28000\n"
    "s_cselect_b32 s76, s76, 0\n"
    "s_add_u32 m0, s76, s87\n"
    "s_nop 0\n"
    "buffer_load_dwordx4 v4, s[44:47], s73 offen lds\n"
    "buffer_load_dwordx4 v5, s[44:47], s73 offen offset:1024 lds\n"
    "buffer_load_dwordx4 v6, s[44:47], s73 offen offset:2048 lds\n"
    "buffer_load_dwordx4 v7, s[44:47], s73 offen offset:3072 lds\n"
    "s_add_u32 m0, s76, s88\n"
    "s_nop 0\n"
    "buffer_load_dwordx4 v4, s[44:47], s74 offen lds\n"
    "buffer_load_dwordx4 v5, s[44:47], s74 offen offset:1024 lds\n"
    "buffer_load_dwordx4 v6, s[44:47], s74 offen offset:2048 lds\n"
    "buffer_load_dwordx4 v7, s[44:47], s74 offen offset:3072 lds\n"
    "s_add_u32 s73, s73, 0x40000\n"
    "s_add_u32 s74, s74, 0x40000\n"
    "s_add_u32 s76, s76, 0x8000\n"
    "s_cmp_lt_u32 s76, 0x28000\n"
    "s_cselect_b32 s76, s76, 0\n"
    "s_add_u32 s56, s32, 0x18000\n"
    "s_addc_u32 s57, s33, 0\n"
    "s_mov_b32 s58, 0x8000\n"
    "s_mov_b32 s59, 0x20000\n"
    "s_lshl_b32 s85, s30, 7\n"
    "v_mov_b32 v24, s94\n"
    "v_mov_b32 v25, s95\n"
    "s_waitcnt vmcnt(16)\n"
    "s_barrier\n"
    "s_cmp_lg_u32 s80, 0\n"
    "s_cbranch_scc1 k_oproj_np\n"
    "s_mov_b64 exec, 1\n buffer_store_dwordx2 v[24:25], off, s[56:59], s85 offset:0 sc1\n s_mov_b64 exec, -1\n"
    "k_oproj_np:\n"
    "s_getpc_b64 s[68:69]\n s_add_u32 s68, s68, k_ffn1@rel32@lo+4\n s_addc_u32 s69, s69, k_ffn1@rel32@hi+12\n"
    "s_mov_b32 s70, k_ffn1_code_end-k_ffn1\n"
    "s_mov_b32 s71, 0x20000\n"
    "s_lshr_b32 s86, s30, 3\n"
    "s_lshl_b32 s86, s86, 2\n"
    "s_add_u32 s86, s86, s80\n"
    "s_lshl_b32 s86, s86, 7\n"
    "v_lshlrev_b32 v23, 14, v1\n"
    "s_mov_b64 exec, 3\n buffer_load_dword v0, v23, s[68:71], s86 offen\n s_mov_b64 exec, -1\n"
    "s_mov_b32 s79, 0xfffe0000\n"
    "s_add_u32 s56, s32, 0x18000\n"
    "s_addc_u32 s57, s33, 0\n"
    "s_mov_b32 s58, 0x8000\n"
    "s_mov_b32 s59, 0x20000\n"
    "s_cmp_eq_u32 s83, 0\n"
    "s_cselect_b32 s85, 0, 15\n"
    "s_cselect_b32 s86, 14, 31\n"
    "v_add_u32 v18, s85, v1\n"
    "v_min_u32 v18, s86, v18\n"
    "s_sub_u32 s85, 15, s81\n"
    "s_min_u32 s85, s85, s81\n"
    "s_lshl_b32 s85, s85, 10\n"
    "v_and_b32 v19, 3, v18\n"
    "v_lshrrev_b32 v18, 2, v18\n"
    "v_lshl_add_u32 v19, v19, 13, s85\n"
    "v_lshl_add_u32 v19, v18, 7, v19\n"
    "s_cmp_lg_u32 s80, 0\n"
    "s_cbranch_scc1 k_oproj_nw\n"
    "s_mov_b32 s91, 1048576\n"
    "k_oproj_h_spin:\n"
    "buffer_load_dwordx2 v[244:245], v19, s[56:59], 0 offen sc1\n"
    "s_waitcnt vmcnt(0)\n"
    "v_cmp_ne_u64 vcc, s[94:95], v[244:245]\n"
    "s_sub_u32 s91, s91, 1\n"
    "s_cmp_eq_u64 vcc, 0\n"
    "s_cbranch_scc1 k_oproj_h_go\n"
    "s_sleep 1\n"
    "s_cmp_lg_u32 s91, 0\n"
    "s_cbranch_scc1 k_oproj_h_spin\n"
    "k_oproj_h_go:\n"
    "k_oproj_nw:\n"
    "s_waitcnt vmcnt(9)\n"
    "s_barrier\n"
    "buffer_load_dwordx4 a[32:35], v2, s[40:43], s72 offen sc1\n"
    "buffer_load_dwordx4 a[36:39], v2, s[40:43], s72 offen offset:1024 sc1\n"
    "buffer_load_dwordx4 a[40:43], v2, s[40:43], s72 offen offset:2048 sc1\n"
    "buffer_load_dwordx4 a[44:47], v2, s[40:43], s72 offen offset:3072 sc1\n"
    "s_add_u32 s72, s72, 0x40000\n"
    "buffer_load_dwordx4 a[48:51], v2, s[40:43], s72 offen sc1\n"
    "buffer_load_dwordx4 a[52:55], v2, s[40:43], s72 offen offset:1024 sc1\n"
    "buffer_load_dwordx4 a[56:59], v2, s[40:43], s72 offen offset:2048 sc1\n"
    "buffer_load_dwordx4 a[60:63], v2, s[40:43], s72 offen offset:3072 sc1\n"
    "s_add_u32 s72, s72, 0x40000\n"
    "buffer_load_dword v0, off, s[56:59], s79\n"
    "buffer_load_dword v0, off, s[56:59], s79\n"
    "buffer_load_dword v0, off, s[56:59], s79\n"
    "buffer_load_dword v0, off, s[56:59], s79\n"
    "buffer_load_dword v0, off, s[56:59], s79\n"
    "buffer_load_dword v0, off, s[56:59], s79\n"
    "buffer_load_dword v0, off, s[56:59], s79\n"
    "buffer_load_dword v0, off, s[56:59], s79\n"
    "buffer_load_dwordx4 a[64:67], v2, s[40:43], s72 offen sc1\n"
    "buffer_load_dwordx4 a[68:71], v2, s[40:43], s72 offen offset:1024 sc1\n"
    "buffer_load_dwordx4 a[72:75], v2, s[40:43], s72 offen offset:2048 sc1\n"
    "buffer_load_dwordx4 a[76:79], v2, s[40:43], s72 offen offset:3072 sc1\n"
    "s_add_u32 s72, s72, 0x40000\n"
    "v_lshrrev_b32 v18, 2, v1\n"
    "v_and_b32 v19, 7, v18\n"
    "v_and_b32 v20, 3, v1\n"
    "v_lshlrev_b32 v20, 3, v20\n"
    "v_lshl_add_u32 v20, v18, 8, v20\n"
    "v_xor_b32 v21, 0, v19\n"
    "v_lshl_add_u32 v8, v21, 5, v20\n"
    "v_xor_b32 v21, 1, v19\n"
    "v_lshl_add_u32 v9, v21, 5, v20\n"
    "v_xor_b32 v21, 2, v19\n"
    "v_lshl_add_u32 v10, v21, 5, v20\n"
    "v_xor_b32 v21, 3, v19\n"
    "v_lshl_add_u32 v11, v21, 5, v20\n"
    "v_xor_b32 v21, 4, v19\n"
    "v_lshl_add_u32 v12, v21, 5, v20\n"
    "v_xor_b32 v21, 5, v19\n"
    "v_lshl_add_u32 v13, v21, 5, v20\n"
    "v_xor_b32 v21, 6, v19\n"
    "v_lshl_add_u32 v14, v21, 5, v20\n"
    "v_xor_b32 v21, 7, v19\n"
    "v_lshl_add_u32 v15, v21, 5, v20\n"
    "s_mov_b32 s77, 0\n"
    "s_mov_b32 s79, 0xfffe0000\n"
    "s_add_u32 s60, s32, 0x4c00000\n"
    "s_addc_u32 s61, s33, 0\n"
    "s_mov_b32 s62, 0x400000\n"
    "s_mov_b32 s63, 0x20000\n"
    "s_add_u32 s64, s32, 0x4000\n"
    "s_addc_u32 s65, s33, 0\n"
    "s_mov_b32 s66, 0x4000\n"
    "s_mov_b32 s67, 0x20000\n"
    "s_lshl_b32 s85, s81, 3\n"
    "s_add_u32 s85, s85, s82\n"
    "s_lshl_b32 s85, s85, 2\n"
    "s_add_u32 s85, s85, s80\n"
    "s_lshl_b32 s89, s85, 13\n"
    "s_lshl_b32 s90, s85, 5\n"
    "v_and_b32 v18, 15, v1\n"
    "v_lshrrev_b32 v19, 4, v1\n"
    "v_lshlrev_b32 v19, 2, v19\n"
    "v_sub_u32 v18, v18, v19\n"
    "v_and_b32 v19, 1, v18\n"
    "v_lshlrev_b32 v19, 4, v19\n"
    "v_mov_b32 v20, 0x3f80\n"
    "v_lshlrev_b32 v19, v19, v20\n"
    "v_cmp_gt_u32 vcc, 2, v18\n"
    "v_subrev_u32 v18, 2, v18\n"
    "v_mov_b32 v224, 0\n"
    "v_cndmask_b32 v226, 0, v19, vcc\n"
    "v_cmp_gt_u32 vcc, 2, v18\n"
    "v_mov_b32 v225, 0\n"
    "v_mov_b32 v228, 0\n"
    "v_cndmask_b32 v227, 0, v19, vcc\n"
    "v_mov_b32 v229, 0\n"
    "v_and_b32 v18, 15, v1\n"
    "v_lshrrev_b32 v20, 4, v1\n"
    "v_lshlrev_b32 v20, 3, v20\n"
    "v_sub_u32 v18, v18, v20\n"
    "v_ashrrev_i32 v18, 1, v18\n"
    "v_cmp_eq_i32 vcc, 0, v18\n"
    "v_cmp_eq_i32 s[92:93], 1, v18\n"
    "s_nop 0\n"
    "v_cndmask_b32 v232, 0, v19, vcc\n"
    "v_cndmask_b32 v233, 0, v19, s[92:93]\n"
    "v_cmp_eq_i32 vcc, 2, v18\n"
    "v_cmp_eq_i32 s[92:93], 3, v18\n"
    "s_nop 0\n"
    "v_cndmask_b32 v234, 0, v19, vcc\n"
    "v_cndmask_b32 v235, 0, v19, s[92:93]\n"
    "v_cmp_eq_i32 vcc, -8, v18\n"
    "v_cmp_eq_i32 s[92:93], -7, v18\n"
    "s_nop 0\n"
    "v_cndmask_b32 v236, 0, v19, vcc\n"
    "v_cndmask_b32 v237, 0, v19, s[92:93]\n"
    "v_cmp_eq_i32 vcc, -6, v18\n"
    "v_cmp_eq_i32 s[92:93], -5, v18\n"
    "s_nop 0\n"
    "v_cndmask_b32 v238, 0, v19, vcc\n"
    "v_cndmask_b32 v239, 0, v19, s[92:93]\n"
    "s_cmp_eq_u32 s83, 0\n"
    "s_cselect_b32 s75, 7, 7\n"
    "v_mov_b32 v32, 0\n"
    "v_mov_b32 v33, 0\n"
    "v_mov_b32 v34, 0\n"
    "v_mov_b32 v35, 0\n"
    "v_mov_b32 v36, 0\n"
    "v_mov_b32 v37, 0\n"
    "v_mov_b32 v38, 0\n"
    "v_mov_b32 v39, 0\n"
    "v_mov_b32 v40, 0\n"
    "v_mov_b32 v41, 0\n"
    "v_mov_b32 v42, 0\n"
    "v_mov_b32 v43, 0\n"
    "v_mov_b32 v44, 0\n"
    "v_mov_b32 v45, 0\n"
    "v_mov_b32 v46, 0\n"
    "v_mov_b32 v47, 0\n"
    "v_mov_b32 v48, 0\n"
    "v_mov_b32 v49, 0\n"
    "v_mov_b32 v50, 0\n"
    "v_mov_b32 v51, 0\n"
    "v_mov_b32 v52, 0\n"
    "v_mov_b32 v53, 0\n"
    "v_mov_b32 v54, 0\n"
    "v_mov_b32 v55, 0\n"
    "v_mov_b32 v56, 0\n"
    "v_mov_b32 v57, 0\n"
    "v_mov_b32 v58, 0\n"
    "v_mov_b32 v59, 0\n"
    "v_mov_b32 v60, 0\n"
    "v_mov_b32 v61, 0\n"
    "v_mov_b32 v62, 0\n"
    "v_mov_b32 v63, 0\n"
    "v_mov_b32 v64, 0\n"
    "v_mov_b32 v65, 0\n"
    "v_mov_b32 v66, 0\n"
    "v_mov_b32 v67, 0\n"
    "v_mov_b32 v68, 0\n"
    "v_mov_b32 v69, 0\n"
    "v_mov_b32 v70, 0\n"
    "v_mov_b32 v71, 0\n"
    "v_mov_b32 v72, 0\n"
    "v_mov_b32 v73, 0\n"
    "v_mov_b32 v74, 0\n"
    "v_mov_b32 v75, 0\n"
    "v_mov_b32 v76, 0\n"
    "v_mov_b32 v77, 0\n"
    "v_mov_b32 v78, 0\n"
    "v_mov_b32 v79, 0\n"
    "v_mov_b32 v80, 0\n"
    "v_mov_b32 v81, 0\n"
    "v_mov_b32 v82, 0\n"
    "v_mov_b32 v83, 0\n"
    "v_mov_b32 v84, 0\n"
    "v_mov_b32 v85, 0\n"
    "v_mov_b32 v86, 0\n"
    "v_mov_b32 v87, 0\n"
    "v_mov_b32 v88, 0\n"
    "v_mov_b32 v89, 0\n"
    "v_mov_b32 v90, 0\n"
    "v_mov_b32 v91, 0\n"
    "v_mov_b32 v92, 0\n"
    "v_mov_b32 v93, 0\n"
    "v_mov_b32 v94, 0\n"
    "v_mov_b32 v95, 0\n"
    "ds_read_b64_tr_b16 a[0:1], v8\n"
    "ds_read_b64_tr_b16 a[2:3], v8 offset:4096\n"
    "ds_read_b64_tr_b16 a[4:5], v8 offset:8192\n"
    "ds_read_b64_tr_b16 a[6:7], v8 offset:12288\n"
    "ds_read_b64_tr_b16 a[8:9], v9\n"
    "ds_read_b64_tr_b16 a[10:11], v9 offset:4096\n"
    "ds_read_b64_tr_b16 a[12:13], v9 offset:8192\n"
    "ds_read_b64_tr_b16 a[14:15], v9 offset:12288\n"
    "ds_read_b64_tr_b16 a[16:17], v10\n"
    "ds_read_b64_tr_b16 a[18:19], v10 offset:4096\n"
    "ds_read_b64_tr_b16 a[20:21], v10 offset:8192\n"
    "ds_read_b64_tr_b16 a[22:23], v10 offset:12288\n"
    "k_oproj_loop:\n"
    "s_waitcnt vmcnt(16) lgkmcnt(8)\n"
    "v_mfma_f32_16x16x32_bf16 v[32:35], a[0:3], a[32:35], v[32:35]\n"
    "ds_read_b64_tr_b16 a[24:25], v11\n"
    "v_mfma_f32_16x16x32_bf16 v[32:35], a[4:7], a[36:39], v[32:35]\n"
    "ds_read_b64_tr_b16 a[26:27], v11 offset:4096\n"
    "buffer_load_dwordx4 a[80:83], v2, s[40:43], s72 offen sc1\n"
    "v_mfma_f32_16x16x32_bf16 v[64:67], a[0:3], a[40:43], v[64:67]\n"
    "s_add_u32 m0, s76, s87\n"
    "ds_read_b64_tr_b16 a[28:29], v11 offset:8192\n"
    "v_mfma_f32_16x16x32_bf16 v[64:67], a[4:7], a[44:47], v[64:67]\n"
    "s_waitcnt lgkmcnt(7)\n"
    "ds_read_b64_tr_b16 a[30:31], v11 offset:12288\n"
    "v_mfma_f32_16x16x32_bf16 v[36:39], a[8:11], a[32:35], v[36:39]\n"
    "ds_read_b64_tr_b16 a[0:1], v12\n"
    "buffer_load_dwordx4 v4, s[44:47], s73 offen lds\n"
    "v_mfma_f32_16x16x32_bf16 v[36:39], a[12:15], a[36:39], v[36:39]\n"
    "ds_read_b64_tr_b16 a[2:3], v12 offset:4096\n"
    "v_mfma_f32_16x16x32_bf16 v[68:71], a[8:11], a[40:43], v[68:71]\n"
    "ds_read_b64_tr_b16 a[4:5], v12 offset:8192\n"
    "v_mfma_f32_16x16x32_bf16 v[68:71], a[12:15], a[44:47], v[68:71]\n"
    "s_waitcnt lgkmcnt(7)\n"
    "ds_read_b64_tr_b16 a[6:7], v12 offset:12288\n"
    "v_mfma_f32_16x16x32_bf16 v[40:43], a[16:19], a[32:35], v[40:43]\n"
    "ds_read_b64_tr_b16 a[8:9], v13\n"
    "buffer_load_dwordx4 a[84:87], v2, s[40:43], s72 offen offset:1024 sc1\n"
    "v_mfma_f32_16x16x32_bf16 v[40:43], a[20:23], a[36:39], v[40:43]\n"
    "ds_read_b64_tr_b16 a[10:11], v13 offset:4096\n"
    "v_mfma_f32_16x16x32_bf16 v[72:75], a[16:19], a[40:43], v[72:75]\n"
    "ds_read_b64_tr_b16 a[12:13], v13 offset:8192\n"
    "v_mfma_f32_16x16x32_bf16 v[72:75], a[20:23], a[44:47], v[72:75]\n"
    "s_waitcnt lgkmcnt(7)\n"
    "ds_read_b64_tr_b16 a[14:15], v13 offset:12288\n"
    "v_mfma_f32_16x16x32_bf16 v[44:47], a[24:27], a[32:35], v[44:47]\n"
    "ds_read_b64_tr_b16 a[16:17], v14\n"
    "buffer_load_dwordx4 v5, s[44:47], s73 offen offset:1024 lds\n"
    "v_mfma_f32_16x16x32_bf16 v[44:47], a[28:31], a[36:39], v[44:47]\n"
    "ds_read_b64_tr_b16 a[18:19], v14 offset:4096\n"
    "v_mfma_f32_16x16x32_bf16 v[76:79], a[24:27], a[40:43], v[76:79]\n"
    "ds_read_b64_tr_b16 a[20:21], v14 offset:8192\n"
    "v_mfma_f32_16x16x32_bf16 v[76:79], a[28:31], a[44:47], v[76:79]\n"
    "s_waitcnt lgkmcnt(7)\n"
    "ds_read_b64_tr_b16 a[22:23], v14 offset:12288\n"
    "v_mfma_f32_16x16x32_bf16 v[48:51], a[0:3], a[32:35], v[48:51]\n"
    "ds_read_b64_tr_b16 a[24:25], v15\n"
    "buffer_load_dwordx4 a[88:91], v2, s[40:43], s72 offen offset:2048 sc1\n"
    "v_mfma_f32_16x16x32_bf16 v[48:51], a[4:7], a[36:39], v[48:51]\n"
    "ds_read_b64_tr_b16 a[26:27], v15 offset:4096\n"
    "v_mfma_f32_16x16x32_bf16 v[80:83], a[0:3], a[40:43], v[80:83]\n"
    "s_add_u32 s77, s77, 1\n"
    "ds_read_b64_tr_b16 a[28:29], v15 offset:8192\n"
    "v_mfma_f32_16x16x32_bf16 v[80:83], a[4:7], a[44:47], v[80:83]\n"
    "s_waitcnt lgkmcnt(7)\n"
    "ds_read_b64_tr_b16 a[30:31], v15 offset:12288\n"
    "v_mfma_f32_16x16x32_bf16 v[52:55], a[8:11], a[32:35], v[52:55]\n"
    "ds_read_b64_tr_b16 a[0:1], v8 offset:16384\n"
    "buffer_load_dwordx4 v6, s[44:47], s73 offen offset:2048 lds\n"
    "v_mfma_f32_16x16x32_bf16 v[52:55], a[12:15], a[36:39], v[52:55]\n"
    "s_cmp_lt_u32 s77, 5\n"
    "s_cselect_b32 s77, s77, 0\n"
    "s_cselect_b32 s78, 0x8000, s79\n"
    "ds_read_b64_tr_b16 a[2:3], v8 offset:20480\n"
    "v_mfma_f32_16x16x32_bf16 v[84:87], a[8:11], a[40:43], v[84:87]\n"
    "ds_read_b64_tr_b16 a[4:5], v8 offset:24576\n"
    "v_mfma_f32_16x16x32_bf16 v[84:87], a[12:15], a[44:47], v[84:87]\n"
    "s_waitcnt lgkmcnt(7)\n"
    "ds_read_b64_tr_b16 a[6:7], v8 offset:28672\n"
    "v_mfma_f32_16x16x32_bf16 v[56:59], a[16:19], a[32:35], v[56:59]\n"
    "ds_read_b64_tr_b16 a[8:9], v9 offset:16384\n"
    "v_add_u32 v8, s78, v8\n"
    "v_mfma_f32_16x16x32_bf16 v[56:59], a[20:23], a[36:39], v[56:59]\n"
    "ds_read_b64_tr_b16 a[10:11], v9 offset:20480\n"
    "buffer_load_dwordx4 a[92:95], v2, s[40:43], s72 offen offset:3072 sc1\n"
    "v_mfma_f32_16x16x32_bf16 v[88:91], a[16:19], a[40:43], v[88:91]\n"
    "s_add_u32 s72, s72, 0x40000\n"
    "ds_read_b64_tr_b16 a[12:13], v9 offset:24576\n"
    "v_mfma_f32_16x16x32_bf16 v[88:91], a[20:23], a[44:47], v[88:91]\n"
    "s_waitcnt lgkmcnt(7)\n"
    "ds_read_b64_tr_b16 a[14:15], v9 offset:28672\n"
    "v_mfma_f32_16x16x32_bf16 v[60:63], a[24:27], a[32:35], v[60:63]\n"
    "ds_read_b64_tr_b16 a[16:17], v10 offset:16384\n"
    "v_add_u32 v9, s78, v9\n"
    "v_mfma_f32_16x16x32_bf16 v[60:63], a[28:31], a[36:39], v[60:63]\n"
    "ds_read_b64_tr_b16 a[18:19], v10 offset:20480\n"
    "buffer_load_dwordx4 v7, s[44:47], s73 offen offset:3072 lds\n"
    "v_mfma_f32_16x16x32_bf16 v[92:95], a[24:27], a[40:43], v[92:95]\n"
    "ds_read_b64_tr_b16 a[20:21], v10 offset:24576\n"
    "v_mfma_f32_16x16x32_bf16 v[92:95], a[28:31], a[44:47], v[92:95]\n"
    "s_waitcnt vmcnt(17) lgkmcnt(7)\n"
    "ds_read_b64_tr_b16 a[22:23], v10 offset:28672\n"
    "v_mfma_f32_16x16x32_bf16 v[32:35], a[0:3], a[48:51], v[32:35]\n"
    "ds_read_b64_tr_b16 a[24:25], v11 offset:16384\n"
    "v_add_u32 v10, s78, v10\n"
    "v_mfma_f32_16x16x32_bf16 v[32:35], a[4:7], a[52:55], v[32:35]\n"
    "ds_read_b64_tr_b16 a[26:27], v11 offset:20480\n"
    "buffer_load_dwordx4 a[32:35], v2, s[40:43], s72 offen sc1\n"
    "v_mfma_f32_16x16x32_bf16 v[64:67], a[0:3], a[56:59], v[64:67]\n"
    "s_add_u32 m0, s76, s88\n"
    "ds_read_b64_tr_b16 a[28:29], v11 offset:24576\n"
    "v_mfma_f32_16x16x32_bf16 v[64:67], a[4:7], a[60:63], v[64:67]\n"
    "s_waitcnt lgkmcnt(7)\n"
    "ds_read_b64_tr_b16 a[30:31], v11 offset:28672\n"
    "v_mfma_f32_16x16x32_bf16 v[36:39], a[8:11], a[48:51], v[36:39]\n"
    "ds_read_b64_tr_b16 a[0:1], v12 offset:16384\n"
    "v_add_u32 v11, s78, v11\n"
    "v_mfma_f32_16x16x32_bf16 v[36:39], a[12:15], a[52:55], v[36:39]\n"
    "ds_read_b64_tr_b16 a[2:3], v12 offset:20480\n"
    "buffer_load_dwordx4 v4, s[44:47], s74 offen lds\n"
    "v_mfma_f32_16x16x32_bf16 v[68:71], a[8:11], a[56:59], v[68:71]\n"
    "ds_read_b64_tr_b16 a[4:5], v12 offset:24576\n"
    "v_mfma_f32_16x16x32_bf16 v[68:71], a[12:15], a[60:63], v[68:71]\n"
    "s_waitcnt lgkmcnt(7)\n"
    "ds_read_b64_tr_b16 a[6:7], v12 offset:28672\n"
    "v_mfma_f32_16x16x32_bf16 v[40:43], a[16:19], a[48:51], v[40:43]\n"
    "ds_read_b64_tr_b16 a[8:9], v13 offset:16384\n"
    "v_add_u32 v12, s78, v12\n"
    "v_mfma_f32_16x16x32_bf16 v[40:43], a[20:23], a[52:55], v[40:43]\n"
    "ds_read_b64_tr_b16 a[10:11], v13 offset:20480\n"
    "buffer_load_dwordx4 a[36:39], v2, s[40:43], s72 offen offset:1024 sc1\n"
    "v_mfma_f32_16x16x32_bf16 v[72:75], a[16:19], a[56:59], v[72:75]\n"
    "ds_read_b64_tr_b16 a[12:13], v13 offset:24576\n"
    "v_mfma_f32_16x16x32_bf16 v[72:75], a[20:23], a[60:63], v[72:75]\n"
    "s_waitcnt lgkmcnt(7)\n"
    "ds_read_b64_tr_b16 a[14:15], v13 offset:28672\n"
    "v_mfma_f32_16x16x32_bf16 v[44:47], a[24:27], a[48:51], v[44:47]\n"
    "ds_read_b64_tr_b16 a[16:17], v14 offset:16384\n"
    "v_add_u32 v13, s78, v13\n"
    "v_mfma_f32_16x16x32_bf16 v[44:47], a[28:31], a[52:55], v[44:47]\n"
    "ds_read_b64_tr_b16 a[18:19], v14 offset:20480\n"
    "buffer_load_dwordx4 v5, s[44:47], s74 offen offset:1024 lds\n"
    "v_mfma_f32_16x16x32_bf16 v[76:79], a[24:27], a[56:59], v[76:79]\n"
    "ds_read_b64_tr_b16 a[20:21], v14 offset:24576\n"
    "v_mfma_f32_16x16x32_bf16 v[76:79], a[28:31], a[60:63], v[76:79]\n"
    "s_waitcnt lgkmcnt(7)\n"
    "ds_read_b64_tr_b16 a[22:23], v14 offset:28672\n"
    "v_mfma_f32_16x16x32_bf16 v[48:51], a[0:3], a[48:51], v[48:51]\n"
    "ds_read_b64_tr_b16 a[24:25], v15 offset:16384\n"
    "v_add_u32 v14, s78, v14\n"
    "v_mfma_f32_16x16x32_bf16 v[48:51], a[4:7], a[52:55], v[48:51]\n"
    "ds_read_b64_tr_b16 a[26:27], v15 offset:20480\n"
    "buffer_load_dwordx4 a[40:43], v2, s[40:43], s72 offen offset:2048 sc1\n"
    "v_mfma_f32_16x16x32_bf16 v[80:83], a[0:3], a[56:59], v[80:83]\n"
    "ds_read_b64_tr_b16 a[28:29], v15 offset:24576\n"
    "v_mfma_f32_16x16x32_bf16 v[80:83], a[4:7], a[60:63], v[80:83]\n"
    "s_waitcnt vmcnt(13) lgkmcnt(7)\n"
    "s_barrier\n"
    "ds_read_b64_tr_b16 a[30:31], v15 offset:28672\n"
    "v_add_u32 v15, s78, v15\n"
    "v_mfma_f32_16x16x32_bf16 v[52:55], a[8:11], a[48:51], v[52:55]\n"
    "ds_read_b64_tr_b16 a[0:1], v8\n"
    "buffer_load_dwordx4 v6, s[44:47], s74 offen offset:2048 lds\n"
    "v_mfma_f32_16x16x32_bf16 v[52:55], a[12:15], a[52:55], v[52:55]\n"
    "ds_read_b64_tr_b16 a[2:3], v8 offset:4096\n"
    "v_mfma_f32_16x16x32_bf16 v[84:87], a[8:11], a[56:59], v[84:87]\n"
    "ds_read_b64_tr_b16 a[4:5], v8 offset:8192\n"
    "v_mfma_f32_16x16x32_bf16 v[84:87], a[12:15], a[60:63], v[84:87]\n"
    "s_waitcnt lgkmcnt(7)\n"
    "ds_read_b64_tr_b16 a[6:7], v8 offset:12288\n"
    "v_mfma_f32_16x16x32_bf16 v[56:59], a[16:19], a[48:51], v[56:59]\n"
    "ds_read_b64_tr_b16 a[8:9], v9\n"
    "buffer_load_dwordx4 a[44:47], v2, s[40:43], s72 offen offset:3072 sc1\n"
    "v_mfma_f32_16x16x32_bf16 v[56:59], a[20:23], a[52:55], v[56:59]\n"
    "s_add_u32 s72, s72, 0x40000\n"
    "ds_read_b64_tr_b16 a[10:11], v9 offset:4096\n"
    "v_mfma_f32_16x16x32_bf16 v[88:91], a[16:19], a[56:59], v[88:91]\n"
    "ds_read_b64_tr_b16 a[12:13], v9 offset:8192\n"
    "v_mfma_f32_16x16x32_bf16 v[88:91], a[20:23], a[60:63], v[88:91]\n"
    "s_waitcnt lgkmcnt(7)\n"
    "ds_read_b64_tr_b16 a[14:15], v9 offset:12288\n"
    "v_mfma_f32_16x16x32_bf16 v[60:63], a[24:27], a[48:51], v[60:63]\n"
    "ds_read_b64_tr_b16 a[16:17], v10\n"
    "buffer_load_dwordx4 v7, s[44:47], s74 offen offset:3072 lds\n"
    "v_mfma_f32_16x16x32_bf16 v[60:63], a[28:31], a[52:55], v[60:63]\n"
    "s_add_u32 s73, s73, 0x40000\n"
    "ds_read_b64_tr_b16 a[18:19], v10 offset:4096\n"
    "v_mfma_f32_16x16x32_bf16 v[92:95], a[24:27], a[56:59], v[92:95]\n"
    "s_add_u32 s74, s74, 0x40000\n"
    "ds_read_b64_tr_b16 a[20:21], v10 offset:8192\n"
    "v_mfma_f32_16x16x32_bf16 v[92:95], a[28:31], a[60:63], v[92:95]\n"
    "s_add_u32 s76, s76, 0x8000\n"
    "s_cmp_lt_u32 s76, 0x28000\n"
    "s_cselect_b32 s76, s76, 0\n"
    "ds_read_b64_tr_b16 a[22:23], v10 offset:12288\n"
    "s_waitcnt vmcnt(16) lgkmcnt(8)\n"
    "v_mfma_f32_16x16x32_bf16 v[32:35], a[0:3], a[64:67], v[32:35]\n"
    "ds_read_b64_tr_b16 a[24:25], v11\n"
    "v_mfma_f32_16x16x32_bf16 v[32:35], a[4:7], a[68:71], v[32:35]\n"
    "ds_read_b64_tr_b16 a[26:27], v11 offset:4096\n"
    "buffer_load_dwordx4 a[48:51], v2, s[40:43], s72 offen sc1\n"
    "v_mfma_f32_16x16x32_bf16 v[64:67], a[0:3], a[72:75], v[64:67]\n"
    "s_add_u32 m0, s76, s87\n"
    "ds_read_b64_tr_b16 a[28:29], v11 offset:8192\n"
    "v_mfma_f32_16x16x32_bf16 v[64:67], a[4:7], a[76:79], v[64:67]\n"
    "s_waitcnt lgkmcnt(7)\n"
    "ds_read_b64_tr_b16 a[30:31], v11 offset:12288\n"
    "v_mfma_f32_16x16x32_bf16 v[36:39], a[8:11], a[64:67], v[36:39]\n"
    "ds_read_b64_tr_b16 a[0:1], v12\n"
    "buffer_load_dwordx4 v4, s[44:47], s73 offen lds\n"
    "v_mfma_f32_16x16x32_bf16 v[36:39], a[12:15], a[68:71], v[36:39]\n"
    "ds_read_b64_tr_b16 a[2:3], v12 offset:4096\n"
    "v_mfma_f32_16x16x32_bf16 v[68:71], a[8:11], a[72:75], v[68:71]\n"
    "ds_read_b64_tr_b16 a[4:5], v12 offset:8192\n"
    "v_mfma_f32_16x16x32_bf16 v[68:71], a[12:15], a[76:79], v[68:71]\n"
    "s_waitcnt lgkmcnt(7)\n"
    "ds_read_b64_tr_b16 a[6:7], v12 offset:12288\n"
    "v_mfma_f32_16x16x32_bf16 v[40:43], a[16:19], a[64:67], v[40:43]\n"
    "ds_read_b64_tr_b16 a[8:9], v13\n"
    "buffer_load_dwordx4 a[52:55], v2, s[40:43], s72 offen offset:1024 sc1\n"
    "v_mfma_f32_16x16x32_bf16 v[40:43], a[20:23], a[68:71], v[40:43]\n"
    "ds_read_b64_tr_b16 a[10:11], v13 offset:4096\n"
    "v_mfma_f32_16x16x32_bf16 v[72:75], a[16:19], a[72:75], v[72:75]\n"
    "ds_read_b64_tr_b16 a[12:13], v13 offset:8192\n"
    "v_mfma_f32_16x16x32_bf16 v[72:75], a[20:23], a[76:79], v[72:75]\n"
    "s_waitcnt lgkmcnt(7)\n"
    "ds_read_b64_tr_b16 a[14:15], v13 offset:12288\n"
    "v_mfma_f32_16x16x32_bf16 v[44:47], a[24:27], a[64:67], v[44:47]\n"
    "ds_read_b64_tr_b16 a[16:17], v14\n"
    "buffer_load_dwordx4 v5, s[44:47], s73 offen offset:1024 lds\n"
    "v_mfma_f32_16x16x32_bf16 v[44:47], a[28:31], a[68:71], v[44:47]\n"
    "ds_read_b64_tr_b16 a[18:19], v14 offset:4096\n"
    "v_mfma_f32_16x16x32_bf16 v[76:79], a[24:27], a[72:75], v[76:79]\n"
    "ds_read_b64_tr_b16 a[20:21], v14 offset:8192\n"
    "v_mfma_f32_16x16x32_bf16 v[76:79], a[28:31], a[76:79], v[76:79]\n"
    "s_waitcnt lgkmcnt(7)\n"
    "ds_read_b64_tr_b16 a[22:23], v14 offset:12288\n"
    "v_mfma_f32_16x16x32_bf16 v[48:51], a[0:3], a[64:67], v[48:51]\n"
    "ds_read_b64_tr_b16 a[24:25], v15\n"
    "buffer_load_dwordx4 a[56:59], v2, s[40:43], s72 offen offset:2048 sc1\n"
    "v_mfma_f32_16x16x32_bf16 v[48:51], a[4:7], a[68:71], v[48:51]\n"
    "ds_read_b64_tr_b16 a[26:27], v15 offset:4096\n"
    "v_mfma_f32_16x16x32_bf16 v[80:83], a[0:3], a[72:75], v[80:83]\n"
    "s_add_u32 s77, s77, 1\n"
    "ds_read_b64_tr_b16 a[28:29], v15 offset:8192\n"
    "v_mfma_f32_16x16x32_bf16 v[80:83], a[4:7], a[76:79], v[80:83]\n"
    "s_waitcnt lgkmcnt(7)\n"
    "ds_read_b64_tr_b16 a[30:31], v15 offset:12288\n"
    "v_mfma_f32_16x16x32_bf16 v[52:55], a[8:11], a[64:67], v[52:55]\n"
    "ds_read_b64_tr_b16 a[0:1], v8 offset:16384\n"
    "buffer_load_dwordx4 v6, s[44:47], s73 offen offset:2048 lds\n"
    "v_mfma_f32_16x16x32_bf16 v[52:55], a[12:15], a[68:71], v[52:55]\n"
    "s_cmp_lt_u32 s77, 5\n"
    "s_cselect_b32 s77, s77, 0\n"
    "s_cselect_b32 s78, 0x8000, s79\n"
    "ds_read_b64_tr_b16 a[2:3], v8 offset:20480\n"
    "v_mfma_f32_16x16x32_bf16 v[84:87], a[8:11], a[72:75], v[84:87]\n"
    "ds_read_b64_tr_b16 a[4:5], v8 offset:24576\n"
    "v_mfma_f32_16x16x32_bf16 v[84:87], a[12:15], a[76:79], v[84:87]\n"
    "s_waitcnt lgkmcnt(7)\n"
    "ds_read_b64_tr_b16 a[6:7], v8 offset:28672\n"
    "v_mfma_f32_16x16x32_bf16 v[56:59], a[16:19], a[64:67], v[56:59]\n"
    "ds_read_b64_tr_b16 a[8:9], v9 offset:16384\n"
    "v_add_u32 v8, s78, v8\n"
    "v_mfma_f32_16x16x32_bf16 v[56:59], a[20:23], a[68:71], v[56:59]\n"
    "ds_read_b64_tr_b16 a[10:11], v9 offset:20480\n"
    "buffer_load_dwordx4 a[60:63], v2, s[40:43], s72 offen offset:3072 sc1\n"
    "v_mfma_f32_16x16x32_bf16 v[88:91], a[16:19], a[72:75], v[88:91]\n"
    "s_add_u32 s72, s72, 0x40000\n"
    "ds_read_b64_tr_b16 a[12:13], v9 offset:24576\n"
    "v_mfma_f32_16x16x32_bf16 v[88:91], a[20:23], a[76:79], v[88:91]\n"
    "s_waitcnt lgkmcnt(7)\n"
    "ds_read_b64_tr_b16 a[14:15], v9 offset:28672\n"
    "v_mfma_f32_16x16x32_bf16 v[60:63], a[24:27], a[64:67], v[60:63]\n"
    "ds_read_b64_tr_b16 a[16:17], v10 offset:16384\n"
    "v_add_u32 v9, s78, v9\n"
    "v_mfma_f32_16x16x32_bf16 v[60:63], a[28:31], a[68:71], v[60:63]\n"
    "ds_read_b64_tr_b16 a[18:19], v10 offset:20480\n"
    "buffer_load_dwordx4 v7, s[44:47], s73 offen offset:3072 lds\n"
    "v_mfma_f32_16x16x32_bf16 v[92:95], a[24:27], a[72:75], v[92:95]\n"
    "ds_read_b64_tr_b16 a[20:21], v10 offset:24576\n"
    "v_mfma_f32_16x16x32_bf16 v[92:95], a[28:31], a[76:79], v[92:95]\n"
    "s_waitcnt vmcnt(17) lgkmcnt(7)\n"
    "ds_read_b64_tr_b16 a[22:23], v10 offset:28672\n"
    "v_mfma_f32_16x16x32_bf16 v[32:35], a[0:3], a[80:83], v[32:35]\n"
    "ds_read_b64_tr_b16 a[24:25], v11 offset:16384\n"
    "v_add_u32 v10, s78, v10\n"
    "v_mfma_f32_16x16x32_bf16 v[32:35], a[4:7], a[84:87], v[32:35]\n"
    "ds_read_b64_tr_b16 a[26:27], v11 offset:20480\n"
    "buffer_load_dwordx4 a[64:67], v2, s[40:43], s72 offen sc1\n"
    "v_mfma_f32_16x16x32_bf16 v[64:67], a[0:3], a[88:91], v[64:67]\n"
    "s_add_u32 m0, s76, s88\n"
    "ds_read_b64_tr_b16 a[28:29], v11 offset:24576\n"
    "v_mfma_f32_16x16x32_bf16 v[64:67], a[4:7], a[92:95], v[64:67]\n"
    "s_waitcnt lgkmcnt(7)\n"
    "ds_read_b64_tr_b16 a[30:31], v11 offset:28672\n"
    "v_mfma_f32_16x16x32_bf16 v[36:39], a[8:11], a[80:83], v[36:39]\n"
    "ds_read_b64_tr_b16 a[0:1], v12 offset:16384\n"
    "v_add_u32 v11, s78, v11\n"
    "v_mfma_f32_16x16x32_bf16 v[36:39], a[12:15], a[84:87], v[36:39]\n"
    "ds_read_b64_tr_b16 a[2:3], v12 offset:20480\n"
    "buffer_load_dwordx4 v4, s[44:47], s74 offen lds\n"
    "v_mfma_f32_16x16x32_bf16 v[68:71], a[8:11], a[88:91], v[68:71]\n"
    "ds_read_b64_tr_b16 a[4:5], v12 offset:24576\n"
    "v_mfma_f32_16x16x32_bf16 v[68:71], a[12:15], a[92:95], v[68:71]\n"
    "s_waitcnt lgkmcnt(7)\n"
    "ds_read_b64_tr_b16 a[6:7], v12 offset:28672\n"
    "v_mfma_f32_16x16x32_bf16 v[40:43], a[16:19], a[80:83], v[40:43]\n"
    "ds_read_b64_tr_b16 a[8:9], v13 offset:16384\n"
    "v_add_u32 v12, s78, v12\n"
    "v_mfma_f32_16x16x32_bf16 v[40:43], a[20:23], a[84:87], v[40:43]\n"
    "ds_read_b64_tr_b16 a[10:11], v13 offset:20480\n"
    "buffer_load_dwordx4 a[68:71], v2, s[40:43], s72 offen offset:1024 sc1\n"
    "v_mfma_f32_16x16x32_bf16 v[72:75], a[16:19], a[88:91], v[72:75]\n"
    "ds_read_b64_tr_b16 a[12:13], v13 offset:24576\n"
    "v_mfma_f32_16x16x32_bf16 v[72:75], a[20:23], a[92:95], v[72:75]\n"
    "s_waitcnt lgkmcnt(7)\n"
    "ds_read_b64_tr_b16 a[14:15], v13 offset:28672\n"
    "v_mfma_f32_16x16x32_bf16 v[44:47], a[24:27], a[80:83], v[44:47]\n"
    "ds_read_b64_tr_b16 a[16:17], v14 offset:16384\n"
    "v_add_u32 v13, s78, v13\n"
    "v_mfma_f32_16x16x32_bf16 v[44:47], a[28:31], a[84:87], v[44:47]\n"
    "ds_read_b64_tr_b16 a[18:19], v14 offset:20480\n"
    "buffer_load_dwordx4 v5, s[44:47], s74 offen offset:1024 lds\n"
    "v_mfma_f32_16x16x32_bf16 v[76:79], a[24:27], a[88:91], v[76:79]\n"
    "ds_read_b64_tr_b16 a[20:21], v14 offset:24576\n"
    "v_mfma_f32_16x16x32_bf16 v[76:79], a[28:31], a[92:95], v[76:79]\n"
    "s_waitcnt lgkmcnt(7)\n"
    "ds_read_b64_tr_b16 a[22:23], v14 offset:28672\n"
    "v_mfma_f32_16x16x32_bf16 v[48:51], a[0:3], a[80:83], v[48:51]\n"
    "ds_read_b64_tr_b16 a[24:25], v15 offset:16384\n"
    "v_add_u32 v14, s78, v14\n"
    "v_mfma_f32_16x16x32_bf16 v[48:51], a[4:7], a[84:87], v[48:51]\n"
    "ds_read_b64_tr_b16 a[26:27], v15 offset:20480\n"
    "buffer_load_dwordx4 a[72:75], v2, s[40:43], s72 offen offset:2048 sc1\n"
    "v_mfma_f32_16x16x32_bf16 v[80:83], a[0:3], a[88:91], v[80:83]\n"
    "ds_read_b64_tr_b16 a[28:29], v15 offset:24576\n"
    "v_mfma_f32_16x16x32_bf16 v[80:83], a[4:7], a[92:95], v[80:83]\n"
    "s_waitcnt vmcnt(13) lgkmcnt(7)\n"
    "s_barrier\n"
    "ds_read_b64_tr_b16 a[30:31], v15 offset:28672\n"
    "v_add_u32 v15, s78, v15\n"
    "v_mfma_f32_16x16x32_bf16 v[52:55], a[8:11], a[80:83], v[52:55]\n"
    "ds_read_b64_tr_b16 a[0:1], v8\n"
    "buffer_load_dwordx4 v6, s[44:47], s74 offen offset:2048 lds\n"
    "v_mfma_f32_16x16x32_bf16 v[52:55], a[12:15], a[84:87], v[52:55]\n"
    "ds_read_b64_tr_b16 a[2:3], v8 offset:4096\n"
    "v_mfma_f32_16x16x32_bf16 v[84:87], a[8:11], a[88:91], v[84:87]\n"
    "ds_read_b64_tr_b16 a[4:5], v8 offset:8192\n"
    "v_mfma_f32_16x16x32_bf16 v[84:87], a[12:15], a[92:95], v[84:87]\n"
    "s_waitcnt lgkmcnt(7)\n"
    "ds_read_b64_tr_b16 a[6:7], v8 offset:12288\n"
    "v_mfma_f32_16x16x32_bf16 v[56:59], a[16:19], a[80:83], v[56:59]\n"
    "ds_read_b64_tr_b16 a[8:9], v9\n"
    "buffer_load_dwordx4 a[76:79], v2, s[40:43], s72 offen offset:3072 sc1\n"
    "v_mfma_f32_16x16x32_bf16 v[56:59], a[20:23], a[84:87], v[56:59]\n"
    "s_add_u32 s72, s72, 0x40000\n"
    "ds_read_b64_tr_b16 a[10:11], v9 offset:4096\n"
    "v_mfma_f32_16x16x32_bf16 v[88:91], a[16:19], a[88:91], v[88:91]\n"
    "ds_read_b64_tr_b16 a[12:13], v9 offset:8192\n"
    "v_mfma_f32_16x16x32_bf16 v[88:91], a[20:23], a[92:95], v[88:91]\n"
    "s_waitcnt lgkmcnt(7)\n"
    "ds_read_b64_tr_b16 a[14:15], v9 offset:12288\n"
    "v_mfma_f32_16x16x32_bf16 v[60:63], a[24:27], a[80:83], v[60:63]\n"
    "s_sub_u32 s75, s75, 1\n"
    "ds_read_b64_tr_b16 a[16:17], v10\n"
    "v_mfma_f32_16x16x32_bf16 v[60:63], a[28:31], a[84:87], v[60:63]\n"
    "ds_read_b64_tr_b16 a[18:19], v10 offset:4096\n"
    "buffer_load_dwordx4 v7, s[44:47], s74 offen offset:3072 lds\n"
    "v_mfma_f32_16x16x32_bf16 v[92:95], a[24:27], a[88:91], v[92:95]\n"
    "s_add_u32 s73, s73, 0x40000\n"
    "ds_read_b64_tr_b16 a[20:21], v10 offset:8192\n"
    "v_mfma_f32_16x16x32_bf16 v[92:95], a[28:31], a[92:95], v[92:95]\n"
    "s_add_u32 s74, s74, 0x40000\n"
    "s_add_u32 s76, s76, 0x8000\n"
    "s_cmp_lt_u32 s76, 0x28000\n"
    "s_cselect_b32 s76, s76, 0\n"
    "ds_read_b64_tr_b16 a[22:23], v10 offset:12288\n"
    "s_cmp_lg_u32 s75, 0\n"
    "s_cbranch_scc1 k_oproj_loop\n"
    "s_cmp_lg_u32 s83, 0\n"
    "s_cbranch_scc1 k_oproj_b\n"
    "s_waitcnt lgkmcnt(8)\n"
    "v_mfma_f32_16x16x32_bf16 v[32:35], a[0:3], a[32:35], v[32:35]\n"
    "s_add_u32 s85, s89, 0x1000\n"
    "ds_read_b64_tr_b16 a[24:25], v11\n"
    "v_mfma_f32_16x16x32_bf16 v[32:35], a[4:7], a[36:39], v[32:35]\n"
    "ds_read_b64_tr_b16 a[26:27], v11 offset:4096\n"
    "v_mfma_f32_16x16x32_bf16 v[64:67], a[0:3], a[40:43], v[64:67]\n"
    "ds_read_b64_tr_b16 a[28:29], v11 offset:8192\n"
    "v_mfma_f32_16x16x32_bf16 v[64:67], a[4:7], a[44:47], v[64:67]\n"
    "s_waitcnt lgkmcnt(7)\n"
    "ds_read_b64_tr_b16 a[30:31], v11 offset:12288\n"
    "v_mfma_f32_16x16x32_bf16 v[36:39], a[8:11], a[32:35], v[36:39]\n"
    "ds_read_b64_tr_b16 a[0:1], v12\n"
    "v_mfma_f32_16x16x32_bf16 v[36:39], a[12:15], a[36:39], v[36:39]\n"
    "ds_read_b64_tr_b16 a[2:3], v12 offset:4096\n"
    "v_mfma_f32_16x16x32_bf16 v[68:71], a[8:11], a[40:43], v[68:71]\n"
    "ds_read_b64_tr_b16 a[4:5], v12 offset:8192\n"
    "v_mfma_f32_16x16x32_bf16 v[68:71], a[12:15], a[44:47], v[68:71]\n"
    "s_waitcnt lgkmcnt(7)\n"
    "ds_read_b64_tr_b16 a[6:7], v12 offset:12288\n"
    "v_mfma_f32_16x16x32_bf16 v[40:43], a[16:19], a[32:35], v[40:43]\n"
    "ds_read_b64_tr_b16 a[8:9], v13\n"
    "v_mfma_f32_16x16x32_bf16 v[40:43], a[20:23], a[36:39], v[40:43]\n"
    "ds_read_b64_tr_b16 a[10:11], v13 offset:4096\n"
    "v_mfma_f32_16x16x32_bf16 v[72:75], a[16:19], a[40:43], v[72:75]\n"
    "ds_read_b64_tr_b16 a[12:13], v13 offset:8192\n"
    "v_mfma_f32_16x16x32_bf16 v[72:75], a[20:23], a[44:47], v[72:75]\n"
    "s_waitcnt lgkmcnt(7)\n"
    "ds_read_b64_tr_b16 a[14:15], v13 offset:12288\n"
    "v_mfma_f32_16x16x32_bf16 v[44:47], a[24:27], a[32:35], v[44:47]\n"
    "ds_read_b64_tr_b16 a[16:17], v14\n"
    "v_mfma_f32_16x16x32_bf16 v[44:47], a[28:31], a[36:39], v[44:47]\n"
    "ds_read_b64_tr_b16 a[18:19], v14 offset:4096\n"
    "v_mfma_f32_16x16x32_bf16 v[76:79], a[24:27], a[40:43], v[76:79]\n"
    "ds_read_b64_tr_b16 a[20:21], v14 offset:8192\n"
    "v_mfma_f32_16x16x32_bf16 v[76:79], a[28:31], a[44:47], v[76:79]\n"
    "s_waitcnt lgkmcnt(7)\n"
    "ds_read_b64_tr_b16 a[22:23], v14 offset:12288\n"
    "v_mfma_f32_16x16x32_bf16 v[48:51], a[0:3], a[32:35], v[48:51]\n"
    "ds_read_b64_tr_b16 a[24:25], v15\n"
    "v_mfma_f32_16x16x32_bf16 v[48:51], a[4:7], a[36:39], v[48:51]\n"
    "ds_read_b64_tr_b16 a[26:27], v15 offset:4096\n"
    "v_mfma_f32_16x16x32_bf16 v[80:83], a[0:3], a[40:43], v[80:83]\n"
    "ds_read_b64_tr_b16 a[28:29], v15 offset:8192\n"
    "v_mfma_f32_16x16x32_bf16 v[80:83], a[4:7], a[44:47], v[80:83]\n"
    "s_waitcnt lgkmcnt(7)\n"
    "ds_read_b64_tr_b16 a[30:31], v15 offset:12288\n"
    "v_mfma_f32_16x16x32_bf16 v[52:55], a[8:11], a[32:35], v[52:55]\n"
    "ds_read_b64_tr_b16 a[0:1], v8 offset:16384\n"
    "v_mfma_f32_16x16x32_bf16 v[52:55], a[12:15], a[36:39], v[52:55]\n"
    "ds_read_b64_tr_b16 a[2:3], v8 offset:20480\n"
    "v_mfma_f32_16x16x32_bf16 v[84:87], a[8:11], a[40:43], v[84:87]\n"
    "ds_read_b64_tr_b16 a[4:5], v8 offset:24576\n"
    "v_mfma_f32_16x16x32_bf16 v[84:87], a[12:15], a[44:47], v[84:87]\n"
    "s_waitcnt lgkmcnt(7)\n"
    "ds_read_b64_tr_b16 a[6:7], v8 offset:28672\n"
    "v_mfma_f32_16x16x32_bf16 v[56:59], a[16:19], a[32:35], v[56:59]\n"
    "ds_read_b64_tr_b16 a[8:9], v9 offset:16384\n"
    "v_mfma_f32_16x16x32_bf16 v[56:59], a[20:23], a[36:39], v[56:59]\n"
    "ds_read_b64_tr_b16 a[10:11], v9 offset:20480\n"
    "v_mfma_f32_16x16x32_bf16 v[88:91], a[16:19], a[40:43], v[88:91]\n"
    "ds_read_b64_tr_b16 a[12:13], v9 offset:24576\n"
    "v_mfma_f32_16x16x32_bf16 v[88:91], a[20:23], a[44:47], v[88:91]\n"
    "s_waitcnt lgkmcnt(7)\n"
    "ds_read_b64_tr_b16 a[14:15], v9 offset:28672\n"
    "v_mfma_f32_16x16x32_bf16 v[60:63], a[24:27], a[32:35], v[60:63]\n"
    "ds_read_b64_tr_b16 a[16:17], v10 offset:16384\n"
    "v_mfma_f32_16x16x32_bf16 v[60:63], a[28:31], a[36:39], v[60:63]\n"
    "ds_read_b64_tr_b16 a[18:19], v10 offset:20480\n"
    "v_mfma_f32_16x16x32_bf16 v[92:95], a[24:27], a[40:43], v[92:95]\n"
    "ds_read_b64_tr_b16 a[20:21], v10 offset:24576\n"
    "v_mfma_f32_16x16x32_bf16 v[92:95], a[28:31], a[44:47], v[92:95]\n"
    "s_waitcnt vmcnt(9) lgkmcnt(7)\n"
    "ds_read_b64_tr_b16 a[22:23], v10 offset:28672\n"
    "v_mfma_f32_16x16x32_bf16 v[32:35], a[0:3], a[48:51], v[32:35]\n"
    "ds_read_b64_tr_b16 a[24:25], v11 offset:16384\n"
    "v_mfma_f32_16x16x32_bf16 v[32:35], a[4:7], a[52:55], v[32:35]\n"
    "ds_read_b64_tr_b16 a[26:27], v11 offset:20480\n"
    "v_mfma_f32_16x16x32_bf16 v[64:67], a[0:3], a[56:59], v[64:67]\n"
    "ds_read_b64_tr_b16 a[28:29], v11 offset:24576\n"
    "v_mfma_f32_16x16x32_bf16 v[64:67], a[4:7], a[60:63], v[64:67]\n"
    "s_waitcnt lgkmcnt(7)\n"
    "ds_read_b64_tr_b16 a[30:31], v11 offset:28672\n"
    "v_mfma_f32_16x16x32_bf16 v[36:39], a[8:11], a[48:51], v[36:39]\n"
    "ds_read_b64_tr_b16 a[0:1], v12 offset:16384\n"
    "v_mfma_f32_16x16x32_bf16 v[36:39], a[12:15], a[52:55], v[36:39]\n"
    "ds_read_b64_tr_b16 a[2:3], v12 offset:20480\n"
    "v_mfma_f32_16x16x32_bf16 v[68:71], a[8:11], a[56:59], v[68:71]\n"
    "ds_read_b64_tr_b16 a[4:5], v12 offset:24576\n"
    "v_mfma_f32_16x16x32_bf16 v[68:71], a[12:15], a[60:63], v[68:71]\n"
    "s_waitcnt lgkmcnt(7)\n"
    "ds_read_b64_tr_b16 a[6:7], v12 offset:28672\n"
    "v_mfma_f32_16x16x32_bf16 v[40:43], a[16:19], a[48:51], v[40:43]\n"
    "ds_read_b64_tr_b16 a[8:9], v13 offset:16384\n"
    "v_mfma_f32_16x16x32_bf16 v[40:43], a[20:23], a[52:55], v[40:43]\n"
    "ds_read_b64_tr_b16 a[10:11], v13 offset:20480\n"
    "v_cvt_pk_bf16_f32 v192, v32, v33\n"
    "v_mfma_f32_16x16x32_bf16 v[72:75], a[16:19], a[56:59], v[72:75]\n"
    "ds_read_b64_tr_b16 a[12:13], v13 offset:24576\n"
    "v_cvt_pk_bf16_f32 v193, v34, v35\n"
    "v_mfma_f32_16x16x32_bf16 v[72:75], a[20:23], a[60:63], v[72:75]\n"
    "s_waitcnt lgkmcnt(7)\n"
    "ds_read_b64_tr_b16 a[14:15], v13 offset:28672\n"
    "v_mfma_f32_16x16x32_bf16 v[44:47], a[24:27], a[48:51], v[44:47]\n"
    "ds_read_b64_tr_b16 a[16:17], v14 offset:16384\n"
    "v_cvt_pk_bf16_f32 v194, v36, v37\n"
    "v_mfma_f32_16x16x32_bf16 v[44:47], a[28:31], a[52:55], v[44:47]\n"
    "ds_read_b64_tr_b16 a[18:19], v14 offset:20480\n"
    "v_cvt_pk_bf16_f32 v195, v38, v39\n"
    "v_mfma_f32_16x16x32_bf16 v[76:79], a[24:27], a[56:59], v[76:79]\n"
    "ds_read_b64_tr_b16 a[20:21], v14 offset:24576\n"
    "buffer_store_dwordx4 v[192:195], v3, s[60:63], s89 offen sc1\n"
    "v_mfma_f32_16x16x32_bf16 v[76:79], a[28:31], a[60:63], v[76:79]\n"
    "s_waitcnt lgkmcnt(7)\n"
    "ds_read_b64_tr_b16 a[22:23], v14 offset:28672\n"
    "v_mfma_f32_16x16x32_bf16 v[48:51], a[0:3], a[48:51], v[48:51]\n"
    "ds_read_b64_tr_b16 a[24:25], v15 offset:16384\n"
    "v_cvt_pk_bf16_f32 v208, v64, v65\n"
    "v_mfma_f32_16x16x32_bf16 v[48:51], a[4:7], a[52:55], v[48:51]\n"
    "ds_read_b64_tr_b16 a[26:27], v15 offset:20480\n"
    "v_cvt_pk_bf16_f32 v209, v66, v67\n"
    "v_cvt_pk_bf16_f32 v196, v40, v41\n"
    "v_mfma_f32_16x16x32_bf16 v[80:83], a[0:3], a[56:59], v[80:83]\n"
    "ds_read_b64_tr_b16 a[28:29], v15 offset:24576\n"
    "v_cvt_pk_bf16_f32 v210, v68, v69\n"
    "v_cvt_pk_bf16_f32 v197, v42, v43\n"
    "v_mfma_f32_16x16x32_bf16 v[80:83], a[4:7], a[60:63], v[80:83]\n"
    "s_waitcnt lgkmcnt(7)\n"
    "ds_read_b64_tr_b16 a[30:31], v15 offset:28672\n"
    "v_mfma_f32_16x16x32_bf16 v[52:55], a[8:11], a[48:51], v[52:55]\n"
    "v_cvt_pk_bf16_f32 v211, v70, v71\n"
    "v_cvt_pk_bf16_f32 v198, v44, v45\n"
    "v_mfma_f32_16x16x32_bf16 v[52:55], a[12:15], a[52:55], v[52:55]\n"
    "buffer_store_dwordx4 v[208:211], v3, s[60:63], s85 offen sc1\n"
    "v_mfma_f32_16x16x32_bf16 v[84:87], a[8:11], a[56:59], v[84:87]\n"
    "v_cvt_pk_bf16_f32 v199, v46, v47\n"
    "v_mfma_f32_16x16x32_bf16 v[84:87], a[12:15], a[60:63], v[84:87]\n"
    "s_waitcnt lgkmcnt(4)\n"
    "v_mfma_f32_16x16x32_bf16 v[56:59], a[16:19], a[48:51], v[56:59]\n"
    "buffer_store_dwordx4 v[196:199], v3, s[60:63], s89 offen offset:1024 sc1\n"
    "v_mfma_f32_16x16x32_bf16 v[56:59], a[20:23], a[52:55], v[56:59]\n"
    "v_cvt_pk_bf16_f32 v212, v72, v73\n"
    "v_cvt_pk_bf16_f32 v213, v74, v75\n"
    "v_mfma_f32_16x16x32_bf16 v[88:91], a[16:19], a[56:59], v[88:91]\n"
    "v_cvt_pk_bf16_f32 v214, v76, v77\n"
    "v_cvt_pk_bf16_f32 v215, v78, v79\n"
    "v_mfma_f32_16x16x32_bf16 v[88:91], a[20:23], a[60:63], v[88:91]\n"
    "s_waitcnt lgkmcnt(0)\n"
    "v_cvt_pk_bf16_f32 v200, v48, v49\n"
    "v_cvt_pk_bf16_f32 v201, v50, v51\n"
    "v_mfma_f32_16x16x32_bf16 v[60:63], a[24:27], a[48:51], v[60:63]\n"
    "buffer_store_dwordx4 v[212:215], v3, s[60:63], s85 offen offset:1024 sc1\n"
    "v_mfma_f32_16x16x32_bf16 v[60:63], a[28:31], a[52:55], v[60:63]\n"
    "v_cvt_pk_bf16_f32 v202, v52, v53\n"
    "v_cvt_pk_bf16_f32 v203, v54, v55\n"
    "v_mfma_f32_16x16x32_bf16 v[92:95], a[24:27], a[56:59], v[92:95]\n"
    "buffer_store_dwordx4 v[200:203], v3, s[60:63], s89 offen offset:2048 sc1\n"
    "v_mfma_f32_16x16x32_bf16 v[92:95], a[28:31], a[60:63], v[92:95]\n"
    "v_cvt_pk_bf16_f32 v216, v80, v81\n"
    "v_cvt_pk_bf16_f32 v217, v82, v83\n"
    "v_cvt_pk_bf16_f32 v218, v84, v85\n"
    "v_cvt_pk_bf16_f32 v219, v86, v87\n"
    "s_nop 7\n"
    "buffer_store_dwordx4 v[216:219], v3, s[60:63], s85 offen offset:2048 sc1\n"
    "v_cvt_pk_bf16_f32 v204, v56, v57\n"
    "v_cvt_pk_bf16_f32 v205, v58, v59\n"
    "v_cvt_pk_bf16_f32 v206, v60, v61\n"
    "v_cvt_pk_bf16_f32 v207, v62, v63\n"
    "buffer_store_dwordx4 v[204:207], v3, s[60:63], s89 offen offset:3072 sc1\n"
    "v_cvt_pk_bf16_f32 v220, v88, v89\n"
    "v_cvt_pk_bf16_f32 v221, v90, v91\n"
    "v_cvt_pk_bf16_f32 v222, v92, v93\n"
    "v_cvt_pk_bf16_f32 v223, v94, v95\n"
    "buffer_store_dwordx4 v[220:223], v3, s[60:63], s85 offen offset:3072 sc1\n"
    "v_mov_b32 v18, s94\n"
    "v_mov_b32 v19, s95\n"
    "s_waitcnt vmcnt(0)\n"
    "s_mov_b64 exec, 1\n buffer_store_dwordx2 v[18:19], off, s[64:67], s90 offset:0 sc1\n s_mov_b64 exec, -1\n"
    "s_cmp_ge_u32 s90, 0x1c00\n"
    "s_cbranch_scc1 k_oproj_ncs\n"
    "v_and_b32 v18, 15, v1\n"
    "v_mul_u32_u24 v18, 56, v18\n"
    "s_lshr_b32 s85, s90, 7\n"
    "v_add_u32 v18, s85, v18\n"
    "v_lshrrev_b32 v18, 2, v18\n"
    "v_lshlrev_b32 v18, 7, v18\n"
    "s_mov_b32 s91, 1048576\n"
    "k_oproj_cs_spin:\n"
    "buffer_load_dwordx2 v[20:21], v18, s[56:59], 0 offen sc1\n"
    "s_waitcnt vmcnt(0)\n"
    "v_cmp_ne_u64 vcc, s[94:95], v[20:21]\n"
    "s_sub_u32 s91, s91, 1\n"
    "s_cmp_eq_u64 vcc, 0\n"
    "s_cbranch_scc1 k_oproj_cs_go\n"
    "s_sleep 1\n"
    "s_cmp_lg_u32 s91, 0\n"
    "s_cbranch_scc1 k_oproj_cs_spin\n"
    "k_oproj_cs_go:\n"
    "s_add_u32 s68, s32, 0x80000\n"
    "s_addc_u32 s69, s33, 0\n"
    "s_mov_b32 s70, 0x38000\n"
    "s_mov_b32 s71, 0x20000\n"
    "s_lshl_b32 s91, s90, 1\n"
    "v_and_b32 v18, 15, v1\n"
    "v_mul_u32_u24 v18, 0x3800, v18\n"
    "v_and_b32 v30, 48, v1\n"
    "v_add_u32 v18, v18, v30\n"
    "buffer_load_dwordx4 v[26:29], v18, s[68:71], s91 offen sc1\n"
    "s_add_u32 s68, s32, 0x10000\n"
    "s_addc_u32 s69, s33, 0\n"
    "s_mov_b32 s70, 0x3800\n"
    "s_mov_b32 s71, 0x20000\n"
    "s_waitcnt vmcnt(0)\n"
    "v_add_f32_dpp v26, v26, v26 row_ror:8 row_mask:0xf bank_mask:0xf\n"
    "v_add_f32_dpp v27, v27, v27 row_ror:8 row_mask:0xf bank_mask:0xf\n"
    "v_add_f32_dpp v28, v28, v28 row_ror:8 row_mask:0xf bank_mask:0xf\n"
    "v_add_f32_dpp v29, v29, v29 row_ror:8 row_mask:0xf bank_mask:0xf\n"
    "v_add_f32_dpp v26, v26, v26 row_ror:4 row_mask:0xf bank_mask:0xf\n"
    "v_add_f32_dpp v27, v27, v27 row_ror:4 row_mask:0xf bank_mask:0xf\n"
    "v_add_f32_dpp v28, v28, v28 row_ror:4 row_mask:0xf bank_mask:0xf\n"
    "v_add_f32_dpp v29, v29, v29 row_ror:4 row_mask:0xf bank_mask:0xf\n"
    "v_add_f32_dpp v26, v26, v26 row_ror:2 row_mask:0xf bank_mask:0xf\n"
    "v_add_f32_dpp v27, v27, v27 row_ror:2 row_mask:0xf bank_mask:0xf\n"
    "v_add_f32_dpp v28, v28, v28 row_ror:2 row_mask:0xf bank_mask:0xf\n"
    "v_add_f32_dpp v29, v29, v29 row_ror:2 row_mask:0xf bank_mask:0xf\n"
    "v_add_f32_dpp v26, v26, v26 row_ror:1 row_mask:0xf bank_mask:0xf\n"
    "v_add_f32_dpp v27, v27, v27 row_ror:1 row_mask:0xf bank_mask:0xf\n"
    "v_add_f32_dpp v28, v28, v28 row_ror:1 row_mask:0xf bank_mask:0xf\n"
    "v_add_f32_dpp v29, v29, v29 row_ror:1 row_mask:0xf bank_mask:0xf\n"
    "buffer_store_dwordx4 v[26:29], v30, s[68:71], s91 offen sc1\n"
    "k_oproj_ncs:\n"
    "s_branch k_oproj_end\n"
    "k_oproj_b:\n"
    "s_waitcnt lgkmcnt(8)\n"
    "v_mfma_f32_16x16x32_bf16 v[32:35], a[0:3], a[32:35], v[32:35]\n"
    "ds_read_b64_tr_b16 a[24:25], v11\n"
    "v_mfma_f32_16x16x32_bf16 v[32:35], a[4:7], a[36:39], v[32:35]\n"
    "ds_read_b64_tr_b16 a[26:27], v11 offset:4096\n"
    "buffer_load_dwordx4 a[80:83], v2, s[40:43], s72 offen sc1\n"
    "v_mfma_f32_16x16x32_bf16 v[64:67], a[0:3], a[40:43], v[64:67]\n"
    "s_add_u32 m0, s76, s87\n"
    "ds_read_b64_tr_b16 a[28:29], v11 offset:8192\n"
    "v_mfma_f32_16x16x32_bf16 v[64:67], a[4:7], a[44:47], v[64:67]\n"
    "s_waitcnt lgkmcnt(7)\n"
    "ds_read_b64_tr_b16 a[30:31], v11 offset:12288\n"
    "v_mfma_f32_16x16x32_bf16 v[36:39], a[8:11], a[32:35], v[36:39]\n"
    "ds_read_b64_tr_b16 a[0:1], v12\n"
    "buffer_load_dwordx4 v4, s[44:47], s73 offen lds\n"
    "v_mfma_f32_16x16x32_bf16 v[36:39], a[12:15], a[36:39], v[36:39]\n"
    "ds_read_b64_tr_b16 a[2:3], v12 offset:4096\n"
    "v_mfma_f32_16x16x32_bf16 v[68:71], a[8:11], a[40:43], v[68:71]\n"
    "ds_read_b64_tr_b16 a[4:5], v12 offset:8192\n"
    "v_mfma_f32_16x16x32_bf16 v[68:71], a[12:15], a[44:47], v[68:71]\n"
    "s_waitcnt lgkmcnt(7)\n"
    "ds_read_b64_tr_b16 a[6:7], v12 offset:12288\n"
    "v_mfma_f32_16x16x32_bf16 v[40:43], a[16:19], a[32:35], v[40:43]\n"
    "ds_read_b64_tr_b16 a[8:9], v13\n"
    "buffer_load_dwordx4 a[84:87], v2, s[40:43], s72 offen offset:1024 sc1\n"
    "v_mfma_f32_16x16x32_bf16 v[40:43], a[20:23], a[36:39], v[40:43]\n"
    "ds_read_b64_tr_b16 a[10:11], v13 offset:4096\n"
    "v_mfma_f32_16x16x32_bf16 v[72:75], a[16:19], a[40:43], v[72:75]\n"
    "ds_read_b64_tr_b16 a[12:13], v13 offset:8192\n"
    "v_mfma_f32_16x16x32_bf16 v[72:75], a[20:23], a[44:47], v[72:75]\n"
    "s_waitcnt lgkmcnt(7)\n"
    "ds_read_b64_tr_b16 a[14:15], v13 offset:12288\n"
    "v_mfma_f32_16x16x32_bf16 v[44:47], a[24:27], a[32:35], v[44:47]\n"
    "ds_read_b64_tr_b16 a[16:17], v14\n"
    "buffer_load_dwordx4 v5, s[44:47], s73 offen offset:1024 lds\n"
    "v_mfma_f32_16x16x32_bf16 v[44:47], a[28:31], a[36:39], v[44:47]\n"
    "ds_read_b64_tr_b16 a[18:19], v14 offset:4096\n"
    "v_mfma_f32_16x16x32_bf16 v[76:79], a[24:27], a[40:43], v[76:79]\n"
    "ds_read_b64_tr_b16 a[20:21], v14 offset:8192\n"
    "v_mfma_f32_16x16x32_bf16 v[76:79], a[28:31], a[44:47], v[76:79]\n"
    "s_waitcnt lgkmcnt(7)\n"
    "ds_read_b64_tr_b16 a[22:23], v14 offset:12288\n"
    "v_mfma_f32_16x16x32_bf16 v[48:51], a[0:3], a[32:35], v[48:51]\n"
    "ds_read_b64_tr_b16 a[24:25], v15\n"
    "buffer_load_dwordx4 a[88:91], v2, s[40:43], s72 offen offset:2048 sc1\n"
    "v_mfma_f32_16x16x32_bf16 v[48:51], a[4:7], a[36:39], v[48:51]\n"
    "ds_read_b64_tr_b16 a[26:27], v15 offset:4096\n"
    "v_mfma_f32_16x16x32_bf16 v[80:83], a[0:3], a[40:43], v[80:83]\n"
    "s_add_u32 s77, s77, 1\n"
    "ds_read_b64_tr_b16 a[28:29], v15 offset:8192\n"
    "v_mfma_f32_16x16x32_bf16 v[80:83], a[4:7], a[44:47], v[80:83]\n"
    "s_waitcnt lgkmcnt(7)\n"
    "ds_read_b64_tr_b16 a[30:31], v15 offset:12288\n"
    "v_mfma_f32_16x16x32_bf16 v[52:55], a[8:11], a[32:35], v[52:55]\n"
    "ds_read_b64_tr_b16 a[0:1], v8 offset:16384\n"
    "buffer_load_dwordx4 v6, s[44:47], s73 offen offset:2048 lds\n"
    "v_mfma_f32_16x16x32_bf16 v[52:55], a[12:15], a[36:39], v[52:55]\n"
    "s_cmp_lt_u32 s77, 5\n"
    "s_cselect_b32 s77, s77, 0\n"
    "s_cselect_b32 s78, 0x8000, s79\n"
    "ds_read_b64_tr_b16 a[2:3], v8 offset:20480\n"
    "v_mfma_f32_16x16x32_bf16 v[84:87], a[8:11], a[40:43], v[84:87]\n"
    "ds_read_b64_tr_b16 a[4:5], v8 offset:24576\n"
    "v_mfma_f32_16x16x32_bf16 v[84:87], a[12:15], a[44:47], v[84:87]\n"
    "s_waitcnt lgkmcnt(7)\n"
    "ds_read_b64_tr_b16 a[6:7], v8 offset:28672\n"
    "v_mfma_f32_16x16x32_bf16 v[56:59], a[16:19], a[32:35], v[56:59]\n"
    "ds_read_b64_tr_b16 a[8:9], v9 offset:16384\n"
    "v_add_u32 v8, s78, v8\n"
    "v_mfma_f32_16x16x32_bf16 v[56:59], a[20:23], a[36:39], v[56:59]\n"
    "ds_read_b64_tr_b16 a[10:11], v9 offset:20480\n"
    "buffer_load_dwordx4 a[92:95], v2, s[40:43], s72 offen offset:3072 sc1\n"
    "v_mfma_f32_16x16x32_bf16 v[88:91], a[16:19], a[40:43], v[88:91]\n"
    "s_add_u32 s72, s72, 0x40000\n"
    "ds_read_b64_tr_b16 a[12:13], v9 offset:24576\n"
    "v_mfma_f32_16x16x32_bf16 v[88:91], a[20:23], a[44:47], v[88:91]\n"
    "s_waitcnt lgkmcnt(7)\n"
    "ds_read_b64_tr_b16 a[14:15], v9 offset:28672\n"
    "v_mfma_f32_16x16x32_bf16 v[60:63], a[24:27], a[32:35], v[60:63]\n"
    "ds_read_b64_tr_b16 a[16:17], v10 offset:16384\n"
    "v_add_u32 v9, s78, v9\n"
    "v_mfma_f32_16x16x32_bf16 v[60:63], a[28:31], a[36:39], v[60:63]\n"
    "ds_read_b64_tr_b16 a[18:19], v10 offset:20480\n"
    "buffer_load_dwordx4 v7, s[44:47], s73 offen offset:3072 lds\n"
    "v_mfma_f32_16x16x32_bf16 v[92:95], a[24:27], a[40:43], v[92:95]\n"
    "ds_read_b64_tr_b16 a[20:21], v10 offset:24576\n"
    "v_mfma_f32_16x16x32_bf16 v[92:95], a[28:31], a[44:47], v[92:95]\n"
    "s_waitcnt vmcnt(17) lgkmcnt(7)\n"
    "ds_read_b64_tr_b16 a[22:23], v10 offset:28672\n"
    "v_mfma_f32_16x16x32_bf16 v[32:35], a[0:3], a[48:51], v[32:35]\n"
    "ds_read_b64_tr_b16 a[24:25], v11 offset:16384\n"
    "v_add_u32 v10, s78, v10\n"
    "v_mfma_f32_16x16x32_bf16 v[32:35], a[4:7], a[52:55], v[32:35]\n"
    "ds_read_b64_tr_b16 a[26:27], v11 offset:20480\n"
    "buffer_load_dwordx4 a[32:35], v2, s[40:43], s72 offen sc1\n"
    "v_mfma_f32_16x16x32_bf16 v[64:67], a[0:3], a[56:59], v[64:67]\n"
    "s_add_u32 m0, s76, s88\n"
    "ds_read_b64_tr_b16 a[28:29], v11 offset:24576\n"
    "v_mfma_f32_16x16x32_bf16 v[64:67], a[4:7], a[60:63], v[64:67]\n"
    "s_waitcnt lgkmcnt(7)\n"
    "ds_read_b64_tr_b16 a[30:31], v11 offset:28672\n"
    "v_mfma_f32_16x16x32_bf16 v[36:39], a[8:11], a[48:51], v[36:39]\n"
    "ds_read_b64_tr_b16 a[0:1], v12 offset:16384\n"
    "v_add_u32 v11, s78, v11\n"
    "v_mfma_f32_16x16x32_bf16 v[36:39], a[12:15], a[52:55], v[36:39]\n"
    "ds_read_b64_tr_b16 a[2:3], v12 offset:20480\n"
    "buffer_load_dwordx4 v4, s[44:47], s74 offen lds\n"
    "v_mfma_f32_16x16x32_bf16 v[68:71], a[8:11], a[56:59], v[68:71]\n"
    "ds_read_b64_tr_b16 a[4:5], v12 offset:24576\n"
    "v_mfma_f32_16x16x32_bf16 v[68:71], a[12:15], a[60:63], v[68:71]\n"
    "s_waitcnt lgkmcnt(7)\n"
    "ds_read_b64_tr_b16 a[6:7], v12 offset:28672\n"
    "v_mfma_f32_16x16x32_bf16 v[40:43], a[16:19], a[48:51], v[40:43]\n"
    "ds_read_b64_tr_b16 a[8:9], v13 offset:16384\n"
    "v_add_u32 v12, s78, v12\n"
    "v_mfma_f32_16x16x32_bf16 v[40:43], a[20:23], a[52:55], v[40:43]\n"
    "ds_read_b64_tr_b16 a[10:11], v13 offset:20480\n"
    "buffer_load_dwordx4 a[36:39], v2, s[40:43], s72 offen offset:1024 sc1\n"
    "v_mfma_f32_16x16x32_bf16 v[72:75], a[16:19], a[56:59], v[72:75]\n"
    "ds_read_b64_tr_b16 a[12:13], v13 offset:24576\n"
    "v_mfma_f32_16x16x32_bf16 v[72:75], a[20:23], a[60:63], v[72:75]\n"
    "s_waitcnt lgkmcnt(7)\n"
    "ds_read_b64_tr_b16 a[14:15], v13 offset:28672\n"
    "v_mfma_f32_16x16x32_bf16 v[44:47], a[24:27], a[48:51], v[44:47]\n"
    "ds_read_b64_tr_b16 a[16:17], v14 offset:16384\n"
    "v_add_u32 v13, s78, v13\n"
    "v_mfma_f32_16x16x32_bf16 v[44:47], a[28:31], a[52:55], v[44:47]\n"
    "ds_read_b64_tr_b16 a[18:19], v14 offset:20480\n"
    "buffer_load_dwordx4 v5, s[44:47], s74 offen offset:1024 lds\n"
    "v_mfma_f32_16x16x32_bf16 v[76:79], a[24:27], a[56:59], v[76:79]\n"
    "ds_read_b64_tr_b16 a[20:21], v14 offset:24576\n"
    "v_mfma_f32_16x16x32_bf16 v[76:79], a[28:31], a[60:63], v[76:79]\n"
    "s_waitcnt lgkmcnt(7)\n"
    "ds_read_b64_tr_b16 a[22:23], v14 offset:28672\n"
    "v_mfma_f32_16x16x32_bf16 v[48:51], a[0:3], a[48:51], v[48:51]\n"
    "ds_read_b64_tr_b16 a[24:25], v15 offset:16384\n"
    "v_add_u32 v14, s78, v14\n"
    "v_mfma_f32_16x16x32_bf16 v[48:51], a[4:7], a[52:55], v[48:51]\n"
    "ds_read_b64_tr_b16 a[26:27], v15 offset:20480\n"
    "buffer_load_dwordx4 a[40:43], v2, s[40:43], s72 offen offset:2048 sc1\n"
    "v_mfma_f32_16x16x32_bf16 v[80:83], a[0:3], a[56:59], v[80:83]\n"
    "ds_read_b64_tr_b16 a[28:29], v15 offset:24576\n"
    "v_mfma_f32_16x16x32_bf16 v[80:83], a[4:7], a[60:63], v[80:83]\n"
    "s_waitcnt vmcnt(13) lgkmcnt(7)\n"
    "s_barrier\n"
    "ds_read_b64_tr_b16 a[30:31], v15 offset:28672\n"
    "v_add_u32 v15, s78, v15\n"
    "v_mfma_f32_16x16x32_bf16 v[52:55], a[8:11], a[48:51], v[52:55]\n"
    "ds_read_b64_tr_b16 a[0:1], v8\n"
    "buffer_load_dwordx4 v6, s[44:47], s74 offen offset:2048 lds\n"
    "v_mfma_f32_16x16x32_bf16 v[52:55], a[12:15], a[52:55], v[52:55]\n"
    "ds_read_b64_tr_b16 a[2:3], v8 offset:4096\n"
    "v_mfma_f32_16x16x32_bf16 v[84:87], a[8:11], a[56:59], v[84:87]\n"
    "ds_read_b64_tr_b16 a[4:5], v8 offset:8192\n"
    "v_mfma_f32_16x16x32_bf16 v[84:87], a[12:15], a[60:63], v[84:87]\n"
    "s_waitcnt lgkmcnt(7)\n"
    "ds_read_b64_tr_b16 a[6:7], v8 offset:12288\n"
    "v_mfma_f32_16x16x32_bf16 v[56:59], a[16:19], a[48:51], v[56:59]\n"
    "ds_read_b64_tr_b16 a[8:9], v9\n"
    "buffer_load_dwordx4 a[44:47], v2, s[40:43], s72 offen offset:3072 sc1\n"
    "v_mfma_f32_16x16x32_bf16 v[56:59], a[20:23], a[52:55], v[56:59]\n"
    "s_add_u32 s72, s72, 0x40000\n"
    "ds_read_b64_tr_b16 a[10:11], v9 offset:4096\n"
    "v_mfma_f32_16x16x32_bf16 v[88:91], a[16:19], a[56:59], v[88:91]\n"
    "ds_read_b64_tr_b16 a[12:13], v9 offset:8192\n"
    "v_mfma_f32_16x16x32_bf16 v[88:91], a[20:23], a[60:63], v[88:91]\n"
    "s_waitcnt lgkmcnt(7)\n"
    "ds_read_b64_tr_b16 a[14:15], v9 offset:12288\n"
    "v_mfma_f32_16x16x32_bf16 v[60:63], a[24:27], a[48:51], v[60:63]\n"
    "ds_read_b64_tr_b16 a[16:17], v10\n"
    "buffer_load_dwordx4 v7, s[44:47], s74 offen offset:3072 lds\n"
    "v_mfma_f32_16x16x32_bf16 v[60:63], a[28:31], a[52:55], v[60:63]\n"
    "s_add_u32 s73, s73, 0x40000\n"
    "ds_read_b64_tr_b16 a[18:19], v10 offset:4096\n"
    "v_mfma_f32_16x16x32_bf16 v[92:95], a[24:27], a[56:59], v[92:95]\n"
    "s_add_u32 s74, s74, 0x40000\n"
    "ds_read_b64_tr_b16 a[20:21], v10 offset:8192\n"
    "v_mfma_f32_16x16x32_bf16 v[92:95], a[28:31], a[60:63], v[92:95]\n"
    "s_add_u32 s76, s76, 0x8000\n"
    "s_cmp_lt_u32 s76, 0x28000\n"
    "s_cselect_b32 s76, s76, 0\n"
    "ds_read_b64_tr_b16 a[22:23], v10 offset:12288\n"
    "s_mov_b32 s48, s36\n"
    "s_mov_b32 s49, s37\n"
    "s_mov_b32 s50, 0x400000\n"
    "s_mov_b32 s51, 0x20000\n"
    "v_lshrrev_b32 v18, 4, v1\n"
    "v_and_b32 v19, 15, v1\n"
    "v_xor_b32 v20, v19, v18\n"
    "v_lshlrev_b32 v18, 11, v18\n"
    "s_lshl_b32 s86, s84, 15\n"
    "s_lshl_b32 s85, s82, 8\n"
    "s_add_u32 s85, s86, s85\n"
    "s_add_u32 s86, s85, 0x8000\n"
    "v_xor_b32 v21, 0, v20\n"
    "v_lshl_add_u32 v21, v21, 4, v18\n"
    "v_add_u32 v192, 0, v21\n"
    "v_xor_b32 v21, 4, v20\n"
    "v_lshl_add_u32 v21, v21, 4, v18\n"
    "v_add_u32 v193, 0x1c00, v21\n"
    "v_xor_b32 v21, 8, v20\n"
    "v_lshl_add_u32 v21, v21, 4, v18\n"
    "v_add_u32 v194, 0x3800, v21\n"
    "v_xor_b32 v21, 12, v20\n"
    "v_lshl_add_u32 v21, v21, 4, v18\n"
    "v_add_u32 v195, 0x5400, v21\n"
    "v_lshrrev_b32 v18, 4, v1\n"
    "v_xor_b32 v18, v18, v19\n"
    "s_add_u32 s91, s87, 0x10000\n"
    "v_lshl_add_u32 v19, v19, 8, s91\n"
    "v_xor_b32 v21, 0, v18\n"
    "v_lshl_add_u32 v200, v21, 4, v19\n"
    "v_xor_b32 v21, 4, v18\n"
    "v_lshl_add_u32 v201, v21, 4, v19\n"
    "v_xor_b32 v21, 8, v18\n"
    "v_lshl_add_u32 v202, v21, 4, v19\n"
    "v_xor_b32 v21, 12, v18\n"
    "v_lshl_add_u32 v203, v21, 4, v19\n"
    "s_nop 1\n"
    "s_waitcnt lgkmcnt(8)\n"
    "v_mfma_f32_16x16x32_bf16 v[32:35], a[0:3], a[64:67], v[32:35]\n"
    "ds_read_b64_tr_b16 a[24:25], v11\n"
    "v_mfma_f32_16x16x32_bf16 v[32:35], a[4:7], a[68:71], v[32:35]\n"
    "ds_read_b64_tr_b16 a[26:27], v11 offset:4096\n"
    "buffer_load_dwordx4 a[48:51], v2, s[40:43], s72 offen sc1\n"
    "v_mfma_f32_16x16x32_bf16 v[64:67], a[0:3], a[72:75], v[64:67]\n"
    "ds_read_b64_tr_b16 a[28:29], v11 offset:8192\n"
    "v_mfma_f32_16x16x32_bf16 v[64:67], a[4:7], a[76:79], v[64:67]\n"
    "s_waitcnt lgkmcnt(7)\n"
    "ds_read_b64_tr_b16 a[30:31], v11 offset:12288\n"
    "v_mfma_f32_16x16x32_bf16 v[36:39], a[8:11], a[64:67], v[36:39]\n"
    "ds_read_b64_tr_b16 a[0:1], v12\n"
    "v_mfma_f32_16x16x32_bf16 v[36:39], a[12:15], a[68:71], v[36:39]\n"
    "ds_read_b64_tr_b16 a[2:3], v12 offset:4096\n"
    "v_mfma_f32_16x16x32_bf16 v[68:71], a[8:11], a[72:75], v[68:71]\n"
    "ds_read_b64_tr_b16 a[4:5], v12 offset:8192\n"
    "v_mfma_f32_16x16x32_bf16 v[68:71], a[12:15], a[76:79], v[68:71]\n"
    "s_waitcnt lgkmcnt(7)\n"
    "ds_read_b64_tr_b16 a[6:7], v12 offset:12288\n"
    "v_mfma_f32_16x16x32_bf16 v[40:43], a[16:19], a[64:67], v[40:43]\n"
    "ds_read_b64_tr_b16 a[8:9], v13\n"
    "buffer_load_dwordx4 a[52:55], v2, s[40:43], s72 offen offset:1024 sc1\n"
    "v_mfma_f32_16x16x32_bf16 v[40:43], a[20:23], a[68:71], v[40:43]\n"
    "ds_read_b64_tr_b16 a[10:11], v13 offset:4096\n"
    "v_mfma_f32_16x16x32_bf16 v[72:75], a[16:19], a[72:75], v[72:75]\n"
    "ds_read_b64_tr_b16 a[12:13], v13 offset:8192\n"
    "v_mfma_f32_16x16x32_bf16 v[72:75], a[20:23], a[76:79], v[72:75]\n"
    "s_waitcnt lgkmcnt(7)\n"
    "ds_read_b64_tr_b16 a[14:15], v13 offset:12288\n"
    "v_mfma_f32_16x16x32_bf16 v[44:47], a[24:27], a[64:67], v[44:47]\n"
    "ds_read_b64_tr_b16 a[16:17], v14\n"
    "v_mfma_f32_16x16x32_bf16 v[44:47], a[28:31], a[68:71], v[44:47]\n"
    "ds_read_b64_tr_b16 a[18:19], v14 offset:4096\n"
    "v_mfma_f32_16x16x32_bf16 v[76:79], a[24:27], a[72:75], v[76:79]\n"
    "ds_read_b64_tr_b16 a[20:21], v14 offset:8192\n"
    "v_mfma_f32_16x16x32_bf16 v[76:79], a[28:31], a[76:79], v[76:79]\n"
    "s_waitcnt lgkmcnt(7)\n"
    "ds_read_b64_tr_b16 a[22:23], v14 offset:12288\n"
    "v_mfma_f32_16x16x32_bf16 v[48:51], a[0:3], a[64:67], v[48:51]\n"
    "ds_read_b64_tr_b16 a[24:25], v15\n"
    "buffer_load_dwordx4 a[56:59], v2, s[40:43], s72 offen offset:2048 sc1\n"
    "v_mfma_f32_16x16x32_bf16 v[48:51], a[4:7], a[68:71], v[48:51]\n"
    "ds_read_b64_tr_b16 a[26:27], v15 offset:4096\n"
    "v_mfma_f32_16x16x32_bf16 v[80:83], a[0:3], a[72:75], v[80:83]\n"
    "s_add_u32 s77, s77, 1\n"
    "ds_read_b64_tr_b16 a[28:29], v15 offset:8192\n"
    "v_mfma_f32_16x16x32_bf16 v[80:83], a[4:7], a[76:79], v[80:83]\n"
    "s_waitcnt lgkmcnt(7)\n"
    "ds_read_b64_tr_b16 a[30:31], v15 offset:12288\n"
    "v_mfma_f32_16x16x32_bf16 v[52:55], a[8:11], a[64:67], v[52:55]\n"
    "ds_read_b64_tr_b16 a[0:1], v8 offset:16384\n"
    "v_mfma_f32_16x16x32_bf16 v[52:55], a[12:15], a[68:71], v[52:55]\n"
    "s_cmp_lt_u32 s77, 5\n"
    "s_cselect_b32 s77, s77, 0\n"
    "s_cselect_b32 s78, 0x8000, s79\n"
    "ds_read_b64_tr_b16 a[2:3], v8 offset:20480\n"
    "v_mfma_f32_16x16x32_bf16 v[84:87], a[8:11], a[72:75], v[84:87]\n"
    "ds_read_b64_tr_b16 a[4:5], v8 offset:24576\n"
    "buffer_load_dwordx4 a[60:63], v2, s[40:43], s72 offen offset:3072 sc1\n"
    "v_mfma_f32_16x16x32_bf16 v[84:87], a[12:15], a[76:79], v[84:87]\n"
    "s_waitcnt lgkmcnt(7)\n"
    "s_add_u32 s72, s72, 0x40000\n"
    "ds_read_b64_tr_b16 a[6:7], v8 offset:28672\n"
    "v_mfma_f32_16x16x32_bf16 v[56:59], a[16:19], a[64:67], v[56:59]\n"
    "buffer_load_dwordx2 v[18:19], off, s[64:67], s90 sc1\n"
    "ds_read_b64_tr_b16 a[8:9], v9 offset:16384\n"
    "v_mfma_f32_16x16x32_bf16 v[56:59], a[20:23], a[68:71], v[56:59]\n"
    "ds_read_b64_tr_b16 a[10:11], v9 offset:20480\n"
    "v_add_u32 v8, s78, v8\n"
    "v_mfma_f32_16x16x32_bf16 v[88:91], a[16:19], a[72:75], v[88:91]\n"
    "ds_read_b64_tr_b16 a[12:13], v9 offset:24576\n"
    "v_mfma_f32_16x16x32_bf16 v[88:91], a[20:23], a[76:79], v[88:91]\n"
    "s_waitcnt lgkmcnt(7)\n"
    "ds_read_b64_tr_b16 a[14:15], v9 offset:28672\n"
    "v_mfma_f32_16x16x32_bf16 v[60:63], a[24:27], a[64:67], v[60:63]\n"
    "ds_read_b64_tr_b16 a[16:17], v10 offset:16384\n"
    "v_add_u32 v9, s78, v9\n"
    "v_mfma_f32_16x16x32_bf16 v[60:63], a[28:31], a[68:71], v[60:63]\n"
    "s_add_u32 m0, s91, 0\n"
    "ds_read_b64_tr_b16 a[18:19], v10 offset:20480\n"
    "v_mfma_f32_16x16x32_bf16 v[92:95], a[24:27], a[72:75], v[92:95]\n"
    "ds_read_b64_tr_b16 a[20:21], v10 offset:24576\n"
    "buffer_load_dwordx4 v192, s[48:51], s85 offen lds\n"
    "v_mfma_f32_16x16x32_bf16 v[92:95], a[28:31], a[76:79], v[92:95]\n"
    "s_waitcnt vmcnt(15) lgkmcnt(7)\n"
    "ds_read_b64_tr_b16 a[22:23], v10 offset:28672\n"
    "v_mfma_f32_16x16x32_bf16 v[32:35], a[0:3], a[80:83], v[32:35]\n"
    "ds_read_b64_tr_b16 a[24:25], v11 offset:16384\n"
    "v_add_u32 v10, s78, v10\n"
    "v_mfma_f32_16x16x32_bf16 v[32:35], a[4:7], a[84:87], v[32:35]\n"
    "ds_read_b64_tr_b16 a[26:27], v11 offset:20480\n"
    "v_mfma_f32_16x16x32_bf16 v[64:67], a[0:3], a[88:91], v[64:67]\n"
    "ds_read_b64_tr_b16 a[28:29], v11 offset:24576\n"
    "buffer_load_dwordx4 v193, s[48:51], s85 offen offset:1024 lds\n"
    "v_mfma_f32_16x16x32_bf16 v[64:67], a[4:7], a[92:95], v[64:67]\n"
    "s_waitcnt lgkmcnt(7)\n"
    "ds_read_b64_tr_b16 a[30:31], v11 offset:28672\n"
    "v_mfma_f32_16x16x32_bf16 v[36:39], a[8:11], a[80:83], v[36:39]\n"
    "ds_read_b64_tr_b16 a[0:1], v12 offset:16384\n"
    "v_add_u32 v11, s78, v11\n"
    "v_mfma_f32_16x16x32_bf16 v[36:39], a[12:15], a[84:87], v[36:39]\n"
    "ds_read_b64_tr_b16 a[2:3], v12 offset:20480\n"
    "v_mfma_f32_16x16x32_bf16 v[68:71], a[8:11], a[88:91], v[68:71]\n"
    "ds_read_b64_tr_b16 a[4:5], v12 offset:24576\n"
    "buffer_load_dwordx4 v194, s[48:51], s85 offen offset:2048 lds\n"
    "v_mfma_f32_16x16x32_bf16 v[68:71], a[12:15], a[92:95], v[68:71]\n"
    "s_waitcnt lgkmcnt(7)\n"
    "ds_read_b64_tr_b16 a[6:7], v12 offset:28672\n"
    "v_mfma_f32_16x16x32_bf16 v[40:43], a[16:19], a[80:83], v[40:43]\n"
    "ds_read_b64_tr_b16 a[8:9], v13 offset:16384\n"
    "v_add_u32 v12, s78, v12\n"
    "v_mfma_f32_16x16x32_bf16 v[40:43], a[20:23], a[84:87], v[40:43]\n"
    "ds_read_b64_tr_b16 a[10:11], v13 offset:20480\n"
    "buffer_load_dwordx4 v195, s[48:51], s85 offen offset:3072 lds\n"
    "v_mfma_f32_16x16x32_bf16 v[72:75], a[16:19], a[88:91], v[72:75]\n"
    "ds_read_b64_tr_b16 a[12:13], v13 offset:24576\n"
    "v_mfma_f32_16x16x32_bf16 v[72:75], a[20:23], a[92:95], v[72:75]\n"
    "s_waitcnt lgkmcnt(7)\n"
    "ds_read_b64_tr_b16 a[14:15], v13 offset:28672\n"
    "v_mfma_f32_16x16x32_bf16 v[44:47], a[24:27], a[80:83], v[44:47]\n"
    "ds_read_b64_tr_b16 a[16:17], v14 offset:16384\n"
    "v_add_u32 v13, s78, v13\n"
    "v_mfma_f32_16x16x32_bf16 v[44:47], a[28:31], a[84:87], v[44:47]\n"
    "s_add_u32 m0, s91, 0x1000\n"
    "ds_read_b64_tr_b16 a[18:19], v14 offset:20480\n"
    "v_mfma_f32_16x16x32_bf16 v[76:79], a[24:27], a[88:91], v[76:79]\n"
    "ds_read_b64_tr_b16 a[20:21], v14 offset:24576\n"
    "v_mfma_f32_16x16x32_bf16 v[76:79], a[28:31], a[92:95], v[76:79]\n"
    "s_waitcnt lgkmcnt(7)\n"
    "ds_read_b64_tr_b16 a[22:23], v14 offset:28672\n"
    "v_mfma_f32_16x16x32_bf16 v[48:51], a[0:3], a[80:83], v[48:51]\n"
    "ds_read_b64_tr_b16 a[24:25], v15 offset:16384\n"
    "v_add_u32 v14, s78, v14\n"
    "v_mfma_f32_16x16x32_bf16 v[48:51], a[4:7], a[84:87], v[48:51]\n"
    "ds_read_b64_tr_b16 a[26:27], v15 offset:20480\n"
    "buffer_load_dwordx4 v192, s[48:51], s86 offen lds\n"
    "v_mfma_f32_16x16x32_bf16 v[80:83], a[0:3], a[88:91], v[80:83]\n"
    "ds_read_b64_tr_b16 a[28:29], v15 offset:24576\n"
    "v_mfma_f32_16x16x32_bf16 v[80:83], a[4:7], a[92:95], v[80:83]\n"
    "s_waitcnt vmcnt(10) lgkmcnt(7)\n"
    "s_barrier\n"
    "ds_read_b64_tr_b16 a[30:31], v15 offset:28672\n"
    "v_add_u32 v15, s78, v15\n"
    "v_mfma_f32_16x16x32_bf16 v[52:55], a[8:11], a[80:83], v[52:55]\n"
    "ds_read_b64_tr_b16 a[0:1], v8\n"
    "buffer_load_dwordx4 v193, s[48:51], s86 offen offset:1024 lds\n"
    "v_mfma_f32_16x16x32_bf16 v[52:55], a[12:15], a[84:87], v[52:55]\n"
    "ds_read_b64_tr_b16 a[2:3], v8 offset:4096\n"
    "v_mfma_f32_16x16x32_bf16 v[84:87], a[8:11], a[88:91], v[84:87]\n"
    "ds_read_b64_tr_b16 a[4:5], v8 offset:8192\n"
    "v_mfma_f32_16x16x32_bf16 v[84:87], a[12:15], a[92:95], v[84:87]\n"
    "s_waitcnt lgkmcnt(7)\n"
    "ds_read_b64_tr_b16 a[6:7], v8 offset:12288\n"
    "v_mfma_f32_16x16x32_bf16 v[56:59], a[16:19], a[80:83], v[56:59]\n"
    "ds_read_b64_tr_b16 a[8:9], v9\n"
    "buffer_load_dwordx4 v194, s[48:51], s86 offen offset:2048 lds\n"
    "v_mfma_f32_16x16x32_bf16 v[56:59], a[20:23], a[84:87], v[56:59]\n"
    "ds_read_b64_tr_b16 a[10:11], v9 offset:4096\n"
    "v_mfma_f32_16x16x32_bf16 v[88:91], a[16:19], a[88:91], v[88:91]\n"
    "ds_read_b64_tr_b16 a[12:13], v9 offset:8192\n"
    "buffer_load_dwordx4 v195, s[48:51], s86 offen offset:3072 lds\n"
    "v_mfma_f32_16x16x32_bf16 v[88:91], a[20:23], a[92:95], v[88:91]\n"
    "s_waitcnt lgkmcnt(7)\n"
    "ds_read_b64_tr_b16 a[14:15], v9 offset:12288\n"
    "v_mfma_f32_16x16x32_bf16 v[60:63], a[24:27], a[80:83], v[60:63]\n"
    "ds_read_b64_tr_b16 a[16:17], v10\n"
    "v_mfma_f32_16x16x32_bf16 v[60:63], a[28:31], a[84:87], v[60:63]\n"
    "ds_read_b64_tr_b16 a[18:19], v10 offset:4096\n"
    "v_mfma_f32_16x16x32_bf16 v[92:95], a[24:27], a[88:91], v[92:95]\n"
    "ds_read_b64_tr_b16 a[20:21], v10 offset:8192\n"
    "v_mfma_f32_16x16x32_bf16 v[92:95], a[28:31], a[92:95], v[92:95]\n"
    "ds_read_b64_tr_b16 a[22:23], v10 offset:12288\n"
    "s_mov_b32 s91, 1048577\n"
    "s_waitcnt vmcnt(8)\n"
    "v_cmp_ne_u64 vcc, s[94:95], v[18:19]\n"
    "s_sub_u32 s91, s91, 1\n"
    "s_cmp_eq_u64 vcc, 0\n"
    "s_cbranch_scc1 k_oproj_go\n"
    "k_oproj_spin:\n"
    "buffer_load_dwordx2 v[18:19], off, s[64:67], s90 sc1\n"
    "s_waitcnt vmcnt(0)\n"
    "v_cmp_ne_u64 vcc, s[94:95], v[18:19]\n"
    "s_sub_u32 s91, s91, 1\n"
    "s_cmp_eq_u64 vcc, 0\n"
    "s_cbranch_scc1 k_oproj_go\n"
    "s_sleep 2\n"
    "s_cmp_lg_u32 s91, 0\n"
    "s_cbranch_scc1 k_oproj_spin\n"
    "k_oproj_go:\n"
    "s_waitcnt lgkmcnt(8)\n"
    "v_mfma_f32_16x16x32_bf16 v[32:35], a[0:3], a[32:35], v[32:35]\n"
    "ds_read_b64_tr_b16 a[24:25], v11\n"
    "v_mfma_f32_16x16x32_bf16 v[32:35], a[4:7], a[36:39], v[32:35]\n"
    "ds_read_b64_tr_b16 a[26:27], v11 offset:4096\n"
    "buffer_load_dwordx4 v[128:131], v3, s[60:63], s89 offen sc1\n"
    "v_mfma_f32_16x16x32_bf16 v[64:67], a[0:3], a[40:43], v[64:67]\n"
    "ds_read_b64_tr_b16 a[28:29], v11 offset:8192\n"
    "v_mfma_f32_16x16x32_bf16 v[64:67], a[4:7], a[44:47], v[64:67]\n"
    "s_waitcnt lgkmcnt(7)\n"
    "ds_read_b64_tr_b16 a[30:31], v11 offset:12288\n"
    "v_mfma_f32_16x16x32_bf16 v[36:39], a[8:11], a[32:35], v[36:39]\n"
    "ds_read_b64_tr_b16 a[0:1], v12\n"
    "buffer_load_dwordx4 v[132:135], v3, s[60:63], s89 offen offset:1024 sc1\n"
    "v_mfma_f32_16x16x32_bf16 v[36:39], a[12:15], a[36:39], v[36:39]\n"
    "ds_read_b64_tr_b16 a[2:3], v12 offset:4096\n"
    "buffer_load_dwordx4 v[136:139], v3, s[60:63], s89 offen offset:2048 sc1\n"
    "v_mfma_f32_16x16x32_bf16 v[68:71], a[8:11], a[40:43], v[68:71]\n"
    "ds_read_b64_tr_b16 a[4:5], v12 offset:8192\n"
    "v_mfma_f32_16x16x32_bf16 v[68:71], a[12:15], a[44:47], v[68:71]\n"
    "s_waitcnt lgkmcnt(7)\n"
    "ds_read_b64_tr_b16 a[6:7], v12 offset:12288\n"
    "v_mfma_f32_16x16x32_bf16 v[40:43], a[16:19], a[32:35], v[40:43]\n"
    "ds_read_b64_tr_b16 a[8:9], v13\n"
    "buffer_load_dwordx4 v[140:143], v3, s[60:63], s89 offen offset:3072 sc1\n"
    "v_mfma_f32_16x16x32_bf16 v[40:43], a[20:23], a[36:39], v[40:43]\n"
    "ds_read_b64_tr_b16 a[10:11], v13 offset:4096\n"
    "v_mfma_f32_16x16x32_bf16 v[72:75], a[16:19], a[40:43], v[72:75]\n"
    "s_add_u32 s89, s89, 0x1000\n"
    "ds_read_b64_tr_b16 a[12:13], v13 offset:8192\n"
    "v_mfma_f32_16x16x32_bf16 v[72:75], a[20:23], a[44:47], v[72:75]\n"
    "s_waitcnt lgkmcnt(7)\n"
    "ds_read_b64_tr_b16 a[14:15], v13 offset:12288\n"
    "v_mfma_f32_16x16x32_bf16 v[44:47], a[24:27], a[32:35], v[44:47]\n"
    "ds_read_b64_tr_b16 a[16:17], v14\n"
    "buffer_load_dwordx4 v[144:147], v3, s[60:63], s89 offen sc1\n"
    "v_mfma_f32_16x16x32_bf16 v[44:47], a[28:31], a[36:39], v[44:47]\n"
    "ds_read_b64_tr_b16 a[18:19], v14 offset:4096\n"
    "v_mfma_f32_16x16x32_bf16 v[76:79], a[24:27], a[40:43], v[76:79]\n"
    "ds_read_b64_tr_b16 a[20:21], v14 offset:8192\n"
    "buffer_load_dwordx4 v[148:151], v3, s[60:63], s89 offen offset:1024 sc1\n"
    "v_mfma_f32_16x16x32_bf16 v[76:79], a[28:31], a[44:47], v[76:79]\n"
    "s_waitcnt lgkmcnt(7)\n"
    "ds_read_b64_tr_b16 a[22:23], v14 offset:12288\n"
    "v_mfma_f32_16x16x32_bf16 v[48:51], a[0:3], a[32:35], v[48:51]\n"
    "ds_read_b64_tr_b16 a[24:25], v15\n"
    "v_mfma_f32_16x16x32_bf16 v[48:51], a[4:7], a[36:39], v[48:51]\n"
    "ds_read_b64_tr_b16 a[26:27], v15 offset:4096\n"
    "buffer_load_dwordx4 v[152:155], v3, s[60:63], s89 offen offset:2048 sc1\n"
    "v_mfma_f32_16x16x32_bf16 v[80:83], a[0:3], a[40:43], v[80:83]\n"
    "ds_read_b64_tr_b16 a[28:29], v15 offset:8192\n"
    "v_mfma_f32_16x16x32_bf16 v[80:83], a[4:7], a[44:47], v[80:83]\n"
    "s_waitcnt lgkmcnt(7)\n"
    "ds_read_b64_tr_b16 a[30:31], v15 offset:12288\n"
    "v_mfma_f32_16x16x32_bf16 v[52:55], a[8:11], a[32:35], v[52:55]\n"
    "ds_read_b64_tr_b16 a[0:1], v8 offset:16384\n"
    "buffer_load_dwordx4 v[156:159], v3, s[60:63], s89 offen offset:3072 sc1\n"
    "v_mfma_f32_16x16x32_bf16 v[52:55], a[12:15], a[36:39], v[52:55]\n"
    "s_add_u32 s89, s89, 0x1000\n"
    "ds_read_b64_tr_b16 a[2:3], v8 offset:20480\n"
    "v_mfma_f32_16x16x32_bf16 v[84:87], a[8:11], a[40:43], v[84:87]\n"
    "ds_read_b64_tr_b16 a[4:5], v8 offset:24576\n"
    "v_mfma_f32_16x16x32_bf16 v[84:87], a[12:15], a[44:47], v[84:87]\n"
    "s_waitcnt lgkmcnt(7)\n"
    "ds_read_b64_tr_b16 a[6:7], v8 offset:28672\n"
    "v_mfma_f32_16x16x32_bf16 v[56:59], a[16:19], a[32:35], v[56:59]\n"
    "ds_read_b64_tr_b16 a[8:9], v9 offset:16384\n"
    "v_mfma_f32_16x16x32_bf16 v[56:59], a[20:23], a[36:39], v[56:59]\n"
    "ds_read_b64_tr_b16 a[10:11], v9 offset:20480\n"
    "v_mfma_f32_16x16x32_bf16 v[88:91], a[16:19], a[40:43], v[88:91]\n"
    "ds_read_b64_tr_b16 a[12:13], v9 offset:24576\n"
    "v_mfma_f32_16x16x32_bf16 v[88:91], a[20:23], a[44:47], v[88:91]\n"
    "s_waitcnt lgkmcnt(7)\n"
    "ds_read_b64_tr_b16 a[14:15], v9 offset:28672\n"
    "v_mfma_f32_16x16x32_bf16 v[60:63], a[24:27], a[32:35], v[60:63]\n"
    "ds_read_b64_tr_b16 a[16:17], v10 offset:16384\n"
    "v_mfma_f32_16x16x32_bf16 v[60:63], a[28:31], a[36:39], v[60:63]\n"
    "ds_read_b64_tr_b16 a[18:19], v10 offset:20480\n"
    "v_mfma_f32_16x16x32_bf16 v[92:95], a[24:27], a[40:43], v[92:95]\n"
    "s_add_u32 s52, s32, 0x100000\n"
    "s_addc_u32 s53, s33, 0\n"
    "ds_read_b64_tr_b16 a[20:21], v10 offset:24576\n"
    "v_mfma_f32_16x16x32_bf16 v[92:95], a[28:31], a[44:47], v[92:95]\n"
    "s_waitcnt lgkmcnt(7)\n"
    "ds_read_b64_tr_b16 a[22:23], v10 offset:28672\n"
    "v_mfma_f32_16x16x32_bf16 v[32:35], a[0:3], a[48:51], v[32:35]\n"
    "ds_read_b64_tr_b16 a[24:25], v11 offset:16384\n"
    "v_mfma_f32_16x16x32_bf16 v[32:35], a[4:7], a[52:55], v[32:35]\n"
    "s_mov_b32 s54, 0x400000\n"
    "ds_read_b64_tr_b16 a[26:27], v11 offset:20480\n"
    "v_mfma_f32_16x16x32_bf16 v[64:67], a[0:3], a[56:59], v[64:67]\n"
    "ds_read_b64_tr_b16 a[28:29], v11 offset:24576\n"
    "v_mfma_f32_16x16x32_bf16 v[64:67], a[4:7], a[60:63], v[64:67]\n"
    "s_waitcnt lgkmcnt(7)\n"
    "ds_read_b64_tr_b16 a[30:31], v11 offset:28672\n"
    "v_mfma_f32_16x16x32_bf16 v[36:39], a[8:11], a[48:51], v[36:39]\n"
    "ds_read_b64_tr_b16 a[0:1], v12 offset:16384\n"
    "v_mfma_f32_16x16x32_bf16 v[36:39], a[12:15], a[52:55], v[36:39]\n"
    "s_mov_b32 s55, 0x20000\n"
    "ds_read_b64_tr_b16 a[2:3], v12 offset:20480\n"
    "v_mfma_f32_16x16x32_bf16 v[68:71], a[8:11], a[56:59], v[68:71]\n"
    "ds_read_b64_tr_b16 a[4:5], v12 offset:24576\n"
    "v_mfma_f32_16x16x32_bf16 v[68:71], a[12:15], a[60:63], v[68:71]\n"
    "s_waitcnt lgkmcnt(7)\n"
    "ds_read_b64_tr_b16 a[6:7], v12 offset:28672\n"
    "v_mfma_f32_16x16x32_bf16 v[40:43], a[16:19], a[48:51], v[40:43]\n"
    "s_waitcnt vmcnt(8)\n"
    "ds_read_b64_tr_b16 a[8:9], v13 offset:16384\n"
    "v_mfma_f32_16x16x32_bf16 v[40:43], a[20:23], a[52:55], v[40:43]\n"
    "s_add_u32 s56, s32, 0x20000\n"
    "s_addc_u32 s57, s33, 0\n"
    "ds_read_b64_tr_b16 a[10:11], v13 offset:20480\n"
    "v_mfma_f32_16x16x32_bf16 v[72:75], a[16:19], a[56:59], v[72:75]\n"
    "ds_read_b64_tr_b16 a[12:13], v13 offset:24576\n"
    "ds_read_b128 v[96:99], v200\n"
    "v_mfma_f32_16x16x32_bf16 v[72:75], a[20:23], a[60:63], v[72:75]\n"
    "s_waitcnt lgkmcnt(8)\n"
    "ds_read_b64_tr_b16 a[14:15], v13 offset:28672\n"
    "v_mfma_f32_16x16x32_bf16 v[44:47], a[24:27], a[48:51], v[44:47]\n"
    "ds_read_b64_tr_b16 a[16:17], v14 offset:16384\n"
    "ds_read_b128 v[100:103], v201\n"
    "v_mfma_f32_16x16x32_bf16 v[44:47], a[28:31], a[52:55], v[44:47]\n"
    "s_mov_b32 s58, 0x20000\n"
    "ds_read_b64_tr_b16 a[18:19], v14 offset:20480\n"
    "v_mfma_f32_16x16x32_bf16 v[76:79], a[24:27], a[56:59], v[76:79]\n"
    "ds_read_b64_tr_b16 a[20:21], v14 offset:24576\n"
    "ds_read_b128 v[104:107], v202\n"
    "v_mfma_f32_16x16x32_bf16 v[76:79], a[28:31], a[60:63], v[76:79]\n"
    "s_waitcnt lgkmcnt(10)\n"
    "ds_read_b64_tr_b16 a[22:23], v14 offset:28672\n"
    "v_mfma_f32_16x16x32_bf16 v[48:51], a[0:3], a[48:51], v[48:51]\n"
    "s_mov_b32 s59, 0x20000\n"
    "ds_read_b64_tr_b16 a[24:25], v15 offset:16384\n"
    "v_mfma_f32_16x16x32_bf16 v[48:51], a[4:7], a[52:55], v[48:51]\n"
    "ds_read_b64_tr_b16 a[26:27], v15 offset:20480\n"
    "v_mfma_f32_16x16x32_bf16 v[80:83], a[0:3], a[56:59], v[80:83]\n"
    "ds_read_b64_tr_b16 a[28:29], v15 offset:24576\n"
    "v_mfma_f32_16x16x32_bf16 v[80:83], a[4:7], a[60:63], v[80:83]\n"
    "s_waitcnt lgkmcnt(9)\n"
    "ds_read_b64_tr_b16 a[30:31], v15 offset:28672\n"
    "v_mfma_f32_16x16x32_bf16 v[52:55], a[8:11], a[48:51], v[52:55]\n"
    "s_lshl_b32 s85, s82, 19\n"
    "ds_read_b128 v[108:111], v203\n"
    "v_mfma_f32_16x16x32_bf16 v[52:55], a[12:15], a[52:55], v[52:55]\n"
    "ds_read_b128 v[112:115], v200 offset:4096\n"
    "v_mfma_f32_16x16x32_bf16 v[84:87], a[8:11], a[56:59], v[84:87]\n"
    "ds_read_b128 v[116:119], v201 offset:4096\n"
    "v_mfma_f32_16x16x32_bf16 v[84:87], a[12:15], a[60:63], v[84:87]\n"
    "s_waitcnt lgkmcnt(7)\n"
    "ds_read_b128 v[120:123], v202 offset:4096\n"
    "v_mfma_f32_16x16x32_bf16 v[56:59], a[16:19], a[48:51], v[56:59]\n"
    "s_add_u32 s86, s85, 0x40000\n"
    "v_mfma_f32_16x16x32_bf16 v[56:59], a[20:23], a[52:55], v[56:59]\n"
    "ds_read_b128 v[124:127], v203 offset:4096\n"
    "v_mfma_f32_16x16x32_bf16 v[88:91], a[16:19], a[56:59], v[88:91]\n"
    "v_mfma_f32_16x16x32_bf16 v[88:91], a[20:23], a[60:63], v[88:91]\n"
    "s_waitcnt lgkmcnt(5)\n"
    "v_mfma_f32_16x16x32_bf16 v[60:63], a[24:27], a[48:51], v[60:63]\n"
    "v_mfma_f32_16x16x32_bf16 v[60:63], a[28:31], a[52:55], v[60:63]\n"
    "v_mfma_f32_16x16x32_bf16 v[92:95], a[24:27], a[56:59], v[92:95]\n"
    "v_mfma_f32_16x16x32_bf16 v[92:95], a[28:31], a[60:63], v[92:95]\n"
    "s_waitcnt lgkmcnt(0)\n"
    "v_mfma_f32_16x16x32_bf16 v[32:35], v[232:235], v[96:99], v[32:35]\n"
    "v_mfma_f32_16x16x32_bf16 v[36:39], v[236:239], v[96:99], v[36:39]\n"
    "v_mfma_f32_16x16x32_bf16 v[40:43], v[232:235], v[100:103], v[40:43]\n"
    "v_mfma_f32_16x16x32_bf16 v[44:47], v[236:239], v[100:103], v[44:47]\n"
    "v_mfma_f32_16x16x32_bf16 v[48:51], v[232:235], v[104:107], v[48:51]\n"
    "v_mfma_f32_16x16x32_bf16 v[52:55], v[236:239], v[104:107], v[52:55]\n"
    "v_mfma_f32_16x16x32_bf16 v[56:59], v[232:235], v[108:111], v[56:59]\n"
    "v_mfma_f32_16x16x32_bf16 v[60:63], v[236:239], v[108:111], v[60:63]\n"
    "v_mfma_f32_16x16x32_bf16 v[64:67], v[232:235], v[112:115], v[64:67]\n"
    "v_mfma_f32_16x16x32_bf16 v[68:71], v[236:239], v[112:115], v[68:71]\n"
    "v_mfma_f32_16x16x32_bf16 v[72:75], v[232:235], v[116:119], v[72:75]\n"
    "v_mfma_f32_16x16x32_bf16 v[76:79], v[236:239], v[116:119], v[76:79]\n"
    "v_mfma_f32_16x16x32_bf16 v[80:83], v[232:235], v[120:123], v[80:83]\n"
    "v_mfma_f32_16x16x32_bf16 v[84:87], v[236:239], v[120:123], v[84:87]\n"
    "v_mfma_f32_16x16x32_bf16 v[88:91], v[232:235], v[124:127], v[88:91]\n"
    "v_mfma_f32_16x16x32_bf16 v[92:95], v[236:239], v[124:127], v[92:95]\n"
    "s_waitcnt vmcnt(7)\n"
    "v_mfma_f32_16x16x32_bf16 v[32:35], v[226:229], v[128:131], v[32:35]\n"
    "v_mfma_f32_16x16x32_bf16 v[36:39], v[224:227], v[128:131], v[36:39]\n"
    "s_waitcnt vmcnt(6)\n"
    "v_mfma_f32_16x16x32_bf16 v[40:43], v[226:229], v[132:135], v[40:43]\n"
    "v_mfma_f32_16x16x32_bf16 v[44:47], v[224:227], v[132:135], v[44:47]\n"
    "s_waitcnt vmcnt(5)\n"
    "v_mfma_f32_16x16x32_bf16 v[48:51], v[226:229], v[136:139], v[48:51]\n"
    "v_mfma_f32_16x16x32_bf16 v[52:55], v[224:227], v[136:139], v[52:55]\n"
    "s_waitcnt vmcnt(4)\n"
    "v_mfma_f32_16x16x32_bf16 v[56:59], v[226:229], v[140:143], v[56:59]\n"
    "v_cvt_pk_bf16_f32 v96, v32, v33\n"
    "v_cvt_pk_bf16_f32 v97, v34, v35\n"
    "v_mfma_f32_16x16x32_bf16 v[60:63], v[224:227], v[140:143], v[60:63]\n"
    "s_waitcnt vmcnt(3)\n"
    "v_cvt_pk_bf16_f32 v98, v36, v37\n"
    "v_cvt_pk_bf16_f32 v99, v38, v39\n"
    "v_mfma_f32_16x16x32_bf16 v[64:67], v[226:229], v[144:147], v[64:67]\n"
    "buffer_store_dwordx4 v[96:99], v2, s[52:55], s85 offen sc1\n"
    "v_mfma_f32_16x16x32_bf16 v[68:71], v[224:227], v[144:147], v[68:71]\n"
    "s_waitcnt vmcnt(3)\n"
    "v_cvt_pk_bf16_f32 v100, v40, v41\n"
    "v_cvt_pk_bf16_f32 v101, v42, v43\n"
    "v_mfma_f32_16x16x32_bf16 v[72:75], v[226:229], v[148:151], v[72:75]\n"
    "v_cvt_pk_bf16_f32 v102, v44, v45\n"
    "v_cvt_pk_bf16_f32 v103, v46, v47\n"
    "v_mfma_f32_16x16x32_bf16 v[76:79], v[224:227], v[148:151], v[76:79]\n"
    "s_waitcnt vmcnt(2)\n"
    "v_mfma_f32_16x16x32_bf16 v[80:83], v[226:229], v[152:155], v[80:83]\n"
    "buffer_store_dwordx4 v[100:103], v2, s[52:55], s85 offen offset:1024 sc1\n"
    "v_mfma_f32_16x16x32_bf16 v[84:87], v[224:227], v[152:155], v[84:87]\n"
    "s_waitcnt vmcnt(2)\n"
    "v_cvt_pk_bf16_f32 v104, v48, v49\n"
    "v_cvt_pk_bf16_f32 v105, v50, v51\n"
    "v_mfma_f32_16x16x32_bf16 v[88:91], v[226:229], v[156:159], v[88:91]\n"
    "v_cvt_pk_bf16_f32 v106, v52, v53\n"
    "v_cvt_pk_bf16_f32 v107, v54, v55\n"
    "v_mfma_f32_16x16x32_bf16 v[92:95], v[224:227], v[156:159], v[92:95]\n"
    "buffer_store_dwordx4 v[104:107], v2, s[52:55], s86 offen sc1\n"
    "s_nop 2\n"
    "v_cvt_pk_bf16_f32 v108, v56, v57\n"
    "v_cvt_pk_bf16_f32 v109, v58, v59\n"
    "v_cvt_pk_bf16_f32 v110, v60, v61\n"
    "v_cvt_pk_bf16_f32 v111, v62, v63\n"
    "buffer_store_dwordx4 v[108:111], v2, s[52:55], s86 offen offset:1024 sc1\n"
    "v_cvt_pk_bf16_f32 v112, v64, v65\n"
    "v_cvt_pk_bf16_f32 v113, v66, v67\n"
    "v_cvt_pk_bf16_f32 v114, v68, v69\n"
    "v_cvt_pk_bf16_f32 v115, v70, v71\n"
    "buffer_store_dwordx4 v[112:115], v2, s[52:55], s85 offen offset:2048 sc1\n"
    "v_cvt_pk_bf16_f32 v116, v72, v73\n"
    "v_cvt_pk_bf16_f32 v117, v74, v75\n"
    "v_cvt_pk_bf16_f32 v118, v76, v77\n"
    "v_cvt_pk_bf16_f32 v119, v78, v79\n"
    "buffer_store_dwordx4 v[116:119], v2, s[52:55], s85 offen offset:3072 sc1\n"
    "v_cvt_pk_bf16_f32 v120, v80, v81\n"
    "v_cvt_pk_bf16_f32 v121, v82, v83\n"
    "v_cvt_pk_bf16_f32 v122, v84, v85\n"
    "v_cvt_pk_bf16_f32 v123, v86, v87\n"
    "buffer_store_dwordx4 v[120:123], v2, s[52:55], s86 offen offset:2048 sc1\n"
    "v_cvt_pk_bf16_f32 v124, v88, v89\n"
    "v_cvt_pk_bf16_f32 v125, v90, v91\n"
    "v_cvt_pk_bf16_f32 v126, v92, v93\n"
    "v_cvt_pk_bf16_f32 v127, v94, v95\n"
    "buffer_store_dwordx4 v[124:127], v2, s[52:55], s86 offen offset:3072 sc1\n"
    "s_lshl_b32 s86, s84, 7\n"
    "s_lshl_b32 s85, s82, 14\n"
    "s_add_u32 s86, s86, s85\n"
    "v_lshl_add_u32 v16, v1, 3, s86\n"
    "v_cmp_gt_u32 vcc, 32, v1\n"
    "v_mov_b32 v17, 0x7ffffff0\n"
    "s_nop 0\n"
    "v_cndmask_b32 v208, v17, v16, vcc\n"
    "k_oproj_end:\n"
    "k_oproj_code_end:\n"
    "k_ffn1:\n"
    "s_mov_b32 s66, s100\n"
    "s_mov_b64 s[34:35], s[18:19]\n"
    "s_and_b32 s69, s30, 7\n"
    "s_lshr_b32 s70, s30, 3\n"
    "s_and_b32 s67, s69, 1\n"
    "s_lshl_b32 s67, s67, 2\n"
    "s_lshr_b32 s71, s70, 3\n"
    "s_add_u32 s67, s67, s71\n"
    "s_lshr_b32 s68, s69, 1\n"
    "s_lshl_b32 s68, s68, 3\n"
    "s_and_b32 s71, s70, 7\n"
    "s_add_u32 s68, s68, s71\n"
    "s_and_b32 s74, s68, 1\n"
    "s_mov_b32 s44, s34\n"
    "s_mov_b32 s45, s35\n"
    "s_mov_b32 s46, 0x700000\n"
    "s_mov_b32 s47, 0x20000\n"
    "s_mul_i32 s69, s66, 0x38000\n"
    "s_mul_i32 s70, s68, 0xe0\n"
    "s_add_u32 s69, s69, s70\n"
    "s_lshl_b32 s70, s74, 1\n"
    "s_mov_b32 s77, 0x124a\n"
    "s_mov_b32 s62, 0x1c00\n"
    "s_mul_i32 s71, s66, 0x1c00\n"
    "s_add_u32 s75, s71, 0x1000\n"
    "s_mov_b32 s61, 0\n"
    "s_mov_b32 s63, 0\n"
    "s_mov_b32 s60, 0\n"
    "s_barrier\n"
    "s_add_u32 m0, s63, s71\n"
    "v_add_u32 v16, 0, v1\n"
    "v_mul_u32_u24 v17, s77, v16\n"
    "v_lshrrev_b32 v17, 16, v17\n"
    "v_mad_i32_i24 v16, v17, -14, v16\n"
    "v_add_u32 v16, s70, v16\n"
    "v_subrev_u32 v18, 14, v16\n"
    "v_min_u32 v16, v16, v18\n"
    "v_mul_u32_u24 v17, s62, v17\n"
    "v_lshl_add_u32 v16, v16, 4, v17\n"
    "v_add_u32 v8, s69, v16\n"
    "buffer_load_dwordx4 v8, s[44:47], s61 offen lds\n"
    "s_sub_u32 s69, s69, 0x400\n"
    "v_add_u32 v16, 64, v1\n"
    "v_mul_u32_u24 v17, s77, v16\n"
    "v_lshrrev_b32 v17, 16, v17\n"
    "v_mad_i32_i24 v16, v17, -14, v16\n"
    "v_add_u32 v16, s70, v16\n"
    "v_subrev_u32 v18, 14, v16\n"
    "v_min_u32 v16, v16, v18\n"
    "v_mul_u32_u24 v17, s62, v17\n"
    "v_lshl_add_u32 v16, v16, 4, v17\n"
    "v_add_u32 v9, s69, v16\n"
    "buffer_load_dwordx4 v9, s[44:47], s61 offen offset:1024 lds\n"
    "s_sub_u32 s69, s69, 0x400\n"
    "v_add_u32 v16, 0x80, v1\n"
    "v_mul_u32_u24 v17, s77, v16\n"
    "v_lshrrev_b32 v17, 16, v17\n"
    "v_mad_i32_i24 v16, v17, -14, v16\n"
    "v_add_u32 v16, s70, v16\n"
    "v_subrev_u32 v18, 14, v16\n"
    "v_min_u32 v16, v16, v18\n"
    "v_mul_u32_u24 v17, s62, v17\n"
    "v_lshl_add_u32 v16, v16, 4, v17\n"
    "v_add_u32 v10, s69, v16\n"
    "buffer_load_dwordx4 v10, s[44:47], s61 offen offset:2048 lds\n"
    "s_sub_u32 s69, s69, 0x400\n"
    "v_add_u32 v16, 0xc0, v1\n"
    "v_mul_u32_u24 v17, s77, v16\n"
    "v_lshrrev_b32 v17, 16, v17\n"
    "v_mad_i32_i24 v16, v17, -14, v16\n"
    "v_add_u32 v16, s70, v16\n"
    "v_subrev_u32 v18, 14, v16\n"
    "v_min_u32 v16, v16, v18\n"
    "v_mul_u32_u24 v17, s62, v17\n"
    "v_lshl_add_u32 v16, v16, 4, v17\n"
    "v_add_u32 v11, s69, v16\n"
    "buffer_load_dwordx4 v11, s[44:47], s61 offen offset:3072 lds\n"
    "s_add_u32 s69, s69, 0xc00\n"
    "s_add_u32 m0, s63, s75\n"
    "v_add_u32 v16, 0x100, v1\n"
    "v_mul_u32_u24 v17, s77, v16\n"
    "v_lshrrev_b32 v17, 16, v17\n"
    "v_mad_i32_i24 v16, v17, -14, v16\n"
    "v_add_u32 v16, s70, v16\n"
    "v_subrev_u32 v18, 14, v16\n"
    "v_min_u32 v16, v16, v18\n"
    "v_mul_u32_u24 v17, s62, v17\n"
    "v_lshl_add_u32 v16, v16, 4, v17\n"
    "v_add_u32 v12, s69, v16\n"
    "buffer_load_dwordx4 v12, s[44:47], s61 offen lds\n"
    "s_sub_u32 s69, s69, 0x400\n"
    "v_add_u32 v16, 0x140, v1\n"
    "v_mul_u32_u24 v17, s77, v16\n"
    "v_lshrrev_b32 v17, 16, v17\n"
    "v_mad_i32_i24 v16, v17, -14, v16\n"
    "v_add_u32 v16, s70, v16\n"
    "v_subrev_u32 v18, 14, v16\n"
    "v_min_u32 v16, v16, v18\n"
    "v_mul_u32_u24 v17, s62, v17\n"
    "v_lshl_add_u32 v16, v16, 4, v17\n"
    "v_add_u32 v13, s69, v16\n"
    "buffer_load_dwordx4 v13, s[44:47], s61 offen offset:1024 lds\n"
    "s_sub_u32 s69, s69, 0x400\n"
    "v_add_u32 v16, 0x180, v1\n"
    "v_mul_u32_u24 v17, s77, v16\n"
    "v_lshrrev_b32 v17, 16, v17\n"
    "v_mad_i32_i24 v16, v17, -14, v16\n"
    "v_add_u32 v16, s70, v16\n"
    "v_subrev_u32 v18, 14, v16\n"
    "v_min_u32 v16, v16, v18\n"
    "v_mul_u32_u24 v17, s62, v17\n"
    "v_lshl_add_u32 v16, v16, 4, v17\n"
    "v_add_u32 v14, s69, v16\n"
    "buffer_load_dwordx4 v14, s[44:47], s61 offen offset:2048 lds\n"
    "s_sub_u32 s69, s69, 0x400\n"
    "s_add_u32 s61, s61, 0xe0000\n"
    "s_add_u32 s63, s63, 0x7000\n"
    "s_cmp_lt_u32 s63, 0x23000\n"
    "s_cselect_b32 s63, s63, 0\n"
    "s_add_u32 s40, s32, 0x100000\n"
    "s_addc_u32 s41, s33, 0\n"
    "s_mov_b32 s42, 0x400000\n"
    "s_mov_b32 s43, 0x20000\n"
    "s_lshl_b32 s69, s67, 4\n"
    "s_lshl_b32 s70, s66, 2\n"
    "s_add_u32 s69, s69, s70\n"
    "s_lshl_b32 s70, s69, 11\n"
    "v_lshl_add_u32 v4, v1, 4, s70\n"
    "v_add_u32 v5, 0x1000, v4\n"
    "s_add_u32 m0, s63, s71\n"
    "s_nop 0\n"
    "buffer_load_dwordx4 v8, s[44:47], s61 offen lds\n"
    "buffer_load_dwordx4 v9, s[44:47], s61 offen offset:1024 lds\n"
    "buffer_load_dwordx4 v10, s[44:47], s61 offen offset:2048 lds\n"
    "buffer_load_dwordx4 v11, s[44:47], s61 offen offset:3072 lds\n"
    "s_add_u32 m0, s63, s75\n"
    "s_nop 0\n"
    "buffer_load_dwordx4 v12, s[44:47], s61 offen lds\n"
    "buffer_load_dwordx4 v13, s[44:47], s61 offen offset:1024 lds\n"
    "buffer_load_dwordx4 v14, s[44:47], s61 offen offset:2048 lds\n"
    "s_add_u32 s61, s61, 0xe0000\n"
    "s_add_u32 s63, s63, 0x7000\n"
    "s_cmp_lt_u32 s63, 0x23000\n"
    "s_cselect_b32 s63, s63, 0\n"
    "s_add_u32 s88, s32, 0x18000\n"
    "s_addc_u32 s89, s33, 0\n"
    "s_mov_b32 s90, 0x8000\n"
    "s_mov_b32 s91, 0x20000\n"
    "s_lshl_b32 s83, s30, 7\n"
    "v_mov_b32 v24, s94\n"
    "v_mov_b32 v25, s95\n"
    "s_waitcnt vmcnt(14)\n"
    "s_barrier\n"
    "s_cmp_lg_u32 s66, 0\n"
    "s_cbranch_scc1 k_ffn1_np\n"
    "s_mov_b64 exec, 1\n buffer_store_dwordx2 v[24:25], off, s[88:91], s83 offset:8 sc1\n s_mov_b64 exec, -1\n"
    "k_ffn1_np:\n"
    "s_add_u32 m0, s63, s71\n"
    "s_nop 0\n"
    "buffer_load_dwordx4 v8, s[44:47], s61 offen lds\n"
    "buffer_load_dwordx4 v9, s[44:47], s61 offen offset:1024 lds\n"
    "buffer_load_dwordx4 v10, s[44:47], s61 offen offset:2048 lds\n"
    "buffer_load_dwordx4 v11, s[44:47], s61 offen offset:3072 lds\n"
    "s_add_u32 m0, s63, s75\n"
    "s_nop 0\n"
    "buffer_load_dwordx4 v12, s[44:47], s61 offen lds\n"
    "buffer_load_dwordx4 v13, s[44:47], s61 offen offset:1024 lds\n"
    "buffer_load_dwordx4 v14, s[44:47], s61 offen offset:2048 lds\n"
    "s_add_u32 s61, s61, 0xe0000\n"
    "s_add_u32 s63, s63, 0x7000\n"
    "s_cmp_lt_u32 s63, 0x23000\n"
    "s_cselect_b32 s63, s63, 0\n"
    "s_getpc_b64 s[88:89]\n s_add_u32 s88, s88, k_ffn2@rel32@lo+4\n s_addc_u32 s89, s89, k_ffn2@rel32@hi+12\n"
    "s_mov_b32 s90, k_ffn2_code_end-k_ffn2\n"
    "s_mov_b32 s91, 0x20000\n"
    "s_lshr_b32 s70, s30, 3\n"
    "s_lshl_b32 s70, s70, 2\n"
    "s_add_u32 s70, s70, s66\n"
    "s_lshl_b32 s70, s70, 7\n"
    "v_lshlrev_b32 v21, 14, v1\n"
    "s_mov_b64 exec, 3\n buffer_load_dword v0, v21, s[88:91], s70 offen\n s_mov_b64 exec, -1\n"
    "s_add_u32 s88, s32, 0x18000\n"
    "s_addc_u32 s89, s33, 0\n"
    "s_mov_b32 s90, 0x8000\n"
    "s_mov_b32 s91, 0x20000\n"
    "s_lshl_b32 s82, s67, 4\n"
    "s_mul_i32 s70, s68, 7\n"
    "s_add_u32 s83, s70, 6\n"
    "s_lshr_b32 s83, s83, 2\n"
    "s_lshr_b32 s70, s70, 2\n"
    "s_sub_u32 s70, s70, 16\n"
    "v_add_u32 v20, s70, v1\n"
    "v_min_u32 v20, s83, v20\n"
    "v_add_u32 v21, s82, v1\n"
    "v_cmp_gt_u32 vcc, 16, v1\n"
    "v_mov_b32 v22, 24\n"
    "v_mov_b32 v23, 0x98\n"
    "v_cndmask_b32 v20, v20, v21, vcc\n"
    "v_cndmask_b32 v22, v22, v23, vcc\n"
    "v_bfe_u32 v21, v20, 2, 1\n"
    "v_lshl_add_u32 v22, v21, 8, v22\n"
    "v_lshrrev_b32 v21, 6, v20\n"
    "v_lshl_add_u32 v22, v21, 9, v22\n"
    "v_and_b32 v21, 3, v20\n"
    "v_lshl_add_u32 v22, v21, 10, v22\n"
    "v_bfe_u32 v21, v20, 3, 3\n"
    "v_lshl_add_u32 v22, v21, 12, v22\n"
    "v_mov_b32 v28, v22\n"
    "v_cmp_gt_u32 vcc, 16, v1\n"
    "v_readfirstlane_b32 s82, v22\n"
    "v_subrev_u32 v22, 16, v22\n"
    "s_nop 0\n"
    "s_sub_u32 s82, s82, 16\n"
    "v_mov_b32 v23, s82\n"
    "v_cndmask_b32 v22, v23, v22, vcc\n"
    "v_mov_b32 v30, s94\n"
    "v_mov_b32 v31, s95\n"
    "s_cmp_lg_u32 s66, 0\n"
    "s_cbranch_scc1 k_ffn1_nw\n"
    "s_mov_b32 s82, 1048576\n"
    "k_ffn1_h_spin:\n"
    "buffer_load_dwordx2 v[236:237], v22, s[88:91], 0 offen sc1\n"
    "s_waitcnt vmcnt(0)\n"
    "v_cmp_ne_u64 vcc, s[94:95], v[236:237]\n"
    "s_sub_u32 s82, s82, 1\n"
    "s_cmp_eq_u64 vcc, 0\n"
    "s_cbranch_scc1 k_ffn1_h_go\n"
    "s_sleep 1\n"
    "s_cmp_lg_u32 s82, 0\n"
    "s_cbranch_scc1 k_ffn1_h_spin\n"
    "k_ffn1_h_go:\n"
    "s_cmp_lg_u32 s30, 0\n"
    "s_cbranch_scc1 k_ffn1_nw\n"
    "s_add_u32 s96, s32, 0x2000\n"
    "s_addc_u32 s97, s33, 0\n"
    "s_mov_b32 s98, 8\n"
    "s_mov_b32 s99, 0x20000\n"
    "v_mov_b32 v24, s94\n"
    "v_mov_b32 v25, s95\n"
    "s_mov_b64 exec, 1\n buffer_store_dwordx2 v[24:25], off, s[96:99], 0 offset:0 sc1\n s_mov_b64 exec, -1\n"
    "k_ffn1_nw:\n"
    "s_waitcnt vmcnt(15)\n"
    "s_barrier\n"
    "buffer_load_dwordx4 a[32:35], v4, s[40:43], s60 offen sc1\n"
    "buffer_load_dwordx4 a[36:39], v4, s[40:43], s60 offen offset:1024 sc1\n"
    "buffer_load_dwordx4 a[40:43], v4, s[40:43], s60 offen offset:2048 sc1\n"
    "buffer_load_dwordx4 a[44:47], v4, s[40:43], s60 offen offset:3072 sc1\n"
    "buffer_load_dwordx4 a[48:51], v5, s[40:43], s60 offen sc1\n"
    "buffer_load_dwordx4 a[52:55], v5, s[40:43], s60 offen offset:1024 sc1\n"
    "buffer_load_dwordx4 a[56:59], v5, s[40:43], s60 offen offset:2048 sc1\n"
    "buffer_load_dwordx4 a[60:63], v5, s[40:43], s60 offen offset:3072 sc1\n"
    "s_add_u32 s60, s60, 0x40000\n"
    "buffer_load_dwordx4 a[64:67], v4, s[40:43], s60 offen sc1\n"
    "buffer_load_dwordx4 a[68:71], v4, s[40:43], s60 offen offset:1024 sc1\n"
    "buffer_load_dwordx4 a[72:75], v4, s[40:43], s60 offen offset:2048 sc1\n"
    "buffer_load_dwordx4 a[76:79], v4, s[40:43], s60 offen offset:3072 sc1\n"
    "buffer_load_dwordx4 a[80:83], v5, s[40:43], s60 offen sc1\n"
    "buffer_load_dwordx4 a[84:87], v5, s[40:43], s60 offen offset:1024 sc1\n"
    "buffer_load_dwordx4 a[88:91], v5, s[40:43], s60 offen offset:2048 sc1\n"
    "buffer_load_dwordx4 a[92:95], v5, s[40:43], s60 offen offset:3072 sc1\n"
    "s_add_u32 s60, s60, 0x40000\n"
    "buffer_load_dwordx4 a[96:99], v4, s[40:43], s60 offen sc1\n"
    "buffer_load_dwordx4 a[100:103], v4, s[40:43], s60 offen offset:1024 sc1\n"
    "buffer_load_dwordx4 a[104:107], v4, s[40:43], s60 offen offset:2048 sc1\n"
    "buffer_load_dwordx4 a[108:111], v4, s[40:43], s60 offen offset:3072 sc1\n"
    "buffer_load_dwordx4 a[112:115], v5, s[40:43], s60 offen sc1\n"
    "buffer_load_dwordx4 a[116:119], v5, s[40:43], s60 offen offset:1024 sc1\n"
    "buffer_load_dwordx4 a[120:123], v5, s[40:43], s60 offen offset:2048 sc1\n"
    "buffer_load_dwordx4 a[124:127], v5, s[40:43], s60 offen offset:3072 sc1\n"
    "s_add_u32 s60, s60, 0x40000\n"
    "v_lshrrev_b32 v16, 2, v1\n"
    "v_mul_u32_u24 v16, 0xe0, v16\n"
    "v_and_b32 v18, 3, v1\n"
    "v_lshl_add_u32 v2, v18, 3, v16\n"
    "s_mov_b32 s64, 0\n"
    "s_mov_b32 s73, 0xfffe4000\n"
    "s_add_u32 s52, s32, 0x2c00000\n"
    "s_addc_u32 s53, s33, 0\n"
    "s_mov_b32 s54, 0xe00000\n"
    "s_mov_b32 s55, 0x20000\n"
    "s_add_u32 s56, s32, 0x20000\n"
    "s_addc_u32 s57, s33, 0\n"
    "s_mov_b32 s58, 0x20000\n"
    "s_mov_b32 s59, 0x20000\n"
    "s_add_u32 s48, s32, 0x10000\n"
    "s_addc_u32 s49, s33, 0\n"
    "s_mov_b32 s50, 0x3800\n"
    "s_mov_b32 s51, 0x20000\n"
    "v_and_b32 v16, 15, v1\n"
    "v_lshrrev_b32 v17, 4, v1\n"
    "v_lshlrev_b32 v18, 15, v17\n"
    "v_lshl_add_u32 v18, v16, 3, v18\n"
    "s_lshl_b32 s70, s69, 7\n"
    "v_add_u32 v18, s70, v18\n"
    "v_add_u32 v6, 0x4000, v18\n"
    "v_lshlrev_b32 v7, 4, v1\n"
    "s_mul_i32 s70, s68, 7\n"
    "s_xor_b32 s77, s74, 1\n"
    "s_mul_i32 s80, s77, 6\n"
    "s_add_u32 s77, s80, s70\n"
    "s_lshl_b32 s84, s70, 6\n"
    "s_lshl_b32 s85, s66, 10\n"
    "s_add_u32 s85, s85, 0x23000\n"
    "s_lshl_b32 s80, s80, 6\n"
    "s_lshl_b32 s81, s74, 6\n"
    "s_add_u32 s80, s80, s85\n"
    "s_add_u32 s81, s81, s85\n"
    "v_lshl_add_u32 v19, v17, 4, s81\n"
    "v_lshl_add_u32 v15, v17, 4, s80\n"
    "s_lshl_b32 s83, s30, 7\n"
    "s_bitcmp1_b32 s30, 0\n"
    "s_cbranch_scc0 k_ffn1_nst\n"
    "v_lshlrev_b32 v32, 16, v96\n"
    "v_and_b32 v33, 0xffff0000, v96\n"
    "v_lshlrev_b32 v34, 16, v97\n"
    "v_and_b32 v35, 0xffff0000, v97\n"
    "v_lshlrev_b32 v36, 16, v98\n"
    "v_and_b32 v37, 0xffff0000, v98\n"
    "v_lshlrev_b32 v38, 16, v99\n"
    "v_and_b32 v39, 0xffff0000, v99\n"
    "v_lshlrev_b32 v40, 16, v100\n"
    "v_and_b32 v41, 0xffff0000, v100\n"
    "v_lshlrev_b32 v42, 16, v101\n"
    "v_and_b32 v43, 0xffff0000, v101\n"
    "v_lshlrev_b32 v44, 16, v102\n"
    "v_and_b32 v45, 0xffff0000, v102\n"
    "v_lshlrev_b32 v46, 16, v103\n"
    "v_and_b32 v47, 0xffff0000, v103\n"
    "v_lshlrev_b32 v48, 16, v104\n"
    "v_and_b32 v49, 0xffff0000, v104\n"
    "v_lshlrev_b32 v50, 16, v105\n"
    "v_and_b32 v51, 0xffff0000, v105\n"
    "v_lshlrev_b32 v52, 16, v106\n"
    "v_and_b32 v53, 0xffff0000, v106\n"
    "v_lshlrev_b32 v54, 16, v107\n"
    "v_and_b32 v55, 0xffff0000, v107\n"
    "v_lshlrev_b32 v56, 16, v108\n"
    "v_and_b32 v57, 0xffff0000, v108\n"
    "v_lshlrev_b32 v58, 16, v109\n"
    "v_and_b32 v59, 0xffff0000, v109\n"
    "v_lshlrev_b32 v60, 16, v110\n"
    "v_and_b32 v61, 0xffff0000, v110\n"
    "v_lshlrev_b32 v62, 16, v111\n"
    "v_and_b32 v63, 0xffff0000, v111\n"
    "v_lshlrev_b32 v64, 16, v112\n"
    "v_and_b32 v65, 0xffff0000, v112\n"
    "v_lshlrev_b32 v66, 16, v113\n"
    "v_and_b32 v67, 0xffff0000, v113\n"
    "v_lshlrev_b32 v68, 16, v114\n"
    "v_and_b32 v69, 0xffff0000, v114\n"
    "v_lshlrev_b32 v70, 16, v115\n"
    "v_and_b32 v71, 0xffff0000, v115\n"
    "v_lshlrev_b32 v72, 16, v116\n"
    "v_and_b32 v73, 0xffff0000, v116\n"
    "v_lshlrev_b32 v74, 16, v117\n"
    "v_and_b32 v75, 0xffff0000, v117\n"
    "v_lshlrev_b32 v76, 16, v118\n"
    "v_and_b32 v77, 0xffff0000, v118\n"
    "v_lshlrev_b32 v78, 16, v119\n"
    "v_and_b32 v79, 0xffff0000, v119\n"
    "v_lshlrev_b32 v80, 16, v120\n"
    "v_and_b32 v81, 0xffff0000, v120\n"
    "v_lshlrev_b32 v82, 16, v121\n"
    "v_and_b32 v83, 0xffff0000, v121\n"
    "v_lshlrev_b32 v84, 16, v122\n"
    "v_and_b32 v85, 0xffff0000, v122\n"
    "v_lshlrev_b32 v86, 16, v123\n"
    "v_and_b32 v87, 0xffff0000, v123\n"
    "v_lshlrev_b32 v88, 16, v124\n"
    "v_and_b32 v89, 0xffff0000, v124\n"
    "v_lshlrev_b32 v90, 16, v125\n"
    "v_and_b32 v91, 0xffff0000, v125\n"
    "v_lshlrev_b32 v92, 16, v126\n"
    "v_and_b32 v93, 0xffff0000, v126\n"
    "v_lshlrev_b32 v94, 16, v127\n"
    "v_and_b32 v95, 0xffff0000, v127\n"
    "v_pk_add_f32 v[192:193], v[32:33], v[34:35]\n"
    "v_pk_add_f32 v[194:195], v[64:65], v[66:67]\n"
    "v_pk_add_f32 v[192:193], v[192:193], v[36:37]\n"
    "v_pk_add_f32 v[194:195], v[194:195], v[68:69]\n"
    "v_pk_add_f32 v[192:193], v[192:193], v[38:39]\n"
    "v_pk_add_f32 v[194:195], v[194:195], v[70:71]\n"
    "v_pk_add_f32 v[192:193], v[192:193], v[40:41]\n"
    "v_pk_add_f32 v[194:195], v[194:195], v[72:73]\n"
    "v_pk_add_f32 v[192:193], v[192:193], v[42:43]\n"
    "v_pk_add_f32 v[194:195], v[194:195], v[74:75]\n"
    "v_pk_add_f32 v[192:193], v[192:193], v[44:45]\n"
    "v_pk_add_f32 v[194:195], v[194:195], v[76:77]\n"
    "v_pk_add_f32 v[192:193], v[192:193], v[46:47]\n"
    "v_pk_add_f32 v[194:195], v[194:195], v[78:79]\n"
    "v_pk_add_f32 v[192:193], v[192:193], v[48:49]\n"
    "v_pk_add_f32 v[194:195], v[194:195], v[80:81]\n"
    "v_pk_add_f32 v[192:193], v[192:193], v[50:51]\n"
    "v_pk_add_f32 v[194:195], v[194:195], v[82:83]\n"
    "v_pk_add_f32 v[192:193], v[192:193], v[52:53]\n"
    "v_pk_add_f32 v[194:195], v[194:195], v[84:85]\n"
    "v_pk_add_f32 v[192:193], v[192:193], v[54:55]\n"
    "v_pk_add_f32 v[194:195], v[194:195], v[86:87]\n"
    "v_pk_add_f32 v[192:193], v[192:193], v[56:57]\n"
    "v_pk_add_f32 v[194:195], v[194:195], v[88:89]\n"
    "v_pk_add_f32 v[192:193], v[192:193], v[58:59]\n"
    "v_pk_add_f32 v[194:195], v[194:195], v[90:91]\n"
    "v_pk_add_f32 v[192:193], v[192:193], v[60:61]\n"
    "v_pk_add_f32 v[194:195], v[194:195], v[92:93]\n"
    "v_pk_add_f32 v[192:193], v[192:193], v[62:63]\n"
    "v_pk_add_f32 v[194:195], v[194:195], v[94:95]\n"
    "v_add_f32 v192, v192, v193\n"
    "v_add_f32 v194, v194, v195\n"
    "v_mov_b32 v193, v192\n"
    "v_mov_b32 v195, v194\n"
    "s_nop 1\n"
    "v_permlane32_swap_b32 v192, v193\n"
    "v_permlane32_swap_b32 v194, v195\n"
    "s_nop 0\n"
    "v_add_f32 v192, v192, v193\n"
    "v_add_f32 v194, v194, v195\n"
    "v_mov_b32 v193, v192\n"
    "v_mov_b32 v195, v194\n"
    "s_nop 1\n"
    "v_permlane16_swap_b32 v192, v193\n"
    "v_permlane16_swap_b32 v194, v195\n"
    "s_nop 0\n"
    "v_add_f32 v192, v192, v193\n"
    "v_add_f32 v194, v194, v195\n"
    "v_mul_f32 v192, 0x3c000000, v192\n"
    "v_mul_f32 v194, 0x3c000000, v194\n"
    "v_pk_add_f32 v[32:33], v[32:33], v[192:193] op_sel_hi:[1,0] neg_lo:[0,1] neg_hi:[0,1]\n"
    "v_pk_add_f32 v[64:65], v[64:65], v[194:195] op_sel_hi:[1,0] neg_lo:[0,1] neg_hi:[0,1]\n"
    "v_pk_add_f32 v[34:35], v[34:35], v[192:193] op_sel_hi:[1,0] neg_lo:[0,1] neg_hi:[0,1]\n"
    "v_pk_add_f32 v[66:67], v[66:67], v[194:195] op_sel_hi:[1,0] neg_lo:[0,1] neg_hi:[0,1]\n"
    "v_pk_add_f32 v[36:37], v[36:37], v[192:193] op_sel_hi:[1,0] neg_lo:[0,1] neg_hi:[0,1]\n"
    "v_pk_add_f32 v[68:69], v[68:69], v[194:195] op_sel_hi:[1,0] neg_lo:[0,1] neg_hi:[0,1]\n"
    "v_pk_add_f32 v[38:39], v[38:39], v[192:193] op_sel_hi:[1,0] neg_lo:[0,1] neg_hi:[0,1]\n"
    "v_pk_add_f32 v[70:71], v[70:71], v[194:195] op_sel_hi:[1,0] neg_lo:[0,1] neg_hi:[0,1]\n"
    "v_pk_add_f32 v[40:41], v[40:41], v[192:193] op_sel_hi:[1,0] neg_lo:[0,1] neg_hi:[0,1]\n"
    "v_pk_add_f32 v[72:73], v[72:73], v[194:195] op_sel_hi:[1,0] neg_lo:[0,1] neg_hi:[0,1]\n"
    "v_pk_add_f32 v[42:43], v[42:43], v[192:193] op_sel_hi:[1,0] neg_lo:[0,1] neg_hi:[0,1]\n"
    "v_pk_add_f32 v[74:75], v[74:75], v[194:195] op_sel_hi:[1,0] neg_lo:[0,1] neg_hi:[0,1]\n"
    "v_pk_add_f32 v[44:45], v[44:45], v[192:193] op_sel_hi:[1,0] neg_lo:[0,1] neg_hi:[0,1]\n"
    "v_pk_add_f32 v[76:77], v[76:77], v[194:195] op_sel_hi:[1,0] neg_lo:[0,1] neg_hi:[0,1]\n"
    "v_pk_add_f32 v[46:47], v[46:47], v[192:193] op_sel_hi:[1,0] neg_lo:[0,1] neg_hi:[0,1]\n"
    "v_pk_add_f32 v[78:79], v[78:79], v[194:195] op_sel_hi:[1,0] neg_lo:[0,1] neg_hi:[0,1]\n"
    "v_pk_add_f32 v[48:49], v[48:49], v[192:193] op_sel_hi:[1,0] neg_lo:[0,1] neg_hi:[0,1]\n"
    "v_pk_add_f32 v[80:81], v[80:81], v[194:195] op_sel_hi:[1,0] neg_lo:[0,1] neg_hi:[0,1]\n"
    "v_pk_add_f32 v[50:51], v[50:51], v[192:193] op_sel_hi:[1,0] neg_lo:[0,1] neg_hi:[0,1]\n"
    "v_pk_add_f32 v[82:83], v[82:83], v[194:195] op_sel_hi:[1,0] neg_lo:[0,1] neg_hi:[0,1]\n"
    "v_pk_add_f32 v[52:53], v[52:53], v[192:193] op_sel_hi:[1,0] neg_lo:[0,1] neg_hi:[0,1]\n"
    "v_pk_add_f32 v[84:85], v[84:85], v[194:195] op_sel_hi:[1,0] neg_lo:[0,1] neg_hi:[0,1]\n"
    "v_pk_add_f32 v[54:55], v[54:55], v[192:193] op_sel_hi:[1,0] neg_lo:[0,1] neg_hi:[0,1]\n"
    "v_pk_add_f32 v[86:87], v[86:87], v[194:195] op_sel_hi:[1,0] neg_lo:[0,1] neg_hi:[0,1]\n"
    "v_pk_add_f32 v[56:57], v[56:57], v[192:193] op_sel_hi:[1,0] neg_lo:[0,1] neg_hi:[0,1]\n"
    "v_pk_add_f32 v[88:89], v[88:89], v[194:195] op_sel_hi:[1,0] neg_lo:[0,1] neg_hi:[0,1]\n"
    "v_pk_add_f32 v[58:59], v[58:59], v[192:193] op_sel_hi:[1,0] neg_lo:[0,1] neg_hi:[0,1]\n"
    "v_pk_add_f32 v[90:91], v[90:91], v[194:195] op_sel_hi:[1,0] neg_lo:[0,1] neg_hi:[0,1]\n"
    "v_pk_add_f32 v[60:61], v[60:61], v[192:193] op_sel_hi:[1,0] neg_lo:[0,1] neg_hi:[0,1]\n"
    "v_pk_add_f32 v[92:93], v[92:93], v[194:195] op_sel_hi:[1,0] neg_lo:[0,1] neg_hi:[0,1]\n"
    "v_pk_add_f32 v[62:63], v[62:63], v[192:193] op_sel_hi:[1,0] neg_lo:[0,1] neg_hi:[0,1]\n"
    "v_pk_add_f32 v[94:95], v[94:95], v[194:195] op_sel_hi:[1,0] neg_lo:[0,1] neg_hi:[0,1]\n"
    "v_pk_mul_f32 v[196:197], v[32:33], v[32:33]\n"
    "v_pk_mul_f32 v[198:199], v[64:65], v[64:65]\n"
    "v_pk_fma_f32 v[196:197], v[34:35], v[34:35], v[196:197]\n"
    "v_pk_fma_f32 v[198:199], v[66:67], v[66:67], v[198:199]\n"
    "v_pk_fma_f32 v[196:197], v[36:37], v[36:37], v[196:197]\n"
    "v_pk_fma_f32 v[198:199], v[68:69], v[68:69], v[198:199]\n"
    "v_pk_fma_f32 v[196:197], v[38:39], v[38:39], v[196:197]\n"
    "v_pk_fma_f32 v[198:199], v[70:71], v[70:71], v[198:199]\n"
    "v_pk_fma_f32 v[196:197], v[40:41], v[40:41], v[196:197]\n"
    "v_pk_fma_f32 v[198:199], v[72:73], v[72:73], v[198:199]\n"
    "v_pk_fma_f32 v[196:197], v[42:43], v[42:43], v[196:197]\n"
    "v_pk_fma_f32 v[198:199], v[74:75], v[74:75], v[198:199]\n"
    "v_pk_fma_f32 v[196:197], v[44:45], v[44:45], v[196:197]\n"
    "v_pk_fma_f32 v[198:199], v[76:77], v[76:77], v[198:199]\n"
    "v_pk_fma_f32 v[196:197], v[46:47], v[46:47], v[196:197]\n"
    "v_pk_fma_f32 v[198:199], v[78:79], v[78:79], v[198:199]\n"
    "v_pk_fma_f32 v[196:197], v[48:49], v[48:49], v[196:197]\n"
    "v_pk_fma_f32 v[198:199], v[80:81], v[80:81], v[198:199]\n"
    "v_pk_fma_f32 v[196:197], v[50:51], v[50:51], v[196:197]\n"
    "v_pk_fma_f32 v[198:199], v[82:83], v[82:83], v[198:199]\n"
    "v_pk_fma_f32 v[196:197], v[52:53], v[52:53], v[196:197]\n"
    "v_pk_fma_f32 v[198:199], v[84:85], v[84:85], v[198:199]\n"
    "v_pk_fma_f32 v[196:197], v[54:55], v[54:55], v[196:197]\n"
    "v_pk_fma_f32 v[198:199], v[86:87], v[86:87], v[198:199]\n"
    "v_pk_fma_f32 v[196:197], v[56:57], v[56:57], v[196:197]\n"
    "v_pk_fma_f32 v[198:199], v[88:89], v[88:89], v[198:199]\n"
    "v_pk_fma_f32 v[196:197], v[58:59], v[58:59], v[196:197]\n"
    "v_pk_fma_f32 v[198:199], v[90:91], v[90:91], v[198:199]\n"
    "v_pk_fma_f32 v[196:197], v[60:61], v[60:61], v[196:197]\n"
    "v_pk_fma_f32 v[198:199], v[92:93], v[92:93], v[198:199]\n"
    "v_pk_fma_f32 v[196:197], v[62:63], v[62:63], v[196:197]\n"
    "v_pk_fma_f32 v[198:199], v[94:95], v[94:95], v[198:199]\n"
    "v_add_f32 v196, v196, v197\n"
    "v_add_f32 v198, v198, v199\n"
    "v_mov_b32 v197, v196\n"
    "v_mov_b32 v199, v198\n"
    "s_nop 1\n"
    "v_permlane32_swap_b32 v196, v197\n"
    "v_permlane32_swap_b32 v198, v199\n"
    "s_nop 0\n"
    "v_add_f32 v196, v196, v197\n"
    "v_add_f32 v198, v198, v199\n"
    "v_mov_b32 v197, v196\n"
    "v_mov_b32 v199, v198\n"
    "s_nop 1\n"
    "v_permlane16_swap_b32 v196, v197\n"
    "v_permlane16_swap_b32 v198, v199\n"
    "s_nop 0\n"
    "v_add_f32 v196, v196, v197\n"
    "v_add_f32 v198, v198, v199\n"
    "v_cmp_gt_u32 vcc, 16, v1\n"
    "s_nop 1\n"
    "v_cndmask_b32 v200, v194, v192, vcc\n"
    "v_cndmask_b32 v201, v198, v196, vcc\n"
    "buffer_store_dwordx2 v[200:201], v208, s[56:59], 0 offen sc1\n"
    "k_ffn1_nst:\n"
    "ds_read_b64_tr_b16 a[0:1], v2\n"
    "ds_read_b64_tr_b16 a[2:3], v2 offset:3584\n"
    "ds_read_b64_tr_b16 a[4:5], v2 offset:7168\n"
    "ds_read_b64_tr_b16 a[6:7], v2 offset:10752\n"
    "ds_read_b64_tr_b16 a[8:9], v2 offset:32\n"
    "ds_read_b64_tr_b16 a[10:11], v2 offset:3616\n"
    "ds_read_b64_tr_b16 a[12:13], v2 offset:7200\n"
    "ds_read_b64_tr_b16 a[14:15], v2 offset:10784\n"
    "s_waitcnt vmcnt(17) lgkmcnt(4)\n"
    "v_mfma_f32_16x16x32_bf16 v[32:35], a[0:3], a[32:35], 0\n"
    "ds_read_b64_tr_b16 a[16:17], v2 offset:64\n"
    "v_mfma_f32_16x16x32_bf16 v[32:35], a[4:7], a[36:39], v[32:35]\n"
    "buffer_load_dwordx4 a[128:131], v4, s[40:43], s60 offen sc1\n"
    "v_mfma_f32_16x16x32_bf16 v[60:63], a[0:3], a[40:43], 0\n"
    "ds_read_b64_tr_b16 a[18:19], v2 offset:3648\n"
    "v_mfma_f32_16x16x32_bf16 v[60:63], a[4:7], a[44:47], v[60:63]\n"
    "v_mfma_f32_16x16x32_bf16 v[88:91], a[0:3], a[48:51], 0\n"
    "ds_read_b64_tr_b16 a[20:21], v2 offset:7232\n"
    "v_mfma_f32_16x16x32_bf16 v[88:91], a[4:7], a[52:55], v[88:91]\n"
    "buffer_load_dwordx4 a[132:135], v4, s[40:43], s60 offen offset:1024 sc1\n"
    "v_mfma_f32_16x16x32_bf16 v[116:119], a[0:3], a[56:59], 0\n"
    "ds_read_b64_tr_b16 a[22:23], v2 offset:10816\n"
    "v_mfma_f32_16x16x32_bf16 v[116:119], a[4:7], a[60:63], v[116:119]\n"
    "s_waitcnt lgkmcnt(4)\n"
    "v_mfma_f32_16x16x32_bf16 v[36:39], a[8:11], a[32:35], 0\n"
    "ds_read_b64_tr_b16 a[24:25], v2 offset:96\n"
    "v_mfma_f32_16x16x32_bf16 v[36:39], a[12:15], a[36:39], v[36:39]\n"
    "s_add_u32 m0, s63, s71\n"
    "v_mfma_f32_16x16x32_bf16 v[64:67], a[8:11], a[40:43], 0\n"
    "ds_read_b64_tr_b16 a[26:27], v2 offset:3680\n"
    "v_mfma_f32_16x16x32_bf16 v[64:67], a[12:15], a[44:47], v[64:67]\n"
    "buffer_load_dwordx4 v8, s[44:47], s61 offen lds\n"
    "v_mfma_f32_16x16x32_bf16 v[92:95], a[8:11], a[48:51], 0\n"
    "ds_read_b64_tr_b16 a[28:29], v2 offset:7264\n"
    "v_mfma_f32_16x16x32_bf16 v[92:95], a[12:15], a[52:55], v[92:95]\n"
    "v_mfma_f32_16x16x32_bf16 v[120:123], a[8:11], a[56:59], 0\n"
    "ds_read_b64_tr_b16 a[30:31], v2 offset:10848\n"
    "v_mfma_f32_16x16x32_bf16 v[120:123], a[12:15], a[60:63], v[120:123]\n"
    "s_waitcnt lgkmcnt(4)\n"
    "v_mfma_f32_16x16x32_bf16 v[40:43], a[16:19], a[32:35], 0\n"
    "ds_read_b64_tr_b16 a[0:1], v2 offset:128\n"
    "v_mfma_f32_16x16x32_bf16 v[40:43], a[20:23], a[36:39], v[40:43]\n"
    "buffer_load_dwordx4 a[136:139], v4, s[40:43], s60 offen offset:2048 sc1\n"
    "v_mfma_f32_16x16x32_bf16 v[68:71], a[16:19], a[40:43], 0\n"
    "ds_read_b64_tr_b16 a[2:3], v2 offset:3712\n"
    "v_mfma_f32_16x16x32_bf16 v[68:71], a[20:23], a[44:47], v[68:71]\n"
    "buffer_load_dwordx4 a[140:143], v4, s[40:43], s60 offen offset:3072 sc1\n"
    "v_mfma_f32_16x16x32_bf16 v[96:99], a[16:19], a[48:51], 0\n"
    "ds_read_b64_tr_b16 a[4:5], v2 offset:7296\n"
    "v_mfma_f32_16x16x32_bf16 v[96:99], a[20:23], a[52:55], v[96:99]\n"
    "v_mfma_f32_16x16x32_bf16 v[124:127], a[16:19], a[56:59], 0\n"
    "ds_read_b64_tr_b16 a[6:7], v2 offset:10880\n"
    "v_mfma_f32_16x16x32_bf16 v[124:127], a[20:23], a[60:63], v[124:127]\n"
    "s_waitcnt lgkmcnt(4)\n"
    "v_mfma_f32_16x16x32_bf16 v[44:47], a[24:27], a[32:35], 0\n"
    "ds_read_b64_tr_b16 a[8:9], v2 offset:160\n"
    "v_mfma_f32_16x16x32_bf16 v[44:47], a[28:31], a[36:39], v[44:47]\n"
    "buffer_load_dwordx4 v9, s[44:47], s61 offen offset:1024 lds\n"
    "v_mfma_f32_16x16x32_bf16 v[72:75], a[24:27], a[40:43], 0\n"
    "ds_read_b64_tr_b16 a[10:11], v2 offset:3744\n"
    "v_mfma_f32_16x16x32_bf16 v[72:75], a[28:31], a[44:47], v[72:75]\n"
    "v_mfma_f32_16x16x32_bf16 v[100:103], a[24:27], a[48:51], 0\n"
    "ds_read_b64_tr_b16 a[12:13], v2 offset:7328\n"
    "v_mfma_f32_16x16x32_bf16 v[100:103], a[28:31], a[52:55], v[100:103]\n"
    "buffer_load_dwordx4 a[144:147], v5, s[40:43], s60 offen sc1\n"
    "v_mfma_f32_16x16x32_bf16 v[128:131], a[24:27], a[56:59], 0\n"
    "ds_read_b64_tr_b16 a[14:15], v2 offset:10912\n"
    "v_mfma_f32_16x16x32_bf16 v[128:131], a[28:31], a[60:63], v[128:131]\n"
    "s_waitcnt lgkmcnt(4)\n"
    "v_mfma_f32_16x16x32_bf16 v[48:51], a[0:3], a[32:35], 0\n"
    "ds_read_b64_tr_b16 a[16:17], v2 offset:192\n"
    "v_mfma_f32_16x16x32_bf16 v[48:51], a[4:7], a[36:39], v[48:51]\n"
    "buffer_load_dwordx4 a[148:151], v5, s[40:43], s60 offen offset:1024 sc1\n"
    "v_mfma_f32_16x16x32_bf16 v[76:79], a[0:3], a[40:43], 0\n"
    "ds_read_b64_tr_b16 a[18:19], v2 offset:3776\n"
    "v_mfma_f32_16x16x32_bf16 v[76:79], a[4:7], a[44:47], v[76:79]\n"
    "v_mfma_f32_16x16x32_bf16 v[104:107], a[0:3], a[48:51], 0\n"
    "ds_read_b64_tr_b16 a[20:21], v2 offset:7360\n"
    "v_mfma_f32_16x16x32_bf16 v[104:107], a[4:7], a[52:55], v[104:107]\n"
    "buffer_load_dwordx4 v10, s[44:47], s61 offen offset:2048 lds\n"
    "v_mfma_f32_16x16x32_bf16 v[132:135], a[0:3], a[56:59], 0\n"
    "ds_read_b64_tr_b16 a[22:23], v2 offset:10944\n"
    "v_mfma_f32_16x16x32_bf16 v[132:135], a[4:7], a[60:63], v[132:135]\n"
    "s_waitcnt lgkmcnt(4)\n"
    "v_mfma_f32_16x16x32_bf16 v[52:55], a[8:11], a[32:35], 0\n"
    "ds_read_b64_tr_b16 a[24:25], v2 offset:14336\n"
    "v_mfma_f32_16x16x32_bf16 v[52:55], a[12:15], a[36:39], v[52:55]\n"
    "v_mfma_f32_16x16x32_bf16 v[80:83], a[8:11], a[40:43], 0\n"
    "ds_read_b64_tr_b16 a[26:27], v2 offset:17920\n"
    "v_mfma_f32_16x16x32_bf16 v[80:83], a[12:15], a[44:47], v[80:83]\n"
    "buffer_load_dwordx4 a[152:155], v5, s[40:43], s60 offen offset:2048 sc1\n"
    "v_mfma_f32_16x16x32_bf16 v[108:111], a[8:11], a[48:51], 0\n"
    "ds_read_b64_tr_b16 a[28:29], v2 offset:21504\n"
    "v_mfma_f32_16x16x32_bf16 v[108:111], a[12:15], a[52:55], v[108:111]\n"
    "v_mfma_f32_16x16x32_bf16 v[136:139], a[8:11], a[56:59], 0\n"
    "ds_read_b64_tr_b16 a[30:31], v2 offset:25088\n"
    "v_mfma_f32_16x16x32_bf16 v[136:139], a[12:15], a[60:63], v[136:139]\n"
    "s_waitcnt lgkmcnt(4)\n"
    "v_mfma_f32_16x16x32_bf16 v[56:59], a[16:19], a[32:35], 0\n"
    "ds_read_b64_tr_b16 a[0:1], v2 offset:14368\n"
    "v_mfma_f32_16x16x32_bf16 v[56:59], a[20:23], a[36:39], v[56:59]\n"
    "buffer_load_dwordx4 a[156:159], v5, s[40:43], s60 offen offset:3072 sc1\n"
    "v_mfma_f32_16x16x32_bf16 v[84:87], a[16:19], a[40:43], 0\n"
    "s_add_u32 s60, s60, 0x40000\n"
    "ds_read_b64_tr_b16 a[2:3], v2 offset:17952\n"
    "v_mfma_f32_16x16x32_bf16 v[84:87], a[20:23], a[44:47], v[84:87]\n"
    "buffer_load_dwordx4 v11, s[44:47], s61 offen offset:3072 lds\n"
    "v_mfma_f32_16x16x32_bf16 v[112:115], a[16:19], a[48:51], 0\n"
    "ds_read_b64_tr_b16 a[4:5], v2 offset:21536\n"
    "v_mfma_f32_16x16x32_bf16 v[112:115], a[20:23], a[52:55], v[112:115]\n"
    "v_mfma_f32_16x16x32_bf16 v[140:143], a[16:19], a[56:59], 0\n"
    "ds_read_b64_tr_b16 a[6:7], v2 offset:25120\n"
    "v_mfma_f32_16x16x32_bf16 v[140:143], a[20:23], a[60:63], v[140:143]\n"
    "s_waitcnt vmcnt(21) lgkmcnt(4)\n"
    "v_mfma_f32_16x16x32_bf16 v[32:35], a[24:27], a[64:67], v[32:35]\n"
    "ds_read_b64_tr_b16 a[8:9], v2 offset:14400\n"
    "v_mfma_f32_16x16x32_bf16 v[32:35], a[28:31], a[68:71], v[32:35]\n"
    "buffer_load_dwordx4 a[32:35], v4, s[40:43], s60 offen sc1\n"
    "v_mfma_f32_16x16x32_bf16 v[60:63], a[24:27], a[72:75], v[60:63]\n"
    "ds_read_b64_tr_b16 a[10:11], v2 offset:17984\n"
    "v_mfma_f32_16x16x32_bf16 v[60:63], a[28:31], a[76:79], v[60:63]\n"
    "v_mfma_f32_16x16x32_bf16 v[88:91], a[24:27], a[80:83], v[88:91]\n"
    "ds_read_b64_tr_b16 a[12:13], v2 offset:21568\n"
    "v_mfma_f32_16x16x32_bf16 v[88:91], a[28:31], a[84:87], v[88:91]\n"
    "v_mfma_f32_16x16x32_bf16 v[116:119], a[24:27], a[88:91], v[116:119]\n"
    "ds_read_b64_tr_b16 a[14:15], v2 offset:25152\n"
    "v_mfma_f32_16x16x32_bf16 v[116:119], a[28:31], a[92:95], v[116:119]\n"
    "s_waitcnt lgkmcnt(4)\n"
    "v_mfma_f32_16x16x32_bf16 v[36:39], a[0:3], a[64:67], v[36:39]\n"
    "ds_read_b64_tr_b16 a[16:17], v2 offset:14432\n"
    "v_mfma_f32_16x16x32_bf16 v[36:39], a[4:7], a[68:71], v[36:39]\n"
    "buffer_load_dwordx4 a[36:39], v4, s[40:43], s60 offen offset:1024 sc1\n"
    "v_mfma_f32_16x16x32_bf16 v[64:67], a[0:3], a[72:75], v[64:67]\n"
    "s_add_u32 m0, s63, s75\n"
    "ds_read_b64_tr_b16 a[18:19], v2 offset:18016\n"
    "v_mfma_f32_16x16x32_bf16 v[64:67], a[4:7], a[76:79], v[64:67]\n"
    "buffer_load_dwordx4 v12, s[44:47], s61 offen lds\n"
    "v_mfma_f32_16x16x32_bf16 v[92:95], a[0:3], a[80:83], v[92:95]\n"
    "ds_read_b64_tr_b16 a[20:21], v2 offset:21600\n"
    "v_mfma_f32_16x16x32_bf16 v[92:95], a[4:7], a[84:87], v[92:95]\n"
    "v_mfma_f32_16x16x32_bf16 v[120:123], a[0:3], a[88:91], v[120:123]\n"
    "ds_read_b64_tr_b16 a[22:23], v2 offset:25184\n"
    "v_mfma_f32_16x16x32_bf16 v[120:123], a[4:7], a[92:95], v[120:123]\n"
    "s_waitcnt lgkmcnt(4)\n"
    "v_mfma_f32_16x16x32_bf16 v[40:43], a[8:11], a[64:67], v[40:43]\n"
    "ds_read_b64_tr_b16 a[24:25], v2 offset:14464\n"
    "v_mfma_f32_16x16x32_bf16 v[40:43], a[12:15], a[68:71], v[40:43]\n"
    "buffer_load_dwordx4 a[40:43], v4, s[40:43], s60 offen offset:2048 sc1\n"
    "v_mfma_f32_16x16x32_bf16 v[68:71], a[8:11], a[72:75], v[68:71]\n"
    "ds_read_b64_tr_b16 a[26:27], v2 offset:18048\n"
    "v_mfma_f32_16x16x32_bf16 v[68:71], a[12:15], a[76:79], v[68:71]\n"
    "v_mfma_f32_16x16x32_bf16 v[96:99], a[8:11], a[80:83], v[96:99]\n"
    "ds_read_b64_tr_b16 a[28:29], v2 offset:21632\n"
    "v_mfma_f32_16x16x32_bf16 v[96:99], a[12:15], a[84:87], v[96:99]\n"
    "buffer_load_dwordx4 a[44:47], v4, s[40:43], s60 offen offset:3072 sc1\n"
    "v_mfma_f32_16x16x32_bf16 v[124:127], a[8:11], a[88:91], v[124:127]\n"
    "ds_read_b64_tr_b16 a[30:31], v2 offset:25216\n"
    "v_mfma_f32_16x16x32_bf16 v[124:127], a[12:15], a[92:95], v[124:127]\n"
    "s_waitcnt lgkmcnt(4)\n"
    "v_mfma_f32_16x16x32_bf16 v[44:47], a[16:19], a[64:67], v[44:47]\n"
    "ds_read_b64_tr_b16 a[0:1], v2 offset:14496\n"
    "v_mfma_f32_16x16x32_bf16 v[44:47], a[20:23], a[68:71], v[44:47]\n"
    "v_mfma_f32_16x16x32_bf16 v[72:75], a[16:19], a[72:75], v[72:75]\n"
    "ds_read_b64_tr_b16 a[2:3], v2 offset:18080\n"
    "v_mfma_f32_16x16x32_bf16 v[72:75], a[20:23], a[76:79], v[72:75]\n"
    "buffer_load_dwordx4 a[48:51], v5, s[40:43], s60 offen sc1\n"
    "v_mfma_f32_16x16x32_bf16 v[100:103], a[16:19], a[80:83], v[100:103]\n"
    "ds_read_b64_tr_b16 a[4:5], v2 offset:21664\n"
    "v_mfma_f32_16x16x32_bf16 v[100:103], a[20:23], a[84:87], v[100:103]\n"
    "v_mfma_f32_16x16x32_bf16 v[128:131], a[16:19], a[88:91], v[128:131]\n"
    "ds_read_b64_tr_b16 a[6:7], v2 offset:25248\n"
    "v_mfma_f32_16x16x32_bf16 v[128:131], a[20:23], a[92:95], v[128:131]\n"
    "s_waitcnt lgkmcnt(4)\n"
    "v_mfma_f32_16x16x32_bf16 v[48:51], a[24:27], a[64:67], v[48:51]\n"
    "ds_read_b64_tr_b16 a[8:9], v2 offset:14528\n"
    "v_mfma_f32_16x16x32_bf16 v[48:51], a[28:31], a[68:71], v[48:51]\n"
    "s_add_u32 s64, s64, 1\n"
    "v_mfma_f32_16x16x32_bf16 v[76:79], a[24:27], a[72:75], v[76:79]\n"
    "s_waitcnt vmcnt(18)\n"
    "ds_read_b64_tr_b16 a[10:11], v2 offset:18112\n"
    "v_mfma_f32_16x16x32_bf16 v[76:79], a[28:31], a[76:79], v[76:79]\n"
    "buffer_load_dwordx4 v13, s[44:47], s61 offen offset:1024 lds\n"
    "v_mfma_f32_16x16x32_bf16 v[104:107], a[24:27], a[80:83], v[104:107]\n"
    "ds_read_b64_tr_b16 a[12:13], v2 offset:21696\n"
    "buffer_load_dwordx4 a[52:55], v5, s[40:43], s60 offen offset:1024 sc1\n"
    "v_mfma_f32_16x16x32_bf16 v[104:107], a[28:31], a[84:87], v[104:107]\n"
    "s_cmp_lt_u32 s64, 5\n"
    "s_cselect_b32 s64, s64, 0\n"
    "s_cselect_b32 s65, 0x7000, s73\n"
    "v_mfma_f32_16x16x32_bf16 v[132:135], a[24:27], a[88:91], v[132:135]\n"
    "ds_read_b64_tr_b16 a[14:15], v2 offset:25280\n"
    "v_add_u32 v2, s65, v2\n"
    "v_mfma_f32_16x16x32_bf16 v[132:135], a[28:31], a[92:95], v[132:135]\n"
    "s_waitcnt lgkmcnt(4)\n"
    "s_barrier\n"
    "v_mfma_f32_16x16x32_bf16 v[52:55], a[0:3], a[64:67], v[52:55]\n"
    "ds_read_b64_tr_b16 a[16:17], v2\n"
    "v_mfma_f32_16x16x32_bf16 v[52:55], a[4:7], a[68:71], v[52:55]\n"
    "buffer_load_dwordx4 a[56:59], v5, s[40:43], s60 offen offset:2048 sc1\n"
    "v_mfma_f32_16x16x32_bf16 v[80:83], a[0:3], a[72:75], v[80:83]\n"
    "ds_read_b64_tr_b16 a[18:19], v2 offset:3584\n"
    "v_mfma_f32_16x16x32_bf16 v[80:83], a[4:7], a[76:79], v[80:83]\n"
    "s_mov_b64 exec, 1\n buffer_store_dwordx2 v[30:31], off, s[88:91], s83 offset:24 sc1\n s_mov_b64 exec, -1\n"
    "v_mfma_f32_16x16x32_bf16 v[108:111], a[0:3], a[80:83], v[108:111]\n"
    "ds_read_b64_tr_b16 a[20:21], v2 offset:7168\n"
    "v_mfma_f32_16x16x32_bf16 v[108:111], a[4:7], a[84:87], v[108:111]\n"
    "v_mfma_f32_16x16x32_bf16 v[136:139], a[0:3], a[88:91], v[136:139]\n"
    "ds_read_b64_tr_b16 a[22:23], v2 offset:10752\n"
    "v_mfma_f32_16x16x32_bf16 v[136:139], a[4:7], a[92:95], v[136:139]\n"
    "s_waitcnt lgkmcnt(4)\n"
    "v_mfma_f32_16x16x32_bf16 v[56:59], a[8:11], a[64:67], v[56:59]\n"
    "ds_read_b64_tr_b16 a[24:25], v2 offset:32\n"
    "v_mfma_f32_16x16x32_bf16 v[56:59], a[12:15], a[68:71], v[56:59]\n"
    "buffer_load_dwordx4 a[60:63], v5, s[40:43], s60 offen offset:3072 sc1\n"
    "v_mfma_f32_16x16x32_bf16 v[84:87], a[8:11], a[72:75], v[84:87]\n"
    "s_add_u32 s60, s60, 0x40000\n"
    "ds_read_b64_tr_b16 a[26:27], v2 offset:3616\n"
    "v_mfma_f32_16x16x32_bf16 v[84:87], a[12:15], a[76:79], v[84:87]\n"
    "buffer_load_dwordx4 v14, s[44:47], s61 offen offset:2048 lds\n"
    "v_mfma_f32_16x16x32_bf16 v[112:115], a[8:11], a[80:83], v[112:115]\n"
    "s_add_u32 s61, s61, 0xe0000\n"
    "ds_read_b64_tr_b16 a[28:29], v2 offset:7200\n"
    "v_mfma_f32_16x16x32_bf16 v[112:115], a[12:15], a[84:87], v[112:115]\n"
    "s_add_u32 s63, s63, 0x7000\n"
    "s_cmp_lt_u32 s63, 0x23000\n"
    "s_cselect_b32 s63, s63, 0\n"
    "v_mfma_f32_16x16x32_bf16 v[140:143], a[8:11], a[88:91], v[140:143]\n"
    "ds_read_b64_tr_b16 a[30:31], v2 offset:10784\n"
    "v_mfma_f32_16x16x32_bf16 v[140:143], a[12:15], a[92:95], v[140:143]\n"
    "s_waitcnt lgkmcnt(4)\n"
    "v_mfma_f32_16x16x32_bf16 v[32:35], a[16:19], a[96:99], v[32:35]\n"
    "ds_read_b64_tr_b16 a[0:1], v2 offset:64\n"
    "v_mfma_f32_16x16x32_bf16 v[32:35], a[20:23], a[100:103], v[32:35]\n"
    "buffer_load_dwordx4 a[64:67], v4, s[40:43], s60 offen sc1\n"
    "v_mfma_f32_16x16x32_bf16 v[60:63], a[16:19], a[104:107], v[60:63]\n"
    "ds_read_b64_tr_b16 a[2:3], v2 offset:3648\n"
    "v_mfma_f32_16x16x32_bf16 v[60:63], a[20:23], a[108:111], v[60:63]\n"
    "v_mfma_f32_16x16x32_bf16 v[88:91], a[16:19], a[112:115], v[88:91]\n"
    "ds_read_b64_tr_b16 a[4:5], v2 offset:7232\n"
    "v_mfma_f32_16x16x32_bf16 v[88:91], a[20:23], a[116:119], v[88:91]\n"
    "buffer_load_dwordx4 a[68:71], v4, s[40:43], s60 offen offset:1024 sc1\n"
    "v_mfma_f32_16x16x32_bf16 v[116:119], a[16:19], a[120:123], v[116:119]\n"
    "ds_read_b64_tr_b16 a[6:7], v2 offset:10816\n"
    "v_mfma_f32_16x16x32_bf16 v[116:119], a[20:23], a[124:127], v[116:119]\n"
    "s_waitcnt lgkmcnt(4)\n"
    "v_mfma_f32_16x16x32_bf16 v[36:39], a[24:27], a[96:99], v[36:39]\n"
    "ds_read_b64_tr_b16 a[8:9], v2 offset:96\n"
    "v_mfma_f32_16x16x32_bf16 v[36:39], a[28:31], a[100:103], v[36:39]\n"
    "s_add_u32 m0, s63, s71\n"
    "v_mfma_f32_16x16x32_bf16 v[64:67], a[24:27], a[104:107], v[64:67]\n"
    "ds_read_b64_tr_b16 a[10:11], v2 offset:3680\n"
    "v_mfma_f32_16x16x32_bf16 v[64:67], a[28:31], a[108:111], v[64:67]\n"
    "buffer_load_dwordx4 v8, s[44:47], s61 offen lds\n"
    "v_mfma_f32_16x16x32_bf16 v[92:95], a[24:27], a[112:115], v[92:95]\n"
    "ds_read_b64_tr_b16 a[12:13], v2 offset:7264\n"
    "v_mfma_f32_16x16x32_bf16 v[92:95], a[28:31], a[116:119], v[92:95]\n"
    "v_mfma_f32_16x16x32_bf16 v[120:123], a[24:27], a[120:123], v[120:123]\n"
    "ds_read_b64_tr_b16 a[14:15], v2 offset:10848\n"
    "v_mfma_f32_16x16x32_bf16 v[120:123], a[28:31], a[124:127], v[120:123]\n"
    "s_waitcnt lgkmcnt(4)\n"
    "v_mfma_f32_16x16x32_bf16 v[40:43], a[0:3], a[96:99], v[40:43]\n"
    "ds_read_b64_tr_b16 a[16:17], v2 offset:128\n"
    "v_mfma_f32_16x16x32_bf16 v[40:43], a[4:7], a[100:103], v[40:43]\n"
    "buffer_load_dwordx4 a[72:75], v4, s[40:43], s60 offen offset:2048 sc1\n"
    "v_mfma_f32_16x16x32_bf16 v[68:71], a[0:3], a[104:107], v[68:71]\n"
    "ds_read_b64_tr_b16 a[18:19], v2 offset:3712\n"
    "v_mfma_f32_16x16x32_bf16 v[68:71], a[4:7], a[108:111], v[68:71]\n"
    "buffer_load_dwordx4 a[76:79], v4, s[40:43], s60 offen offset:3072 sc1\n"
    "v_mfma_f32_16x16x32_bf16 v[96:99], a[0:3], a[112:115], v[96:99]\n"
    "ds_read_b64_tr_b16 a[20:21], v2 offset:7296\n"
    "v_mfma_f32_16x16x32_bf16 v[96:99], a[4:7], a[116:119], v[96:99]\n"
    "v_mfma_f32_16x16x32_bf16 v[124:127], a[0:3], a[120:123], v[124:127]\n"
    "ds_read_b64_tr_b16 a[22:23], v2 offset:10880\n"
    "v_mfma_f32_16x16x32_bf16 v[124:127], a[4:7], a[124:127], v[124:127]\n"
    "s_waitcnt lgkmcnt(4)\n"
    "v_mfma_f32_16x16x32_bf16 v[44:47], a[8:11], a[96:99], v[44:47]\n"
    "ds_read_b64_tr_b16 a[24:25], v2 offset:160\n"
    "v_mfma_f32_16x16x32_bf16 v[44:47], a[12:15], a[100:103], v[44:47]\n"
    "buffer_load_dwordx4 v9, s[44:47], s61 offen offset:1024 lds\n"
    "v_mfma_f32_16x16x32_bf16 v[72:75], a[8:11], a[104:107], v[72:75]\n"
    "ds_read_b64_tr_b16 a[26:27], v2 offset:3744\n"
    "v_mfma_f32_16x16x32_bf16 v[72:75], a[12:15], a[108:111], v[72:75]\n"
    "v_mfma_f32_16x16x32_bf16 v[100:103], a[8:11], a[112:115], v[100:103]\n"
    "ds_read_b64_tr_b16 a[28:29], v2 offset:7328\n"
    "v_mfma_f32_16x16x32_bf16 v[100:103], a[12:15], a[116:119], v[100:103]\n"
    "buffer_load_dwordx4 a[80:83], v5, s[40:43], s60 offen sc1\n"
    "v_mfma_f32_16x16x32_bf16 v[128:131], a[8:11], a[120:123], v[128:131]\n"
    "ds_read_b64_tr_b16 a[30:31], v2 offset:10912\n"
    "v_mfma_f32_16x16x32_bf16 v[128:131], a[12:15], a[124:127], v[128:131]\n"
    "s_waitcnt lgkmcnt(4)\n"
    "v_mfma_f32_16x16x32_bf16 v[48:51], a[16:19], a[96:99], v[48:51]\n"
    "ds_read_b64_tr_b16 a[0:1], v2 offset:192\n"
    "v_mfma_f32_16x16x32_bf16 v[48:51], a[20:23], a[100:103], v[48:51]\n"
    "buffer_load_dwordx4 a[84:87], v5, s[40:43], s60 offen offset:1024 sc1\n"
    "v_mfma_f32_16x16x32_bf16 v[76:79], a[16:19], a[104:107], v[76:79]\n"
    "ds_read_b64_tr_b16 a[2:3], v2 offset:3776\n"
    "v_mfma_f32_16x16x32_bf16 v[76:79], a[20:23], a[108:111], v[76:79]\n"
    "v_mfma_f32_16x16x32_bf16 v[104:107], a[16:19], a[112:115], v[104:107]\n"
    "ds_read_b64_tr_b16 a[4:5], v2 offset:7360\n"
    "v_mfma_f32_16x16x32_bf16 v[104:107], a[20:23], a[116:119], v[104:107]\n"
    "buffer_load_dwordx4 v10, s[44:47], s61 offen offset:2048 lds\n"
    "v_mfma_f32_16x16x32_bf16 v[132:135], a[16:19], a[120:123], v[132:135]\n"
    "ds_read_b64_tr_b16 a[6:7], v2 offset:10944\n"
    "v_mfma_f32_16x16x32_bf16 v[132:135], a[20:23], a[124:127], v[132:135]\n"
    "s_waitcnt lgkmcnt(4)\n"
    "v_mfma_f32_16x16x32_bf16 v[52:55], a[24:27], a[96:99], v[52:55]\n"
    "ds_read_b64_tr_b16 a[8:9], v2 offset:14336\n"
    "v_mfma_f32_16x16x32_bf16 v[52:55], a[28:31], a[100:103], v[52:55]\n"
    "v_mfma_f32_16x16x32_bf16 v[80:83], a[24:27], a[104:107], v[80:83]\n"
    "ds_read_b64_tr_b16 a[10:11], v2 offset:17920\n"
    "v_mfma_f32_16x16x32_bf16 v[80:83], a[28:31], a[108:111], v[80:83]\n"
    "buffer_load_dwordx4 a[88:91], v5, s[40:43], s60 offen offset:2048 sc1\n"
    "v_mfma_f32_16x16x32_bf16 v[108:111], a[24:27], a[112:115], v[108:111]\n"
    "ds_read_b64_tr_b16 a[12:13], v2 offset:21504\n"
    "v_mfma_f32_16x16x32_bf16 v[108:111], a[28:31], a[116:119], v[108:111]\n"
    "v_mfma_f32_16x16x32_bf16 v[136:139], a[24:27], a[120:123], v[136:139]\n"
    "ds_read_b64_tr_b16 a[14:15], v2 offset:25088\n"
    "v_mfma_f32_16x16x32_bf16 v[136:139], a[28:31], a[124:127], v[136:139]\n"
    "s_waitcnt lgkmcnt(4)\n"
    "v_mfma_f32_16x16x32_bf16 v[56:59], a[0:3], a[96:99], v[56:59]\n"
    "ds_read_b64_tr_b16 a[16:17], v2 offset:14368\n"
    "v_mfma_f32_16x16x32_bf16 v[56:59], a[4:7], a[100:103], v[56:59]\n"
    "buffer_load_dwordx4 a[92:95], v5, s[40:43], s60 offen offset:3072 sc1\n"
    "v_mfma_f32_16x16x32_bf16 v[84:87], a[0:3], a[104:107], v[84:87]\n"
    "s_add_u32 s60, s60, 0x40000\n"
    "ds_read_b64_tr_b16 a[18:19], v2 offset:17952\n"
    "v_mfma_f32_16x16x32_bf16 v[84:87], a[4:7], a[108:111], v[84:87]\n"
    "buffer_load_dwordx4 v11, s[44:47], s61 offen offset:3072 lds\n"
    "v_mfma_f32_16x16x32_bf16 v[112:115], a[0:3], a[112:115], v[112:115]\n"
    "ds_read_b64_tr_b16 a[20:21], v2 offset:21536\n"
    "v_mfma_f32_16x16x32_bf16 v[112:115], a[4:7], a[116:119], v[112:115]\n"
    "v_mfma_f32_16x16x32_bf16 v[140:143], a[0:3], a[120:123], v[140:143]\n"
    "ds_read_b64_tr_b16 a[22:23], v2 offset:25120\n"
    "v_mfma_f32_16x16x32_bf16 v[140:143], a[4:7], a[124:127], v[140:143]\n"
    "s_waitcnt vmcnt(25) lgkmcnt(4)\n"
    "v_mfma_f32_16x16x32_bf16 v[32:35], a[8:11], a[128:131], v[32:35]\n"
    "ds_read_b64_tr_b16 a[24:25], v2 offset:14400\n"
    "v_mfma_f32_16x16x32_bf16 v[32:35], a[12:15], a[132:135], v[32:35]\n"
    "buffer_load_dwordx4 a[96:99], v4, s[40:43], s60 offen sc1\n"
    "v_mfma_f32_16x16x32_bf16 v[60:63], a[8:11], a[136:139], v[60:63]\n"
    "ds_read_b64_tr_b16 a[26:27], v2 offset:17984\n"
    "v_mfma_f32_16x16x32_bf16 v[60:63], a[12:15], a[140:143], v[60:63]\n"
    "v_mfma_f32_16x16x32_bf16 v[88:91], a[8:11], a[144:147], v[88:91]\n"
    "ds_read_b64_tr_b16 a[28:29], v2 offset:21568\n"
    "v_mfma_f32_16x16x32_bf16 v[88:91], a[12:15], a[148:151], v[88:91]\n"
    "v_mfma_f32_16x16x32_bf16 v[116:119], a[8:11], a[152:155], v[116:119]\n"
    "ds_read_b64_tr_b16 a[30:31], v2 offset:25152\n"
    "v_mfma_f32_16x16x32_bf16 v[116:119], a[12:15], a[156:159], v[116:119]\n"
    "s_waitcnt lgkmcnt(4)\n"
    "v_mfma_f32_16x16x32_bf16 v[36:39], a[16:19], a[128:131], v[36:39]\n"
    "ds_read_b64_tr_b16 a[0:1], v2 offset:14432\n"
    "v_mfma_f32_16x16x32_bf16 v[36:39], a[20:23], a[132:135], v[36:39]\n"
    "buffer_load_dwordx4 a[100:103], v4, s[40:43], s60 offen offset:1024 sc1\n"
    "v_mfma_f32_16x16x32_bf16 v[64:67], a[16:19], a[136:139], v[64:67]\n"
    "s_add_u32 m0, s63, s75\n"
    "ds_read_b64_tr_b16 a[2:3], v2 offset:18016\n"
    "v_mfma_f32_16x16x32_bf16 v[64:67], a[20:23], a[140:143], v[64:67]\n"
    "buffer_load_dwordx4 v12, s[44:47], s61 offen lds\n"
    "v_mfma_f32_16x16x32_bf16 v[92:95], a[16:19], a[144:147], v[92:95]\n"
    "ds_read_b64_tr_b16 a[4:5], v2 offset:21600\n"
    "v_mfma_f32_16x16x32_bf16 v[92:95], a[20:23], a[148:151], v[92:95]\n"
    "v_mfma_f32_16x16x32_bf16 v[120:123], a[16:19], a[152:155], v[120:123]\n"
    "buffer_load_dwordx2 v[30:31], v28, s[88:91], 0 offen sc1\n"
    "ds_read_b64_tr_b16 a[6:7], v2 offset:25184\n"
    "v_mfma_f32_16x16x32_bf16 v[120:123], a[20:23], a[156:159], v[120:123]\n"
    "s_waitcnt lgkmcnt(4)\n"
    "v_mfma_f32_16x16x32_bf16 v[40:43], a[24:27], a[128:131], v[40:43]\n"
    "ds_read_b64_tr_b16 a[8:9], v2 offset:14464\n"
    "v_mfma_f32_16x16x32_bf16 v[40:43], a[28:31], a[132:135], v[40:43]\n"
    "buffer_load_dwordx4 a[104:107], v4, s[40:43], s60 offen offset:2048 sc1\n"
    "v_mfma_f32_16x16x32_bf16 v[68:71], a[24:27], a[136:139], v[68:71]\n"
    "ds_read_b64_tr_b16 a[10:11], v2 offset:18048\n"
    "v_mfma_f32_16x16x32_bf16 v[68:71], a[28:31], a[140:143], v[68:71]\n"
    "v_mfma_f32_16x16x32_bf16 v[96:99], a[24:27], a[144:147], v[96:99]\n"
    "ds_read_b64_tr_b16 a[12:13], v2 offset:21632\n"
    "v_mfma_f32_16x16x32_bf16 v[96:99], a[28:31], a[148:151], v[96:99]\n"
    "buffer_load_dwordx4 a[108:111], v4, s[40:43], s60 offen offset:3072 sc1\n"
    "v_mfma_f32_16x16x32_bf16 v[124:127], a[24:27], a[152:155], v[124:127]\n"
    "ds_read_b64_tr_b16 a[14:15], v2 offset:25216\n"
    "v_mfma_f32_16x16x32_bf16 v[124:127], a[28:31], a[156:159], v[124:127]\n"
    "s_waitcnt lgkmcnt(4)\n"
    "v_mfma_f32_16x16x32_bf16 v[44:47], a[0:3], a[128:131], v[44:47]\n"
    "ds_read_b64_tr_b16 a[16:17], v2 offset:14496\n"
    "v_mfma_f32_16x16x32_bf16 v[44:47], a[4:7], a[132:135], v[44:47]\n"
    "v_mfma_f32_16x16x32_bf16 v[72:75], a[0:3], a[136:139], v[72:75]\n"
    "ds_read_b64_tr_b16 a[18:19], v2 offset:18080\n"
    "v_mfma_f32_16x16x32_bf16 v[72:75], a[4:7], a[140:143], v[72:75]\n"
    "buffer_load_dwordx4 a[112:115], v5, s[40:43], s60 offen sc1\n"
    "v_mfma_f32_16x16x32_bf16 v[100:103], a[0:3], a[144:147], v[100:103]\n"
    "ds_read_b64_tr_b16 a[20:21], v2 offset:21664\n"
    "v_mfma_f32_16x16x32_bf16 v[100:103], a[4:7], a[148:151], v[100:103]\n"
    "v_mfma_f32_16x16x32_bf16 v[128:131], a[0:3], a[152:155], v[128:131]\n"
    "ds_read_b64_tr_b16 a[22:23], v2 offset:25248\n"
    "v_mfma_f32_16x16x32_bf16 v[128:131], a[4:7], a[156:159], v[128:131]\n"
    "s_waitcnt lgkmcnt(4)\n"
    "v_mfma_f32_16x16x32_bf16 v[48:51], a[8:11], a[128:131], v[48:51]\n"
    "ds_read_b64_tr_b16 a[24:25], v2 offset:14528\n"
    "v_mfma_f32_16x16x32_bf16 v[48:51], a[12:15], a[132:135], v[48:51]\n"
    "buffer_load_dwordx4 v13, s[44:47], s61 offen offset:1024 lds\n"
    "v_mfma_f32_16x16x32_bf16 v[76:79], a[8:11], a[136:139], v[76:79]\n"
    "s_add_u32 s64, s64, 1\n"
    "ds_read_b64_tr_b16 a[26:27], v2 offset:18112\n"
    "v_mfma_f32_16x16x32_bf16 v[76:79], a[12:15], a[140:143], v[76:79]\n"
    "buffer_load_dwordx4 a[116:119], v5, s[40:43], s60 offen offset:1024 sc1\n"
    "v_mfma_f32_16x16x32_bf16 v[104:107], a[8:11], a[144:147], v[104:107]\n"
    "ds_read_b64_tr_b16 a[28:29], v2 offset:21696\n"
    "v_mfma_f32_16x16x32_bf16 v[104:107], a[12:15], a[148:151], v[104:107]\n"
    "s_cmp_lt_u32 s64, 5\n"
    "s_cselect_b32 s64, s64, 0\n"
    "s_cselect_b32 s65, 0x7000, s73\n"
    "v_mfma_f32_16x16x32_bf16 v[132:135], a[8:11], a[152:155], v[132:135]\n"
    "ds_read_b64_tr_b16 a[30:31], v2 offset:25280\n"
    "v_add_u32 v2, s65, v2\n"
    "v_mfma_f32_16x16x32_bf16 v[132:135], a[12:15], a[156:159], v[132:135]\n"
    "s_waitcnt lgkmcnt(4)\n"
    "s_barrier\n"
    "v_mfma_f32_16x16x32_bf16 v[52:55], a[16:19], a[128:131], v[52:55]\n"
    "ds_read_b64_tr_b16 a[0:1], v2\n"
    "v_mfma_f32_16x16x32_bf16 v[52:55], a[20:23], a[132:135], v[52:55]\n"
    "buffer_load_dwordx4 a[120:123], v5, s[40:43], s60 offen offset:2048 sc1\n"
    "v_mfma_f32_16x16x32_bf16 v[80:83], a[16:19], a[136:139], v[80:83]\n"
    "ds_read_b64_tr_b16 a[2:3], v2 offset:3584\n"
    "v_mfma_f32_16x16x32_bf16 v[80:83], a[20:23], a[140:143], v[80:83]\n"
    "v_mfma_f32_16x16x32_bf16 v[108:111], a[16:19], a[144:147], v[108:111]\n"
    "ds_read_b64_tr_b16 a[4:5], v2 offset:7168\n"
    "v_mfma_f32_16x16x32_bf16 v[108:111], a[20:23], a[148:151], v[108:111]\n"
    "v_mfma_f32_16x16x32_bf16 v[136:139], a[16:19], a[152:155], v[136:139]\n"
    "ds_read_b64_tr_b16 a[6:7], v2 offset:10752\n"
    "v_mfma_f32_16x16x32_bf16 v[136:139], a[20:23], a[156:159], v[136:139]\n"
    "s_waitcnt lgkmcnt(4)\n"
    "v_mfma_f32_16x16x32_bf16 v[56:59], a[24:27], a[128:131], v[56:59]\n"
    "ds_read_b64_tr_b16 a[8:9], v2 offset:32\n"
    "v_mfma_f32_16x16x32_bf16 v[56:59], a[28:31], a[132:135], v[56:59]\n"
    "buffer_load_dwordx4 a[124:127], v5, s[40:43], s60 offen offset:3072 sc1\n"
    "v_mfma_f32_16x16x32_bf16 v[84:87], a[24:27], a[136:139], v[84:87]\n"
    "s_add_u32 s60, s60, 0x40000\n"
    "ds_read_b64_tr_b16 a[10:11], v2 offset:3616\n"
    "v_mfma_f32_16x16x32_bf16 v[84:87], a[28:31], a[140:143], v[84:87]\n"
    "buffer_load_dwordx4 v14, s[44:47], s61 offen offset:2048 lds\n"
    "v_mfma_f32_16x16x32_bf16 v[112:115], a[24:27], a[144:147], v[112:115]\n"
    "s_add_u32 s61, s61, 0xe0000\n"
    "ds_read_b64_tr_b16 a[12:13], v2 offset:7200\n"
    "v_mfma_f32_16x16x32_bf16 v[112:115], a[28:31], a[148:151], v[112:115]\n"
    "s_add_u32 s63, s63, 0x7000\n"
    "s_cmp_lt_u32 s63, 0x23000\n"
    "s_cselect_b32 s63, s63, 0\n"
    "v_mfma_f32_16x16x32_bf16 v[140:143], a[24:27], a[152:155], v[140:143]\n"
    "ds_read_b64_tr_b16 a[14:15], v2 offset:10784\n"
    "v_mfma_f32_16x16x32_bf16 v[140:143], a[28:31], a[156:159], v[140:143]\n"
    "s_mov_b32 s82, 1048577\n"
    "s_waitcnt vmcnt(8)\n"
    "v_cmp_ne_u64 vcc, s[94:95], v[30:31]\n"
    "s_sub_u32 s82, s82, 1\n"
    "s_cmp_eq_u64 vcc, 0\n"
    "s_cbranch_scc1 k_ffn1_s_go\n"
    "k_ffn1_s_spin:\n"
    "buffer_load_dwordx2 v[30:31], v28, s[88:91], 0 offen sc1\n"
    "s_waitcnt vmcnt(0)\n"
    "v_cmp_ne_u64 vcc, s[94:95], v[30:31]\n"
    "s_sub_u32 s82, s82, 1\n"
    "s_cmp_eq_u64 vcc, 0\n"
    "s_cbranch_scc1 k_ffn1_s_go\n"
    "s_sleep 1\n"
    "s_cmp_lg_u32 s82, 0\n"
    "s_cbranch_scc1 k_ffn1_s_spin\n"
    "k_ffn1_s_go:\n"
    "s_waitcnt lgkmcnt(4)\n"
    "v_mfma_f32_16x16x32_bf16 v[32:35], a[0:3], a[32:35], v[32:35]\n"
    "ds_read_b64_tr_b16 a[16:17], v2 offset:64\n"
    "v_mfma_f32_16x16x32_bf16 v[32:35], a[4:7], a[36:39], v[32:35]\n"
    "buffer_load_dwordx4 a[128:131], v4, s[40:43], s60 offen sc1\n"
    "v_mfma_f32_16x16x32_bf16 v[60:63], a[0:3], a[40:43], v[60:63]\n"
    "ds_read_b64_tr_b16 a[18:19], v2 offset:3648\n"
    "v_mfma_f32_16x16x32_bf16 v[60:63], a[4:7], a[44:47], v[60:63]\n"
    "buffer_load_dwordx2 v[180:181], v18, s[56:59], 0 offen sc1\n"
    "v_mfma_f32_16x16x32_bf16 v[88:91], a[0:3], a[48:51], v[88:91]\n"
    "ds_read_b64_tr_b16 a[20:21], v2 offset:7232\n"
    "v_mfma_f32_16x16x32_bf16 v[88:91], a[4:7], a[52:55], v[88:91]\n"
    "buffer_load_dwordx4 a[132:135], v4, s[40:43], s60 offen offset:1024 sc1\n"
    "v_mfma_f32_16x16x32_bf16 v[116:119], a[0:3], a[56:59], v[116:119]\n"
    "ds_read_b64_tr_b16 a[22:23], v2 offset:10816\n"
    "v_mfma_f32_16x16x32_bf16 v[116:119], a[4:7], a[60:63], v[116:119]\n"
    "s_waitcnt lgkmcnt(4)\n"
    "v_mfma_f32_16x16x32_bf16 v[36:39], a[8:11], a[32:35], v[36:39]\n"
    "ds_read_b64_tr_b16 a[24:25], v2 offset:96\n"
    "v_mfma_f32_16x16x32_bf16 v[36:39], a[12:15], a[36:39], v[36:39]\n"
    "s_add_u32 m0, s63, s71\n"
    "buffer_load_dwordx2 v[182:183], v6, s[56:59], 0 offen sc1\n"
    "v_mfma_f32_16x16x32_bf16 v[64:67], a[8:11], a[40:43], v[64:67]\n"
    "ds_read_b64_tr_b16 a[26:27], v2 offset:3680\n"
    "v_mfma_f32_16x16x32_bf16 v[64:67], a[12:15], a[44:47], v[64:67]\n"
    "buffer_load_dwordx4 v8, s[44:47], s61 offen lds\n"
    "v_mfma_f32_16x16x32_bf16 v[92:95], a[8:11], a[48:51], v[92:95]\n"
    "ds_read_b64_tr_b16 a[28:29], v2 offset:7264\n"
    "v_mfma_f32_16x16x32_bf16 v[92:95], a[12:15], a[52:55], v[92:95]\n"
    "buffer_load_dwordx2 v[188:189], v18, s[56:59], 0 offen offset:128 sc1\n"
    "v_mfma_f32_16x16x32_bf16 v[120:123], a[8:11], a[56:59], v[120:123]\n"
    "ds_read_b64_tr_b16 a[30:31], v2 offset:10848\n"
    "v_mfma_f32_16x16x32_bf16 v[120:123], a[12:15], a[60:63], v[120:123]\n"
    "s_waitcnt lgkmcnt(4)\n"
    "v_mfma_f32_16x16x32_bf16 v[40:43], a[16:19], a[32:35], v[40:43]\n"
    "ds_read_b64_tr_b16 a[0:1], v2 offset:128\n"
    "v_mfma_f32_16x16x32_bf16 v[40:43], a[20:23], a[36:39], v[40:43]\n"
    "buffer_load_dwordx4 a[136:139], v4, s[40:43], s60 offen offset:2048 sc1\n"
    "v_mfma_f32_16x16x32_bf16 v[68:71], a[16:19], a[40:43], v[68:71]\n"
    "ds_read_b64_tr_b16 a[2:3], v2 offset:3712\n"
    "v_mfma_f32_16x16x32_bf16 v[68:71], a[20:23], a[44:47], v[68:71]\n"
    "buffer_load_dwordx4 a[140:143], v4, s[40:43], s60 offen offset:3072 sc1\n"
    "v_mfma_f32_16x16x32_bf16 v[96:99], a[16:19], a[48:51], v[96:99]\n"
    "ds_read_b64_tr_b16 a[4:5], v2 offset:7296\n"
    "v_mfma_f32_16x16x32_bf16 v[96:99], a[20:23], a[52:55], v[96:99]\n"
    "buffer_load_dwordx2 v[190:191], v6, s[56:59], 0 offen offset:128 sc1\n"
    "v_mfma_f32_16x16x32_bf16 v[124:127], a[16:19], a[56:59], v[124:127]\n"
    "ds_read_b64_tr_b16 a[6:7], v2 offset:10880\n"
    "v_mfma_f32_16x16x32_bf16 v[124:127], a[20:23], a[60:63], v[124:127]\n"
    "s_waitcnt lgkmcnt(4)\n"
    "v_mfma_f32_16x16x32_bf16 v[44:47], a[24:27], a[32:35], v[44:47]\n"
    "ds_read_b64_tr_b16 a[8:9], v2 offset:160\n"
    "v_mfma_f32_16x16x32_bf16 v[44:47], a[28:31], a[36:39], v[44:47]\n"
    "buffer_load_dwordx4 v9, s[44:47], s61 offen offset:1024 lds\n"
    "v_mfma_f32_16x16x32_bf16 v[72:75], a[24:27], a[40:43], v[72:75]\n"
    "ds_read_b64_tr_b16 a[10:11], v2 offset:3744\n"
    "v_mfma_f32_16x16x32_bf16 v[72:75], a[28:31], a[44:47], v[72:75]\n"
    "buffer_load_dwordx2 v[196:197], v18, s[56:59], 0 offen offset:256 sc1\n"
    "v_mfma_f32_16x16x32_bf16 v[100:103], a[24:27], a[48:51], v[100:103]\n"
    "ds_read_b64_tr_b16 a[12:13], v2 offset:7328\n"
    "v_mfma_f32_16x16x32_bf16 v[100:103], a[28:31], a[52:55], v[100:103]\n"
    "buffer_load_dwordx4 a[144:147], v5, s[40:43], s60 offen sc1\n"
    "v_mfma_f32_16x16x32_bf16 v[128:131], a[24:27], a[56:59], v[128:131]\n"
    "ds_read_b64_tr_b16 a[14:15], v2 offset:10912\n"
    "v_mfma_f32_16x16x32_bf16 v[128:131], a[28:31], a[60:63], v[128:131]\n"
    "s_waitcnt lgkmcnt(4)\n"
    "v_mfma_f32_16x16x32_bf16 v[48:51], a[0:3], a[32:35], v[48:51]\n"
    "ds_read_b64_tr_b16 a[16:17], v2 offset:192\n"
    "v_mfma_f32_16x16x32_bf16 v[48:51], a[4:7], a[36:39], v[48:51]\n"
    "buffer_load_dwordx4 a[148:151], v5, s[40:43], s60 offen offset:1024 sc1\n"
    "v_mfma_f32_16x16x32_bf16 v[76:79], a[0:3], a[40:43], v[76:79]\n"
    "ds_read_b64_tr_b16 a[18:19], v2 offset:3776\n"
    "v_mfma_f32_16x16x32_bf16 v[76:79], a[4:7], a[44:47], v[76:79]\n"
    "buffer_load_dwordx2 v[198:199], v6, s[56:59], 0 offen offset:256 sc1\n"
    "v_mfma_f32_16x16x32_bf16 v[104:107], a[0:3], a[48:51], v[104:107]\n"
    "ds_read_b64_tr_b16 a[20:21], v2 offset:7360\n"
    "v_mfma_f32_16x16x32_bf16 v[104:107], a[4:7], a[52:55], v[104:107]\n"
    "buffer_load_dwordx4 v10, s[44:47], s61 offen offset:2048 lds\n"
    "v_mfma_f32_16x16x32_bf16 v[132:135], a[0:3], a[56:59], v[132:135]\n"
    "ds_read_b64_tr_b16 a[22:23], v2 offset:10944\n"
    "v_mfma_f32_16x16x32_bf16 v[132:135], a[4:7], a[60:63], v[132:135]\n"
    "s_waitcnt lgkmcnt(4)\n"
    "v_mfma_f32_16x16x32_bf16 v[52:55], a[8:11], a[32:35], v[52:55]\n"
    "ds_read_b64_tr_b16 a[24:25], v2 offset:14336\n"
    "v_mfma_f32_16x16x32_bf16 v[52:55], a[12:15], a[36:39], v[52:55]\n"
    "buffer_load_dwordx2 v[204:205], v18, s[56:59], 0 offen offset:384 sc1\n"
    "v_mfma_f32_16x16x32_bf16 v[80:83], a[8:11], a[40:43], v[80:83]\n"
    "ds_read_b64_tr_b16 a[26:27], v2 offset:17920\n"
    "v_mfma_f32_16x16x32_bf16 v[80:83], a[12:15], a[44:47], v[80:83]\n"
    "buffer_load_dwordx4 a[152:155], v5, s[40:43], s60 offen offset:2048 sc1\n"
    "v_mfma_f32_16x16x32_bf16 v[108:111], a[8:11], a[48:51], v[108:111]\n"
    "ds_read_b64_tr_b16 a[28:29], v2 offset:21504\n"
    "v_mfma_f32_16x16x32_bf16 v[108:111], a[12:15], a[52:55], v[108:111]\n"
    "buffer_load_dwordx2 v[206:207], v6, s[56:59], 0 offen offset:384 sc1\n"
    "v_mfma_f32_16x16x32_bf16 v[136:139], a[8:11], a[56:59], v[136:139]\n"
    "ds_read_b64_tr_b16 a[30:31], v2 offset:25088\n"
    "v_mfma_f32_16x16x32_bf16 v[136:139], a[12:15], a[60:63], v[136:139]\n"
    "s_waitcnt lgkmcnt(4)\n"
    "v_mfma_f32_16x16x32_bf16 v[56:59], a[16:19], a[32:35], v[56:59]\n"
    "ds_read_b64_tr_b16 a[0:1], v2 offset:14368\n"
    "v_mfma_f32_16x16x32_bf16 v[56:59], a[20:23], a[36:39], v[56:59]\n"
    "buffer_load_dwordx4 a[156:159], v5, s[40:43], s60 offen offset:3072 sc1\n"
    "v_mfma_f32_16x16x32_bf16 v[84:87], a[16:19], a[40:43], v[84:87]\n"
    "s_add_u32 s60, s60, 0x40000\n"
    "ds_read_b64_tr_b16 a[2:3], v2 offset:17952\n"
    "v_mfma_f32_16x16x32_bf16 v[84:87], a[20:23], a[44:47], v[84:87]\n"
    "buffer_load_dwordx4 v11, s[44:47], s61 offen offset:3072 lds\n"
    "v_mfma_f32_16x16x32_bf16 v[112:115], a[16:19], a[48:51], v[112:115]\n"
    "ds_read_b64_tr_b16 a[4:5], v2 offset:21536\n"
    "v_mfma_f32_16x16x32_bf16 v[112:115], a[20:23], a[52:55], v[112:115]\n"
    "s_mov_b32 s62, m0\n s_mov_b32 m0, s85\n s_nop 0\n buffer_load_dwordx4 v7, s[48:51], s84 offen sc1 lds\n s_mov_b32 m0, s62\n"
    "v_mfma_f32_16x16x32_bf16 v[140:143], a[16:19], a[56:59], v[140:143]\n"
    "ds_read_b64_tr_b16 a[6:7], v2 offset:25120\n"
    "v_mfma_f32_16x16x32_bf16 v[140:143], a[20:23], a[60:63], v[140:143]\n"
    "s_waitcnt lgkmcnt(4)\n"
    "v_mfma_f32_16x16x32_bf16 v[32:35], a[24:27], a[64:67], v[32:35]\n"
    "ds_read_b64_tr_b16 a[8:9], v2 offset:14400\n"
    "v_mfma_f32_16x16x32_bf16 v[32:35], a[28:31], a[68:71], v[32:35]\n"
    "buffer_load_dwordx4 a[32:35], v4, s[40:43], s60 offen sc1\n"
    "v_mfma_f32_16x16x32_bf16 v[60:63], a[24:27], a[72:75], v[60:63]\n"
    "ds_read_b64_tr_b16 a[10:11], v2 offset:17984\n"
    "v_mfma_f32_16x16x32_bf16 v[60:63], a[28:31], a[76:79], v[60:63]\n"
    "v_mfma_f32_16x16x32_bf16 v[88:91], a[24:27], a[80:83], v[88:91]\n"
    "ds_read_b64_tr_b16 a[12:13], v2 offset:21568\n"
    "v_mfma_f32_16x16x32_bf16 v[88:91], a[28:31], a[84:87], v[88:91]\n"
    "v_mfma_f32_16x16x32_bf16 v[116:119], a[24:27], a[88:91], v[116:119]\n"
    "ds_read_b64_tr_b16 a[14:15], v2 offset:25152\n"
    "v_mfma_f32_16x16x32_bf16 v[116:119], a[28:31], a[92:95], v[116:119]\n"
    "s_waitcnt lgkmcnt(4)\n"
    "v_mfma_f32_16x16x32_bf16 v[36:39], a[0:3], a[64:67], v[36:39]\n"
    "ds_read_b64_tr_b16 a[16:17], v2 offset:14432\n"
    "v_mfma_f32_16x16x32_bf16 v[36:39], a[4:7], a[68:71], v[36:39]\n"
    "buffer_load_dwordx4 a[36:39], v4, s[40:43], s60 offen offset:1024 sc1\n"
    "v_mfma_f32_16x16x32_bf16 v[64:67], a[0:3], a[72:75], v[64:67]\n"
    "s_add_u32 m0, s63, s75\n"
    "ds_read_b64_tr_b16 a[18:19], v2 offset:18016\n"
    "v_mfma_f32_16x16x32_bf16 v[64:67], a[4:7], a[76:79], v[64:67]\n"
    "buffer_load_dwordx4 v12, s[44:47], s61 offen lds\n"
    "v_mfma_f32_16x16x32_bf16 v[92:95], a[0:3], a[80:83], v[92:95]\n"
    "ds_read_b64_tr_b16 a[20:21], v2 offset:21600\n"
    "v_mfma_f32_16x16x32_bf16 v[92:95], a[4:7], a[84:87], v[92:95]\n"
    "v_mfma_f32_16x16x32_bf16 v[120:123], a[0:3], a[88:91], v[120:123]\n"
    "ds_read_b64_tr_b16 a[22:23], v2 offset:25184\n"
    "v_mfma_f32_16x16x32_bf16 v[120:123], a[4:7], a[92:95], v[120:123]\n"
    "s_waitcnt lgkmcnt(4)\n"
    "v_mfma_f32_16x16x32_bf16 v[40:43], a[8:11], a[64:67], v[40:43]\n"
    "ds_read_b64_tr_b16 a[24:25], v2 offset:14464\n"
    "v_mfma_f32_16x16x32_bf16 v[40:43], a[12:15], a[68:71], v[40:43]\n"
    "buffer_load_dwordx4 a[40:43], v4, s[40:43], s60 offen offset:2048 sc1\n"
    "v_mfma_f32_16x16x32_bf16 v[68:71], a[8:11], a[72:75], v[68:71]\n"
    "ds_read_b64_tr_b16 a[26:27], v2 offset:18048\n"
    "v_mfma_f32_16x16x32_bf16 v[68:71], a[12:15], a[76:79], v[68:71]\n"
    "v_mfma_f32_16x16x32_bf16 v[96:99], a[8:11], a[80:83], v[96:99]\n"
    "ds_read_b64_tr_b16 a[28:29], v2 offset:21632\n"
    "v_mfma_f32_16x16x32_bf16 v[96:99], a[12:15], a[84:87], v[96:99]\n"
    "buffer_load_dwordx4 a[44:47], v4, s[40:43], s60 offen offset:3072 sc1\n"
    "v_mfma_f32_16x16x32_bf16 v[124:127], a[8:11], a[88:91], v[124:127]\n"
    "ds_read_b64_tr_b16 a[30:31], v2 offset:25216\n"
    "v_mfma_f32_16x16x32_bf16 v[124:127], a[12:15], a[92:95], v[124:127]\n"
    "s_waitcnt lgkmcnt(4)\n"
    "v_mfma_f32_16x16x32_bf16 v[44:47], a[16:19], a[64:67], v[44:47]\n"
    "ds_read_b64_tr_b16 a[0:1], v2 offset:14496\n"
    "v_mfma_f32_16x16x32_bf16 v[44:47], a[20:23], a[68:71], v[44:47]\n"
    "v_mfma_f32_16x16x32_bf16 v[72:75], a[16:19], a[72:75], v[72:75]\n"
    "ds_read_b64_tr_b16 a[2:3], v2 offset:18080\n"
    "v_mfma_f32_16x16x32_bf16 v[72:75], a[20:23], a[76:79], v[72:75]\n"
    "buffer_load_dwordx4 a[48:51], v5, s[40:43], s60 offen sc1\n"
    "v_mfma_f32_16x16x32_bf16 v[100:103], a[16:19], a[80:83], v[100:103]\n"
    "ds_read_b64_tr_b16 a[4:5], v2 offset:21664\n"
    "v_mfma_f32_16x16x32_bf16 v[100:103], a[20:23], a[84:87], v[100:103]\n"
    "v_mfma_f32_16x16x32_bf16 v[128:131], a[16:19], a[88:91], v[128:131]\n"
    "ds_read_b64_tr_b16 a[6:7], v2 offset:25248\n"
    "v_mfma_f32_16x16x32_bf16 v[128:131], a[20:23], a[92:95], v[128:131]\n"
    "s_waitcnt lgkmcnt(4)\n"
    "v_mfma_f32_16x16x32_bf16 v[48:51], a[24:27], a[64:67], v[48:51]\n"
    "ds_read_b64_tr_b16 a[8:9], v2 offset:14528\n"
    "v_mfma_f32_16x16x32_bf16 v[48:51], a[28:31], a[68:71], v[48:51]\n"
    "buffer_load_dwordx4 v13, s[44:47], s61 offen offset:1024 lds\n"
    "v_mfma_f32_16x16x32_bf16 v[76:79], a[24:27], a[72:75], v[76:79]\n"
    "s_add_u32 s64, s64, 1\n"
    "ds_read_b64_tr_b16 a[10:11], v2 offset:18112\n"
    "v_mfma_f32_16x16x32_bf16 v[76:79], a[28:31], a[76:79], v[76:79]\n"
    "buffer_load_dwordx4 a[52:55], v5, s[40:43], s60 offen offset:1024 sc1\n"
    "v_mfma_f32_16x16x32_bf16 v[104:107], a[24:27], a[80:83], v[104:107]\n"
    "ds_read_b64_tr_b16 a[12:13], v2 offset:21696\n"
    "v_mfma_f32_16x16x32_bf16 v[104:107], a[28:31], a[84:87], v[104:107]\n"
    "s_cmp_lt_u32 s64, 5\n"
    "s_cselect_b32 s64, s64, 0\n"
    "s_cselect_b32 s65, 0x7000, s73\n"
    "v_mfma_f32_16x16x32_bf16 v[132:135], a[24:27], a[88:91], v[132:135]\n"
    "ds_read_b64_tr_b16 a[14:15], v2 offset:25280\n"
    "v_add_u32 v2, s65, v2\n"
    "v_mfma_f32_16x16x32_bf16 v[132:135], a[28:31], a[92:95], v[132:135]\n"
    "s_waitcnt lgkmcnt(4)\n"
    "s_barrier\n"
    "v_mfma_f32_16x16x32_bf16 v[52:55], a[0:3], a[64:67], v[52:55]\n"
    "ds_read_b64_tr_b16 a[16:17], v2\n"
    "v_mfma_f32_16x16x32_bf16 v[52:55], a[4:7], a[68:71], v[52:55]\n"
    "buffer_load_dwordx4 a[56:59], v5, s[40:43], s60 offen offset:2048 sc1\n"
    "v_mfma_f32_16x16x32_bf16 v[80:83], a[0:3], a[72:75], v[80:83]\n"
    "ds_read_b64_tr_b16 a[18:19], v2 offset:3584\n"
    "v_mfma_f32_16x16x32_bf16 v[80:83], a[4:7], a[76:79], v[80:83]\n"
    "v_mfma_f32_16x16x32_bf16 v[108:111], a[0:3], a[80:83], v[108:111]\n"
    "ds_read_b64_tr_b16 a[20:21], v2 offset:7168\n"
    "v_mfma_f32_16x16x32_bf16 v[108:111], a[4:7], a[84:87], v[108:111]\n"
    "v_mfma_f32_16x16x32_bf16 v[136:139], a[0:3], a[88:91], v[136:139]\n"
    "ds_read_b64_tr_b16 a[22:23], v2 offset:10752\n"
    "v_mfma_f32_16x16x32_bf16 v[136:139], a[4:7], a[92:95], v[136:139]\n"
    "s_waitcnt lgkmcnt(4)\n"
    "v_mfma_f32_16x16x32_bf16 v[56:59], a[8:11], a[64:67], v[56:59]\n"
    "ds_read_b64_tr_b16 a[24:25], v2 offset:32\n"
    "v_mfma_f32_16x16x32_bf16 v[56:59], a[12:15], a[68:71], v[56:59]\n"
    "buffer_load_dwordx4 a[60:63], v5, s[40:43], s60 offen offset:3072 sc1\n"
    "v_mfma_f32_16x16x32_bf16 v[84:87], a[8:11], a[72:75], v[84:87]\n"
    "s_add_u32 s60, s60, 0x40000\n"
    "ds_read_b64_tr_b16 a[26:27], v2 offset:3616\n"
    "v_mfma_f32_16x16x32_bf16 v[84:87], a[12:15], a[76:79], v[84:87]\n"
    "buffer_load_dwordx4 v14, s[44:47], s61 offen offset:2048 lds\n"
    "v_mfma_f32_16x16x32_bf16 v[112:115], a[8:11], a[80:83], v[112:115]\n"
    "s_add_u32 s61, s61, 0xe0000\n"
    "ds_read_b64_tr_b16 a[28:29], v2 offset:7200\n"
    "v_mfma_f32_16x16x32_bf16 v[112:115], a[12:15], a[84:87], v[112:115]\n"
    "s_add_u32 s63, s63, 0x7000\n"
    "s_cmp_lt_u32 s63, 0x23000\n"
    "s_cselect_b32 s63, s63, 0\n"
    "v_mfma_f32_16x16x32_bf16 v[140:143], a[8:11], a[88:91], v[140:143]\n"
    "ds_read_b64_tr_b16 a[30:31], v2 offset:10784\n"
    "v_mfma_f32_16x16x32_bf16 v[140:143], a[12:15], a[92:95], v[140:143]\n"
    "s_waitcnt vmcnt(33) lgkmcnt(4)\n"
    "v_mfma_f32_16x16x32_bf16 v[32:35], a[16:19], a[96:99], v[32:35]\n"
    "ds_read_b64_tr_b16 a[0:1], v2 offset:64\n"
    "v_mfma_f32_16x16x32_bf16 v[32:35], a[20:23], a[100:103], v[32:35]\n"
    "buffer_load_dwordx4 a[64:67], v4, s[40:43], s60 offen sc1\n"
    "v_mfma_f32_16x16x32_bf16 v[60:63], a[16:19], a[104:107], v[60:63]\n"
    "ds_read_b64_tr_b16 a[2:3], v2 offset:3648\n"
    "v_mfma_f32_16x16x32_bf16 v[60:63], a[20:23], a[108:111], v[60:63]\n"
    "v_mfma_f32_16x16x32_bf16 v[88:91], a[16:19], a[112:115], v[88:91]\n"
    "ds_read_b64_tr_b16 a[4:5], v2 offset:7232\n"
    "v_mfma_f32_16x16x32_bf16 v[88:91], a[20:23], a[116:119], v[88:91]\n"
    "buffer_load_dwordx4 a[68:71], v4, s[40:43], s60 offen offset:1024 sc1\n"
    "v_mfma_f32_16x16x32_bf16 v[116:119], a[16:19], a[120:123], v[116:119]\n"
    "ds_read_b64_tr_b16 a[6:7], v2 offset:10816\n"
    "v_mfma_f32_16x16x32_bf16 v[116:119], a[20:23], a[124:127], v[116:119]\n"
    "s_waitcnt lgkmcnt(4)\n"
    "v_mfma_f32_16x16x32_bf16 v[36:39], a[24:27], a[96:99], v[36:39]\n"
    "ds_read_b64_tr_b16 a[8:9], v2 offset:96\n"
    "v_mfma_f32_16x16x32_bf16 v[36:39], a[28:31], a[100:103], v[36:39]\n"
    "s_add_u32 m0, s63, s71\n"
    "v_mfma_f32_16x16x32_bf16 v[64:67], a[24:27], a[104:107], v[64:67]\n"
    "ds_read_b64_tr_b16 a[10:11], v2 offset:3680\n"
    "v_mfma_f32_16x16x32_bf16 v[64:67], a[28:31], a[108:111], v[64:67]\n"
    "buffer_load_dwordx4 v8, s[44:47], s61 offen lds\n"
    "v_mfma_f32_16x16x32_bf16 v[92:95], a[24:27], a[112:115], v[92:95]\n"
    "ds_read_b64_tr_b16 a[12:13], v2 offset:7264\n"
    "v_mfma_f32_16x16x32_bf16 v[92:95], a[28:31], a[116:119], v[92:95]\n"
    "v_mfma_f32_16x16x32_bf16 v[120:123], a[24:27], a[120:123], v[120:123]\n"
    "ds_read_b64_tr_b16 a[14:15], v2 offset:10848\n"
    "v_mfma_f32_16x16x32_bf16 v[120:123], a[28:31], a[124:127], v[120:123]\n"
    "s_waitcnt lgkmcnt(4)\n"
    "v_mfma_f32_16x16x32_bf16 v[40:43], a[0:3], a[96:99], v[40:43]\n"
    "ds_read_b64_tr_b16 a[16:17], v2 offset:128\n"
    "v_mfma_f32_16x16x32_bf16 v[40:43], a[4:7], a[100:103], v[40:43]\n"
    "buffer_load_dwordx4 a[72:75], v4, s[40:43], s60 offen offset:2048 sc1\n"
    "v_mfma_f32_16x16x32_bf16 v[68:71], a[0:3], a[104:107], v[68:71]\n"
    "ds_read_b64_tr_b16 a[18:19], v2 offset:3712\n"
    "v_mfma_f32_16x16x32_bf16 v[68:71], a[4:7], a[108:111], v[68:71]\n"
    "buffer_load_dwordx4 a[76:79], v4, s[40:43], s60 offen offset:3072 sc1\n"
    "v_mfma_f32_16x16x32_bf16 v[96:99], a[0:3], a[112:115], v[96:99]\n"
    "ds_read_b64_tr_b16 a[20:21], v2 offset:7296\n"
    "v_mfma_f32_16x16x32_bf16 v[96:99], a[4:7], a[116:119], v[96:99]\n"
    "v_mfma_f32_16x16x32_bf16 v[124:127], a[0:3], a[120:123], v[124:127]\n"
    "ds_read_b64_tr_b16 a[22:23], v2 offset:10880\n"
    "v_mfma_f32_16x16x32_bf16 v[124:127], a[4:7], a[124:127], v[124:127]\n"
    "s_waitcnt lgkmcnt(4)\n"
    "v_mfma_f32_16x16x32_bf16 v[44:47], a[8:11], a[96:99], v[44:47]\n"
    "ds_read_b64_tr_b16 a[24:25], v2 offset:160\n"
    "v_mfma_f32_16x16x32_bf16 v[44:47], a[12:15], a[100:103], v[44:47]\n"
    "buffer_load_dwordx4 v9, s[44:47], s61 offen offset:1024 lds\n"
    "v_mfma_f32_16x16x32_bf16 v[72:75], a[8:11], a[104:107], v[72:75]\n"
    "ds_read_b64_tr_b16 a[26:27], v2 offset:3744\n"
    "v_mfma_f32_16x16x32_bf16 v[72:75], a[12:15], a[108:111], v[72:75]\n"
    "v_mfma_f32_16x16x32_bf16 v[100:103], a[8:11], a[112:115], v[100:103]\n"
    "ds_read_b64_tr_b16 a[28:29], v2 offset:7328\n"
    "v_mfma_f32_16x16x32_bf16 v[100:103], a[12:15], a[116:119], v[100:103]\n"
    "buffer_load_dwordx4 a[80:83], v5, s[40:43], s60 offen sc1\n"
    "v_mfma_f32_16x16x32_bf16 v[128:131], a[8:11], a[120:123], v[128:131]\n"
    "s_waitcnt vmcnt(21)\n"
    "ds_read_b64_tr_b16 a[30:31], v2 offset:10912\n"
    "v_mfma_f32_16x16x32_bf16 v[128:131], a[12:15], a[124:127], v[128:131]\n"
    "s_waitcnt lgkmcnt(4)\n"
    "v_add_f32 v20, v180, v182\n"
    "v_mfma_f32_16x16x32_bf16 v[48:51], a[16:19], a[96:99], v[48:51]\n"
    "ds_read_b64_tr_b16 a[0:1], v2 offset:192\n"
    "v_mfma_f32_16x16x32_bf16 v[48:51], a[20:23], a[100:103], v[48:51]\n"
    "buffer_load_dwordx4 a[84:87], v5, s[40:43], s60 offen offset:1024 sc1\n"
    "v_mfma_f32_16x16x32_bf16 v[76:79], a[16:19], a[104:107], v[76:79]\n"
    "ds_read_b64_tr_b16 a[2:3], v2 offset:3776\n"
    "v_add_f32 v21, v188, v190\n"
    "v_mfma_f32_16x16x32_bf16 v[76:79], a[20:23], a[108:111], v[76:79]\n"
    "v_add_f32 v22, v196, v198\n"
    "v_mfma_f32_16x16x32_bf16 v[104:107], a[16:19], a[112:115], v[104:107]\n"
    "ds_read_b64_tr_b16 a[4:5], v2 offset:7360\n"
    "v_add_f32 v23, v204, v206\n"
    "v_mfma_f32_16x16x32_bf16 v[104:107], a[20:23], a[116:119], v[104:107]\n"
    "buffer_load_dwordx4 v10, s[44:47], s61 offen offset:2048 lds\n"
    "v_mfma_f32_16x16x32_bf16 v[132:135], a[16:19], a[120:123], v[132:135]\n"
    "ds_read_b64_tr_b16 a[6:7], v2 offset:10944\n"
    "v_mov_b32 v24, v20\n"
    "v_mfma_f32_16x16x32_bf16 v[132:135], a[20:23], a[124:127], v[132:135]\n"
    "s_waitcnt lgkmcnt(4)\n"
    "v_mov_b32 v25, v21\n"
    "v_mfma_f32_16x16x32_bf16 v[52:55], a[24:27], a[96:99], v[52:55]\n"
    "ds_read_b64_tr_b16 a[8:9], v2 offset:14336\n"
    "v_mfma_f32_16x16x32_bf16 v[52:55], a[28:31], a[100:103], v[52:55]\n"
    "v_mov_b32 v26, v22\n"
    "v_mfma_f32_16x16x32_bf16 v[80:83], a[24:27], a[104:107], v[80:83]\n"
    "ds_read_b64_tr_b16 a[10:11], v2 offset:17920\n"
    "v_mfma_f32_16x16x32_bf16 v[80:83], a[28:31], a[108:111], v[80:83]\n"
    "buffer_load_dwordx4 a[88:91], v5, s[40:43], s60 offen offset:2048 sc1\n"
    "v_mfma_f32_16x16x32_bf16 v[108:111], a[24:27], a[112:115], v[108:111]\n"
    "ds_read_b64_tr_b16 a[12:13], v2 offset:21504\n"
    "v_mov_b32 v27, v23\n"
    "v_mfma_f32_16x16x32_bf16 v[108:111], a[28:31], a[116:119], v[108:111]\n"
    "s_nop 1\n"
    "v_mfma_f32_16x16x32_bf16 v[136:139], a[24:27], a[120:123], v[136:139]\n"
    "ds_read_b64_tr_b16 a[14:15], v2 offset:25088\n"
    "v_permlane32_swap_b32 v20, v24\n"
    "v_mfma_f32_16x16x32_bf16 v[136:139], a[28:31], a[124:127], v[136:139]\n"
    "s_waitcnt lgkmcnt(4)\n"
    "v_permlane32_swap_b32 v21, v25\n"
    "v_mfma_f32_16x16x32_bf16 v[56:59], a[0:3], a[96:99], v[56:59]\n"
    "ds_read_b64_tr_b16 a[16:17], v2 offset:14368\n"
    "v_mfma_f32_16x16x32_bf16 v[56:59], a[4:7], a[100:103], v[56:59]\n"
    "buffer_load_dwordx4 a[92:95], v5, s[40:43], s60 offen offset:3072 sc1\n"
    "v_mfma_f32_16x16x32_bf16 v[84:87], a[0:3], a[104:107], v[84:87]\n"
    "s_add_u32 s60, s60, 0x40000\n"
    "ds_read_b64_tr_b16 a[18:19], v2 offset:17952\n"
    "v_mfma_f32_16x16x32_bf16 v[84:87], a[4:7], a[108:111], v[84:87]\n"
    "buffer_load_dwordx4 v11, s[44:47], s61 offen offset:3072 lds\n"
    "v_mfma_f32_16x16x32_bf16 v[112:115], a[0:3], a[112:115], v[112:115]\n"
    "ds_read_b64_tr_b16 a[20:21], v2 offset:21536\n"
    "v_permlane32_swap_b32 v22, v26\n"
    "v_mfma_f32_16x16x32_bf16 v[112:115], a[4:7], a[116:119], v[112:115]\n"
    "v_permlane32_swap_b32 v23, v27\n"
    "v_mfma_f32_16x16x32_bf16 v[140:143], a[0:3], a[120:123], v[140:143]\n"
    "s_nop 0\n"
    "ds_read_b64_tr_b16 a[22:23], v2 offset:25120\n"
    "v_mfma_f32_16x16x32_bf16 v[140:143], a[4:7], a[124:127], v[140:143]\n"
    "s_waitcnt vmcnt(25) lgkmcnt(4)\n"
    "v_add_f32 v20, v20, v24\n"
    "v_mfma_f32_16x16x32_bf16 v[32:35], a[8:11], a[128:131], v[32:35]\n"
    "ds_read_b64_tr_b16 a[24:25], v2 offset:14400\n"
    "v_add_f32 v21, v21, v25\n"
    "v_mfma_f32_16x16x32_bf16 v[32:35], a[12:15], a[132:135], v[32:35]\n"
    "buffer_load_dwordx4 a[96:99], v4, s[40:43], s60 offen sc1\n"
    "v_mfma_f32_16x16x32_bf16 v[60:63], a[8:11], a[136:139], v[60:63]\n"
    "ds_read_b64_tr_b16 a[26:27], v2 offset:17984\n"
    "v_add_f32 v22, v22, v26\n"
    "v_mfma_f32_16x16x32_bf16 v[60:63], a[12:15], a[140:143], v[60:63]\n"
    "v_add_f32 v23, v23, v27\n"
    "v_mfma_f32_16x16x32_bf16 v[88:91], a[8:11], a[144:147], v[88:91]\n"
    "ds_read_b64_tr_b16 a[28:29], v2 offset:21568\n"
    "v_mov_b32 v24, v20\n"
    "v_mfma_f32_16x16x32_bf16 v[88:91], a[12:15], a[148:151], v[88:91]\n"
    "v_mfma_f32_16x16x32_bf16 v[116:119], a[8:11], a[152:155], v[116:119]\n"
    "ds_read_b64_tr_b16 a[30:31], v2 offset:25152\n"
    "v_mov_b32 v25, v21\n"
    "v_mfma_f32_16x16x32_bf16 v[116:119], a[12:15], a[156:159], v[116:119]\n"
    "s_waitcnt lgkmcnt(4)\n"
    "v_mfma_f32_16x16x32_bf16 v[36:39], a[16:19], a[128:131], v[36:39]\n"
    "ds_read_b64_tr_b16 a[0:1], v2 offset:14432\n"
    "v_mov_b32 v26, v22\n"
    "v_mfma_f32_16x16x32_bf16 v[36:39], a[20:23], a[132:135], v[36:39]\n"
    "buffer_load_dwordx4 a[100:103], v4, s[40:43], s60 offen offset:1024 sc1\n"
    "v_mfma_f32_16x16x32_bf16 v[64:67], a[16:19], a[136:139], v[64:67]\n"
    "s_add_u32 m0, s63, s75\n"
    "ds_read_b64_tr_b16 a[2:3], v2 offset:18016\n"
    "v_mfma_f32_16x16x32_bf16 v[64:67], a[20:23], a[140:143], v[64:67]\n"
    "buffer_load_dwordx4 v12, s[44:47], s61 offen lds\n"
    "v_mfma_f32_16x16x32_bf16 v[92:95], a[16:19], a[144:147], v[92:95]\n"
    "ds_read_b64_tr_b16 a[4:5], v2 offset:21600\n"
    "v_mov_b32 v27, v23\n"
    "v_mfma_f32_16x16x32_bf16 v[92:95], a[20:23], a[148:151], v[92:95]\n"
    "s_nop 1\n"
    "v_mfma_f32_16x16x32_bf16 v[120:123], a[16:19], a[152:155], v[120:123]\n"
    "ds_read_b64_tr_b16 a[6:7], v2 offset:25184\n"
    "v_permlane16_swap_b32 v20, v24\n"
    "v_mfma_f32_16x16x32_bf16 v[120:123], a[20:23], a[156:159], v[120:123]\n"
    "s_waitcnt lgkmcnt(4)\n"
    "v_permlane16_swap_b32 v21, v25\n"
    "v_mfma_f32_16x16x32_bf16 v[40:43], a[24:27], a[128:131], v[40:43]\n"
    "ds_read_b64_tr_b16 a[8:9], v2 offset:14464\n"
    "v_permlane16_swap_b32 v22, v26\n"
    "v_mfma_f32_16x16x32_bf16 v[40:43], a[28:31], a[132:135], v[40:43]\n"
    "buffer_load_dwordx4 a[104:107], v4, s[40:43], s60 offen offset:2048 sc1\n"
    "v_mfma_f32_16x16x32_bf16 v[68:71], a[24:27], a[136:139], v[68:71]\n"
    "ds_read_b64_tr_b16 a[10:11], v2 offset:18048\n"
    "v_permlane16_swap_b32 v23, v27\n"
    "v_mfma_f32_16x16x32_bf16 v[68:71], a[28:31], a[140:143], v[68:71]\n"
    "s_nop 0\n"
    "v_mfma_f32_16x16x32_bf16 v[96:99], a[24:27], a[144:147], v[96:99]\n"
    "ds_read_b64_tr_b16 a[12:13], v2 offset:21632\n"
    "v_mfma_f32_16x16x32_bf16 v[96:99], a[28:31], a[148:151], v[96:99]\n"
    "buffer_load_dwordx4 a[108:111], v4, s[40:43], s60 offen offset:3072 sc1\n"
    "v_mfma_f32_16x16x32_bf16 v[124:127], a[24:27], a[152:155], v[124:127]\n"
    "ds_read_b64_tr_b16 a[14:15], v2 offset:25216\n"
    "v_add_f32 v20, v20, v24\n"
    "v_mfma_f32_16x16x32_bf16 v[124:127], a[28:31], a[156:159], v[124:127]\n"
    "s_waitcnt lgkmcnt(4)\n"
    "v_add_f32 v21, v21, v25\n"
    "v_mfma_f32_16x16x32_bf16 v[44:47], a[0:3], a[128:131], v[44:47]\n"
    "ds_read_b64_tr_b16 a[16:17], v2 offset:14496\n"
    "v_add_f32 v22, v22, v26\n"
    "v_mfma_f32_16x16x32_bf16 v[44:47], a[4:7], a[132:135], v[44:47]\n"
    "v_mfma_f32_16x16x32_bf16 v[72:75], a[0:3], a[136:139], v[72:75]\n"
    "ds_read_b64_tr_b16 a[18:19], v2 offset:18080\n"
    "v_add_f32 v23, v23, v27\n"
    "v_mfma_f32_16x16x32_bf16 v[72:75], a[4:7], a[140:143], v[72:75]\n"
    "buffer_load_dwordx4 a[112:115], v5, s[40:43], s60 offen sc1\n"
    "v_mfma_f32_16x16x32_bf16 v[100:103], a[0:3], a[144:147], v[100:103]\n"
    "ds_read_b64_tr_b16 a[20:21], v2 offset:21664\n"
    "v_mul_f32 v20, 0x3e000000, v20\n"
    "v_mfma_f32_16x16x32_bf16 v[100:103], a[4:7], a[148:151], v[100:103]\n"
    "v_mul_f32 v21, 0x3e000000, v21\n"
    "v_mfma_f32_16x16x32_bf16 v[128:131], a[0:3], a[152:155], v[128:131]\n"
    "ds_read_b64_tr_b16 a[22:23], v2 offset:25248\n"
    "v_mul_f32 v22, 0x3e000000, v22\n"
    "v_mfma_f32_16x16x32_bf16 v[128:131], a[4:7], a[156:159], v[128:131]\n"
    "s_waitcnt lgkmcnt(4)\n"
    "v_mfma_f32_16x16x32_bf16 v[48:51], a[8:11], a[128:131], v[48:51]\n"
    "ds_read_b64_tr_b16 a[24:25], v2 offset:14528\n"
    "v_mul_f32 v23, 0x3e000000, v23\n"
    "v_mfma_f32_16x16x32_bf16 v[48:51], a[12:15], a[132:135], v[48:51]\n"
    "buffer_load_dwordx4 v13, s[44:47], s61 offen offset:1024 lds\n"
    "v_mfma_f32_16x16x32_bf16 v[76:79], a[8:11], a[136:139], v[76:79]\n"
    "s_add_u32 s64, s64, 1\n"
    "ds_read_b64_tr_b16 a[26:27], v2 offset:18112\n"
    "v_mfma_f32_16x16x32_bf16 v[76:79], a[12:15], a[140:143], v[76:79]\n"
    "buffer_load_dwordx4 a[116:119], v5, s[40:43], s60 offen offset:1024 sc1\n"
    "v_mfma_f32_16x16x32_bf16 v[104:107], a[8:11], a[144:147], v[104:107]\n"
    "ds_read_b64_tr_b16 a[28:29], v2 offset:21696\n"
    "v_add_f32 v28, v181, v183\n"
    "v_mfma_f32_16x16x32_bf16 v[104:107], a[12:15], a[148:151], v[104:107]\n"
    "s_cmp_lt_u32 s64, 5\n"
    "s_cselect_b32 s64, s64, 0\n"
    "s_cselect_b32 s65, 0x7000, s73\n"
    "v_mfma_f32_16x16x32_bf16 v[132:135], a[8:11], a[152:155], v[132:135]\n"
    "ds_read_b64_tr_b16 a[30:31], v2 offset:25280\n"
    "v_add_u32 v2, s65, v2\n"
    "v_mfma_f32_16x16x32_bf16 v[132:135], a[12:15], a[156:159], v[132:135]\n"
    "s_waitcnt lgkmcnt(4)\n"
    "s_barrier\n"
    "v_add_f32 v29, v189, v191\n"
    "v_mfma_f32_16x16x32_bf16 v[52:55], a[16:19], a[128:131], v[52:55]\n"
    "ds_read_b64_tr_b16 a[0:1], v2\n"
    "v_add_f32 v30, v197, v199\n"
    "v_mfma_f32_16x16x32_bf16 v[52:55], a[20:23], a[132:135], v[52:55]\n"
    "buffer_load_dwordx4 a[120:123], v5, s[40:43], s60 offen offset:2048 sc1\n"
    "v_mfma_f32_16x16x32_bf16 v[80:83], a[16:19], a[136:139], v[80:83]\n"
    "ds_read_b64_tr_b16 a[2:3], v2 offset:3584\n"
    "v_add_f32 v31, v205, v207\n"
    "v_mfma_f32_16x16x32_bf16 v[80:83], a[20:23], a[140:143], v[80:83]\n"
    "v_sub_f32 v180, v180, v20\n"
    "v_sub_f32 v188, v188, v21\n"
    "v_mfma_f32_16x16x32_bf16 v[108:111], a[16:19], a[144:147], v[108:111]\n"
    "ds_read_b64_tr_b16 a[4:5], v2 offset:7168\n"
    "v_sub_f32 v196, v196, v22\n"
    "v_mfma_f32_16x16x32_bf16 v[108:111], a[20:23], a[148:151], v[108:111]\n"
    "v_sub_f32 v204, v204, v23\n"
    "v_mfma_f32_16x16x32_bf16 v[136:139], a[16:19], a[152:155], v[136:139]\n"
    "ds_read_b64_tr_b16 a[6:7], v2 offset:10752\n"
    "v_mfma_f32_16x16x32_bf16 v[136:139], a[20:23], a[156:159], v[136:139]\n"
    "s_waitcnt lgkmcnt(4)\n"
    "v_mul_f32 v181, 0x43000000, v180\n"
    "v_mfma_f32_16x16x32_bf16 v[56:59], a[24:27], a[128:131], v[56:59]\n"
    "ds_read_b64_tr_b16 a[8:9], v2 offset:32\n"
    "v_mul_f32 v189, 0x43000000, v188\n"
    "v_mfma_f32_16x16x32_bf16 v[56:59], a[28:31], a[132:135], v[56:59]\n"
    "buffer_load_dwordx4 a[124:127], v5, s[40:43], s60 offen offset:3072 sc1\n"
    "v_mfma_f32_16x16x32_bf16 v[84:87], a[24:27], a[136:139], v[84:87]\n"
    "s_add_u32 s60, s60, 0x40000\n"
    "ds_read_b64_tr_b16 a[10:11], v2 offset:3616\n"
    "v_mfma_f32_16x16x32_bf16 v[84:87], a[28:31], a[140:143], v[84:87]\n"
    "buffer_load_dwordx4 v14, s[44:47], s61 offen offset:2048 lds\n"
    "v_mfma_f32_16x16x32_bf16 v[112:115], a[24:27], a[144:147], v[112:115]\n"
    "s_add_u32 s61, s61, 0xe0000\n"
    "ds_read_b64_tr_b16 a[12:13], v2 offset:7200\n"
    "v_mfma_f32_16x16x32_bf16 v[112:115], a[28:31], a[148:151], v[112:115]\n"
    "s_add_u32 s63, s63, 0x7000\n"
    "s_cmp_lt_u32 s63, 0x23000\n"
    "s_cselect_b32 s63, s63, 0\n"
    "v_mfma_f32_16x16x32_bf16 v[140:143], a[24:27], a[152:155], v[140:143]\n"
    "ds_read_b64_tr_b16 a[14:15], v2 offset:10784\n"
    "v_mul_f32 v197, 0x43000000, v196\n"
    "v_mfma_f32_16x16x32_bf16 v[140:143], a[28:31], a[156:159], v[140:143]\n"
    "s_waitcnt vmcnt(24) lgkmcnt(4)\n"
    "v_mfma_f32_16x16x32_bf16 v[32:35], a[0:3], a[32:35], v[32:35]\n"
    "ds_read_b64_tr_b16 a[16:17], v2 offset:64\n"
    "v_mfma_f32_16x16x32_bf16 v[32:35], a[4:7], a[36:39], v[32:35]\n"
    "buffer_load_dwordx4 a[128:131], v4, s[40:43], s60 offen sc1\n"
    "v_mfma_f32_16x16x32_bf16 v[60:63], a[0:3], a[40:43], v[60:63]\n"
    "ds_read_b64_tr_b16 a[18:19], v2 offset:3648\n"
    "v_mul_f32 v205, 0x43000000, v204\n"
    "v_mfma_f32_16x16x32_bf16 v[60:63], a[4:7], a[44:47], v[60:63]\n"
    "v_fmac_f32 v28, v181, v180\n"
    "v_fmac_f32 v29, v189, v188\n"
    "v_mfma_f32_16x16x32_bf16 v[88:91], a[0:3], a[48:51], v[88:91]\n"
    "ds_read_b64_tr_b16 a[20:21], v2 offset:7232\n"
    "v_fmac_f32 v30, v197, v196\n"
    "v_mfma_f32_16x16x32_bf16 v[88:91], a[4:7], a[52:55], v[88:91]\n"
    "buffer_load_dwordx4 a[132:135], v4, s[40:43], s60 offen offset:1024 sc1\n"
    "v_mfma_f32_16x16x32_bf16 v[116:119], a[0:3], a[56:59], v[116:119]\n"
    "ds_read_b64_tr_b16 a[22:23], v2 offset:10816\n"
    "v_fmac_f32 v31, v205, v204\n"
    "v_mfma_f32_16x16x32_bf16 v[116:119], a[4:7], a[60:63], v[116:119]\n"
    "s_waitcnt lgkmcnt(4)\n"
    "v_sub_f32 v182, v182, v20\n"
    "v_sub_f32 v190, v190, v21\n"
    "v_mfma_f32_16x16x32_bf16 v[36:39], a[8:11], a[32:35], v[36:39]\n"
    "ds_read_b64_tr_b16 a[24:25], v2 offset:96\n"
    "v_sub_f32 v198, v198, v22\n"
    "v_mfma_f32_16x16x32_bf16 v[36:39], a[12:15], a[36:39], v[36:39]\n"
    "s_add_u32 m0, s63, s71\n"
    "v_sub_f32 v206, v206, v23\n"
    "v_mfma_f32_16x16x32_bf16 v[64:67], a[8:11], a[40:43], v[64:67]\n"
    "ds_read_b64_tr_b16 a[26:27], v2 offset:3680\n"
    "v_mul_f32 v183, 0x43000000, v182\n"
    "v_mfma_f32_16x16x32_bf16 v[64:67], a[12:15], a[44:47], v[64:67]\n"
    "buffer_load_dwordx4 v8, s[44:47], s61 offen lds\n"
    "v_mfma_f32_16x16x32_bf16 v[92:95], a[8:11], a[48:51], v[92:95]\n"
    "ds_read_b64_tr_b16 a[28:29], v2 offset:7264\n"
    "v_mul_f32 v191, 0x43000000, v190\n"
    "v_mfma_f32_16x16x32_bf16 v[92:95], a[12:15], a[52:55], v[92:95]\n"
    "v_mul_f32 v199, 0x43000000, v198\n"
    "v_mul_f32 v207, 0x43000000, v206\n"
    "v_mfma_f32_16x16x32_bf16 v[120:123], a[8:11], a[56:59], v[120:123]\n"
    "ds_read_b64_tr_b16 a[30:31], v2 offset:10848\n"
    "v_fmac_f32 v28, v183, v182\n"
    "v_mfma_f32_16x16x32_bf16 v[120:123], a[12:15], a[60:63], v[120:123]\n"
    "s_waitcnt lgkmcnt(4)\n"
    "v_fmac_f32 v29, v191, v190\n"
    "v_mfma_f32_16x16x32_bf16 v[40:43], a[16:19], a[32:35], v[40:43]\n"
    "ds_read_b64_tr_b16 a[0:1], v2 offset:128\n"
    "v_fmac_f32 v30, v199, v198\n"
    "v_mfma_f32_16x16x32_bf16 v[40:43], a[20:23], a[36:39], v[40:43]\n"
    "buffer_load_dwordx4 a[136:139], v4, s[40:43], s60 offen offset:2048 sc1\n"
    "v_mfma_f32_16x16x32_bf16 v[68:71], a[16:19], a[40:43], v[68:71]\n"
    "ds_read_b64_tr_b16 a[2:3], v2 offset:3712\n"
    "v_fmac_f32 v31, v207, v206\n"
    "v_mfma_f32_16x16x32_bf16 v[68:71], a[20:23], a[44:47], v[68:71]\n"
    "buffer_load_dwordx4 a[140:143], v4, s[40:43], s60 offen offset:3072 sc1\n"
    "v_mfma_f32_16x16x32_bf16 v[96:99], a[16:19], a[48:51], v[96:99]\n"
    "ds_read_b64_tr_b16 a[4:5], v2 offset:7296\n"
    "v_mov_b32 v24, v28\n"
    "v_mfma_f32_16x16x32_bf16 v[96:99], a[20:23], a[52:55], v[96:99]\n"
    "v_mov_b32 v25, v29\n"
    "v_mov_b32 v26, v30\n"
    "v_mfma_f32_16x16x32_bf16 v[124:127], a[16:19], a[56:59], v[124:127]\n"
    "ds_read_b64_tr_b16 a[6:7], v2 offset:10880\n"
    "v_mov_b32 v27, v31\n"
    "v_mfma_f32_16x16x32_bf16 v[124:127], a[20:23], a[60:63], v[124:127]\n"
    "s_waitcnt lgkmcnt(4)\n"
    "s_nop 1\n"
    "v_mfma_f32_16x16x32_bf16 v[44:47], a[24:27], a[32:35], v[44:47]\n"
    "ds_read_b64_tr_b16 a[8:9], v2 offset:160\n"
    "v_permlane32_swap_b32 v28, v24\n"
    "v_mfma_f32_16x16x32_bf16 v[44:47], a[28:31], a[36:39], v[44:47]\n"
    "buffer_load_dwordx4 v9, s[44:47], s61 offen offset:1024 lds\n"
    "v_mfma_f32_16x16x32_bf16 v[72:75], a[24:27], a[40:43], v[72:75]\n"
    "ds_read_b64_tr_b16 a[10:11], v2 offset:3744\n"
    "v_permlane32_swap_b32 v29, v25\n"
    "v_mfma_f32_16x16x32_bf16 v[72:75], a[28:31], a[44:47], v[72:75]\n"
    "v_permlane32_swap_b32 v30, v26\n"
    "v_permlane32_swap_b32 v31, v27\n"
    "v_mfma_f32_16x16x32_bf16 v[100:103], a[24:27], a[48:51], v[100:103]\n"
    "s_nop 0\n"
    "ds_read_b64_tr_b16 a[12:13], v2 offset:7328\n"
    "v_mfma_f32_16x16x32_bf16 v[100:103], a[28:31], a[52:55], v[100:103]\n"
    "buffer_load_dwordx4 a[144:147], v5, s[40:43], s60 offen sc1\n"
    "v_mfma_f32_16x16x32_bf16 v[128:131], a[24:27], a[56:59], v[128:131]\n"
    "ds_read_b64_tr_b16 a[14:15], v2 offset:10912\n"
    "v_add_f32 v28, v28, v24\n"
    "v_mfma_f32_16x16x32_bf16 v[128:131], a[28:31], a[60:63], v[128:131]\n"
    "s_waitcnt lgkmcnt(4)\n"
    "v_add_f32 v29, v29, v25\n"
    "v_add_f32 v30, v30, v26\n"
    "v_mfma_f32_16x16x32_bf16 v[48:51], a[0:3], a[32:35], v[48:51]\n"
    "ds_read_b64_tr_b16 a[16:17], v2 offset:192\n"
    "v_add_f32 v31, v31, v27\n"
    "v_mfma_f32_16x16x32_bf16 v[48:51], a[4:7], a[36:39], v[48:51]\n"
    "buffer_load_dwordx4 a[148:151], v5, s[40:43], s60 offen offset:1024 sc1\n"
    "v_mfma_f32_16x16x32_bf16 v[76:79], a[0:3], a[40:43], v[76:79]\n"
    "ds_read_b64_tr_b16 a[18:19], v2 offset:3776\n"
    "v_mov_b32 v24, v28\n"
    "v_mfma_f32_16x16x32_bf16 v[76:79], a[4:7], a[44:47], v[76:79]\n"
    "v_mov_b32 v25, v29\n"
    "v_mov_b32 v26, v30\n"
    "v_mfma_f32_16x16x32_bf16 v[104:107], a[0:3], a[48:51], v[104:107]\n"
    "ds_read_b64_tr_b16 a[20:21], v2 offset:7360\n"
    "v_mov_b32 v27, v31\n"
    "v_mfma_f32_16x16x32_bf16 v[104:107], a[4:7], a[52:55], v[104:107]\n"
    "buffer_load_dwordx4 v10, s[44:47], s61 offen offset:2048 lds\n"
    "v_mfma_f32_16x16x32_bf16 v[132:135], a[0:3], a[56:59], v[132:135]\n"
    "s_nop 1\n"
    "ds_read_b64_tr_b16 a[22:23], v2 offset:10944\n"
    "v_mfma_f32_16x16x32_bf16 v[132:135], a[4:7], a[60:63], v[132:135]\n"
    "s_waitcnt lgkmcnt(4)\n"
    "v_permlane16_swap_b32 v28, v24\n"
    "v_permlane16_swap_b32 v29, v25\n"
    "v_mfma_f32_16x16x32_bf16 v[52:55], a[8:11], a[32:35], v[52:55]\n"
    "ds_read_b64_tr_b16 a[24:25], v2 offset:14336\n"
    "v_permlane16_swap_b32 v30, v26\n"
    "v_mfma_f32_16x16x32_bf16 v[52:55], a[12:15], a[36:39], v[52:55]\n"
    "v_permlane16_swap_b32 v31, v27\n"
    "v_mfma_f32_16x16x32_bf16 v[80:83], a[8:11], a[40:43], v[80:83]\n"
    "s_nop 0\n"
    "ds_read_b64_tr_b16 a[26:27], v2 offset:17920\n"
    "v_mfma_f32_16x16x32_bf16 v[80:83], a[12:15], a[44:47], v[80:83]\n"
    "buffer_load_dwordx4 a[152:155], v5, s[40:43], s60 offen offset:2048 sc1\n"
    "v_mfma_f32_16x16x32_bf16 v[108:111], a[8:11], a[48:51], v[108:111]\n"
    "ds_read_b64_tr_b16 a[28:29], v2 offset:21504\n"
    "v_add_f32 v28, v28, v24\n"
    "v_mfma_f32_16x16x32_bf16 v[108:111], a[12:15], a[52:55], v[108:111]\n"
    "ds_read_b128 v[144:147], v19\n"
    "v_add_f32 v29, v29, v25\n"
    "v_add_f32 v30, v30, v26\n"
    "v_mfma_f32_16x16x32_bf16 v[136:139], a[8:11], a[56:59], v[136:139]\n"
    "ds_read_b64_tr_b16 a[30:31], v2 offset:25088\n"
    "v_add_f32 v31, v31, v27\n"
    "v_mfma_f32_16x16x32_bf16 v[136:139], a[12:15], a[60:63], v[136:139]\n"
    "s_waitcnt lgkmcnt(5)\n"
    "v_mov_b32 v24, 0x3727c5ac\n"
    "v_mov_b32 v25, 0x3a800000\n"
    "v_mfma_f32_16x16x32_bf16 v[56:59], a[16:19], a[32:35], v[56:59]\n"
    "ds_read_b64_tr_b16 a[0:1], v2 offset:14368\n"
    "v_fma_f32 v28, v28, v25, v24\n"
    "v_mfma_f32_16x16x32_bf16 v[56:59], a[20:23], a[36:39], v[56:59]\n"
    "buffer_load_dwordx4 a[156:159], v5, s[40:43], s60 offen offset:3072 sc1\n"
    "v_mfma_f32_16x16x32_bf16 v[84:87], a[16:19], a[40:43], v[84:87]\n"
    "s_add_u32 s60, s60, 0x40000\n"
    "ds_read_b64_tr_b16 a[2:3], v2 offset:17952\n"
    "v_mfma_f32_16x16x32_bf16 v[84:87], a[20:23], a[44:47], v[84:87]\n"
    "buffer_load_dwordx4 v11, s[44:47], s61 offen offset:3072 lds\n"
    "v_mfma_f32_16x16x32_bf16 v[112:115], a[16:19], a[48:51], v[112:115]\n"
    "ds_read_b64_tr_b16 a[4:5], v2 offset:21536\n"
    "v_fma_f32 v29, v29, v25, v24\n"
    "v_mfma_f32_16x16x32_bf16 v[112:115], a[20:23], a[52:55], v[112:115]\n"
    "ds_read_b128 v[148:151], v19 offset:64\n"
    "v_fma_f32 v30, v30, v25, v24\n"
    "v_fma_f32 v31, v31, v25, v24\n"
    "v_mfma_f32_16x16x32_bf16 v[140:143], a[16:19], a[56:59], v[140:143]\n"
    "ds_read_b64_tr_b16 a[6:7], v2 offset:25120\n"
    "v_mfma_f32_16x16x32_bf16 v[140:143], a[20:23], a[60:63], v[140:143]\n"
    "s_waitcnt vmcnt(24) lgkmcnt(5)\n"
    "v_rsq_f32 v28, v28\n"
    "v_mfma_f32_16x16x32_bf16 v[32:35], a[24:27], a[64:67], v[32:35]\n"
    "ds_read_b64_tr_b16 a[8:9], v2 offset:14400\n"
    "v_mfma_f32_16x16x32_bf16 v[32:35], a[28:31], a[68:71], v[32:35]\n"
    "buffer_load_dwordx4 a[32:35], v4, s[40:43], s60 offen sc1\n"
    "v_mfma_f32_16x16x32_bf16 v[60:63], a[24:27], a[72:75], v[60:63]\n"
    "ds_read_b64_tr_b16 a[10:11], v2 offset:17984\n"
    "v_mfma_f32_16x16x32_bf16 v[60:63], a[28:31], a[76:79], v[60:63]\n"
    "v_rsq_f32 v29, v29\n"
    "v_mfma_f32_16x16x32_bf16 v[88:91], a[24:27], a[80:83], v[88:91]\n"
    "ds_read_b64_tr_b16 a[12:13], v2 offset:21568\n"
    "ds_read_b128 v[152:155], v19 offset:128\n"
    "v_mfma_f32_16x16x32_bf16 v[88:91], a[28:31], a[84:87], v[88:91]\n"
    "v_rsq_f32 v30, v30\n"
    "v_mfma_f32_16x16x32_bf16 v[116:119], a[24:27], a[88:91], v[116:119]\n"
    "ds_read_b64_tr_b16 a[14:15], v2 offset:25152\n"
    "v_mfma_f32_16x16x32_bf16 v[116:119], a[28:31], a[92:95], v[116:119]\n"
    "s_waitcnt lgkmcnt(5)\n"
    "v_rsq_f32 v31, v31\n"
    "v_mfma_f32_16x16x32_bf16 v[36:39], a[0:3], a[64:67], v[36:39]\n"
    "ds_read_b64_tr_b16 a[16:17], v2 offset:14432\n"
    "v_mul_f32 v172, 0xbfb8aa3b, v28\n"
    "v_mfma_f32_16x16x32_bf16 v[36:39], a[4:7], a[68:71], v[36:39]\n"
    "buffer_load_dwordx4 a[36:39], v4, s[40:43], s60 offen offset:1024 sc1\n"
    "v_mfma_f32_16x16x32_bf16 v[64:67], a[0:3], a[72:75], v[64:67]\n"
    "s_add_u32 m0, s63, s75\n"
    "ds_read_b64_tr_b16 a[18:19], v2 offset:18016\n"
    "v_mfma_f32_16x16x32_bf16 v[64:67], a[4:7], a[76:79], v[64:67]\n"
    "buffer_load_dwordx4 v12, s[44:47], s61 offen lds\n"
    "v_mfma_f32_16x16x32_bf16 v[92:95], a[0:3], a[80:83], v[92:95]\n"
    "ds_read_b64_tr_b16 a[20:21], v2 offset:21600\n"
    "v_mul_f32 v174, 0xbfb8aa3b, v29\n"
    "v_mfma_f32_16x16x32_bf16 v[92:95], a[4:7], a[84:87], v[92:95]\n"
    "ds_read_b128 v[156:159], v19 offset:192\n"
    "v_mul_f32 v176, 0xbfb8aa3b, v30\n"
    "v_mul_f32 v178, 0xbfb8aa3b, v31\n"
    "v_mfma_f32_16x16x32_bf16 v[120:123], a[0:3], a[88:91], v[120:123]\n"
    "ds_read_b64_tr_b16 a[22:23], v2 offset:25184\n"
    "v_mov_b32 v16, 0xbfb8aa3b\n"
    "v_mfma_f32_16x16x32_bf16 v[120:123], a[4:7], a[92:95], v[120:123]\n"
    "s_waitcnt lgkmcnt(5)\n"
    "v_mfma_f32_16x16x32_bf16 v[40:43], a[8:11], a[64:67], v[40:43]\n"
    "ds_read_b64_tr_b16 a[24:25], v2 offset:14464\n"
    "v_mfma_f32_16x16x32_bf16 v[40:43], a[12:15], a[68:71], v[40:43]\n"
    "buffer_load_dwordx4 a[40:43], v4, s[40:43], s60 offen offset:2048 sc1\n"
    "v_mfma_f32_16x16x32_bf16 v[68:71], a[8:11], a[72:75], v[68:71]\n"
    "ds_read_b64_tr_b16 a[26:27], v2 offset:18048\n"
    "v_mfma_f32_16x16x32_bf16 v[68:71], a[12:15], a[76:79], v[68:71]\n"
    "ds_read_b128 v[160:163], v19 offset:256\n"
    "v_mfma_f32_16x16x32_bf16 v[96:99], a[8:11], a[80:83], v[96:99]\n"
    "ds_read_b64_tr_b16 a[28:29], v2 offset:21632\n"
    "v_mfma_f32_16x16x32_bf16 v[96:99], a[12:15], a[84:87], v[96:99]\n"
    "buffer_load_dwordx4 a[44:47], v4, s[40:43], s60 offen offset:3072 sc1\n"
    "v_mfma_f32_16x16x32_bf16 v[124:127], a[8:11], a[88:91], v[124:127]\n"
    "ds_read_b64_tr_b16 a[30:31], v2 offset:25216\n"
    "v_mfma_f32_16x16x32_bf16 v[124:127], a[12:15], a[92:95], v[124:127]\n"
    "s_waitcnt lgkmcnt(5)\n"
    "v_mfma_f32_16x16x32_bf16 v[44:47], a[16:19], a[64:67], v[44:47]\n"
    "ds_read_b64_tr_b16 a[0:1], v2 offset:14496\n"
    "v_mfma_f32_16x16x32_bf16 v[44:47], a[20:23], a[68:71], v[44:47]\n"
    "ds_read_b128 v[164:167], v19 offset:320\n"
    "v_mfma_f32_16x16x32_bf16 v[72:75], a[16:19], a[72:75], v[72:75]\n"
    "ds_read_b64_tr_b16 a[2:3], v2 offset:18080\n"
    "v_mfma_f32_16x16x32_bf16 v[72:75], a[20:23], a[76:79], v[72:75]\n"
    "buffer_load_dwordx4 a[48:51], v5, s[40:43], s60 offen sc1\n"
    "v_mfma_f32_16x16x32_bf16 v[100:103], a[16:19], a[80:83], v[100:103]\n"
    "ds_read_b64_tr_b16 a[4:5], v2 offset:21664\n"
    "v_mfma_f32_16x16x32_bf16 v[100:103], a[20:23], a[84:87], v[100:103]\n"
    "v_mfma_f32_16x16x32_bf16 v[128:131], a[16:19], a[88:91], v[128:131]\n"
    "ds_read_b64_tr_b16 a[6:7], v2 offset:25248\n"
    "v_mfma_f32_16x16x32_bf16 v[128:131], a[20:23], a[92:95], v[128:131]\n"
    "s_waitcnt lgkmcnt(5)\n"
    "ds_read_b128 v[168:171], v15\n"
    "v_mfma_f32_16x16x32_bf16 v[48:51], a[24:27], a[64:67], v[48:51]\n"
    "ds_read_b64_tr_b16 a[8:9], v2 offset:14528\n"
    "v_mfma_f32_16x16x32_bf16 v[48:51], a[28:31], a[68:71], v[48:51]\n"
    "buffer_load_dwordx4 v13, s[44:47], s61 offen offset:1024 lds\n"
    "v_mfma_f32_16x16x32_bf16 v[76:79], a[24:27], a[72:75], v[76:79]\n"
    "s_add_u32 s64, s64, 1\n"
    "ds_read_b64_tr_b16 a[10:11], v2 offset:18112\n"
    "v_mfma_f32_16x16x32_bf16 v[76:79], a[28:31], a[76:79], v[76:79]\n"
    "buffer_load_dwordx4 a[52:55], v5, s[40:43], s60 offen offset:1024 sc1\n"
    "v_mfma_f32_16x16x32_bf16 v[104:107], a[24:27], a[80:83], v[104:107]\n"
    "ds_read_b64_tr_b16 a[12:13], v2 offset:21696\n"
    "v_mfma_f32_16x16x32_bf16 v[104:107], a[28:31], a[84:87], v[104:107]\n"
    "s_cmp_lt_u32 s64, 5\n"
    "s_cselect_b32 s64, s64, 0\n"
    "s_cselect_b32 s65, 0x7000, s73\n"
    "v_mfma_f32_16x16x32_bf16 v[132:135], a[24:27], a[88:91], v[132:135]\n"
    "ds_read_b64_tr_b16 a[14:15], v2 offset:25280\n"
    "v_add_u32 v2, s65, v2\n"
    "v_mfma_f32_16x16x32_bf16 v[132:135], a[28:31], a[92:95], v[132:135]\n"
    "s_waitcnt lgkmcnt(5)\n"
    "s_barrier\n"
    "s_waitcnt lgkmcnt(4)\n"
    "v_mfma_f32_16x16x32_bf16 v[52:55], a[0:3], a[64:67], v[52:55]\n"
    "ds_read_b64_tr_b16 a[16:17], v2\n"
    "v_mfma_f32_16x16x32_bf16 v[52:55], a[4:7], a[68:71], v[52:55]\n"
    "buffer_load_dwordx4 a[56:59], v5, s[40:43], s60 offen offset:2048 sc1\n"
    "v_mfma_f32_16x16x32_bf16 v[80:83], a[0:3], a[72:75], v[80:83]\n"
    "ds_read_b64_tr_b16 a[18:19], v2 offset:3584\n"
    "v_mfma_f32_16x16x32_bf16 v[80:83], a[4:7], a[76:79], v[80:83]\n"
    "v_mfma_f32_16x16x32_bf16 v[108:111], a[0:3], a[80:83], v[108:111]\n"
    "ds_read_b64_tr_b16 a[20:21], v2 offset:7168\n"
    "v_mfma_f32_16x16x32_bf16 v[108:111], a[4:7], a[84:87], v[108:111]\n"
    "v_mfma_f32_16x16x32_bf16 v[136:139], a[0:3], a[88:91], v[136:139]\n"
    "ds_read_b64_tr_b16 a[22:23], v2 offset:10752\n"
    "v_mfma_f32_16x16x32_bf16 v[136:139], a[4:7], a[92:95], v[136:139]\n"
    "s_waitcnt lgkmcnt(4)\n"
    "v_mfma_f32_16x16x32_bf16 v[56:59], a[8:11], a[64:67], v[56:59]\n"
    "ds_read_b64_tr_b16 a[24:25], v2 offset:32\n"
    "v_mfma_f32_16x16x32_bf16 v[56:59], a[12:15], a[68:71], v[56:59]\n"
    "buffer_load_dwordx4 a[60:63], v5, s[40:43], s60 offen offset:3072 sc1\n"
    "v_mfma_f32_16x16x32_bf16 v[84:87], a[8:11], a[72:75], v[84:87]\n"
    "s_add_u32 s60, s60, 0x40000\n"
    "ds_read_b64_tr_b16 a[26:27], v2 offset:3616\n"
    "v_mfma_f32_16x16x32_bf16 v[84:87], a[12:15], a[76:79], v[84:87]\n"
    "buffer_load_dwordx4 v14, s[44:47], s61 offen offset:2048 lds\n"
    "v_mfma_f32_16x16x32_bf16 v[112:115], a[8:11], a[80:83], v[112:115]\n"
    "s_add_u32 s61, s61, 0xe0000\n"
    "ds_read_b64_tr_b16 a[28:29], v2 offset:7200\n"
    "v_mfma_f32_16x16x32_bf16 v[112:115], a[12:15], a[84:87], v[112:115]\n"
    "s_add_u32 s63, s63, 0x7000\n"
    "s_cmp_lt_u32 s63, 0x23000\n"
    "s_cselect_b32 s63, s63, 0\n"
    "v_mfma_f32_16x16x32_bf16 v[140:143], a[8:11], a[88:91], v[140:143]\n"
    "ds_read_b64_tr_b16 a[30:31], v2 offset:10784\n"
    "v_mfma_f32_16x16x32_bf16 v[140:143], a[12:15], a[92:95], v[140:143]\n"
    "s_waitcnt vmcnt(24) lgkmcnt(4)\n"
    "v_mfma_f32_16x16x32_bf16 v[32:35], a[16:19], a[96:99], v[32:35]\n"
    "ds_read_b64_tr_b16 a[0:1], v2 offset:64\n"
    "v_fma_f32 v48, -v20, v160, v48\n"
    "v_mfma_f32_16x16x32_bf16 v[32:35], a[20:23], a[100:103], v[32:35]\n"
    "buffer_load_dwordx4 a[64:67], v4, s[40:43], s60 offen sc1\n"
    "v_mfma_f32_16x16x32_bf16 v[60:63], a[16:19], a[104:107], v[60:63]\n"
    "ds_read_b64_tr_b16 a[2:3], v2 offset:3648\n"
    "v_fma_f32 v49, -v20, v161, v49\n"
    "v_mfma_f32_16x16x32_bf16 v[60:63], a[20:23], a[108:111], v[60:63]\n"
    "v_fma_f32 v50, -v20, v162, v50\n"
    "v_fma_f32 v51, -v20, v163, v51\n"
    "v_mfma_f32_16x16x32_bf16 v[88:91], a[16:19], a[112:115], v[88:91]\n"
    "ds_read_b64_tr_b16 a[4:5], v2 offset:7232\n"
    "v_fma_f32 v76, -v21, v160, v76\n"
    "v_mfma_f32_16x16x32_bf16 v[88:91], a[20:23], a[116:119], v[88:91]\n"
    "v_fma_f32 v77, -v21, v161, v77\n"
    "v_fma_f32 v78, -v21, v162, v78\n"
    "v_mfma_f32_16x16x32_bf16 v[116:119], a[16:19], a[120:123], v[116:119]\n"
    "ds_read_b64_tr_b16 a[6:7], v2 offset:10816\n"
    "v_fma_f32 v79, -v21, v163, v79\n"
    "v_mfma_f32_16x16x32_bf16 v[116:119], a[20:23], a[124:127], v[116:119]\n"
    "s_waitcnt lgkmcnt(4)\n"
    "v_mfma_f32_16x16x32_bf16 v[36:39], a[24:27], a[96:99], v[36:39]\n"
    "ds_read_b64_tr_b16 a[8:9], v2 offset:96\n"
    "v_fma_f32 v52, -v20, v164, v52\n"
    "v_mfma_f32_16x16x32_bf16 v[36:39], a[28:31], a[100:103], v[36:39]\n"
    "buffer_load_dwordx4 a[68:71], v4, s[40:43], s60 offen offset:1024 sc1\n"
    "v_mfma_f32_16x16x32_bf16 v[64:67], a[24:27], a[104:107], v[64:67]\n"
    "ds_read_b64_tr_b16 a[10:11], v2 offset:3680\n"
    "v_fma_f32 v53, -v20, v165, v53\n"
    "v_mfma_f32_16x16x32_bf16 v[64:67], a[28:31], a[108:111], v[64:67]\n"
    "v_fma_f32 v54, -v20, v166, v54\n"
    "v_fma_f32 v55, -v20, v167, v55\n"
    "v_mfma_f32_16x16x32_bf16 v[92:95], a[24:27], a[112:115], v[92:95]\n"
    "ds_read_b64_tr_b16 a[12:13], v2 offset:7264\n"
    "v_fma_f32 v80, -v21, v164, v80\n"
    "v_mfma_f32_16x16x32_bf16 v[92:95], a[28:31], a[116:119], v[92:95]\n"
    "v_fma_f32 v81, -v21, v165, v81\n"
    "v_fma_f32 v82, -v21, v166, v82\n"
    "v_mfma_f32_16x16x32_bf16 v[120:123], a[24:27], a[120:123], v[120:123]\n"
    "ds_read_b64_tr_b16 a[14:15], v2 offset:10848\n"
    "v_fma_f32 v83, -v21, v167, v83\n"
    "v_mfma_f32_16x16x32_bf16 v[120:123], a[28:31], a[124:127], v[120:123]\n"
    "s_waitcnt lgkmcnt(4)\n"
    "v_mfma_f32_16x16x32_bf16 v[40:43], a[0:3], a[96:99], v[40:43]\n"
    "ds_read_b64_tr_b16 a[16:17], v2 offset:128\n"
    "v_fma_f32 v56, -v20, v168, v56\n"
    "v_mfma_f32_16x16x32_bf16 v[40:43], a[4:7], a[100:103], v[40:43]\n"
    "buffer_load_dwordx4 a[72:75], v4, s[40:43], s60 offen offset:2048 sc1\n"
    "v_mfma_f32_16x16x32_bf16 v[68:71], a[0:3], a[104:107], v[68:71]\n"
    "ds_read_b64_tr_b16 a[18:19], v2 offset:3712\n"
    "v_fma_f32 v57, -v20, v169, v57\n"
    "v_mfma_f32_16x16x32_bf16 v[68:71], a[4:7], a[108:111], v[68:71]\n"
    "v_fma_f32 v58, -v20, v170, v58\n"
    "v_fma_f32 v59, -v20, v171, v59\n"
    "v_mfma_f32_16x16x32_bf16 v[96:99], a[0:3], a[112:115], v[96:99]\n"
    "ds_read_b64_tr_b16 a[20:21], v2 offset:7296\n"
    "v_fma_f32 v84, -v21, v168, v84\n"
    "v_mfma_f32_16x16x32_bf16 v[96:99], a[4:7], a[116:119], v[96:99]\n"
    "buffer_load_dwordx4 a[76:79], v4, s[40:43], s60 offen offset:3072 sc1\n"
    "v_mfma_f32_16x16x32_bf16 v[124:127], a[0:3], a[120:123], v[124:127]\n"
    "ds_read_b64_tr_b16 a[22:23], v2 offset:10880\n"
    "v_fma_f32 v85, -v21, v169, v85\n"
    "v_mfma_f32_16x16x32_bf16 v[124:127], a[4:7], a[124:127], v[124:127]\n"
    "s_waitcnt lgkmcnt(4)\n"
    "v_fma_f32 v86, -v21, v170, v86\n"
    "v_fma_f32 v87, -v21, v171, v87\n"
    "v_mfma_f32_16x16x32_bf16 v[44:47], a[8:11], a[96:99], v[44:47]\n"
    "ds_read_b64_tr_b16 a[24:25], v2 offset:160\n"
    "v_fma_f32 v32, -v20, v144, v32\n"
    "v_mfma_f32_16x16x32_bf16 v[44:47], a[12:15], a[100:103], v[44:47]\n"
    "v_fma_f32 v33, -v20, v145, v33\n"
    "v_fma_f32 v34, -v20, v146, v34\n"
    "v_mfma_f32_16x16x32_bf16 v[72:75], a[8:11], a[104:107], v[72:75]\n"
    "ds_read_b64_tr_b16 a[26:27], v2 offset:3744\n"
    "v_fma_f32 v35, -v20, v147, v35\n"
    "v_mfma_f32_16x16x32_bf16 v[72:75], a[12:15], a[108:111], v[72:75]\n"
    "v_fma_f32 v60, -v21, v144, v60\n"
    "v_fma_f32 v61, -v21, v145, v61\n"
    "v_mfma_f32_16x16x32_bf16 v[100:103], a[8:11], a[112:115], v[100:103]\n"
    "ds_read_b64_tr_b16 a[28:29], v2 offset:7328\n"
    "v_fma_f32 v62, -v21, v146, v62\n"
    "v_mfma_f32_16x16x32_bf16 v[100:103], a[12:15], a[116:119], v[100:103]\n"
    "buffer_load_dwordx4 a[80:83], v5, s[40:43], s60 offen sc1\n"
    "v_mfma_f32_16x16x32_bf16 v[128:131], a[8:11], a[120:123], v[128:131]\n"
    "ds_read_b64_tr_b16 a[30:31], v2 offset:10912\n"
    "v_fma_f32 v63, -v21, v147, v63\n"
    "v_mfma_f32_16x16x32_bf16 v[128:131], a[12:15], a[124:127], v[128:131]\n"
    "s_waitcnt lgkmcnt(4)\n"
    "v_mfma_f32_16x16x32_bf16 v[48:51], a[16:19], a[96:99], v[48:51]\n"
    "ds_read_b64_tr_b16 a[0:1], v2 offset:192\n"
    "v_fma_f32 v36, -v20, v148, v36\n"
    "v_mfma_f32_16x16x32_bf16 v[48:51], a[20:23], a[100:103], v[48:51]\n"
    "v_fma_f32 v37, -v20, v149, v37\n"
    "v_fma_f32 v38, -v20, v150, v38\n"
    "v_mfma_f32_16x16x32_bf16 v[76:79], a[16:19], a[104:107], v[76:79]\n"
    "ds_read_b64_tr_b16 a[2:3], v2 offset:3776\n"
    "v_fma_f32 v39, -v20, v151, v39\n"
    "v_mfma_f32_16x16x32_bf16 v[76:79], a[20:23], a[108:111], v[76:79]\n"
    "buffer_load_dwordx4 a[84:87], v5, s[40:43], s60 offen offset:1024 sc1\n"
    "v_mfma_f32_16x16x32_bf16 v[104:107], a[16:19], a[112:115], v[104:107]\n"
    "ds_read_b64_tr_b16 a[4:5], v2 offset:7360\n"
    "v_fma_f32 v64, -v21, v148, v64\n"
    "v_mfma_f32_16x16x32_bf16 v[104:107], a[20:23], a[116:119], v[104:107]\n"
    "v_fma_f32 v65, -v21, v149, v65\n"
    "v_fma_f32 v66, -v21, v150, v66\n"
    "v_mfma_f32_16x16x32_bf16 v[132:135], a[16:19], a[120:123], v[132:135]\n"
    "ds_read_b64_tr_b16 a[6:7], v2 offset:10944\n"
    "v_fma_f32 v67, -v21, v151, v67\n"
    "v_mfma_f32_16x16x32_bf16 v[132:135], a[20:23], a[124:127], v[132:135]\n"
    "s_waitcnt lgkmcnt(4)\n"
    "v_mfma_f32_16x16x32_bf16 v[52:55], a[24:27], a[96:99], v[52:55]\n"
    "ds_read_b64_tr_b16 a[8:9], v2 offset:14336\n"
    "v_fma_f32 v40, -v20, v152, v40\n"
    "v_mfma_f32_16x16x32_bf16 v[52:55], a[28:31], a[100:103], v[52:55]\n"
    "v_fma_f32 v41, -v20, v153, v41\n"
    "v_fma_f32 v42, -v20, v154, v42\n"
    "v_mfma_f32_16x16x32_bf16 v[80:83], a[24:27], a[104:107], v[80:83]\n"
    "ds_read_b64_tr_b16 a[10:11], v2 offset:17920\n"
    "v_fma_f32 v43, -v20, v155, v43\n"
    "v_mfma_f32_16x16x32_bf16 v[80:83], a[28:31], a[108:111], v[80:83]\n"
    "buffer_load_dwordx4 a[88:91], v5, s[40:43], s60 offen offset:2048 sc1\n"
    "v_mfma_f32_16x16x32_bf16 v[108:111], a[24:27], a[112:115], v[108:111]\n"
    "ds_read_b64_tr_b16 a[12:13], v2 offset:21504\n"
    "v_fma_f32 v68, -v21, v152, v68\n"
    "v_mfma_f32_16x16x32_bf16 v[108:111], a[28:31], a[116:119], v[108:111]\n"
    "v_fma_f32 v69, -v21, v153, v69\n"
    "v_fma_f32 v70, -v21, v154, v70\n"
    "v_mfma_f32_16x16x32_bf16 v[136:139], a[24:27], a[120:123], v[136:139]\n"
    "ds_read_b64_tr_b16 a[14:15], v2 offset:25088\n"
    "v_fma_f32 v71, -v21, v155, v71\n"
    "v_mfma_f32_16x16x32_bf16 v[136:139], a[28:31], a[124:127], v[136:139]\n"
    "s_waitcnt lgkmcnt(4)\n"
    "v_mfma_f32_16x16x32_bf16 v[56:59], a[0:3], a[96:99], v[56:59]\n"
    "ds_read_b64_tr_b16 a[16:17], v2 offset:14368\n"
    "v_fma_f32 v44, -v20, v156, v44\n"
    "v_mfma_f32_16x16x32_bf16 v[56:59], a[4:7], a[100:103], v[56:59]\n"
    "buffer_load_dwordx4 a[92:95], v5, s[40:43], s60 offen offset:3072 sc1\n"
    "v_mfma_f32_16x16x32_bf16 v[84:87], a[0:3], a[104:107], v[84:87]\n"
    "s_add_u32 s60, s60, 0x40000\n"
    "ds_read_b64_tr_b16 a[18:19], v2 offset:17952\n"
    "v_mfma_f32_16x16x32_bf16 v[84:87], a[4:7], a[108:111], v[84:87]\n"
    "v_fma_f32 v45, -v20, v157, v45\n"
    "v_fma_f32 v46, -v20, v158, v46\n"
    "v_mfma_f32_16x16x32_bf16 v[112:115], a[0:3], a[112:115], v[112:115]\n"
    "ds_read_b64_tr_b16 a[20:21], v2 offset:21536\n"
    "v_fma_f32 v47, -v20, v159, v47\n"
    "v_mfma_f32_16x16x32_bf16 v[112:115], a[4:7], a[116:119], v[112:115]\n"
    "v_fma_f32 v72, -v21, v156, v72\n"
    "v_fma_f32 v73, -v21, v157, v73\n"
    "v_mfma_f32_16x16x32_bf16 v[140:143], a[0:3], a[120:123], v[140:143]\n"
    "ds_read_b64_tr_b16 a[22:23], v2 offset:25120\n"
    "v_fma_f32 v74, -v21, v158, v74\n"
    "v_mfma_f32_16x16x32_bf16 v[140:143], a[4:7], a[124:127], v[140:143]\n"
    "s_waitcnt vmcnt(20) lgkmcnt(4)\n"
    "v_fma_f32 v75, -v21, v159, v75\n"
    "v_mfma_f32_16x16x32_bf16 v[32:35], a[8:11], a[128:131], v[32:35]\n"
    "ds_read_b64_tr_b16 a[24:25], v2 offset:14400\n"
    "v_fma_f32 v104, -v22, v160, v104\n"
    "v_mfma_f32_16x16x32_bf16 v[32:35], a[12:15], a[132:135], v[32:35]\n"
    "buffer_load_dwordx4 a[96:99], v4, s[40:43], s60 offen sc1\n"
    "v_mfma_f32_16x16x32_bf16 v[60:63], a[8:11], a[136:139], v[60:63]\n"
    "ds_read_b64_tr_b16 a[26:27], v2 offset:17984\n"
    "v_fma_f32 v105, -v22, v161, v105\n"
    "v_mfma_f32_16x16x32_bf16 v[60:63], a[12:15], a[140:143], v[60:63]\n"
    "v_fma_f32 v106, -v22, v162, v106\n"
    "v_fma_f32 v107, -v22, v163, v107\n"
    "v_mfma_f32_16x16x32_bf16 v[88:91], a[8:11], a[144:147], v[88:91]\n"
    "ds_read_b64_tr_b16 a[28:29], v2 offset:21568\n"
    "v_fma_f32 v132, -v23, v160, v132\n"
    "v_mfma_f32_16x16x32_bf16 v[88:91], a[12:15], a[148:151], v[88:91]\n"
    "v_fma_f32 v133, -v23, v161, v133\n"
    "v_fma_f32 v134, -v23, v162, v134\n"
    "v_mfma_f32_16x16x32_bf16 v[116:119], a[8:11], a[152:155], v[116:119]\n"
    "ds_read_b64_tr_b16 a[30:31], v2 offset:25152\n"
    "v_fma_f32 v135, -v23, v163, v135\n"
    "v_mfma_f32_16x16x32_bf16 v[116:119], a[12:15], a[156:159], v[116:119]\n"
    "s_waitcnt lgkmcnt(4)\n"
    "v_mfma_f32_16x16x32_bf16 v[36:39], a[16:19], a[128:131], v[36:39]\n"
    "ds_read_b64_tr_b16 a[0:1], v2 offset:14432\n"
    "v_fma_f32 v108, -v22, v164, v108\n"
    "v_mfma_f32_16x16x32_bf16 v[36:39], a[20:23], a[132:135], v[36:39]\n"
    "buffer_load_dwordx4 a[100:103], v4, s[40:43], s60 offen offset:1024 sc1\n"
    "v_mfma_f32_16x16x32_bf16 v[64:67], a[16:19], a[136:139], v[64:67]\n"
    "ds_read_b64_tr_b16 a[2:3], v2 offset:18016\n"
    "v_fma_f32 v109, -v22, v165, v109\n"
    "v_mfma_f32_16x16x32_bf16 v[64:67], a[20:23], a[140:143], v[64:67]\n"
    "v_fma_f32 v110, -v22, v166, v110\n"
    "v_fma_f32 v111, -v22, v167, v111\n"
    "v_mfma_f32_16x16x32_bf16 v[92:95], a[16:19], a[144:147], v[92:95]\n"
    "ds_read_b64_tr_b16 a[4:5], v2 offset:21600\n"
    "v_fma_f32 v136, -v23, v164, v136\n"
    "v_mfma_f32_16x16x32_bf16 v[92:95], a[20:23], a[148:151], v[92:95]\n"
    "v_fma_f32 v137, -v23, v165, v137\n"
    "v_fma_f32 v138, -v23, v166, v138\n"
    "v_mfma_f32_16x16x32_bf16 v[120:123], a[16:19], a[152:155], v[120:123]\n"
    "ds_read_b64_tr_b16 a[6:7], v2 offset:25184\n"
    "v_fma_f32 v139, -v23, v167, v139\n"
    "v_mfma_f32_16x16x32_bf16 v[120:123], a[20:23], a[156:159], v[120:123]\n"
    "s_waitcnt lgkmcnt(4)\n"
    "v_mfma_f32_16x16x32_bf16 v[40:43], a[24:27], a[128:131], v[40:43]\n"
    "ds_read_b64_tr_b16 a[8:9], v2 offset:14464\n"
    "v_fma_f32 v112, -v22, v168, v112\n"
    "v_mfma_f32_16x16x32_bf16 v[40:43], a[28:31], a[132:135], v[40:43]\n"
    "buffer_load_dwordx4 a[104:107], v4, s[40:43], s60 offen offset:2048 sc1\n"
    "v_mfma_f32_16x16x32_bf16 v[68:71], a[24:27], a[136:139], v[68:71]\n"
    "ds_read_b64_tr_b16 a[10:11], v2 offset:18048\n"
    "v_fma_f32 v113, -v22, v169, v113\n"
    "v_mfma_f32_16x16x32_bf16 v[68:71], a[28:31], a[140:143], v[68:71]\n"
    "v_fma_f32 v114, -v22, v170, v114\n"
    "v_fma_f32 v115, -v22, v171, v115\n"
    "v_mfma_f32_16x16x32_bf16 v[96:99], a[24:27], a[144:147], v[96:99]\n"
    "ds_read_b64_tr_b16 a[12:13], v2 offset:21632\n"
    "v_fma_f32 v140, -v23, v168, v140\n"
    "v_mfma_f32_16x16x32_bf16 v[96:99], a[28:31], a[148:151], v[96:99]\n"
    "buffer_load_dwordx4 a[108:111], v4, s[40:43], s60 offen offset:3072 sc1\n"
    "v_mfma_f32_16x16x32_bf16 v[124:127], a[24:27], a[152:155], v[124:127]\n"
    "ds_read_b64_tr_b16 a[14:15], v2 offset:25216\n"
    "v_fma_f32 v141, -v23, v169, v141\n"
    "v_mfma_f32_16x16x32_bf16 v[124:127], a[28:31], a[156:159], v[124:127]\n"
    "s_waitcnt lgkmcnt(4)\n"
    "v_fma_f32 v142, -v23, v170, v142\n"
    "v_fma_f32 v143, -v23, v171, v143\n"
    "v_mfma_f32_16x16x32_bf16 v[44:47], a[0:3], a[128:131], v[44:47]\n"
    "ds_read_b64_tr_b16 a[16:17], v2 offset:14496\n"
    "v_fma_f32 v88, -v22, v144, v88\n"
    "v_mfma_f32_16x16x32_bf16 v[44:47], a[4:7], a[132:135], v[44:47]\n"
    "v_fma_f32 v89, -v22, v145, v89\n"
    "v_fma_f32 v90, -v22, v146, v90\n"
    "v_mfma_f32_16x16x32_bf16 v[72:75], a[0:3], a[136:139], v[72:75]\n"
    "ds_read_b64_tr_b16 a[18:19], v2 offset:18080\n"
    "v_fma_f32 v91, -v22, v147, v91\n"
    "v_mfma_f32_16x16x32_bf16 v[72:75], a[4:7], a[140:143], v[72:75]\n"
    "v_fma_f32 v116, -v23, v144, v116\n"
    "v_fma_f32 v117, -v23, v145, v117\n"
    "v_mfma_f32_16x16x32_bf16 v[100:103], a[0:3], a[144:147], v[100:103]\n"
    "ds_read_b64_tr_b16 a[20:21], v2 offset:21664\n"
    "v_fma_f32 v118, -v23, v146, v118\n"
    "v_mfma_f32_16x16x32_bf16 v[100:103], a[4:7], a[148:151], v[100:103]\n"
    "buffer_load_dwordx4 a[112:115], v5, s[40:43], s60 offen sc1\n"
    "v_mfma_f32_16x16x32_bf16 v[128:131], a[0:3], a[152:155], v[128:131]\n"
    "ds_read_b64_tr_b16 a[22:23], v2 offset:25248\n"
    "v_fma_f32 v119, -v23, v147, v119\n"
    "v_mfma_f32_16x16x32_bf16 v[128:131], a[4:7], a[156:159], v[128:131]\n"
    "s_waitcnt lgkmcnt(4)\n"
    "v_mfma_f32_16x16x32_bf16 v[48:51], a[8:11], a[128:131], v[48:51]\n"
    "ds_read_b64_tr_b16 a[24:25], v2 offset:14528\n"
    "v_fma_f32 v92, -v22, v148, v92\n"
    "v_mfma_f32_16x16x32_bf16 v[48:51], a[12:15], a[132:135], v[48:51]\n"
    "v_fma_f32 v93, -v22, v149, v93\n"
    "v_fma_f32 v94, -v22, v150, v94\n"
    "v_mfma_f32_16x16x32_bf16 v[76:79], a[8:11], a[136:139], v[76:79]\n"
    "s_add_u32 s64, s64, 1\n"
    "ds_read_b64_tr_b16 a[26:27], v2 offset:18112\n"
    "v_mfma_f32_16x16x32_bf16 v[76:79], a[12:15], a[140:143], v[76:79]\n"
    "buffer_load_dwordx4 a[116:119], v5, s[40:43], s60 offen offset:1024 sc1\n"
    "v_mfma_f32_16x16x32_bf16 v[104:107], a[8:11], a[144:147], v[104:107]\n"
    "ds_read_b64_tr_b16 a[28:29], v2 offset:21696\n"
    "v_fma_f32 v95, -v22, v151, v95\n"
    "v_mfma_f32_16x16x32_bf16 v[104:107], a[12:15], a[148:151], v[104:107]\n"
    "s_cmp_lt_u32 s64, 5\n"
    "s_cselect_b32 s64, s64, 0\n"
    "s_cselect_b32 s65, 0x7000, s73\n"
    "v_mfma_f32_16x16x32_bf16 v[132:135], a[8:11], a[152:155], v[132:135]\n"
    "ds_read_b64_tr_b16 a[30:31], v2 offset:25280\n"
    "v_add_u32 v2, s65, v2\n"
    "v_mfma_f32_16x16x32_bf16 v[132:135], a[12:15], a[156:159], v[132:135]\n"
    "s_waitcnt lgkmcnt(4)\n"
    "s_barrier\n"
    "v_fma_f32 v120, -v23, v148, v120\n"
    "v_mfma_f32_16x16x32_bf16 v[52:55], a[16:19], a[128:131], v[52:55]\n"
    "ds_read_b64_tr_b16 a[0:1], v2\n"
    "v_fma_f32 v121, -v23, v149, v121\n"
    "v_mfma_f32_16x16x32_bf16 v[52:55], a[20:23], a[132:135], v[52:55]\n"
    "v_fma_f32 v122, -v23, v150, v122\n"
    "v_fma_f32 v123, -v23, v151, v123\n"
    "v_mfma_f32_16x16x32_bf16 v[80:83], a[16:19], a[136:139], v[80:83]\n"
    "ds_read_b64_tr_b16 a[2:3], v2 offset:3584\n"
    "v_fma_f32 v96, -v22, v152, v96\n"
    "v_mfma_f32_16x16x32_bf16 v[80:83], a[20:23], a[140:143], v[80:83]\n"
    "buffer_load_dwordx4 a[120:123], v5, s[40:43], s60 offen offset:2048 sc1\n"
    "v_mfma_f32_16x16x32_bf16 v[108:111], a[16:19], a[144:147], v[108:111]\n"
    "ds_read_b64_tr_b16 a[4:5], v2 offset:7168\n"
    "v_fma_f32 v97, -v22, v153, v97\n"
    "v_mfma_f32_16x16x32_bf16 v[108:111], a[20:23], a[148:151], v[108:111]\n"
    "v_fma_f32 v98, -v22, v154, v98\n"
    "v_fma_f32 v99, -v22, v155, v99\n"
    "v_mfma_f32_16x16x32_bf16 v[136:139], a[16:19], a[152:155], v[136:139]\n"
    "ds_read_b64_tr_b16 a[6:7], v2 offset:10752\n"
    "v_fma_f32 v124, -v23, v152, v124\n"
    "v_mfma_f32_16x16x32_bf16 v[136:139], a[20:23], a[156:159], v[136:139]\n"
    "s_waitcnt lgkmcnt(4)\n"
    "v_fma_f32 v125, -v23, v153, v125\n"
    "v_fma_f32 v126, -v23, v154, v126\n"
    "v_mfma_f32_16x16x32_bf16 v[56:59], a[24:27], a[128:131], v[56:59]\n"
    "ds_read_b64_tr_b16 a[8:9], v2 offset:32\n"
    "v_fma_f32 v127, -v23, v155, v127\n"
    "v_mfma_f32_16x16x32_bf16 v[56:59], a[28:31], a[132:135], v[56:59]\n"
    "buffer_load_dwordx4 a[124:127], v5, s[40:43], s60 offen offset:3072 sc1\n"
    "v_mfma_f32_16x16x32_bf16 v[84:87], a[24:27], a[136:139], v[84:87]\n"
    "s_add_u32 s60, s60, 0x40000\n"
    "ds_read_b64_tr_b16 a[10:11], v2 offset:3616\n"
    "v_mfma_f32_16x16x32_bf16 v[84:87], a[28:31], a[140:143], v[84:87]\n"
    "v_fma_f32 v100, -v22, v156, v100\n"
    "v_fma_f32 v101, -v22, v157, v101\n"
    "v_mfma_f32_16x16x32_bf16 v[112:115], a[24:27], a[144:147], v[112:115]\n"
    "ds_read_b64_tr_b16 a[12:13], v2 offset:7200\n"
    "v_fma_f32 v102, -v22, v158, v102\n"
    "v_mfma_f32_16x16x32_bf16 v[112:115], a[28:31], a[148:151], v[112:115]\n"
    "v_fma_f32 v103, -v22, v159, v103\n"
    "v_fma_f32 v128, -v23, v156, v128\n"
    "v_mfma_f32_16x16x32_bf16 v[140:143], a[24:27], a[152:155], v[140:143]\n"
    "ds_read_b64_tr_b16 a[14:15], v2 offset:10784\n"
    "v_fma_f32 v129, -v23, v157, v129\n"
    "v_mfma_f32_16x16x32_bf16 v[140:143], a[28:31], a[156:159], v[140:143]\n"
    "v_fma_f32 v130, -v23, v158, v130\n"
    "v_fma_f32 v131, -v23, v159, v131\n"
    "s_waitcnt vmcnt(17) lgkmcnt(4)\n"
    "v_mfma_f32_16x16x32_bf16 v[32:35], a[0:3], a[32:35], v[32:35]\n"
    "ds_read_b64_tr_b16 a[16:17], v2 offset:64\n"
    "v_mfma_f32_16x16x32_bf16 v[32:35], a[4:7], a[36:39], v[32:35]\n"
    "buffer_load_dwordx4 a[128:131], v4, s[40:43], s60 offen sc1\n"
    "v_mfma_f32_16x16x32_bf16 v[60:63], a[0:3], a[40:43], v[60:63]\n"
    "ds_read_b64_tr_b16 a[18:19], v2 offset:3648\n"
    "v_mfma_f32_16x16x32_bf16 v[60:63], a[4:7], a[44:47], v[60:63]\n"
    "buffer_load_dwordx4 a[132:135], v4, s[40:43], s60 offen offset:1024 sc1\n"
    "v_mfma_f32_16x16x32_bf16 v[88:91], a[0:3], a[48:51], v[88:91]\n"
    "ds_read_b64_tr_b16 a[20:21], v2 offset:7232\n"
    "v_mfma_f32_16x16x32_bf16 v[88:91], a[4:7], a[52:55], v[88:91]\n"
    "buffer_load_dwordx4 a[136:139], v4, s[40:43], s60 offen offset:2048 sc1\n"
    "v_mfma_f32_16x16x32_bf16 v[116:119], a[0:3], a[56:59], v[116:119]\n"
    "ds_read_b64_tr_b16 a[22:23], v2 offset:10816\n"
    "v_mfma_f32_16x16x32_bf16 v[116:119], a[4:7], a[60:63], v[116:119]\n"
    "s_waitcnt lgkmcnt(4)\n"
    "v_mfma_f32_16x16x32_bf16 v[36:39], a[8:11], a[32:35], v[36:39]\n"
    "ds_read_b64_tr_b16 a[24:25], v2 offset:96\n"
    "v_mfma_f32_16x16x32_bf16 v[36:39], a[12:15], a[36:39], v[36:39]\n"
    "buffer_load_dwordx4 a[140:143], v4, s[40:43], s60 offen offset:3072 sc1\n"
    "v_mfma_f32_16x16x32_bf16 v[64:67], a[8:11], a[40:43], v[64:67]\n"
    "ds_read_b64_tr_b16 a[26:27], v2 offset:3680\n"
    "v_mfma_f32_16x16x32_bf16 v[64:67], a[12:15], a[44:47], v[64:67]\n"
    "buffer_load_dwordx4 a[144:147], v5, s[40:43], s60 offen sc1\n"
    "v_mfma_f32_16x16x32_bf16 v[92:95], a[8:11], a[48:51], v[92:95]\n"
    "ds_read_b64_tr_b16 a[28:29], v2 offset:7264\n"
    "v_mfma_f32_16x16x32_bf16 v[92:95], a[12:15], a[52:55], v[92:95]\n"
    "buffer_load_dwordx4 a[148:151], v5, s[40:43], s60 offen offset:1024 sc1\n"
    "v_mfma_f32_16x16x32_bf16 v[120:123], a[8:11], a[56:59], v[120:123]\n"
    "ds_read_b64_tr_b16 a[30:31], v2 offset:10848\n"
    "v_mfma_f32_16x16x32_bf16 v[120:123], a[12:15], a[60:63], v[120:123]\n"
    "s_waitcnt lgkmcnt(4)\n"
    "v_mfma_f32_16x16x32_bf16 v[40:43], a[16:19], a[32:35], v[40:43]\n"
    "ds_read_b64_tr_b16 a[0:1], v2 offset:128\n"
    "v_mfma_f32_16x16x32_bf16 v[40:43], a[20:23], a[36:39], v[40:43]\n"
    "buffer_load_dwordx4 a[152:155], v5, s[40:43], s60 offen offset:2048 sc1\n"
    "v_mfma_f32_16x16x32_bf16 v[68:71], a[16:19], a[40:43], v[68:71]\n"
    "ds_read_b64_tr_b16 a[2:3], v2 offset:3712\n"
    "v_mfma_f32_16x16x32_bf16 v[68:71], a[20:23], a[44:47], v[68:71]\n"
    "buffer_load_dwordx4 a[156:159], v5, s[40:43], s60 offen offset:3072 sc1\n"
    "v_mfma_f32_16x16x32_bf16 v[96:99], a[16:19], a[48:51], v[96:99]\n"
    "ds_read_b64_tr_b16 a[4:5], v2 offset:7296\n"
    "v_mfma_f32_16x16x32_bf16 v[96:99], a[20:23], a[52:55], v[96:99]\n"
    "v_mfma_f32_16x16x32_bf16 v[124:127], a[16:19], a[56:59], v[124:127]\n"
    "ds_read_b64_tr_b16 a[6:7], v2 offset:10880\n"
    "v_mfma_f32_16x16x32_bf16 v[124:127], a[20:23], a[60:63], v[124:127]\n"
    "s_waitcnt lgkmcnt(4)\n"
    "v_mfma_f32_16x16x32_bf16 v[44:47], a[24:27], a[32:35], v[44:47]\n"
    "s_mul_i32 s70, s68, 7\n"
    "ds_read_b64_tr_b16 a[8:9], v2 offset:160\n"
    "v_mfma_f32_16x16x32_bf16 v[44:47], a[28:31], a[36:39], v[44:47]\n"
    "s_add_u32 s70, s70, s74\n"
    "v_mfma_f32_16x16x32_bf16 v[72:75], a[24:27], a[40:43], v[72:75]\n"
    "s_lshr_b32 s70, s70, 1\n"
    "ds_read_b64_tr_b16 a[10:11], v2 offset:3744\n"
    "v_mfma_f32_16x16x32_bf16 v[72:75], a[28:31], a[44:47], v[72:75]\n"
    "s_lshr_b32 s77, s77, 1\n"
    "v_mfma_f32_16x16x32_bf16 v[100:103], a[24:27], a[48:51], v[100:103]\n"
    "s_lshr_b32 s84, s70, 1\n"
    "ds_read_b64_tr_b16 a[12:13], v2 offset:7328\n"
    "v_mfma_f32_16x16x32_bf16 v[100:103], a[28:31], a[52:55], v[100:103]\n"
    "s_lshl_b32 s84, s84, 18\n"
    "v_mfma_f32_16x16x32_bf16 v[128:131], a[24:27], a[56:59], v[128:131]\n"
    "s_and_b32 s85, s70, 1\n"
    "ds_read_b64_tr_b16 a[14:15], v2 offset:10912\n"
    "v_mfma_f32_16x16x32_bf16 v[128:131], a[28:31], a[60:63], v[128:131]\n"
    "s_waitcnt lgkmcnt(4)\n"
    "s_lshl_b32 s85, s85, 10\n"
    "v_mfma_f32_16x16x32_bf16 v[48:51], a[0:3], a[32:35], v[48:51]\n"
    "s_add_u32 s80, s84, s85\n"
    "ds_read_b64_tr_b16 a[16:17], v2 offset:192\n"
    "v_mfma_f32_16x16x32_bf16 v[48:51], a[4:7], a[36:39], v[48:51]\n"
    "s_add_u32 s70, s70, 1\n"
    "v_mfma_f32_16x16x32_bf16 v[76:79], a[0:3], a[40:43], v[76:79]\n"
    "s_lshr_b32 s84, s70, 1\n"
    "ds_read_b64_tr_b16 a[18:19], v2 offset:3776\n"
    "v_mfma_f32_16x16x32_bf16 v[76:79], a[4:7], a[44:47], v[76:79]\n"
    "s_lshl_b32 s84, s84, 18\n"
    "v_mfma_f32_16x16x32_bf16 v[104:107], a[0:3], a[48:51], v[104:107]\n"
    "s_and_b32 s85, s70, 1\n"
    "ds_read_b64_tr_b16 a[20:21], v2 offset:7360\n"
    "v_mfma_f32_16x16x32_bf16 v[104:107], a[4:7], a[52:55], v[104:107]\n"
    "s_lshl_b32 s85, s85, 10\n"
    "v_mfma_f32_16x16x32_bf16 v[132:135], a[0:3], a[56:59], v[132:135]\n"
    "s_add_u32 s81, s84, s85\n"
    "ds_read_b64_tr_b16 a[22:23], v2 offset:10944\n"
    "v_mfma_f32_16x16x32_bf16 v[132:135], a[4:7], a[60:63], v[132:135]\n"
    "s_waitcnt lgkmcnt(4)\n"
    "s_add_u32 s70, s70, 1\n"
    "v_mfma_f32_16x16x32_bf16 v[52:55], a[8:11], a[32:35], v[52:55]\n"
    "s_lshr_b32 s84, s70, 1\n"
    "ds_read_b64_tr_b16 a[24:25], v2 offset:14336\n"
    "v_mfma_f32_16x16x32_bf16 v[52:55], a[12:15], a[36:39], v[52:55]\n"
    "s_lshl_b32 s84, s84, 18\n"
    "v_mfma_f32_16x16x32_bf16 v[80:83], a[8:11], a[40:43], v[80:83]\n"
    "s_and_b32 s85, s70, 1\n"
    "ds_read_b64_tr_b16 a[26:27], v2 offset:17920\n"
    "v_mfma_f32_16x16x32_bf16 v[80:83], a[12:15], a[44:47], v[80:83]\n"
    "s_lshl_b32 s85, s85, 10\n"
    "v_mfma_f32_16x16x32_bf16 v[108:111], a[8:11], a[48:51], v[108:111]\n"
    "s_add_u32 s82, s84, s85\n"
    "ds_read_b64_tr_b16 a[28:29], v2 offset:21504\n"
    "v_mfma_f32_16x16x32_bf16 v[108:111], a[12:15], a[52:55], v[108:111]\n"
    "s_lshr_b32 s84, s77, 1\n"
    "v_mfma_f32_16x16x32_bf16 v[136:139], a[8:11], a[56:59], v[136:139]\n"
    "s_lshl_b32 s84, s84, 18\n"
    "ds_read_b64_tr_b16 a[30:31], v2 offset:25088\n"
    "v_mfma_f32_16x16x32_bf16 v[136:139], a[12:15], a[60:63], v[136:139]\n"
    "s_waitcnt vmcnt(24) lgkmcnt(4)\n"
    "s_barrier\n"
    "s_and_b32 s85, s77, 1\n"
    "v_mfma_f32_16x16x32_bf16 v[56:59], a[16:19], a[32:35], v[56:59]\n"
    "s_lshl_b32 s85, s85, 10\n"
    "ds_read_b64_tr_b16 a[0:1], v2 offset:28672\n"
    "v_mfma_f32_16x16x32_bf16 v[56:59], a[20:23], a[36:39], v[56:59]\n"
    "s_add_u32 s83, s84, s85\n"
    "v_mfma_f32_16x16x32_bf16 v[84:87], a[16:19], a[40:43], v[84:87]\n"
    "s_lshl_b32 s84, s74, 3\n"
    "ds_read_b64_tr_b16 a[2:3], v2 offset:32256\n"
    "v_mfma_f32_16x16x32_bf16 v[84:87], a[20:23], a[44:47], v[84:87]\n"
    "s_add_u32 s83, s83, s84\n"
    "v_mfma_f32_16x16x32_bf16 v[112:115], a[16:19], a[48:51], v[112:115]\n"
    "ds_read_b64_tr_b16 a[4:5], v2 offset:35840\n"
    "v_mfma_f32_16x16x32_bf16 v[112:115], a[20:23], a[52:55], v[112:115]\n"
    "v_mfma_f32_16x16x32_bf16 v[140:143], a[16:19], a[56:59], v[140:143]\n"
    "ds_read_b64_tr_b16 a[6:7], v2 offset:39424\n"
    "v_mfma_f32_16x16x32_bf16 v[140:143], a[20:23], a[60:63], v[140:143]\n"
    "s_waitcnt vmcnt(16) lgkmcnt(4)\n"
    "v_mfma_f32_16x16x32_bf16 v[32:35], a[24:27], a[64:67], v[32:35]\n"
    "ds_read_b64_tr_b16 a[8:9], v2 offset:43008\n"
    "v_mfma_f32_16x16x32_bf16 v[32:35], a[28:31], a[68:71], v[32:35]\n"
    "v_mfma_f32_16x16x32_bf16 v[60:63], a[24:27], a[72:75], v[60:63]\n"
    "ds_read_b64_tr_b16 a[10:11], v2 offset:46592\n"
    "v_mfma_f32_16x16x32_bf16 v[60:63], a[28:31], a[76:79], v[60:63]\n"
    "v_mfma_f32_16x16x32_bf16 v[88:91], a[24:27], a[80:83], v[88:91]\n"
    "ds_read_b64_tr_b16 a[12:13], v2 offset:50176\n"
    "v_mfma_f32_16x16x32_bf16 v[88:91], a[28:31], a[84:87], v[88:91]\n"
    "v_mfma_f32_16x16x32_bf16 v[116:119], a[24:27], a[88:91], v[116:119]\n"
    "ds_read_b64_tr_b16 a[14:15], v2 offset:53760\n"
    "v_mfma_f32_16x16x32_bf16 v[116:119], a[28:31], a[92:95], v[116:119]\n"
    "s_waitcnt vmcnt(8) lgkmcnt(4)\n"
    "v_mfma_f32_16x16x32_bf16 v[32:35], a[0:3], a[96:99], v[32:35]\n"
    "ds_read_b64_tr_b16 a[16:17], v2 offset:14368\n"
    "v_mfma_f32_16x16x32_bf16 v[32:35], a[4:7], a[100:103], v[32:35]\n"
    "v_mfma_f32_16x16x32_bf16 v[60:63], a[0:3], a[104:107], v[60:63]\n"
    "ds_read_b64_tr_b16 a[18:19], v2 offset:17952\n"
    "v_mfma_f32_16x16x32_bf16 v[60:63], a[4:7], a[108:111], v[60:63]\n"
    "v_mfma_f32_16x16x32_bf16 v[88:91], a[0:3], a[112:115], v[88:91]\n"
    "ds_read_b64_tr_b16 a[20:21], v2 offset:21536\n"
    "v_mfma_f32_16x16x32_bf16 v[88:91], a[4:7], a[116:119], v[88:91]\n"
    "v_mfma_f32_16x16x32_bf16 v[116:119], a[0:3], a[120:123], v[116:119]\n"
    "ds_read_b64_tr_b16 a[22:23], v2 offset:25120\n"
    "v_mfma_f32_16x16x32_bf16 v[116:119], a[4:7], a[124:127], v[116:119]\n"
    "s_waitcnt vmcnt(0) lgkmcnt(4)\n"
    "v_mfma_f32_16x16x32_bf16 v[32:35], a[8:11], a[128:131], v[32:35]\n"
    "ds_read_b64_tr_b16 a[24:25], v2 offset:28704\n"
    "v_mfma_f32_16x16x32_bf16 v[32:35], a[12:15], a[132:135], v[32:35]\n"
    "v_mfma_f32_16x16x32_bf16 v[60:63], a[8:11], a[136:139], v[60:63]\n"
    "ds_read_b64_tr_b16 a[26:27], v2 offset:32288\n"
    "v_mfma_f32_16x16x32_bf16 v[60:63], a[12:15], a[140:143], v[60:63]\n"
    "v_mfma_f32_16x16x32_bf16 v[88:91], a[8:11], a[144:147], v[88:91]\n"
    "ds_read_b64_tr_b16 a[28:29], v2 offset:35872\n"
    "v_mfma_f32_16x16x32_bf16 v[88:91], a[12:15], a[148:151], v[88:91]\n"
    "v_mfma_f32_16x16x32_bf16 v[116:119], a[8:11], a[152:155], v[116:119]\n"
    "ds_read_b64_tr_b16 a[30:31], v2 offset:39456\n"
    "v_mfma_f32_16x16x32_bf16 v[116:119], a[12:15], a[156:159], v[116:119]\n"
    "s_waitcnt lgkmcnt(4)\n"
    "v_mul_f32 v32, v32, v172\n"
    "v_mul_f32 v33, v33, v172\n"
    "v_mfma_f32_16x16x32_bf16 v[36:39], a[16:19], a[64:67], v[36:39]\n"
    "ds_read_b64_tr_b16 a[0:1], v2 offset:43040\n"
    "v_mul_f32 v34, v34, v172\n"
    "v_mfma_f32_16x16x32_bf16 v[36:39], a[20:23], a[68:71], v[36:39]\n"
    "v_mul_f32 v35, v35, v172\n"
    "v_mul_f32 v60, v60, v174\n"
    "v_mfma_f32_16x16x32_bf16 v[64:67], a[16:19], a[72:75], v[64:67]\n"
    "ds_read_b64_tr_b16 a[2:3], v2 offset:46624\n"
    "v_mul_f32 v61, v61, v174\n"
    "v_mfma_f32_16x16x32_bf16 v[64:67], a[20:23], a[76:79], v[64:67]\n"
    "v_exp_f32 v180, v32\n"
    "v_mfma_f32_16x16x32_bf16 v[92:95], a[16:19], a[80:83], v[92:95]\n"
    "ds_read_b64_tr_b16 a[4:5], v2 offset:50208\n"
    "v_mul_f32 v62, v62, v174\n"
    "v_mfma_f32_16x16x32_bf16 v[92:95], a[20:23], a[84:87], v[92:95]\n"
    "v_exp_f32 v181, v33\n"
    "v_mfma_f32_16x16x32_bf16 v[120:123], a[16:19], a[88:91], v[120:123]\n"
    "ds_read_b64_tr_b16 a[6:7], v2 offset:53792\n"
    "v_mul_f32 v63, v63, v174\n"
    "v_mfma_f32_16x16x32_bf16 v[120:123], a[20:23], a[92:95], v[120:123]\n"
    "s_waitcnt lgkmcnt(4)\n"
    "v_exp_f32 v182, v34\n"
    "v_mfma_f32_16x16x32_bf16 v[36:39], a[24:27], a[96:99], v[36:39]\n"
    "ds_read_b64_tr_b16 a[8:9], v2 offset:14400\n"
    "v_mul_f32 v88, v88, v176\n"
    "v_mfma_f32_16x16x32_bf16 v[36:39], a[28:31], a[100:103], v[36:39]\n"
    "v_exp_f32 v183, v35\n"
    "v_mfma_f32_16x16x32_bf16 v[64:67], a[24:27], a[104:107], v[64:67]\n"
    "ds_read_b64_tr_b16 a[10:11], v2 offset:17984\n"
    "v_fma_f32 v180, v180, v16, v16\n"
    "v_mfma_f32_16x16x32_bf16 v[64:67], a[28:31], a[108:111], v[64:67]\n"
    "v_exp_f32 v184, v60\n"
    "v_mfma_f32_16x16x32_bf16 v[92:95], a[24:27], a[112:115], v[92:95]\n"
    "ds_read_b64_tr_b16 a[12:13], v2 offset:21568\n"
    "v_fma_f32 v181, v181, v16, v16\n"
    "v_mfma_f32_16x16x32_bf16 v[92:95], a[28:31], a[116:119], v[92:95]\n"
    "v_exp_f32 v185, v61\n"
    "v_mfma_f32_16x16x32_bf16 v[120:123], a[24:27], a[120:123], v[120:123]\n"
    "ds_read_b64_tr_b16 a[14:15], v2 offset:25152\n"
    "v_fma_f32 v182, v182, v16, v16\n"
    "v_mfma_f32_16x16x32_bf16 v[120:123], a[28:31], a[124:127], v[120:123]\n"
    "s_waitcnt lgkmcnt(4)\n"
    "v_exp_f32 v186, v62\n"
    "v_mfma_f32_16x16x32_bf16 v[36:39], a[0:3], a[128:131], v[36:39]\n"
    "ds_read_b64_tr_b16 a[16:17], v2 offset:28736\n"
    "v_fma_f32 v183, v183, v16, v16\n"
    "v_mfma_f32_16x16x32_bf16 v[36:39], a[4:7], a[132:135], v[36:39]\n"
    "v_rcp_f32 v180, v180\n"
    "v_mfma_f32_16x16x32_bf16 v[64:67], a[0:3], a[136:139], v[64:67]\n"
    "ds_read_b64_tr_b16 a[18:19], v2 offset:32320\n"
    "v_mul_f32 v89, v89, v176\n"
    "v_mfma_f32_16x16x32_bf16 v[64:67], a[4:7], a[140:143], v[64:67]\n"
    "v_rcp_f32 v181, v181\n"
    "v_mfma_f32_16x16x32_bf16 v[92:95], a[0:3], a[144:147], v[92:95]\n"
    "ds_read_b64_tr_b16 a[20:21], v2 offset:35904\n"
    "v_mul_f32 v90, v90, v176\n"
    "v_mfma_f32_16x16x32_bf16 v[92:95], a[4:7], a[148:151], v[92:95]\n"
    "v_rcp_f32 v182, v182\n"
    "v_mfma_f32_16x16x32_bf16 v[120:123], a[0:3], a[152:155], v[120:123]\n"
    "ds_read_b64_tr_b16 a[22:23], v2 offset:39488\n"
    "v_mul_f32 v91, v91, v176\n"
    "v_mfma_f32_16x16x32_bf16 v[120:123], a[4:7], a[156:159], v[120:123]\n"
    "s_waitcnt lgkmcnt(4)\n"
    "v_rcp_f32 v183, v183\n"
    "v_mfma_f32_16x16x32_bf16 v[40:43], a[8:11], a[64:67], v[40:43]\n"
    "ds_read_b64_tr_b16 a[24:25], v2 offset:43072\n"
    "v_mul_f32 v32, v32, v180\n"
    "v_mfma_f32_16x16x32_bf16 v[40:43], a[12:15], a[68:71], v[40:43]\n"
    "v_exp_f32 v187, v63\n"
    "v_mfma_f32_16x16x32_bf16 v[68:71], a[8:11], a[72:75], v[68:71]\n"
    "ds_read_b64_tr_b16 a[26:27], v2 offset:46656\n"
    "v_mul_f32 v33, v33, v181\n"
    "v_mfma_f32_16x16x32_bf16 v[68:71], a[12:15], a[76:79], v[68:71]\n"
    "v_exp_f32 v188, v88\n"
    "v_mfma_f32_16x16x32_bf16 v[96:99], a[8:11], a[80:83], v[96:99]\n"
    "ds_read_b64_tr_b16 a[28:29], v2 offset:50240\n"
    "v_mul_f32 v34, v34, v182\n"
    "v_mfma_f32_16x16x32_bf16 v[96:99], a[12:15], a[84:87], v[96:99]\n"
    "v_exp_f32 v189, v89\n"
    "v_mfma_f32_16x16x32_bf16 v[124:127], a[8:11], a[88:91], v[124:127]\n"
    "ds_read_b64_tr_b16 a[30:31], v2 offset:53824\n"
    "v_mul_f32 v35, v35, v183\n"
    "v_mfma_f32_16x16x32_bf16 v[124:127], a[12:15], a[92:95], v[124:127]\n"
    "s_waitcnt lgkmcnt(4)\n"
    "v_exp_f32 v190, v90\n"
    "v_mfma_f32_16x16x32_bf16 v[40:43], a[16:19], a[96:99], v[40:43]\n"
    "ds_read_b64_tr_b16 a[0:1], v2 offset:14432\n"
    "v_cvt_pk_bf16_f32 v196, v32, v33\n"
    "v_mfma_f32_16x16x32_bf16 v[40:43], a[20:23], a[100:103], v[40:43]\n"
    "v_exp_f32 v191, v91\n"
    "v_mfma_f32_16x16x32_bf16 v[68:71], a[16:19], a[104:107], v[68:71]\n"
    "ds_read_b64_tr_b16 a[2:3], v2 offset:18016\n"
    "v_cvt_pk_bf16_f32 v197, v34, v35\n"
    "v_mfma_f32_16x16x32_bf16 v[68:71], a[20:23], a[108:111], v[68:71]\n"
    "v_fma_f32 v184, v184, v16, v16\n"
    "v_fma_f32 v185, v185, v16, v16\n"
    "v_mfma_f32_16x16x32_bf16 v[96:99], a[16:19], a[112:115], v[96:99]\n"
    "ds_read_b64_tr_b16 a[4:5], v2 offset:21600\n"
    "v_fma_f32 v186, v186, v16, v16\n"
    "v_mfma_f32_16x16x32_bf16 v[96:99], a[20:23], a[116:119], v[96:99]\n"
    "v_fma_f32 v187, v187, v16, v16\n"
    "v_fma_f32 v188, v188, v16, v16\n"
    "v_mfma_f32_16x16x32_bf16 v[124:127], a[16:19], a[120:123], v[124:127]\n"
    "ds_read_b64_tr_b16 a[6:7], v2 offset:25184\n"
    "v_fma_f32 v189, v189, v16, v16\n"
    "v_mfma_f32_16x16x32_bf16 v[124:127], a[20:23], a[124:127], v[124:127]\n"
    "s_waitcnt lgkmcnt(4)\n"
    "v_rcp_f32 v184, v184\n"
    "v_mfma_f32_16x16x32_bf16 v[40:43], a[24:27], a[128:131], v[40:43]\n"
    "ds_read_b64_tr_b16 a[8:9], v2 offset:28768\n"
    "v_fma_f32 v190, v190, v16, v16\n"
    "v_mfma_f32_16x16x32_bf16 v[40:43], a[28:31], a[132:135], v[40:43]\n"
    "v_rcp_f32 v185, v185\n"
    "v_mfma_f32_16x16x32_bf16 v[68:71], a[24:27], a[136:139], v[68:71]\n"
    "ds_read_b64_tr_b16 a[10:11], v2 offset:32352\n"
    "v_fma_f32 v191, v191, v16, v16\n"
    "v_mfma_f32_16x16x32_bf16 v[68:71], a[28:31], a[140:143], v[68:71]\n"
    "v_rcp_f32 v186, v186\n"
    "v_mfma_f32_16x16x32_bf16 v[96:99], a[24:27], a[144:147], v[96:99]\n"
    "ds_read_b64_tr_b16 a[12:13], v2 offset:35936\n"
    "v_mul_f32 v116, v116, v178\n"
    "v_mfma_f32_16x16x32_bf16 v[96:99], a[28:31], a[148:151], v[96:99]\n"
    "v_rcp_f32 v187, v187\n"
    "v_mfma_f32_16x16x32_bf16 v[124:127], a[24:27], a[152:155], v[124:127]\n"
    "ds_read_b64_tr_b16 a[14:15], v2 offset:39520\n"
    "v_mul_f32 v60, v60, v184\n"
    "v_mfma_f32_16x16x32_bf16 v[124:127], a[28:31], a[156:159], v[124:127]\n"
    "s_waitcnt lgkmcnt(4)\n"
    "v_rcp_f32 v188, v188\n"
    "v_mfma_f32_16x16x32_bf16 v[44:47], a[0:3], a[64:67], v[44:47]\n"
    "ds_read_b64_tr_b16 a[16:17], v2 offset:43104\n"
    "v_mul_f32 v61, v61, v185\n"
    "v_mfma_f32_16x16x32_bf16 v[44:47], a[4:7], a[68:71], v[44:47]\n"
    "v_rcp_f32 v189, v189\n"
    "v_mfma_f32_16x16x32_bf16 v[72:75], a[0:3], a[72:75], v[72:75]\n"
    "ds_read_b64_tr_b16 a[18:19], v2 offset:46688\n"
    "v_mul_f32 v62, v62, v186\n"
    "v_mfma_f32_16x16x32_bf16 v[72:75], a[4:7], a[76:79], v[72:75]\n"
    "v_rcp_f32 v190, v190\n"
    "v_mfma_f32_16x16x32_bf16 v[100:103], a[0:3], a[80:83], v[100:103]\n"
    "ds_read_b64_tr_b16 a[20:21], v2 offset:50272\n"
    "v_mul_f32 v63, v63, v187\n"
    "v_mfma_f32_16x16x32_bf16 v[100:103], a[4:7], a[84:87], v[100:103]\n"
    "v_rcp_f32 v191, v191\n"
    "v_mfma_f32_16x16x32_bf16 v[128:131], a[0:3], a[88:91], v[128:131]\n"
    "ds_read_b64_tr_b16 a[22:23], v2 offset:53856\n"
    "v_cvt_pk_bf16_f32 v204, v60, v61\n"
    "v_mfma_f32_16x16x32_bf16 v[128:131], a[4:7], a[92:95], v[128:131]\n"
    "s_waitcnt lgkmcnt(4)\n"
    "v_cvt_pk_bf16_f32 v205, v62, v63\n"
    "v_mul_f32 v88, v88, v188\n"
    "v_mfma_f32_16x16x32_bf16 v[44:47], a[8:11], a[96:99], v[44:47]\n"
    "ds_read_b64_tr_b16 a[24:25], v2 offset:14464\n"
    "v_mul_f32 v89, v89, v189\n"
    "v_mfma_f32_16x16x32_bf16 v[44:47], a[12:15], a[100:103], v[44:47]\n"
    "v_mul_f32 v90, v90, v190\n"
    "v_mul_f32 v91, v91, v191\n"
    "v_mfma_f32_16x16x32_bf16 v[72:75], a[8:11], a[104:107], v[72:75]\n"
    "ds_read_b64_tr_b16 a[26:27], v2 offset:18048\n"
    "v_cvt_pk_bf16_f32 v212, v88, v89\n"
    "v_mfma_f32_16x16x32_bf16 v[72:75], a[12:15], a[108:111], v[72:75]\n"
    "v_cvt_pk_bf16_f32 v213, v90, v91\n"
    "v_mul_f32 v117, v117, v178\n"
    "v_mfma_f32_16x16x32_bf16 v[100:103], a[8:11], a[112:115], v[100:103]\n"
    "ds_read_b64_tr_b16 a[28:29], v2 offset:21632\n"
    "v_mul_f32 v118, v118, v178\n"
    "v_mfma_f32_16x16x32_bf16 v[100:103], a[12:15], a[116:119], v[100:103]\n"
    "v_mul_f32 v119, v119, v178\n"
    "v_mul_f32 v36, v36, v172\n"
    "v_mfma_f32_16x16x32_bf16 v[128:131], a[8:11], a[120:123], v[128:131]\n"
    "ds_read_b64_tr_b16 a[30:31], v2 offset:25216\n"
    "v_mul_f32 v37, v37, v172\n"
    "v_mfma_f32_16x16x32_bf16 v[128:131], a[12:15], a[124:127], v[128:131]\n"
    "s_waitcnt lgkmcnt(4)\n"
    "v_exp_f32 v192, v116\n"
    "v_mfma_f32_16x16x32_bf16 v[44:47], a[16:19], a[128:131], v[44:47]\n"
    "ds_read_b64_tr_b16 a[0:1], v2 offset:28800\n"
    "v_mul_f32 v38, v38, v172\n"
    "v_mfma_f32_16x16x32_bf16 v[44:47], a[20:23], a[132:135], v[44:47]\n"
    "v_exp_f32 v193, v117\n"
    "v_mfma_f32_16x16x32_bf16 v[72:75], a[16:19], a[136:139], v[72:75]\n"
    "ds_read_b64_tr_b16 a[2:3], v2 offset:32384\n"
    "v_mul_f32 v39, v39, v172\n"
    "v_mfma_f32_16x16x32_bf16 v[72:75], a[20:23], a[140:143], v[72:75]\n"
    "v_exp_f32 v194, v118\n"
    "v_mfma_f32_16x16x32_bf16 v[100:103], a[16:19], a[144:147], v[100:103]\n"
    "ds_read_b64_tr_b16 a[4:5], v2 offset:35968\n"
    "v_mul_f32 v64, v64, v174\n"
    "v_mfma_f32_16x16x32_bf16 v[100:103], a[20:23], a[148:151], v[100:103]\n"
    "v_exp_f32 v195, v119\n"
    "v_mfma_f32_16x16x32_bf16 v[128:131], a[16:19], a[152:155], v[128:131]\n"
    "ds_read_b64_tr_b16 a[6:7], v2 offset:39552\n"
    "v_fma_f32 v192, v192, v16, v16\n"
    "v_mfma_f32_16x16x32_bf16 v[128:131], a[20:23], a[156:159], v[128:131]\n"
    "s_waitcnt lgkmcnt(4)\n"
    "v_exp_f32 v180, v36\n"
    "v_mfma_f32_16x16x32_bf16 v[48:51], a[24:27], a[64:67], v[48:51]\n"
    "ds_read_b64_tr_b16 a[8:9], v2 offset:43136\n"
    "v_fma_f32 v193, v193, v16, v16\n"
    "v_mfma_f32_16x16x32_bf16 v[48:51], a[28:31], a[68:71], v[48:51]\n"
    "v_exp_f32 v181, v37\n"
    "v_mfma_f32_16x16x32_bf16 v[76:79], a[24:27], a[72:75], v[76:79]\n"
    "ds_read_b64_tr_b16 a[10:11], v2 offset:46720\n"
    "v_fma_f32 v194, v194, v16, v16\n"
    "v_mfma_f32_16x16x32_bf16 v[76:79], a[28:31], a[76:79], v[76:79]\n"
    "v_exp_f32 v182, v38\n"
    "v_mfma_f32_16x16x32_bf16 v[104:107], a[24:27], a[80:83], v[104:107]\n"
    "ds_read_b64_tr_b16 a[12:13], v2 offset:50304\n"
    "v_fma_f32 v195, v195, v16, v16\n"
    "v_mfma_f32_16x16x32_bf16 v[104:107], a[28:31], a[84:87], v[104:107]\n"
    "v_rcp_f32 v192, v192\n"
    "v_mfma_f32_16x16x32_bf16 v[132:135], a[24:27], a[88:91], v[132:135]\n"
    "ds_read_b64_tr_b16 a[14:15], v2 offset:53888\n"
    "v_mul_f32 v65, v65, v174\n"
    "v_mfma_f32_16x16x32_bf16 v[132:135], a[28:31], a[92:95], v[132:135]\n"
    "s_waitcnt lgkmcnt(4)\n"
    "v_rcp_f32 v193, v193\n"
    "v_mfma_f32_16x16x32_bf16 v[48:51], a[0:3], a[96:99], v[48:51]\n"
    "ds_read_b64_tr_b16 a[16:17], v2 offset:14496\n"
    "v_mul_f32 v66, v66, v174\n"
    "v_mfma_f32_16x16x32_bf16 v[48:51], a[4:7], a[100:103], v[48:51]\n"
    "v_rcp_f32 v194, v194\n"
    "v_mfma_f32_16x16x32_bf16 v[76:79], a[0:3], a[104:107], v[76:79]\n"
    "ds_read_b64_tr_b16 a[18:19], v2 offset:18080\n"
    "v_mul_f32 v67, v67, v174\n"
    "v_mfma_f32_16x16x32_bf16 v[76:79], a[4:7], a[108:111], v[76:79]\n"
    "v_rcp_f32 v195, v195\n"
    "v_mfma_f32_16x16x32_bf16 v[104:107], a[0:3], a[112:115], v[104:107]\n"
    "ds_read_b64_tr_b16 a[20:21], v2 offset:21664\n"
    "v_mul_f32 v116, v116, v192\n"
    "v_mfma_f32_16x16x32_bf16 v[104:107], a[4:7], a[116:119], v[104:107]\n"
    "v_exp_f32 v183, v39\n"
    "v_mfma_f32_16x16x32_bf16 v[132:135], a[0:3], a[120:123], v[132:135]\n"
    "ds_read_b64_tr_b16 a[22:23], v2 offset:25248\n"
    "v_mul_f32 v117, v117, v193\n"
    "v_mfma_f32_16x16x32_bf16 v[132:135], a[4:7], a[124:127], v[132:135]\n"
    "s_waitcnt lgkmcnt(4)\n"
    "v_exp_f32 v184, v64\n"
    "v_mfma_f32_16x16x32_bf16 v[48:51], a[8:11], a[128:131], v[48:51]\n"
    "ds_read_b64_tr_b16 a[24:25], v2 offset:28832\n"
    "v_mul_f32 v118, v118, v194\n"
    "v_mfma_f32_16x16x32_bf16 v[48:51], a[12:15], a[132:135], v[48:51]\n"
    "v_exp_f32 v185, v65\n"
    "v_mfma_f32_16x16x32_bf16 v[76:79], a[8:11], a[136:139], v[76:79]\n"
    "ds_read_b64_tr_b16 a[26:27], v2 offset:32416\n"
    "v_mul_f32 v119, v119, v195\n"
    "v_mfma_f32_16x16x32_bf16 v[76:79], a[12:15], a[140:143], v[76:79]\n"
    "v_exp_f32 v186, v66\n"
    "v_mfma_f32_16x16x32_bf16 v[104:107], a[8:11], a[144:147], v[104:107]\n"
    "ds_read_b64_tr_b16 a[28:29], v2 offset:36000\n"
    "v_cvt_pk_bf16_f32 v220, v116, v117\n"
    "v_mfma_f32_16x16x32_bf16 v[104:107], a[12:15], a[148:151], v[104:107]\n"
    "v_exp_f32 v187, v67\n"
    "v_mfma_f32_16x16x32_bf16 v[132:135], a[8:11], a[152:155], v[132:135]\n"
    "ds_read_b64_tr_b16 a[30:31], v2 offset:39584\n"
    "v_cvt_pk_bf16_f32 v221, v118, v119\n"
    "v_mfma_f32_16x16x32_bf16 v[132:135], a[12:15], a[156:159], v[132:135]\n"
    "s_waitcnt lgkmcnt(4)\n"
    "v_fma_f32 v180, v180, v16, v16\n"
    "v_fma_f32 v181, v181, v16, v16\n"
    "v_mfma_f32_16x16x32_bf16 v[52:55], a[16:19], a[64:67], v[52:55]\n"
    "ds_read_b64_tr_b16 a[0:1], v2 offset:43168\n"
    "v_fma_f32 v182, v182, v16, v16\n"
    "v_mfma_f32_16x16x32_bf16 v[52:55], a[20:23], a[68:71], v[52:55]\n"
    "v_fma_f32 v183, v183, v16, v16\n"
    "v_fma_f32 v184, v184, v16, v16\n"
    "v_mfma_f32_16x16x32_bf16 v[80:83], a[16:19], a[72:75], v[80:83]\n"
    "ds_read_b64_tr_b16 a[2:3], v2 offset:46752\n"
    "v_fma_f32 v185, v185, v16, v16\n"
    "v_mfma_f32_16x16x32_bf16 v[80:83], a[20:23], a[76:79], v[80:83]\n"
    "v_rcp_f32 v180, v180\n"
    "v_mfma_f32_16x16x32_bf16 v[108:111], a[16:19], a[80:83], v[108:111]\n"
    "ds_read_b64_tr_b16 a[4:5], v2 offset:50336\n"
    "v_fma_f32 v186, v186, v16, v16\n"
    "v_mfma_f32_16x16x32_bf16 v[108:111], a[20:23], a[84:87], v[108:111]\n"
    "v_rcp_f32 v181, v181\n"
    "v_mfma_f32_16x16x32_bf16 v[136:139], a[16:19], a[88:91], v[136:139]\n"
    "ds_read_b64_tr_b16 a[6:7], v2 offset:53920\n"
    "v_fma_f32 v187, v187, v16, v16\n"
    "v_mfma_f32_16x16x32_bf16 v[136:139], a[20:23], a[92:95], v[136:139]\n"
    "s_waitcnt lgkmcnt(4)\n"
    "v_rcp_f32 v182, v182\n"
    "v_mfma_f32_16x16x32_bf16 v[52:55], a[24:27], a[96:99], v[52:55]\n"
    "ds_read_b64_tr_b16 a[8:9], v2 offset:14528\n"
    "v_mul_f32 v92, v92, v176\n"
    "v_mfma_f32_16x16x32_bf16 v[52:55], a[28:31], a[100:103], v[52:55]\n"
    "v_rcp_f32 v183, v183\n"
    "v_mfma_f32_16x16x32_bf16 v[80:83], a[24:27], a[104:107], v[80:83]\n"
    "ds_read_b64_tr_b16 a[10:11], v2 offset:18112\n"
    "v_mul_f32 v36, v36, v180\n"
    "v_mfma_f32_16x16x32_bf16 v[80:83], a[28:31], a[108:111], v[80:83]\n"
    "v_rcp_f32 v184, v184\n"
    "v_mfma_f32_16x16x32_bf16 v[108:111], a[24:27], a[112:115], v[108:111]\n"
    "ds_read_b64_tr_b16 a[12:13], v2 offset:21696\n"
    "v_mul_f32 v37, v37, v181\n"
    "v_mfma_f32_16x16x32_bf16 v[108:111], a[28:31], a[116:119], v[108:111]\n"
    "v_rcp_f32 v185, v185\n"
    "v_mfma_f32_16x16x32_bf16 v[136:139], a[24:27], a[120:123], v[136:139]\n"
    "ds_read_b64_tr_b16 a[14:15], v2 offset:25280\n"
    "v_mul_f32 v38, v38, v182\n"
    "v_mfma_f32_16x16x32_bf16 v[136:139], a[28:31], a[124:127], v[136:139]\n"
    "s_waitcnt lgkmcnt(4)\n"
    "v_rcp_f32 v186, v186\n"
    "v_mfma_f32_16x16x32_bf16 v[52:55], a[0:3], a[128:131], v[52:55]\n"
    "ds_read_b64_tr_b16 a[16:17], v2 offset:28864\n"
    "v_mul_f32 v39, v39, v183\n"
    "v_mfma_f32_16x16x32_bf16 v[52:55], a[4:7], a[132:135], v[52:55]\n"
    "v_rcp_f32 v187, v187\n"
    "v_mfma_f32_16x16x32_bf16 v[80:83], a[0:3], a[136:139], v[80:83]\n"
    "ds_read_b64_tr_b16 a[18:19], v2 offset:32448\n"
    "v_cvt_pk_bf16_f32 v198, v36, v37\n"
    "v_mfma_f32_16x16x32_bf16 v[80:83], a[4:7], a[140:143], v[80:83]\n"
    "v_cvt_pk_bf16_f32 v199, v38, v39\n"
    "v_mul_f32 v64, v64, v184\n"
    "v_mfma_f32_16x16x32_bf16 v[108:111], a[0:3], a[144:147], v[108:111]\n"
    "ds_read_b64_tr_b16 a[20:21], v2 offset:36032\n"
    "v_mul_f32 v65, v65, v185\n"
    "v_mfma_f32_16x16x32_bf16 v[108:111], a[4:7], a[148:151], v[108:111]\n"
    "buffer_store_dwordx4 v[196:199], v4, s[52:55], s80 offen sc1\n"
    "v_mfma_f32_16x16x32_bf16 v[136:139], a[0:3], a[152:155], v[136:139]\n"
    "ds_read_b64_tr_b16 a[22:23], v2 offset:39616\n"
    "v_mul_f32 v66, v66, v186\n"
    "v_mfma_f32_16x16x32_bf16 v[136:139], a[4:7], a[156:159], v[136:139]\n"
    "s_waitcnt lgkmcnt(4)\n"
    "v_mul_f32 v67, v67, v187\n"
    "v_cvt_pk_bf16_f32 v206, v64, v65\n"
    "v_mfma_f32_16x16x32_bf16 v[56:59], a[8:11], a[64:67], v[56:59]\n"
    "ds_read_b64_tr_b16 a[24:25], v2 offset:43200\n"
    "v_cvt_pk_bf16_f32 v207, v66, v67\n"
    "v_mfma_f32_16x16x32_bf16 v[56:59], a[12:15], a[68:71], v[56:59]\n"
    "buffer_store_dwordx4 v[204:207], v4, s[52:55], s80 offen offset:2048 sc1\n"
    "v_mfma_f32_16x16x32_bf16 v[84:87], a[8:11], a[72:75], v[84:87]\n"
    "ds_read_b64_tr_b16 a[26:27], v2 offset:46784\n"
    "v_mul_f32 v93, v93, v176\n"
    "v_mfma_f32_16x16x32_bf16 v[84:87], a[12:15], a[76:79], v[84:87]\n"
    "v_mul_f32 v94, v94, v176\n"
    "v_mul_f32 v95, v95, v176\n"
    "v_mfma_f32_16x16x32_bf16 v[112:115], a[8:11], a[80:83], v[112:115]\n"
    "ds_read_b64_tr_b16 a[28:29], v2 offset:50368\n"
    "v_mfma_f32_16x16x32_bf16 v[112:115], a[12:15], a[84:87], v[112:115]\n"
    "v_exp_f32 v188, v92\n"
    "v_mfma_f32_16x16x32_bf16 v[140:143], a[8:11], a[88:91], v[140:143]\n"
    "ds_read_b64_tr_b16 a[30:31], v2 offset:53952\n"
    "v_mfma_f32_16x16x32_bf16 v[140:143], a[12:15], a[92:95], v[140:143]\n"
    "s_waitcnt lgkmcnt(4)\n"
    "v_exp_f32 v189, v93\n"
    "v_mfma_f32_16x16x32_bf16 v[56:59], a[16:19], a[96:99], v[56:59]\n"
    "v_exp_f32 v190, v94\n"
    "v_mfma_f32_16x16x32_bf16 v[56:59], a[20:23], a[100:103], v[56:59]\n"
    "v_exp_f32 v191, v95\n"
    "v_mfma_f32_16x16x32_bf16 v[84:87], a[16:19], a[104:107], v[84:87]\n"
    "v_fma_f32 v188, v188, v16, v16\n"
    "v_fma_f32 v189, v189, v16, v16\n"
    "v_mfma_f32_16x16x32_bf16 v[84:87], a[20:23], a[108:111], v[84:87]\n"
    "v_fma_f32 v190, v190, v16, v16\n"
    "v_fma_f32 v191, v191, v16, v16\n"
    "v_mfma_f32_16x16x32_bf16 v[112:115], a[16:19], a[112:115], v[112:115]\n"
    "v_rcp_f32 v188, v188\n"
    "v_mfma_f32_16x16x32_bf16 v[112:115], a[20:23], a[116:119], v[112:115]\n"
    "v_rcp_f32 v189, v189\n"
    "v_mfma_f32_16x16x32_bf16 v[140:143], a[16:19], a[120:123], v[140:143]\n"
    "v_rcp_f32 v190, v190\n"
    "v_mfma_f32_16x16x32_bf16 v[140:143], a[20:23], a[124:127], v[140:143]\n"
    "s_waitcnt lgkmcnt(0)\n"
    "v_rcp_f32 v191, v191\n"
    "v_mfma_f32_16x16x32_bf16 v[56:59], a[24:27], a[128:131], v[56:59]\n"
    "v_mul_f32 v92, v92, v188\n"
    "v_mul_f32 v93, v93, v189\n"
    "v_mfma_f32_16x16x32_bf16 v[56:59], a[28:31], a[132:135], v[56:59]\n"
    "v_mul_f32 v94, v94, v190\n"
    "v_mul_f32 v95, v95, v191\n"
    "v_mfma_f32_16x16x32_bf16 v[84:87], a[24:27], a[136:139], v[84:87]\n"
    "v_cvt_pk_bf16_f32 v214, v92, v93\n"
    "v_cvt_pk_bf16_f32 v215, v94, v95\n"
    "v_mfma_f32_16x16x32_bf16 v[84:87], a[28:31], a[140:143], v[84:87]\n"
    "buffer_store_dwordx4 v[212:215], v5, s[52:55], s80 offen sc1\n"
    "v_mfma_f32_16x16x32_bf16 v[112:115], a[24:27], a[144:147], v[112:115]\n"
    "v_mfma_f32_16x16x32_bf16 v[112:115], a[28:31], a[148:151], v[112:115]\n"
    "v_mfma_f32_16x16x32_bf16 v[140:143], a[24:27], a[152:155], v[140:143]\n"
    "v_mfma_f32_16x16x32_bf16 v[140:143], a[28:31], a[156:159], v[140:143]\n"
    "s_nop 4\n"
    "v_pk_mul_f32 v[120:121], v[120:121], v[178:179] op_sel_hi:[1,0]\n"
    "v_pk_mul_f32 v[122:123], v[122:123], v[178:179] op_sel_hi:[1,0]\n"
    "v_exp_f32 v144, v120\n"
    "v_exp_f32 v145, v121\n"
    "v_exp_f32 v146, v122\n"
    "v_exp_f32 v147, v123\n"
    "v_pk_fma_f32 v[144:145], v[144:145], v[16:17], v[16:17] op_sel_hi:[1,0,0]\n"
    "v_pk_fma_f32 v[146:147], v[146:147], v[16:17], v[16:17] op_sel_hi:[1,0,0]\n"
    "v_rcp_f32 v144, v144\n"
    "v_rcp_f32 v145, v145\n"
    "v_rcp_f32 v146, v146\n"
    "v_rcp_f32 v147, v147\n"
    "v_pk_mul_f32 v[120:121], v[120:121], v[144:145]\n"
    "v_pk_mul_f32 v[122:123], v[122:123], v[146:147]\n"
    "v_cvt_pk_bf16_f32 v222, v120, v121\n"
    "v_cvt_pk_bf16_f32 v223, v122, v123\n"
    "buffer_store_dwordx4 v[220:223], v5, s[52:55], s80 offen offset:2048 sc1\n"
    "v_pk_mul_f32 v[40:41], v[40:41], v[172:173] op_sel_hi:[1,0]\n"
    "v_pk_mul_f32 v[42:43], v[42:43], v[172:173] op_sel_hi:[1,0]\n"
    "v_pk_mul_f32 v[44:45], v[44:45], v[172:173] op_sel_hi:[1,0]\n"
    "v_pk_mul_f32 v[46:47], v[46:47], v[172:173] op_sel_hi:[1,0]\n"
    "v_exp_f32 v144, v40\n"
    "v_exp_f32 v145, v41\n"
    "v_exp_f32 v146, v42\n"
    "v_exp_f32 v147, v43\n"
    "v_exp_f32 v148, v44\n"
    "v_exp_f32 v149, v45\n"
    "v_exp_f32 v150, v46\n"
    "v_exp_f32 v151, v47\n"
    "v_pk_fma_f32 v[144:145], v[144:145], v[16:17], v[16:17] op_sel_hi:[1,0,0]\n"
    "v_pk_fma_f32 v[146:147], v[146:147], v[16:17], v[16:17] op_sel_hi:[1,0,0]\n"
    "v_pk_fma_f32 v[148:149], v[148:149], v[16:17], v[16:17] op_sel_hi:[1,0,0]\n"
    "v_pk_fma_f32 v[150:151], v[150:151], v[16:17], v[16:17] op_sel_hi:[1,0,0]\n"
    "v_rcp_f32 v144, v144\n"
    "v_rcp_f32 v145, v145\n"
    "v_rcp_f32 v146, v146\n"
    "v_rcp_f32 v147, v147\n"
    "v_rcp_f32 v148, v148\n"
    "v_rcp_f32 v149, v149\n"
    "v_rcp_f32 v150, v150\n"
    "v_rcp_f32 v151, v151\n"
    "v_pk_mul_f32 v[40:41], v[40:41], v[144:145]\n"
    "v_pk_mul_f32 v[42:43], v[42:43], v[146:147]\n"
    "v_pk_mul_f32 v[44:45], v[44:45], v[148:149]\n"
    "v_pk_mul_f32 v[46:47], v[46:47], v[150:151]\n"
    "v_cvt_pk_bf16_f32 v200, v40, v41\n"
    "v_cvt_pk_bf16_f32 v201, v42, v43\n"
    "v_cvt_pk_bf16_f32 v202, v44, v45\n"
    "v_cvt_pk_bf16_f32 v203, v46, v47\n"
    "buffer_store_dwordx4 v[200:203], v4, s[52:55], s81 offen sc1\n"
    "v_pk_mul_f32 v[68:69], v[68:69], v[174:175] op_sel_hi:[1,0]\n"
    "v_pk_mul_f32 v[70:71], v[70:71], v[174:175] op_sel_hi:[1,0]\n"
    "v_pk_mul_f32 v[72:73], v[72:73], v[174:175] op_sel_hi:[1,0]\n"
    "v_pk_mul_f32 v[74:75], v[74:75], v[174:175] op_sel_hi:[1,0]\n"
    "v_exp_f32 v144, v68\n"
    "v_exp_f32 v145, v69\n"
    "v_exp_f32 v146, v70\n"
    "v_exp_f32 v147, v71\n"
    "v_exp_f32 v148, v72\n"
    "v_exp_f32 v149, v73\n"
    "v_exp_f32 v150, v74\n"
    "v_exp_f32 v151, v75\n"
    "v_pk_fma_f32 v[144:145], v[144:145], v[16:17], v[16:17] op_sel_hi:[1,0,0]\n"
    "v_pk_fma_f32 v[146:147], v[146:147], v[16:17], v[16:17] op_sel_hi:[1,0,0]\n"
    "v_pk_fma_f32 v[148:149], v[148:149], v[16:17], v[16:17] op_sel_hi:[1,0,0]\n"
    "v_pk_fma_f32 v[150:151], v[150:151], v[16:17], v[16:17] op_sel_hi:[1,0,0]\n"
    "v_rcp_f32 v144, v144\n"
    "v_rcp_f32 v145, v145\n"
    "v_rcp_f32 v146, v146\n"
    "v_rcp_f32 v147, v147\n"
    "v_rcp_f32 v148, v148\n"
    "v_rcp_f32 v149, v149\n"
    "v_rcp_f32 v150, v150\n"
    "v_rcp_f32 v151, v151\n"
    "v_pk_mul_f32 v[68:69], v[68:69], v[144:145]\n"
    "v_pk_mul_f32 v[70:71], v[70:71], v[146:147]\n"
    "v_pk_mul_f32 v[72:73], v[72:73], v[148:149]\n"
    "v_pk_mul_f32 v[74:75], v[74:75], v[150:151]\n"
    "v_cvt_pk_bf16_f32 v208, v68, v69\n"
    "v_cvt_pk_bf16_f32 v209, v70, v71\n"
    "v_cvt_pk_bf16_f32 v210, v72, v73\n"
    "v_cvt_pk_bf16_f32 v211, v74, v75\n"
    "buffer_store_dwordx4 v[208:211], v4, s[52:55], s81 offen offset:2048 sc1\n"
    "v_pk_mul_f32 v[96:97], v[96:97], v[176:177] op_sel_hi:[1,0]\n"
    "v_pk_mul_f32 v[98:99], v[98:99], v[176:177] op_sel_hi:[1,0]\n"
    "v_pk_mul_f32 v[100:101], v[100:101], v[176:177] op_sel_hi:[1,0]\n"
    "v_pk_mul_f32 v[102:103], v[102:103], v[176:177] op_sel_hi:[1,0]\n"
    "v_exp_f32 v144, v96\n"
    "v_exp_f32 v145, v97\n"
    "v_exp_f32 v146, v98\n"
    "v_exp_f32 v147, v99\n"
    "v_exp_f32 v148, v100\n"
    "v_exp_f32 v149, v101\n"
    "v_exp_f32 v150, v102\n"
    "v_exp_f32 v151, v103\n"
    "v_pk_fma_f32 v[144:145], v[144:145], v[16:17], v[16:17] op_sel_hi:[1,0,0]\n"
    "v_pk_fma_f32 v[146:147], v[146:147], v[16:17], v[16:17] op_sel_hi:[1,0,0]\n"
    "v_pk_fma_f32 v[148:149], v[148:149], v[16:17], v[16:17] op_sel_hi:[1,0,0]\n"
    "v_pk_fma_f32 v[150:151], v[150:151], v[16:17], v[16:17] op_sel_hi:[1,0,0]\n"
    "v_rcp_f32 v144, v144\n"
    "v_rcp_f32 v145, v145\n"
    "v_rcp_f32 v146, v146\n"
    "v_rcp_f32 v147, v147\n"
    "v_rcp_f32 v148, v148\n"
    "v_rcp_f32 v149, v149\n"
    "v_rcp_f32 v150, v150\n"
    "v_rcp_f32 v151, v151\n"
    "v_pk_mul_f32 v[96:97], v[96:97], v[144:145]\n"
    "v_pk_mul_f32 v[98:99], v[98:99], v[146:147]\n"
    "v_pk_mul_f32 v[100:101], v[100:101], v[148:149]\n"
    "v_pk_mul_f32 v[102:103], v[102:103], v[150:151]\n"
    "v_cvt_pk_bf16_f32 v216, v96, v97\n"
    "v_cvt_pk_bf16_f32 v217, v98, v99\n"
    "v_cvt_pk_bf16_f32 v218, v100, v101\n"
    "v_cvt_pk_bf16_f32 v219, v102, v103\n"
    "buffer_store_dwordx4 v[216:219], v5, s[52:55], s81 offen sc1\n"
    "v_pk_mul_f32 v[124:125], v[124:125], v[178:179] op_sel_hi:[1,0]\n"
    "v_pk_mul_f32 v[126:127], v[126:127], v[178:179] op_sel_hi:[1,0]\n"
    "v_pk_mul_f32 v[128:129], v[128:129], v[178:179] op_sel_hi:[1,0]\n"
    "v_pk_mul_f32 v[130:131], v[130:131], v[178:179] op_sel_hi:[1,0]\n"
    "v_exp_f32 v144, v124\n"
    "v_exp_f32 v145, v125\n"
    "v_exp_f32 v146, v126\n"
    "v_exp_f32 v147, v127\n"
    "v_exp_f32 v148, v128\n"
    "v_exp_f32 v149, v129\n"
    "v_exp_f32 v150, v130\n"
    "v_exp_f32 v151, v131\n"
    "v_pk_fma_f32 v[144:145], v[144:145], v[16:17], v[16:17] op_sel_hi:[1,0,0]\n"
    "v_pk_fma_f32 v[146:147], v[146:147], v[16:17], v[16:17] op_sel_hi:[1,0,0]\n"
    "v_pk_fma_f32 v[148:149], v[148:149], v[16:17], v[16:17] op_sel_hi:[1,0,0]\n"
    "v_pk_fma_f32 v[150:151], v[150:151], v[16:17], v[16:17] op_sel_hi:[1,0,0]\n"
    "v_rcp_f32 v144, v144\n"
    "v_rcp_f32 v145, v145\n"
    "v_rcp_f32 v146, v146\n"
    "v_rcp_f32 v147, v147\n"
    "v_rcp_f32 v148, v148\n"
    "v_rcp_f32 v149, v149\n"
    "v_rcp_f32 v150, v150\n"
    "v_rcp_f32 v151, v151\n"
    "v_pk_mul_f32 v[124:125], v[124:125], v[144:145]\n"
    "v_pk_mul_f32 v[126:127], v[126:127], v[146:147]\n"
    "v_pk_mul_f32 v[128:129], v[128:129], v[148:149]\n"
    "v_pk_mul_f32 v[130:131], v[130:131], v[150:151]\n"
    "v_cvt_pk_bf16_f32 v224, v124, v125\n"
    "v_cvt_pk_bf16_f32 v225, v126, v127\n"
    "v_cvt_pk_bf16_f32 v226, v128, v129\n"
    "v_cvt_pk_bf16_f32 v227, v130, v131\n"
    "buffer_store_dwordx4 v[224:227], v5, s[52:55], s81 offen offset:2048 sc1\n"
    "v_pk_mul_f32 v[48:49], v[48:49], v[172:173] op_sel_hi:[1,0]\n"
    "v_pk_mul_f32 v[50:51], v[50:51], v[172:173] op_sel_hi:[1,0]\n"
    "v_pk_mul_f32 v[52:53], v[52:53], v[172:173] op_sel_hi:[1,0]\n"
    "v_pk_mul_f32 v[54:55], v[54:55], v[172:173] op_sel_hi:[1,0]\n"
    "v_exp_f32 v144, v48\n"
    "v_exp_f32 v145, v49\n"
    "v_exp_f32 v146, v50\n"
    "v_exp_f32 v147, v51\n"
    "v_exp_f32 v148, v52\n"
    "v_exp_f32 v149, v53\n"
    "v_exp_f32 v150, v54\n"
    "v_exp_f32 v151, v55\n"
    "v_pk_fma_f32 v[144:145], v[144:145], v[16:17], v[16:17] op_sel_hi:[1,0,0]\n"
    "v_pk_fma_f32 v[146:147], v[146:147], v[16:17], v[16:17] op_sel_hi:[1,0,0]\n"
    "v_pk_fma_f32 v[148:149], v[148:149], v[16:17], v[16:17] op_sel_hi:[1,0,0]\n"
    "v_pk_fma_f32 v[150:151], v[150:151], v[16:17], v[16:17] op_sel_hi:[1,0,0]\n"
    "v_rcp_f32 v144, v144\n"
    "v_rcp_f32 v145, v145\n"
    "v_rcp_f32 v146, v146\n"
    "v_rcp_f32 v147, v147\n"
    "v_rcp_f32 v148, v148\n"
    "v_rcp_f32 v149, v149\n"
    "v_rcp_f32 v150, v150\n"
    "v_rcp_f32 v151, v151\n"
    "v_pk_mul_f32 v[48:49], v[48:49], v[144:145]\n"
    "v_pk_mul_f32 v[50:51], v[50:51], v[146:147]\n"
    "v_pk_mul_f32 v[52:53], v[52:53], v[148:149]\n"
    "v_pk_mul_f32 v[54:55], v[54:55], v[150:151]\n"
    "v_cvt_pk_bf16_f32 v196, v48, v49\n"
    "v_cvt_pk_bf16_f32 v197, v50, v51\n"
    "v_cvt_pk_bf16_f32 v198, v52, v53\n"
    "v_cvt_pk_bf16_f32 v199, v54, v55\n"
    "buffer_store_dwordx4 v[196:199], v4, s[52:55], s82 offen sc1\n"
    "v_pk_mul_f32 v[76:77], v[76:77], v[174:175] op_sel_hi:[1,0]\n"
    "v_pk_mul_f32 v[78:79], v[78:79], v[174:175] op_sel_hi:[1,0]\n"
    "v_pk_mul_f32 v[80:81], v[80:81], v[174:175] op_sel_hi:[1,0]\n"
    "v_pk_mul_f32 v[82:83], v[82:83], v[174:175] op_sel_hi:[1,0]\n"
    "v_exp_f32 v144, v76\n"
    "v_exp_f32 v145, v77\n"
    "v_exp_f32 v146, v78\n"
    "v_exp_f32 v147, v79\n"
    "v_exp_f32 v148, v80\n"
    "v_exp_f32 v149, v81\n"
    "v_exp_f32 v150, v82\n"
    "v_exp_f32 v151, v83\n"
    "v_pk_fma_f32 v[144:145], v[144:145], v[16:17], v[16:17] op_sel_hi:[1,0,0]\n"
    "v_pk_fma_f32 v[146:147], v[146:147], v[16:17], v[16:17] op_sel_hi:[1,0,0]\n"
    "v_pk_fma_f32 v[148:149], v[148:149], v[16:17], v[16:17] op_sel_hi:[1,0,0]\n"
    "v_pk_fma_f32 v[150:151], v[150:151], v[16:17], v[16:17] op_sel_hi:[1,0,0]\n"
    "v_rcp_f32 v144, v144\n"
    "v_rcp_f32 v145, v145\n"
    "v_rcp_f32 v146, v146\n"
    "v_rcp_f32 v147, v147\n"
    "v_rcp_f32 v148, v148\n"
    "v_rcp_f32 v149, v149\n"
    "v_rcp_f32 v150, v150\n"
    "v_rcp_f32 v151, v151\n"
    "v_pk_mul_f32 v[76:77], v[76:77], v[144:145]\n"
    "v_pk_mul_f32 v[78:79], v[78:79], v[146:147]\n"
    "v_pk_mul_f32 v[80:81], v[80:81], v[148:149]\n"
    "v_pk_mul_f32 v[82:83], v[82:83], v[150:151]\n"
    "v_cvt_pk_bf16_f32 v204, v76, v77\n"
    "v_cvt_pk_bf16_f32 v205, v78, v79\n"
    "v_cvt_pk_bf16_f32 v206, v80, v81\n"
    "v_cvt_pk_bf16_f32 v207, v82, v83\n"
    "buffer_store_dwordx4 v[204:207], v4, s[52:55], s82 offen offset:2048 sc1\n"
    "v_pk_mul_f32 v[104:105], v[104:105], v[176:177] op_sel_hi:[1,0]\n"
    "v_pk_mul_f32 v[106:107], v[106:107], v[176:177] op_sel_hi:[1,0]\n"
    "v_pk_mul_f32 v[108:109], v[108:109], v[176:177] op_sel_hi:[1,0]\n"
    "v_pk_mul_f32 v[110:111], v[110:111], v[176:177] op_sel_hi:[1,0]\n"
    "v_exp_f32 v144, v104\n"
    "v_exp_f32 v145, v105\n"
    "v_exp_f32 v146, v106\n"
    "v_exp_f32 v147, v107\n"
    "v_exp_f32 v148, v108\n"
    "v_exp_f32 v149, v109\n"
    "v_exp_f32 v150, v110\n"
    "v_exp_f32 v151, v111\n"
    "v_pk_fma_f32 v[144:145], v[144:145], v[16:17], v[16:17] op_sel_hi:[1,0,0]\n"
    "v_pk_fma_f32 v[146:147], v[146:147], v[16:17], v[16:17] op_sel_hi:[1,0,0]\n"
    "v_pk_fma_f32 v[148:149], v[148:149], v[16:17], v[16:17] op_sel_hi:[1,0,0]\n"
    "v_pk_fma_f32 v[150:151], v[150:151], v[16:17], v[16:17] op_sel_hi:[1,0,0]\n"
    "v_rcp_f32 v144, v144\n"
    "v_rcp_f32 v145, v145\n"
    "v_rcp_f32 v146, v146\n"
    "v_rcp_f32 v147, v147\n"
    "v_rcp_f32 v148, v148\n"
    "v_rcp_f32 v149, v149\n"
    "v_rcp_f32 v150, v150\n"
    "v_rcp_f32 v151, v151\n"
    "v_pk_mul_f32 v[104:105], v[104:105], v[144:145]\n"
    "v_pk_mul_f32 v[106:107], v[106:107], v[146:147]\n"
    "v_pk_mul_f32 v[108:109], v[108:109], v[148:149]\n"
    "v_pk_mul_f32 v[110:111], v[110:111], v[150:151]\n"
    "v_cvt_pk_bf16_f32 v212, v104, v105\n"
    "v_cvt_pk_bf16_f32 v213, v106, v107\n"
    "v_cvt_pk_bf16_f32 v214, v108, v109\n"
    "v_cvt_pk_bf16_f32 v215, v110, v111\n"
    "buffer_store_dwordx4 v[212:215], v5, s[52:55], s82 offen sc1\n"
    "v_pk_mul_f32 v[132:133], v[132:133], v[178:179] op_sel_hi:[1,0]\n"
    "v_pk_mul_f32 v[134:135], v[134:135], v[178:179] op_sel_hi:[1,0]\n"
    "v_pk_mul_f32 v[136:137], v[136:137], v[178:179] op_sel_hi:[1,0]\n"
    "v_pk_mul_f32 v[138:139], v[138:139], v[178:179] op_sel_hi:[1,0]\n"
    "v_exp_f32 v144, v132\n"
    "v_exp_f32 v145, v133\n"
    "v_exp_f32 v146, v134\n"
    "v_exp_f32 v147, v135\n"
    "v_exp_f32 v148, v136\n"
    "v_exp_f32 v149, v137\n"
    "v_exp_f32 v150, v138\n"
    "v_exp_f32 v151, v139\n"
    "v_pk_fma_f32 v[144:145], v[144:145], v[16:17], v[16:17] op_sel_hi:[1,0,0]\n"
    "v_pk_fma_f32 v[146:147], v[146:147], v[16:17], v[16:17] op_sel_hi:[1,0,0]\n"
    "v_pk_fma_f32 v[148:149], v[148:149], v[16:17], v[16:17] op_sel_hi:[1,0,0]\n"
    "v_pk_fma_f32 v[150:151], v[150:151], v[16:17], v[16:17] op_sel_hi:[1,0,0]\n"
    "v_rcp_f32 v144, v144\n"
    "v_rcp_f32 v145, v145\n"
    "v_rcp_f32 v146, v146\n"
    "v_rcp_f32 v147, v147\n"
    "v_rcp_f32 v148, v148\n"
    "v_rcp_f32 v149, v149\n"
    "v_rcp_f32 v150, v150\n"
    "v_rcp_f32 v151, v151\n"
    "v_pk_mul_f32 v[132:133], v[132:133], v[144:145]\n"
    "v_pk_mul_f32 v[134:135], v[134:135], v[146:147]\n"
    "v_pk_mul_f32 v[136:137], v[136:137], v[148:149]\n"
    "v_pk_mul_f32 v[138:139], v[138:139], v[150:151]\n"
    "v_cvt_pk_bf16_f32 v220, v132, v133\n"
    "v_cvt_pk_bf16_f32 v221, v134, v135\n"
    "v_cvt_pk_bf16_f32 v222, v136, v137\n"
    "v_cvt_pk_bf16_f32 v223, v138, v139\n"
    "buffer_store_dwordx4 v[220:223], v5, s[52:55], s82 offen offset:2048 sc1\n"
    "v_pk_mul_f32 v[56:57], v[56:57], v[172:173] op_sel_hi:[1,0]\n"
    "v_pk_mul_f32 v[58:59], v[58:59], v[172:173] op_sel_hi:[1,0]\n"
    "v_exp_f32 v144, v56\n"
    "v_exp_f32 v145, v57\n"
    "v_exp_f32 v146, v58\n"
    "v_exp_f32 v147, v59\n"
    "v_pk_fma_f32 v[144:145], v[144:145], v[16:17], v[16:17] op_sel_hi:[1,0,0]\n"
    "v_pk_fma_f32 v[146:147], v[146:147], v[16:17], v[16:17] op_sel_hi:[1,0,0]\n"
    "v_rcp_f32 v144, v144\n"
    "v_rcp_f32 v145, v145\n"
    "v_rcp_f32 v146, v146\n"
    "v_rcp_f32 v147, v147\n"
    "v_pk_mul_f32 v[56:57], v[56:57], v[144:145]\n"
    "v_pk_mul_f32 v[58:59], v[58:59], v[146:147]\n"
    "v_cvt_pk_bf16_f32 v200, v56, v57\n"
    "v_cvt_pk_bf16_f32 v201, v58, v59\n"
    "buffer_store_dwordx2 v[200:201], v4, s[52:55], s83 offen sc1\n"
    "v_pk_mul_f32 v[84:85], v[84:85], v[174:175] op_sel_hi:[1,0]\n"
    "v_pk_mul_f32 v[86:87], v[86:87], v[174:175] op_sel_hi:[1,0]\n"
    "v_exp_f32 v144, v84\n"
    "v_exp_f32 v145, v85\n"
    "v_exp_f32 v146, v86\n"
    "v_exp_f32 v147, v87\n"
    "v_pk_fma_f32 v[144:145], v[144:145], v[16:17], v[16:17] op_sel_hi:[1,0,0]\n"
    "v_pk_fma_f32 v[146:147], v[146:147], v[16:17], v[16:17] op_sel_hi:[1,0,0]\n"
    "v_rcp_f32 v144, v144\n"
    "v_rcp_f32 v145, v145\n"
    "v_rcp_f32 v146, v146\n"
    "v_rcp_f32 v147, v147\n"
    "v_pk_mul_f32 v[84:85], v[84:85], v[144:145]\n"
    "v_pk_mul_f32 v[86:87], v[86:87], v[146:147]\n"
    "v_cvt_pk_bf16_f32 v208, v84, v85\n"
    "v_cvt_pk_bf16_f32 v209, v86, v87\n"
    "buffer_store_dwordx2 v[208:209], v4, s[52:55], s83 offen offset:2048 sc1\n"
    "v_pk_mul_f32 v[112:113], v[112:113], v[176:177] op_sel_hi:[1,0]\n"
    "v_pk_mul_f32 v[114:115], v[114:115], v[176:177] op_sel_hi:[1,0]\n"
    "v_exp_f32 v144, v112\n"
    "v_exp_f32 v145, v113\n"
    "v_exp_f32 v146, v114\n"
    "v_exp_f32 v147, v115\n"
    "v_pk_fma_f32 v[144:145], v[144:145], v[16:17], v[16:17] op_sel_hi:[1,0,0]\n"
    "v_pk_fma_f32 v[146:147], v[146:147], v[16:17], v[16:17] op_sel_hi:[1,0,0]\n"
    "v_rcp_f32 v144, v144\n"
    "v_rcp_f32 v145, v145\n"
    "v_rcp_f32 v146, v146\n"
    "v_rcp_f32 v147, v147\n"
    "v_pk_mul_f32 v[112:113], v[112:113], v[144:145]\n"
    "v_pk_mul_f32 v[114:115], v[114:115], v[146:147]\n"
    "v_cvt_pk_bf16_f32 v216, v112, v113\n"
    "v_cvt_pk_bf16_f32 v217, v114, v115\n"
    "buffer_store_dwordx2 v[216:217], v5, s[52:55], s83 offen sc1\n"
    "v_pk_mul_f32 v[140:141], v[140:141], v[178:179] op_sel_hi:[1,0]\n"
    "v_pk_mul_f32 v[142:143], v[142:143], v[178:179] op_sel_hi:[1,0]\n"
    "v_exp_f32 v144, v140\n"
    "v_exp_f32 v145, v141\n"
    "v_exp_f32 v146, v142\n"
    "v_exp_f32 v147, v143\n"
    "v_pk_fma_f32 v[144:145], v[144:145], v[16:17], v[16:17] op_sel_hi:[1,0,0]\n"
    "v_pk_fma_f32 v[146:147], v[146:147], v[16:17], v[16:17] op_sel_hi:[1,0,0]\n"
    "v_rcp_f32 v144, v144\n"
    "v_rcp_f32 v145, v145\n"
    "v_rcp_f32 v146, v146\n"
    "v_rcp_f32 v147, v147\n"
    "v_pk_mul_f32 v[140:141], v[140:141], v[144:145]\n"
    "v_pk_mul_f32 v[142:143], v[142:143], v[146:147]\n"
    "v_cvt_pk_bf16_f32 v224, v140, v141\n"
    "v_cvt_pk_bf16_f32 v225, v142, v143\n"
    "buffer_store_dwordx2 v[224:225], v5, s[52:55], s83 offen offset:2048 sc1\n"
    "k_ffn1_code_end:\n"
    "k_ffn2:\n"
    "s_mov_b32 s80, s100\n"
    "s_mov_b64 s[34:35], s[20:21]\n"
    "s_mov_b64 s[36:37], s[24:25]\n"
    "s_and_b32 s83, s30, 1\n"
    "s_lshr_b32 s86, s30, 3\n"
    "s_bfe_u32 s81, s30, 0x10002\n"
    "s_lshl_b32 s81, s81, 3\n"
    "s_lshr_b32 s75, s86, 2\n"
    "s_add_u32 s81, s81, s75\n"
    "s_and_b32 s82, s30, 2\n"
    "s_lshl_b32 s82, s82, 1\n"
    "s_and_b32 s75, s86, 3\n"
    "s_add_u32 s82, s82, s75\n"
    "s_mov_b32 s44, s34\n"
    "s_mov_b32 s45, s35\n"
    "s_mov_b32 s46, 0x700000\n"
    "s_mov_b32 s47, 0x20000\n"
    "s_add_u32 s40, s32, 0x2c00000\n"
    "s_addc_u32 s41, s33, 0\n"
    "s_mov_b32 s42, 0xe00000\n"
    "s_mov_b32 s43, 0x20000\n"
    "s_cmp_eq_u32 s83, 0\n"
    "s_cselect_b32 s46, 0x340000, s46\n"
    "s_cselect_b32 s42, 0x680000, s42\n"
    "s_mul_i32 s72, s83, 0x680000\n"
    "s_mul_i32 s73, s83, 0x340000\n"
    "v_lshrrev_b32 v18, 4, v1\n"
    "v_bfe_u32 v19, v1, 1, 3\n"
    "v_and_b32 v20, 1, v1\n"
    "v_lshlrev_b32 v20, 4, v20\n"
    "v_lshl_add_u32 v20, v18, 11, v20\n"
    "v_xor_b32 v19, v19, v18\n"
    "v_xor_b32 v21, 4, v19\n"
    "s_lshl_b32 s85, s82, 8\n"
    "v_add_u32 v20, s85, v20\n"
    "v_lshl_add_u32 v4, v19, 5, v20\n"
    "v_lshl_add_u32 v5, v21, 5, v20\n"
    "v_add_u32 v6, 0x3800, v4\n"
    "v_add_u32 v7, 0x5400, v5\n"
    "v_add_u32 v5, 0x1c00, v5\n"
    "s_lshl_b32 s85, s80, 16\n"
    "s_add_u32 s73, s73, s85\n"
    "s_add_u32 s74, s73, 0x8000\n"
    "s_lshl_b32 s87, s80, 13\n"
    "s_add_u32 s88, s87, 0x1000\n"
    "s_mov_b32 s76, 0\n"
    "s_lshl_b32 s84, s81, 3\n"
    "s_lshl_b32 s86, s80, 1\n"
    "s_add_u32 s84, s84, s86\n"
    "s_lshl_b32 s86, s84, 11\n"
    "v_lshlrev_b32 v3, 4, v1\n"
    "v_add_u32 v2, s86, v3\n"
    "s_barrier\n"
    "s_add_u32 m0, s76, s87\n"
    "s_nop 0\n"
    "buffer_load_dwordx4 v4, s[44:47], s73 offen lds\n"
    "buffer_load_dwordx4 v5, s[44:47], s73 offen offset:1024 lds\n"
    "buffer_load_dwordx4 v6, s[44:47], s73 offen offset:2048 lds\n"
    "buffer_load_dwordx4 v7, s[44:47], s73 offen offset:3072 lds\n"
    "s_add_u32 m0, s76, s88\n"
    "s_nop 0\n"
    "buffer_load_dwordx4 v4, s[44:47], s74 offen lds\n"
    "buffer_load_dwordx4 v5, s[44:47], s74 offen offset:1024 lds\n"
    "buffer_load_dwordx4 v6, s[44:47], s74 offen offset:2048 lds\n"
    "buffer_load_dwordx4 v7, s[44:47], s74 offen offset:3072 lds\n"
    "s_add_u32 s73, s73, 0x40000\n"
    "s_add_u32 s74, s74, 0x40000\n"
    "s_add_u32 s76, s76, 0x8000\n"
    "s_cmp_lt_u32 s76, 0x28000\n"
    "s_cselect_b32 s76, s76, 0\n"
    "s_add_u32 m0, s76, s87\n"
    "s_nop 0\n"
    "buffer_load_dwordx4 v4, s[44:47], s73 offen lds\n"
    "buffer_load_dwordx4 v5, s[44:47], s73 offen offset:1024 lds\n"
    "buffer_load_dwordx4 v6, s[44:47], s73 offen offset:2048 lds\n"
    "buffer_load_dwordx4 v7, s[44:47], s73 offen offset:3072 lds\n"
    "s_add_u32 m0, s76, s88\n"
    "s_nop 0\n"
    "buffer_load_dwordx4 v4, s[44:47], s74 offen lds\n"
    "buffer_load_dwordx4 v5, s[44:47], s74 offen offset:1024 lds\n"
    "buffer_load_dwordx4 v6, s[44:47], s74 offen offset:2048 lds\n"
    "buffer_load_dwordx4 v7, s[44:47], s74 offen offset:3072 lds\n"
    "s_add_u32 s73, s73, 0x40000\n"
    "s_add_u32 s74, s74, 0x40000\n"
    "s_add_u32 s76, s76, 0x8000\n"
    "s_cmp_lt_u32 s76, 0x28000\n"
    "s_cselect_b32 s76, s76, 0\n"
    "s_add_u32 s56, s32, 0x18000\n"
    "s_addc_u32 s57, s33, 0\n"
    "s_mov_b32 s58, 0x8000\n"
    "s_mov_b32 s59, 0x20000\n"
    "s_lshl_b32 s85, s30, 7\n"
    "v_mov_b32 v24, s94\n"
    "v_mov_b32 v25, s95\n"
    "s_waitcnt vmcnt(16)\n"
    "s_barrier\n"
    "s_cmp_lg_u32 s80, 0\n"
    "s_cbranch_scc1 k_ffn2_np\n"
    "s_mov_b64 exec, 1\n buffer_store_dwordx2 v[24:25], off, s[56:59], s85 offset:16 sc1\n s_mov_b64 exec, -1\n"
    "k_ffn2_np:\n"
    "s_mov_b32 s79, 0xfffe0000\n"
    "s_add_u32 s56, s32, 0x18000\n"
    "s_addc_u32 s57, s33, 0\n"
    "s_mov_b32 s58, 0x8000\n"
    "s_mov_b32 s59, 0x20000\n"
    "s_cmp_eq_u32 s83, 0\n"
    "s_cselect_b32 s85, 0, 14\n"
    "s_cselect_b32 s86, 14, 31\n"
    "v_add_u32 v18, s85, v1\n"
    "v_min_u32 v18, s86, v18\n"
    "s_bfe_u32 s85, s81, 0x20001\n"
    "s_lshl_b32 s85, s85, 13\n"
    "s_lshr_b32 s86, s81, 3\n"
    "s_lshl_b32 s86, s86, 7\n"
    "s_add_u32 s85, s85, s86\n"
    "s_add_u32 s85, s85, 16\n"
    "v_and_b32 v19, 7, v18\n"
    "v_lshrrev_b32 v18, 3, v18\n"
    "v_lshl_add_u32 v19, v19, 10, s85\n"
    "v_lshl_add_u32 v19, v18, 8, v19\n"
    "s_cmp_lg_u32 s80, 0\n"
    "s_cbranch_scc1 k_ffn2_nw\n"
    "s_mov_b32 s91, 1048576\n"
    "k_ffn2_h_spin:\n"
    "buffer_load_dwordx2 v[244:245], v19, s[56:59], 0 offen sc1\n"
    "s_waitcnt vmcnt(0)\n"
    "v_cmp_ne_u64 vcc, s[94:95], v[244:245]\n"
    "s_sub_u32 s91, s91, 1\n"
    "s_cmp_eq_u64 vcc, 0\n"
    "s_cbranch_scc1 k_ffn2_h_go\n"
    "s_sleep 1\n"
    "s_cmp_lg_u32 s91, 0\n"
    "s_cbranch_scc1 k_ffn2_h_spin\n"
    "k_ffn2_h_go:\n"
    "k_ffn2_nw:\n"
    "s_waitcnt vmcnt(8)\n"
    "s_barrier\n"
    "buffer_load_dwordx4 a[32:35], v2, s[40:43], s72 offen sc1\n"
    "buffer_load_dwordx4 a[36:39], v2, s[40:43], s72 offen offset:1024 sc1\n"
    "buffer_load_dwordx4 a[40:43], v2, s[40:43], s72 offen offset:2048 sc1\n"
    "buffer_load_dwordx4 a[44:47], v2, s[40:43], s72 offen offset:3072 sc1\n"
    "s_add_u32 s72, s72, 0x40000\n"
    "buffer_load_dwordx4 a[48:51], v2, s[40:43], s72 offen sc1\n"
    "buffer_load_dwordx4 a[52:55], v2, s[40:43], s72 offen offset:1024 sc1\n"
    "buffer_load_dwordx4 a[56:59], v2, s[40:43], s72 offen offset:2048 sc1\n"
    "buffer_load_dwordx4 a[60:63], v2, s[40:43], s72 offen offset:3072 sc1\n"
    "s_add_u32 s72, s72, 0x40000\n"
    "buffer_load_dword v0, off, s[56:59], s79\n"
    "buffer_load_dword v0, off, s[56:59], s79\n"
    "buffer_load_dword v0, off, s[56:59], s79\n"
    "buffer_load_dword v0, off, s[56:59], s79\n"
    "buffer_load_dword v0, off, s[56:59], s79\n"
    "buffer_load_dword v0, off, s[56:59], s79\n"
    "buffer_load_dword v0, off, s[56:59], s79\n"
    "buffer_load_dword v0, off, s[56:59], s79\n"
    "buffer_load_dwordx4 a[64:67], v2, s[40:43], s72 offen sc1\n"
    "buffer_load_dwordx4 a[68:71], v2, s[40:43], s72 offen offset:1024 sc1\n"
    "buffer_load_dwordx4 a[72:75], v2, s[40:43], s72 offen offset:2048 sc1\n"
    "buffer_load_dwordx4 a[76:79], v2, s[40:43], s72 offen offset:3072 sc1\n"
    "s_add_u32 s72, s72, 0x40000\n"
    "v_lshrrev_b32 v18, 2, v1\n"
    "v_and_b32 v19, 7, v18\n"
    "v_and_b32 v20, 3, v1\n"
    "v_lshlrev_b32 v20, 3, v20\n"
    "v_lshl_add_u32 v20, v18, 8, v20\n"
    "v_xor_b32 v21, 0, v19\n"
    "v_lshl_add_u32 v8, v21, 5, v20\n"
    "v_xor_b32 v21, 1, v19\n"
    "v_lshl_add_u32 v9, v21, 5, v20\n"
    "v_xor_b32 v21, 2, v19\n"
    "v_lshl_add_u32 v10, v21, 5, v20\n"
    "v_xor_b32 v21, 3, v19\n"
    "v_lshl_add_u32 v11, v21, 5, v20\n"
    "v_xor_b32 v21, 4, v19\n"
    "v_lshl_add_u32 v12, v21, 5, v20\n"
    "v_xor_b32 v21, 5, v19\n"
    "v_lshl_add_u32 v13, v21, 5, v20\n"
    "v_xor_b32 v21, 6, v19\n"
    "v_lshl_add_u32 v14, v21, 5, v20\n"
    "v_xor_b32 v21, 7, v19\n"
    "v_lshl_add_u32 v15, v21, 5, v20\n"
    "s_mov_b32 s77, 0\n"
    "s_mov_b32 s79, 0xfffe0000\n"
    "s_add_u32 s60, s32, 0x4c00000\n"
    "s_addc_u32 s61, s33, 0\n"
    "s_mov_b32 s62, 0x400000\n"
    "s_mov_b32 s63, 0x20000\n"
    "s_add_u32 s64, s32, 0x8000\n"
    "s_addc_u32 s65, s33, 0\n"
    "s_mov_b32 s66, 0x4000\n"
    "s_mov_b32 s67, 0x20000\n"
    "s_lshl_b32 s85, s81, 3\n"
    "s_add_u32 s85, s85, s82\n"
    "s_lshl_b32 s85, s85, 2\n"
    "s_add_u32 s85, s85, s80\n"
    "s_lshl_b32 s89, s85, 13\n"
    "s_lshl_b32 s90, s85, 5\n"
    "v_and_b32 v18, 15, v1\n"
    "v_lshrrev_b32 v19, 4, v1\n"
    "v_lshlrev_b32 v19, 2, v19\n"
    "v_sub_u32 v18, v18, v19\n"
    "v_and_b32 v19, 1, v18\n"
    "v_lshlrev_b32 v19, 4, v19\n"
    "v_mov_b32 v20, 0x3f80\n"
    "v_lshlrev_b32 v19, v19, v20\n"
    "v_cmp_gt_u32 vcc, 2, v18\n"
    "v_subrev_u32 v18, 2, v18\n"
    "v_mov_b32 v224, 0\n"
    "v_cndmask_b32 v226, 0, v19, vcc\n"
    "v_cmp_gt_u32 vcc, 2, v18\n"
    "v_mov_b32 v225, 0\n"
    "v_mov_b32 v228, 0\n"
    "v_cndmask_b32 v227, 0, v19, vcc\n"
    "v_mov_b32 v229, 0\n"
    "s_cmp_eq_u32 s83, 0\n"
    "s_cselect_b32 s75, 6, 6\n"
    "v_mov_b32 v32, 0\n"
    "v_mov_b32 v33, 0\n"
    "v_mov_b32 v34, 0\n"
    "v_mov_b32 v35, 0\n"
    "v_mov_b32 v36, 0\n"
    "v_mov_b32 v37, 0\n"
    "v_mov_b32 v38, 0\n"
    "v_mov_b32 v39, 0\n"
    "v_mov_b32 v40, 0\n"
    "v_mov_b32 v41, 0\n"
    "v_mov_b32 v42, 0\n"
    "v_mov_b32 v43, 0\n"
    "v_mov_b32 v44, 0\n"
    "v_mov_b32 v45, 0\n"
    "v_mov_b32 v46, 0\n"
    "v_mov_b32 v47, 0\n"
    "v_mov_b32 v48, 0\n"
    "v_mov_b32 v49, 0\n"
    "v_mov_b32 v50, 0\n"
    "v_mov_b32 v51, 0\n"
    "v_mov_b32 v52, 0\n"
    "v_mov_b32 v53, 0\n"
    "v_mov_b32 v54, 0\n"
    "v_mov_b32 v55, 0\n"
    "v_mov_b32 v56, 0\n"
    "v_mov_b32 v57, 0\n"
    "v_mov_b32 v58, 0\n"
    "v_mov_b32 v59, 0\n"
    "v_mov_b32 v60, 0\n"
    "v_mov_b32 v61, 0\n"
    "v_mov_b32 v62, 0\n"
    "v_mov_b32 v63, 0\n"
    "v_mov_b32 v64, 0\n"
    "v_mov_b32 v65, 0\n"
    "v_mov_b32 v66, 0\n"
    "v_mov_b32 v67, 0\n"
    "v_mov_b32 v68, 0\n"
    "v_mov_b32 v69, 0\n"
    "v_mov_b32 v70, 0\n"
    "v_mov_b32 v71, 0\n"
    "v_mov_b32 v72, 0\n"
    "v_mov_b32 v73, 0\n"
    "v_mov_b32 v74, 0\n"
    "v_mov_b32 v75, 0\n"
    "v_mov_b32 v76, 0\n"
    "v_mov_b32 v77, 0\n"
    "v_mov_b32 v78, 0\n"
    "v_mov_b32 v79, 0\n"
    "v_mov_b32 v80, 0\n"
    "v_mov_b32 v81, 0\n"
    "v_mov_b32 v82, 0\n"
    "v_mov_b32 v83, 0\n"
    "v_mov_b32 v84, 0\n"
    "v_mov_b32 v85, 0\n"
    "v_mov_b32 v86, 0\n"
    "v_mov_b32 v87, 0\n"
    "v_mov_b32 v88, 0\n"
    "v_mov_b32 v89, 0\n"
    "v_mov_b32 v90, 0\n"
    "v_mov_b32 v91, 0\n"
    "v_mov_b32 v92, 0\n"
    "v_mov_b32 v93, 0\n"
    "v_mov_b32 v94, 0\n"
    "v_mov_b32 v95, 0\n"
    "ds_read_b64_tr_b16 a[0:1], v8\n"
    "ds_read_b64_tr_b16 a[2:3], v8 offset:4096\n"
    "ds_read_b64_tr_b16 a[4:5], v8 offset:8192\n"
    "ds_read_b64_tr_b16 a[6:7], v8 offset:12288\n"
    "ds_read_b64_tr_b16 a[8:9], v9\n"
    "ds_read_b64_tr_b16 a[10:11], v9 offset:4096\n"
    "ds_read_b64_tr_b16 a[12:13], v9 offset:8192\n"
    "ds_read_b64_tr_b16 a[14:15], v9 offset:12288\n"
    "ds_read_b64_tr_b16 a[16:17], v10\n"
    "ds_read_b64_tr_b16 a[18:19], v10 offset:4096\n"
    "ds_read_b64_tr_b16 a[20:21], v10 offset:8192\n"
    "ds_read_b64_tr_b16 a[22:23], v10 offset:12288\n"
    "k_ffn2_loop:\n"
    "s_waitcnt vmcnt(16) lgkmcnt(8)\n"
    "v_mfma_f32_16x16x32_bf16 v[32:35], a[0:3], a[32:35], v[32:35]\n"
    "ds_read_b64_tr_b16 a[24:25], v11\n"
    "v_mfma_f32_16x16x32_bf16 v[32:35], a[4:7], a[36:39], v[32:35]\n"
    "ds_read_b64_tr_b16 a[26:27], v11 offset:4096\n"
    "buffer_load_dwordx4 a[80:83], v2, s[40:43], s72 offen sc1\n"
    "v_mfma_f32_16x16x32_bf16 v[64:67], a[0:3], a[40:43], v[64:67]\n"
    "s_add_u32 m0, s76, s87\n"
    "ds_read_b64_tr_b16 a[28:29], v11 offset:8192\n"
    "v_mfma_f32_16x16x32_bf16 v[64:67], a[4:7], a[44:47], v[64:67]\n"
    "s_waitcnt lgkmcnt(7)\n"
    "ds_read_b64_tr_b16 a[30:31], v11 offset:12288\n"
    "v_mfma_f32_16x16x32_bf16 v[36:39], a[8:11], a[32:35], v[36:39]\n"
    "ds_read_b64_tr_b16 a[0:1], v12\n"
    "buffer_load_dwordx4 v4, s[44:47], s73 offen lds\n"
    "v_mfma_f32_16x16x32_bf16 v[36:39], a[12:15], a[36:39], v[36:39]\n"
    "ds_read_b64_tr_b16 a[2:3], v12 offset:4096\n"
    "v_mfma_f32_16x16x32_bf16 v[68:71], a[8:11], a[40:43], v[68:71]\n"
    "ds_read_b64_tr_b16 a[4:5], v12 offset:8192\n"
    "v_mfma_f32_16x16x32_bf16 v[68:71], a[12:15], a[44:47], v[68:71]\n"
    "s_waitcnt lgkmcnt(7)\n"
    "ds_read_b64_tr_b16 a[6:7], v12 offset:12288\n"
    "v_mfma_f32_16x16x32_bf16 v[40:43], a[16:19], a[32:35], v[40:43]\n"
    "ds_read_b64_tr_b16 a[8:9], v13\n"
    "buffer_load_dwordx4 a[84:87], v2, s[40:43], s72 offen offset:1024 sc1\n"
    "v_mfma_f32_16x16x32_bf16 v[40:43], a[20:23], a[36:39], v[40:43]\n"
    "ds_read_b64_tr_b16 a[10:11], v13 offset:4096\n"
    "v_mfma_f32_16x16x32_bf16 v[72:75], a[16:19], a[40:43], v[72:75]\n"
    "ds_read_b64_tr_b16 a[12:13], v13 offset:8192\n"
    "v_mfma_f32_16x16x32_bf16 v[72:75], a[20:23], a[44:47], v[72:75]\n"
    "s_waitcnt lgkmcnt(7)\n"
    "ds_read_b64_tr_b16 a[14:15], v13 offset:12288\n"
    "v_mfma_f32_16x16x32_bf16 v[44:47], a[24:27], a[32:35], v[44:47]\n"
    "ds_read_b64_tr_b16 a[16:17], v14\n"
    "buffer_load_dwordx4 v5, s[44:47], s73 offen offset:1024 lds\n"
    "v_mfma_f32_16x16x32_bf16 v[44:47], a[28:31], a[36:39], v[44:47]\n"
    "ds_read_b64_tr_b16 a[18:19], v14 offset:4096\n"
    "v_mfma_f32_16x16x32_bf16 v[76:79], a[24:27], a[40:43], v[76:79]\n"
    "ds_read_b64_tr_b16 a[20:21], v14 offset:8192\n"
    "v_mfma_f32_16x16x32_bf16 v[76:79], a[28:31], a[44:47], v[76:79]\n"
    "s_waitcnt lgkmcnt(7)\n"
    "ds_read_b64_tr_b16 a[22:23], v14 offset:12288\n"
    "v_mfma_f32_16x16x32_bf16 v[48:51], a[0:3], a[32:35], v[48:51]\n"
    "ds_read_b64_tr_b16 a[24:25], v15\n"
    "buffer_load_dwordx4 a[88:91], v2, s[40:43], s72 offen offset:2048 sc1\n"
    "v_mfma_f32_16x16x32_bf16 v[48:51], a[4:7], a[36:39], v[48:51]\n"
    "ds_read_b64_tr_b16 a[26:27], v15 offset:4096\n"
    "v_mfma_f32_16x16x32_bf16 v[80:83], a[0:3], a[40:43], v[80:83]\n"
    "s_add_u32 s77, s77, 1\n"
    "ds_read_b64_tr_b16 a[28:29], v15 offset:8192\n"
    "v_mfma_f32_16x16x32_bf16 v[80:83], a[4:7], a[44:47], v[80:83]\n"
    "s_waitcnt lgkmcnt(7)\n"
    "ds_read_b64_tr_b16 a[30:31], v15 offset:12288\n"
    "v_mfma_f32_16x16x32_bf16 v[52:55], a[8:11], a[32:35], v[52:55]\n"
    "ds_read_b64_tr_b16 a[0:1], v8 offset:16384\n"
    "buffer_load_dwordx4 v6, s[44:47], s73 offen offset:2048 lds\n"
    "v_mfma_f32_16x16x32_bf16 v[52:55], a[12:15], a[36:39], v[52:55]\n"
    "s_cmp_lt_u32 s77, 5\n"
    "s_cselect_b32 s77, s77, 0\n"
    "s_cselect_b32 s78, 0x8000, s79\n"
    "ds_read_b64_tr_b16 a[2:3], v8 offset:20480\n"
    "v_mfma_f32_16x16x32_bf16 v[84:87], a[8:11], a[40:43], v[84:87]\n"
    "ds_read_b64_tr_b16 a[4:5], v8 offset:24576\n"
    "v_mfma_f32_16x16x32_bf16 v[84:87], a[12:15], a[44:47], v[84:87]\n"
    "s_waitcnt lgkmcnt(7)\n"
    "ds_read_b64_tr_b16 a[6:7], v8 offset:28672\n"
    "v_mfma_f32_16x16x32_bf16 v[56:59], a[16:19], a[32:35], v[56:59]\n"
    "ds_read_b64_tr_b16 a[8:9], v9 offset:16384\n"
    "v_add_u32 v8, s78, v8\n"
    "v_mfma_f32_16x16x32_bf16 v[56:59], a[20:23], a[36:39], v[56:59]\n"
    "ds_read_b64_tr_b16 a[10:11], v9 offset:20480\n"
    "buffer_load_dwordx4 a[92:95], v2, s[40:43], s72 offen offset:3072 sc1\n"
    "v_mfma_f32_16x16x32_bf16 v[88:91], a[16:19], a[40:43], v[88:91]\n"
    "s_add_u32 s72, s72, 0x40000\n"
    "ds_read_b64_tr_b16 a[12:13], v9 offset:24576\n"
    "v_mfma_f32_16x16x32_bf16 v[88:91], a[20:23], a[44:47], v[88:91]\n"
    "s_waitcnt lgkmcnt(7)\n"
    "ds_read_b64_tr_b16 a[14:15], v9 offset:28672\n"
    "v_mfma_f32_16x16x32_bf16 v[60:63], a[24:27], a[32:35], v[60:63]\n"
    "ds_read_b64_tr_b16 a[16:17], v10 offset:16384\n"
    "v_add_u32 v9, s78, v9\n"
    "v_mfma_f32_16x16x32_bf16 v[60:63], a[28:31], a[36:39], v[60:63]\n"
    "ds_read_b64_tr_b16 a[18:19], v10 offset:20480\n"
    "buffer_load_dwordx4 v7, s[44:47], s73 offen offset:3072 lds\n"
    "v_mfma_f32_16x16x32_bf16 v[92:95], a[24:27], a[40:43], v[92:95]\n"
    "ds_read_b64_tr_b16 a[20:21], v10 offset:24576\n"
    "v_mfma_f32_16x16x32_bf16 v[92:95], a[28:31], a[44:47], v[92:95]\n"
    "s_waitcnt vmcnt(17) lgkmcnt(7)\n"
    "ds_read_b64_tr_b16 a[22:23], v10 offset:28672\n"
    "v_mfma_f32_16x16x32_bf16 v[32:35], a[0:3], a[48:51], v[32:35]\n"
    "ds_read_b64_tr_b16 a[24:25], v11 offset:16384\n"
    "v_add_u32 v10, s78, v10\n"
    "v_mfma_f32_16x16x32_bf16 v[32:35], a[4:7], a[52:55], v[32:35]\n"
    "ds_read_b64_tr_b16 a[26:27], v11 offset:20480\n"
    "buffer_load_dwordx4 a[32:35], v2, s[40:43], s72 offen sc1\n"
    "v_mfma_f32_16x16x32_bf16 v[64:67], a[0:3], a[56:59], v[64:67]\n"
    "s_add_u32 m0, s76, s88\n"
    "ds_read_b64_tr_b16 a[28:29], v11 offset:24576\n"
    "v_mfma_f32_16x16x32_bf16 v[64:67], a[4:7], a[60:63], v[64:67]\n"
    "s_waitcnt lgkmcnt(7)\n"
    "ds_read_b64_tr_b16 a[30:31], v11 offset:28672\n"
    "v_mfma_f32_16x16x32_bf16 v[36:39], a[8:11], a[48:51], v[36:39]\n"
    "ds_read_b64_tr_b16 a[0:1], v12 offset:16384\n"
    "v_add_u32 v11, s78, v11\n"
    "v_mfma_f32_16x16x32_bf16 v[36:39], a[12:15], a[52:55], v[36:39]\n"
    "ds_read_b64_tr_b16 a[2:3], v12 offset:20480\n"
    "buffer_load_dwordx4 v4, s[44:47], s74 offen lds\n"
    "v_mfma_f32_16x16x32_bf16 v[68:71], a[8:11], a[56:59], v[68:71]\n"
    "ds_read_b64_tr_b16 a[4:5], v12 offset:24576\n"
    "v_mfma_f32_16x16x32_bf16 v[68:71], a[12:15], a[60:63], v[68:71]\n"
    "s_waitcnt lgkmcnt(7)\n"
    "ds_read_b64_tr_b16 a[6:7], v12 offset:28672\n"
    "v_mfma_f32_16x16x32_bf16 v[40:43], a[16:19], a[48:51], v[40:43]\n"
    "ds_read_b64_tr_b16 a[8:9], v13 offset:16384\n"
    "v_add_u32 v12, s78, v12\n"
    "v_mfma_f32_16x16x32_bf16 v[40:43], a[20:23], a[52:55], v[40:43]\n"
    "ds_read_b64_tr_b16 a[10:11], v13 offset:20480\n"
    "buffer_load_dwordx4 a[36:39], v2, s[40:43], s72 offen offset:1024 sc1\n"
    "v_mfma_f32_16x16x32_bf16 v[72:75], a[16:19], a[56:59], v[72:75]\n"
    "ds_read_b64_tr_b16 a[12:13], v13 offset:24576\n"
    "v_mfma_f32_16x16x32_bf16 v[72:75], a[20:23], a[60:63], v[72:75]\n"
    "s_waitcnt lgkmcnt(7)\n"
    "ds_read_b64_tr_b16 a[14:15], v13 offset:28672\n"
    "v_mfma_f32_16x16x32_bf16 v[44:47], a[24:27], a[48:51], v[44:47]\n"
    "ds_read_b64_tr_b16 a[16:17], v14 offset:16384\n"
    "v_add_u32 v13, s78, v13\n"
    "v_mfma_f32_16x16x32_bf16 v[44:47], a[28:31], a[52:55], v[44:47]\n"
    "ds_read_b64_tr_b16 a[18:19], v14 offset:20480\n"
    "buffer_load_dwordx4 v5, s[44:47], s74 offen offset:1024 lds\n"
    "v_mfma_f32_16x16x32_bf16 v[76:79], a[24:27], a[56:59], v[76:79]\n"
    "ds_read_b64_tr_b16 a[20:21], v14 offset:24576\n"
    "v_mfma_f32_16x16x32_bf16 v[76:79], a[28:31], a[60:63], v[76:79]\n"
    "s_waitcnt lgkmcnt(7)\n"
    "ds_read_b64_tr_b16 a[22:23], v14 offset:28672\n"
    "v_mfma_f32_16x16x32_bf16 v[48:51], a[0:3], a[48:51], v[48:51]\n"
    "ds_read_b64_tr_b16 a[24:25], v15 offset:16384\n"
    "v_add_u32 v14, s78, v14\n"
    "v_mfma_f32_16x16x32_bf16 v[48:51], a[4:7], a[52:55], v[48:51]\n"
    "ds_read_b64_tr_b16 a[26:27], v15 offset:20480\n"
    "buffer_load_dwordx4 a[40:43], v2, s[40:43], s72 offen offset:2048 sc1\n"
    "v_mfma_f32_16x16x32_bf16 v[80:83], a[0:3], a[56:59], v[80:83]\n"
    "ds_read_b64_tr_b16 a[28:29], v15 offset:24576\n"
    "v_mfma_f32_16x16x32_bf16 v[80:83], a[4:7], a[60:63], v[80:83]\n"
    "s_waitcnt vmcnt(13) lgkmcnt(7)\n"
    "s_barrier\n"
    "ds_read_b64_tr_b16 a[30:31], v15 offset:28672\n"
    "v_add_u32 v15, s78, v15\n"
    "v_mfma_f32_16x16x32_bf16 v[52:55], a[8:11], a[48:51], v[52:55]\n"
    "ds_read_b64_tr_b16 a[0:1], v8\n"
    "buffer_load_dwordx4 v6, s[44:47], s74 offen offset:2048 lds\n"
    "v_mfma_f32_16x16x32_bf16 v[52:55], a[12:15], a[52:55], v[52:55]\n"
    "ds_read_b64_tr_b16 a[2:3], v8 offset:4096\n"
    "v_mfma_f32_16x16x32_bf16 v[84:87], a[8:11], a[56:59], v[84:87]\n"
    "ds_read_b64_tr_b16 a[4:5], v8 offset:8192\n"
    "v_mfma_f32_16x16x32_bf16 v[84:87], a[12:15], a[60:63], v[84:87]\n"
    "s_waitcnt lgkmcnt(7)\n"
    "ds_read_b64_tr_b16 a[6:7], v8 offset:12288\n"
    "v_mfma_f32_16x16x32_bf16 v[56:59], a[16:19], a[48:51], v[56:59]\n"
    "ds_read_b64_tr_b16 a[8:9], v9\n"
    "buffer_load_dwordx4 a[44:47], v2, s[40:43], s72 offen offset:3072 sc1\n"
    "v_mfma_f32_16x16x32_bf16 v[56:59], a[20:23], a[52:55], v[56:59]\n"
    "s_add_u32 s72, s72, 0x40000\n"
    "ds_read_b64_tr_b16 a[10:11], v9 offset:4096\n"
    "v_mfma_f32_16x16x32_bf16 v[88:91], a[16:19], a[56:59], v[88:91]\n"
    "ds_read_b64_tr_b16 a[12:13], v9 offset:8192\n"
    "v_mfma_f32_16x16x32_bf16 v[88:91], a[20:23], a[60:63], v[88:91]\n"
    "s_waitcnt lgkmcnt(7)\n"
    "ds_read_b64_tr_b16 a[14:15], v9 offset:12288\n"
    "v_mfma_f32_16x16x32_bf16 v[60:63], a[24:27], a[48:51], v[60:63]\n"
    "ds_read_b64_tr_b16 a[16:17], v10\n"
    "buffer_load_dwordx4 v7, s[44:47], s74 offen offset:3072 lds\n"
    "v_mfma_f32_16x16x32_bf16 v[60:63], a[28:31], a[52:55], v[60:63]\n"
    "s_add_u32 s73, s73, 0x40000\n"
    "ds_read_b64_tr_b16 a[18:19], v10 offset:4096\n"
    "v_mfma_f32_16x16x32_bf16 v[92:95], a[24:27], a[56:59], v[92:95]\n"
    "s_add_u32 s74, s74, 0x40000\n"
    "ds_read_b64_tr_b16 a[20:21], v10 offset:8192\n"
    "v_mfma_f32_16x16x32_bf16 v[92:95], a[28:31], a[60:63], v[92:95]\n"
    "s_add_u32 s76, s76, 0x8000\n"
    "s_cmp_lt_u32 s76, 0x28000\n"
    "s_cselect_b32 s76, s76, 0\n"
    "ds_read_b64_tr_b16 a[22:23], v10 offset:12288\n"
    "s_waitcnt vmcnt(16) lgkmcnt(8)\n"
    "v_mfma_f32_16x16x32_bf16 v[32:35], a[0:3], a[64:67], v[32:35]\n"
    "ds_read_b64_tr_b16 a[24:25], v11\n"
    "v_mfma_f32_16x16x32_bf16 v[32:35], a[4:7], a[68:71], v[32:35]\n"
    "ds_read_b64_tr_b16 a[26:27], v11 offset:4096\n"
    "buffer_load_dwordx4 a[48:51], v2, s[40:43], s72 offen sc1\n"
    "v_mfma_f32_16x16x32_bf16 v[64:67], a[0:3], a[72:75], v[64:67]\n"
    "s_add_u32 m0, s76, s87\n"
    "ds_read_b64_tr_b16 a[28:29], v11 offset:8192\n"
    "v_mfma_f32_16x16x32_bf16 v[64:67], a[4:7], a[76:79], v[64:67]\n"
    "s_waitcnt lgkmcnt(7)\n"
    "ds_read_b64_tr_b16 a[30:31], v11 offset:12288\n"
    "v_mfma_f32_16x16x32_bf16 v[36:39], a[8:11], a[64:67], v[36:39]\n"
    "ds_read_b64_tr_b16 a[0:1], v12\n"
    "buffer_load_dwordx4 v4, s[44:47], s73 offen lds\n"
    "v_mfma_f32_16x16x32_bf16 v[36:39], a[12:15], a[68:71], v[36:39]\n"
    "ds_read_b64_tr_b16 a[2:3], v12 offset:4096\n"
    "v_mfma_f32_16x16x32_bf16 v[68:71], a[8:11], a[72:75], v[68:71]\n"
    "ds_read_b64_tr_b16 a[4:5], v12 offset:8192\n"
    "v_mfma_f32_16x16x32_bf16 v[68:71], a[12:15], a[76:79], v[68:71]\n"
    "s_waitcnt lgkmcnt(7)\n"
    "ds_read_b64_tr_b16 a[6:7], v12 offset:12288\n"
    "v_mfma_f32_16x16x32_bf16 v[40:43], a[16:19], a[64:67], v[40:43]\n"
    "ds_read_b64_tr_b16 a[8:9], v13\n"
    "buffer_load_dwordx4 a[52:55], v2, s[40:43], s72 offen offset:1024 sc1\n"
    "v_mfma_f32_16x16x32_bf16 v[40:43], a[20:23], a[68:71], v[40:43]\n"
    "ds_read_b64_tr_b16 a[10:11], v13 offset:4096\n"
    "v_mfma_f32_16x16x32_bf16 v[72:75], a[16:19], a[72:75], v[72:75]\n"
    "ds_read_b64_tr_b16 a[12:13], v13 offset:8192\n"
    "v_mfma_f32_16x16x32_bf16 v[72:75], a[20:23], a[76:79], v[72:75]\n"
    "s_waitcnt lgkmcnt(7)\n"
    "ds_read_b64_tr_b16 a[14:15], v13 offset:12288\n"
    "v_mfma_f32_16x16x32_bf16 v[44:47], a[24:27], a[64:67], v[44:47]\n"
    "ds_read_b64_tr_b16 a[16:17], v14\n"
    "buffer_load_dwordx4 v5, s[44:47], s73 offen offset:1024 lds\n"
    "v_mfma_f32_16x16x32_bf16 v[44:47], a[28:31], a[68:71], v[44:47]\n"
    "ds_read_b64_tr_b16 a[18:19], v14 offset:4096\n"
    "v_mfma_f32_16x16x32_bf16 v[76:79], a[24:27], a[72:75], v[76:79]\n"
    "ds_read_b64_tr_b16 a[20:21], v14 offset:8192\n"
    "v_mfma_f32_16x16x32_bf16 v[76:79], a[28:31], a[76:79], v[76:79]\n"
    "s_waitcnt lgkmcnt(7)\n"
    "ds_read_b64_tr_b16 a[22:23], v14 offset:12288\n"
    "v_mfma_f32_16x16x32_bf16 v[48:51], a[0:3], a[64:67], v[48:51]\n"
    "ds_read_b64_tr_b16 a[24:25], v15\n"
    "buffer_load_dwordx4 a[56:59], v2, s[40:43], s72 offen offset:2048 sc1\n"
    "v_mfma_f32_16x16x32_bf16 v[48:51], a[4:7], a[68:71], v[48:51]\n"
    "ds_read_b64_tr_b16 a[26:27], v15 offset:4096\n"
    "v_mfma_f32_16x16x32_bf16 v[80:83], a[0:3], a[72:75], v[80:83]\n"
    "s_add_u32 s77, s77, 1\n"
    "ds_read_b64_tr_b16 a[28:29], v15 offset:8192\n"
    "v_mfma_f32_16x16x32_bf16 v[80:83], a[4:7], a[76:79], v[80:83]\n"
    "s_waitcnt lgkmcnt(7)\n"
    "ds_read_b64_tr_b16 a[30:31], v15 offset:12288\n"
    "v_mfma_f32_16x16x32_bf16 v[52:55], a[8:11], a[64:67], v[52:55]\n"
    "ds_read_b64_tr_b16 a[0:1], v8 offset:16384\n"
    "buffer_load_dwordx4 v6, s[44:47], s73 offen offset:2048 lds\n"
    "v_mfma_f32_16x16x32_bf16 v[52:55], a[12:15], a[68:71], v[52:55]\n"
    "s_cmp_lt_u32 s77, 5\n"
    "s_cselect_b32 s77, s77, 0\n"
    "s_cselect_b32 s78, 0x8000, s79\n"
    "ds_read_b64_tr_b16 a[2:3], v8 offset:20480\n"
    "v_mfma_f32_16x16x32_bf16 v[84:87], a[8:11], a[72:75], v[84:87]\n"
    "ds_read_b64_tr_b16 a[4:5], v8 offset:24576\n"
    "v_mfma_f32_16x16x32_bf16 v[84:87], a[12:15], a[76:79], v[84:87]\n"
    "s_waitcnt lgkmcnt(7)\n"
    "ds_read_b64_tr_b16 a[6:7], v8 offset:28672\n"
    "v_mfma_f32_16x16x32_bf16 v[56:59], a[16:19], a[64:67], v[56:59]\n"
    "ds_read_b64_tr_b16 a[8:9], v9 offset:16384\n"
    "v_add_u32 v8, s78, v8\n"
    "v_mfma_f32_16x16x32_bf16 v[56:59], a[20:23], a[68:71], v[56:59]\n"
    "ds_read_b64_tr_b16 a[10:11], v9 offset:20480\n"
    "buffer_load_dwordx4 a[60:63], v2, s[40:43], s72 offen offset:3072 sc1\n"
    "v_mfma_f32_16x16x32_bf16 v[88:91], a[16:19], a[72:75], v[88:91]\n"
    "s_add_u32 s72, s72, 0x40000\n"
    "ds_read_b64_tr_b16 a[12:13], v9 offset:24576\n"
    "v_mfma_f32_16x16x32_bf16 v[88:91], a[20:23], a[76:79], v[88:91]\n"
    "s_waitcnt lgkmcnt(7)\n"
    "ds_read_b64_tr_b16 a[14:15], v9 offset:28672\n"
    "v_mfma_f32_16x16x32_bf16 v[60:63], a[24:27], a[64:67], v[60:63]\n"
    "ds_read_b64_tr_b16 a[16:17], v10 offset:16384\n"
    "v_add_u32 v9, s78, v9\n"
    "v_mfma_f32_16x16x32_bf16 v[60:63], a[28:31], a[68:71], v[60:63]\n"
    "ds_read_b64_tr_b16 a[18:19], v10 offset:20480\n"
    "buffer_load_dwordx4 v7, s[44:47], s73 offen offset:3072 lds\n"
    "v_mfma_f32_16x16x32_bf16 v[92:95], a[24:27], a[72:75], v[92:95]\n"
    "ds_read_b64_tr_b16 a[20:21], v10 offset:24576\n"
    "v_mfma_f32_16x16x32_bf16 v[92:95], a[28:31], a[76:79], v[92:95]\n"
    "s_waitcnt vmcnt(17) lgkmcnt(7)\n"
    "ds_read_b64_tr_b16 a[22:23], v10 offset:28672\n"
    "v_mfma_f32_16x16x32_bf16 v[32:35], a[0:3], a[80:83], v[32:35]\n"
    "ds_read_b64_tr_b16 a[24:25], v11 offset:16384\n"
    "v_add_u32 v10, s78, v10\n"
    "v_mfma_f32_16x16x32_bf16 v[32:35], a[4:7], a[84:87], v[32:35]\n"
    "ds_read_b64_tr_b16 a[26:27], v11 offset:20480\n"
    "buffer_load_dwordx4 a[64:67], v2, s[40:43], s72 offen sc1\n"
    "v_mfma_f32_16x16x32_bf16 v[64:67], a[0:3], a[88:91], v[64:67]\n"
    "s_add_u32 m0, s76, s88\n"
    "ds_read_b64_tr_b16 a[28:29], v11 offset:24576\n"
    "v_mfma_f32_16x16x32_bf16 v[64:67], a[4:7], a[92:95], v[64:67]\n"
    "s_waitcnt lgkmcnt(7)\n"
    "ds_read_b64_tr_b16 a[30:31], v11 offset:28672\n"
    "v_mfma_f32_16x16x32_bf16 v[36:39], a[8:11], a[80:83], v[36:39]\n"
    "ds_read_b64_tr_b16 a[0:1], v12 offset:16384\n"
    "v_add_u32 v11, s78, v11\n"
    "v_mfma_f32_16x16x32_bf16 v[36:39], a[12:15], a[84:87], v[36:39]\n"
    "ds_read_b64_tr_b16 a[2:3], v12 offset:20480\n"
    "buffer_load_dwordx4 v4, s[44:47], s74 offen lds\n"
    "v_mfma_f32_16x16x32_bf16 v[68:71], a[8:11], a[88:91], v[68:71]\n"
    "ds_read_b64_tr_b16 a[4:5], v12 offset:24576\n"
    "v_mfma_f32_16x16x32_bf16 v[68:71], a[12:15], a[92:95], v[68:71]\n"
    "s_waitcnt lgkmcnt(7)\n"
    "ds_read_b64_tr_b16 a[6:7], v12 offset:28672\n"
    "v_mfma_f32_16x16x32_bf16 v[40:43], a[16:19], a[80:83], v[40:43]\n"
    "ds_read_b64_tr_b16 a[8:9], v13 offset:16384\n"
    "v_add_u32 v12, s78, v12\n"
    "v_mfma_f32_16x16x32_bf16 v[40:43], a[20:23], a[84:87], v[40:43]\n"
    "ds_read_b64_tr_b16 a[10:11], v13 offset:20480\n"
    "buffer_load_dwordx4 a[68:71], v2, s[40:43], s72 offen offset:1024 sc1\n"
    "v_mfma_f32_16x16x32_bf16 v[72:75], a[16:19], a[88:91], v[72:75]\n"
    "ds_read_b64_tr_b16 a[12:13], v13 offset:24576\n"
    "v_mfma_f32_16x16x32_bf16 v[72:75], a[20:23], a[92:95], v[72:75]\n"
    "s_waitcnt lgkmcnt(7)\n"
    "ds_read_b64_tr_b16 a[14:15], v13 offset:28672\n"
    "v_mfma_f32_16x16x32_bf16 v[44:47], a[24:27], a[80:83], v[44:47]\n"
    "ds_read_b64_tr_b16 a[16:17], v14 offset:16384\n"
    "v_add_u32 v13, s78, v13\n"
    "v_mfma_f32_16x16x32_bf16 v[44:47], a[28:31], a[84:87], v[44:47]\n"
    "ds_read_b64_tr_b16 a[18:19], v14 offset:20480\n"
    "buffer_load_dwordx4 v5, s[44:47], s74 offen offset:1024 lds\n"
    "v_mfma_f32_16x16x32_bf16 v[76:79], a[24:27], a[88:91], v[76:79]\n"
    "ds_read_b64_tr_b16 a[20:21], v14 offset:24576\n"
    "v_mfma_f32_16x16x32_bf16 v[76:79], a[28:31], a[92:95], v[76:79]\n"
    "s_waitcnt lgkmcnt(7)\n"
    "ds_read_b64_tr_b16 a[22:23], v14 offset:28672\n"
    "v_mfma_f32_16x16x32_bf16 v[48:51], a[0:3], a[80:83], v[48:51]\n"
    "ds_read_b64_tr_b16 a[24:25], v15 offset:16384\n"
    "v_add_u32 v14, s78, v14\n"
    "v_mfma_f32_16x16x32_bf16 v[48:51], a[4:7], a[84:87], v[48:51]\n"
    "ds_read_b64_tr_b16 a[26:27], v15 offset:20480\n"
    "buffer_load_dwordx4 a[72:75], v2, s[40:43], s72 offen offset:2048 sc1\n"
    "v_mfma_f32_16x16x32_bf16 v[80:83], a[0:3], a[88:91], v[80:83]\n"
    "ds_read_b64_tr_b16 a[28:29], v15 offset:24576\n"
    "v_mfma_f32_16x16x32_bf16 v[80:83], a[4:7], a[92:95], v[80:83]\n"
    "s_waitcnt vmcnt(13) lgkmcnt(7)\n"
    "s_barrier\n"
    "ds_read_b64_tr_b16 a[30:31], v15 offset:28672\n"
    "v_add_u32 v15, s78, v15\n"
    "v_mfma_f32_16x16x32_bf16 v[52:55], a[8:11], a[80:83], v[52:55]\n"
    "ds_read_b64_tr_b16 a[0:1], v8\n"
    "buffer_load_dwordx4 v6, s[44:47], s74 offen offset:2048 lds\n"
    "v_mfma_f32_16x16x32_bf16 v[52:55], a[12:15], a[84:87], v[52:55]\n"
    "ds_read_b64_tr_b16 a[2:3], v8 offset:4096\n"
    "v_mfma_f32_16x16x32_bf16 v[84:87], a[8:11], a[88:91], v[84:87]\n"
    "ds_read_b64_tr_b16 a[4:5], v8 offset:8192\n"
    "v_mfma_f32_16x16x32_bf16 v[84:87], a[12:15], a[92:95], v[84:87]\n"
    "s_waitcnt lgkmcnt(7)\n"
    "ds_read_b64_tr_b16 a[6:7], v8 offset:12288\n"
    "v_mfma_f32_16x16x32_bf16 v[56:59], a[16:19], a[80:83], v[56:59]\n"
    "ds_read_b64_tr_b16 a[8:9], v9\n"
    "buffer_load_dwordx4 a[76:79], v2, s[40:43], s72 offen offset:3072 sc1\n"
    "v_mfma_f32_16x16x32_bf16 v[56:59], a[20:23], a[84:87], v[56:59]\n"
    "s_add_u32 s72, s72, 0x40000\n"
    "ds_read_b64_tr_b16 a[10:11], v9 offset:4096\n"
    "v_mfma_f32_16x16x32_bf16 v[88:91], a[16:19], a[88:91], v[88:91]\n"
    "ds_read_b64_tr_b16 a[12:13], v9 offset:8192\n"
    "v_mfma_f32_16x16x32_bf16 v[88:91], a[20:23], a[92:95], v[88:91]\n"
    "s_waitcnt lgkmcnt(7)\n"
    "ds_read_b64_tr_b16 a[14:15], v9 offset:12288\n"
    "v_mfma_f32_16x16x32_bf16 v[60:63], a[24:27], a[80:83], v[60:63]\n"
    "s_sub_u32 s75, s75, 1\n"
    "ds_read_b64_tr_b16 a[16:17], v10\n"
    "v_mfma_f32_16x16x32_bf16 v[60:63], a[28:31], a[84:87], v[60:63]\n"
    "ds_read_b64_tr_b16 a[18:19], v10 offset:4096\n"
    "buffer_load_dwordx4 v7, s[44:47], s74 offen offset:3072 lds\n"
    "v_mfma_f32_16x16x32_bf16 v[92:95], a[24:27], a[88:91], v[92:95]\n"
    "s_add_u32 s73, s73, 0x40000\n"
    "ds_read_b64_tr_b16 a[20:21], v10 offset:8192\n"
    "v_mfma_f32_16x16x32_bf16 v[92:95], a[28:31], a[92:95], v[92:95]\n"
    "s_add_u32 s74, s74, 0x40000\n"
    "s_add_u32 s76, s76, 0x8000\n"
    "s_cmp_lt_u32 s76, 0x28000\n"
    "s_cselect_b32 s76, s76, 0\n"
    "ds_read_b64_tr_b16 a[22:23], v10 offset:12288\n"
    "s_cmp_lg_u32 s75, 0\n"
    "s_cbranch_scc1 k_ffn2_loop\n"
    "s_cmp_lg_u32 s83, 0\n"
    "s_cbranch_scc1 k_ffn2_b\n"
    "s_waitcnt lgkmcnt(8)\n"
    "v_mfma_f32_16x16x32_bf16 v[32:35], a[0:3], a[32:35], v[32:35]\n"
    "s_add_u32 s85, s89, 0x1000\n"
    "ds_read_b64_tr_b16 a[24:25], v11\n"
    "v_mfma_f32_16x16x32_bf16 v[32:35], a[4:7], a[36:39], v[32:35]\n"
    "ds_read_b64_tr_b16 a[26:27], v11 offset:4096\n"
    "v_mfma_f32_16x16x32_bf16 v[64:67], a[0:3], a[40:43], v[64:67]\n"
    "ds_read_b64_tr_b16 a[28:29], v11 offset:8192\n"
    "v_mfma_f32_16x16x32_bf16 v[64:67], a[4:7], a[44:47], v[64:67]\n"
    "s_waitcnt lgkmcnt(7)\n"
    "ds_read_b64_tr_b16 a[30:31], v11 offset:12288\n"
    "v_mfma_f32_16x16x32_bf16 v[36:39], a[8:11], a[32:35], v[36:39]\n"
    "ds_read_b64_tr_b16 a[0:1], v12\n"
    "v_mfma_f32_16x16x32_bf16 v[36:39], a[12:15], a[36:39], v[36:39]\n"
    "ds_read_b64_tr_b16 a[2:3], v12 offset:4096\n"
    "v_mfma_f32_16x16x32_bf16 v[68:71], a[8:11], a[40:43], v[68:71]\n"
    "ds_read_b64_tr_b16 a[4:5], v12 offset:8192\n"
    "v_mfma_f32_16x16x32_bf16 v[68:71], a[12:15], a[44:47], v[68:71]\n"
    "s_waitcnt lgkmcnt(7)\n"
    "ds_read_b64_tr_b16 a[6:7], v12 offset:12288\n"
    "v_mfma_f32_16x16x32_bf16 v[40:43], a[16:19], a[32:35], v[40:43]\n"
    "ds_read_b64_tr_b16 a[8:9], v13\n"
    "v_mfma_f32_16x16x32_bf16 v[40:43], a[20:23], a[36:39], v[40:43]\n"
    "ds_read_b64_tr_b16 a[10:11], v13 offset:4096\n"
    "v_mfma_f32_16x16x32_bf16 v[72:75], a[16:19], a[40:43], v[72:75]\n"
    "ds_read_b64_tr_b16 a[12:13], v13 offset:8192\n"
    "v_mfma_f32_16x16x32_bf16 v[72:75], a[20:23], a[44:47], v[72:75]\n"
    "s_waitcnt lgkmcnt(7)\n"
    "ds_read_b64_tr_b16 a[14:15], v13 offset:12288\n"
    "v_mfma_f32_16x16x32_bf16 v[44:47], a[24:27], a[32:35], v[44:47]\n"
    "ds_read_b64_tr_b16 a[16:17], v14\n"
    "v_mfma_f32_16x16x32_bf16 v[44:47], a[28:31], a[36:39], v[44:47]\n"
    "ds_read_b64_tr_b16 a[18:19], v14 offset:4096\n"
    "v_mfma_f32_16x16x32_bf16 v[76:79], a[24:27], a[40:43], v[76:79]\n"
    "ds_read_b64_tr_b16 a[20:21], v14 offset:8192\n"
    "v_mfma_f32_16x16x32_bf16 v[76:79], a[28:31], a[44:47], v[76:79]\n"
    "s_waitcnt lgkmcnt(7)\n"
    "ds_read_b64_tr_b16 a[22:23], v14 offset:12288\n"
    "v_mfma_f32_16x16x32_bf16 v[48:51], a[0:3], a[32:35], v[48:51]\n"
    "ds_read_b64_tr_b16 a[24:25], v15\n"
    "v_mfma_f32_16x16x32_bf16 v[48:51], a[4:7], a[36:39], v[48:51]\n"
    "ds_read_b64_tr_b16 a[26:27], v15 offset:4096\n"
    "v_mfma_f32_16x16x32_bf16 v[80:83], a[0:3], a[40:43], v[80:83]\n"
    "ds_read_b64_tr_b16 a[28:29], v15 offset:8192\n"
    "v_mfma_f32_16x16x32_bf16 v[80:83], a[4:7], a[44:47], v[80:83]\n"
    "s_waitcnt lgkmcnt(7)\n"
    "ds_read_b64_tr_b16 a[30:31], v15 offset:12288\n"
    "v_mfma_f32_16x16x32_bf16 v[52:55], a[8:11], a[32:35], v[52:55]\n"
    "ds_read_b64_tr_b16 a[0:1], v8 offset:16384\n"
    "v_mfma_f32_16x16x32_bf16 v[52:55], a[12:15], a[36:39], v[52:55]\n"
    "ds_read_b64_tr_b16 a[2:3], v8 offset:20480\n"
    "v_mfma_f32_16x16x32_bf16 v[84:87], a[8:11], a[40:43], v[84:87]\n"
    "ds_read_b64_tr_b16 a[4:5], v8 offset:24576\n"
    "v_mfma_f32_16x16x32_bf16 v[84:87], a[12:15], a[44:47], v[84:87]\n"
    "s_waitcnt lgkmcnt(7)\n"
    "ds_read_b64_tr_b16 a[6:7], v8 offset:28672\n"
    "v_mfma_f32_16x16x32_bf16 v[56:59], a[16:19], a[32:35], v[56:59]\n"
    "ds_read_b64_tr_b16 a[8:9], v9 offset:16384\n"
    "v_mfma_f32_16x16x32_bf16 v[56:59], a[20:23], a[36:39], v[56:59]\n"
    "ds_read_b64_tr_b16 a[10:11], v9 offset:20480\n"
    "v_mfma_f32_16x16x32_bf16 v[88:91], a[16:19], a[40:43], v[88:91]\n"
    "ds_read_b64_tr_b16 a[12:13], v9 offset:24576\n"
    "v_mfma_f32_16x16x32_bf16 v[88:91], a[20:23], a[44:47], v[88:91]\n"
    "s_waitcnt lgkmcnt(7)\n"
    "ds_read_b64_tr_b16 a[14:15], v9 offset:28672\n"
    "v_mfma_f32_16x16x32_bf16 v[60:63], a[24:27], a[32:35], v[60:63]\n"
    "ds_read_b64_tr_b16 a[16:17], v10 offset:16384\n"
    "v_mfma_f32_16x16x32_bf16 v[60:63], a[28:31], a[36:39], v[60:63]\n"
    "ds_read_b64_tr_b16 a[18:19], v10 offset:20480\n"
    "v_mfma_f32_16x16x32_bf16 v[92:95], a[24:27], a[40:43], v[92:95]\n"
    "ds_read_b64_tr_b16 a[20:21], v10 offset:24576\n"
    "v_mfma_f32_16x16x32_bf16 v[92:95], a[28:31], a[44:47], v[92:95]\n"
    "s_waitcnt vmcnt(9) lgkmcnt(7)\n"
    "ds_read_b64_tr_b16 a[22:23], v10 offset:28672\n"
    "v_mfma_f32_16x16x32_bf16 v[32:35], a[0:3], a[48:51], v[32:35]\n"
    "ds_read_b64_tr_b16 a[24:25], v11 offset:16384\n"
    "v_mfma_f32_16x16x32_bf16 v[32:35], a[4:7], a[52:55], v[32:35]\n"
    "ds_read_b64_tr_b16 a[26:27], v11 offset:20480\n"
    "v_mfma_f32_16x16x32_bf16 v[64:67], a[0:3], a[56:59], v[64:67]\n"
    "ds_read_b64_tr_b16 a[28:29], v11 offset:24576\n"
    "v_mfma_f32_16x16x32_bf16 v[64:67], a[4:7], a[60:63], v[64:67]\n"
    "s_waitcnt lgkmcnt(7)\n"
    "ds_read_b64_tr_b16 a[30:31], v11 offset:28672\n"
    "v_mfma_f32_16x16x32_bf16 v[36:39], a[8:11], a[48:51], v[36:39]\n"
    "ds_read_b64_tr_b16 a[0:1], v12 offset:16384\n"
    "v_mfma_f32_16x16x32_bf16 v[36:39], a[12:15], a[52:55], v[36:39]\n"
    "ds_read_b64_tr_b16 a[2:3], v12 offset:20480\n"
    "v_mfma_f32_16x16x32_bf16 v[68:71], a[8:11], a[56:59], v[68:71]\n"
    "ds_read_b64_tr_b16 a[4:5], v12 offset:24576\n"
    "v_mfma_f32_16x16x32_bf16 v[68:71], a[12:15], a[60:63], v[68:71]\n"
    "s_waitcnt lgkmcnt(7)\n"
    "ds_read_b64_tr_b16 a[6:7], v12 offset:28672\n"
    "v_mfma_f32_16x16x32_bf16 v[40:43], a[16:19], a[48:51], v[40:43]\n"
    "ds_read_b64_tr_b16 a[8:9], v13 offset:16384\n"
    "v_mfma_f32_16x16x32_bf16 v[40:43], a[20:23], a[52:55], v[40:43]\n"
    "ds_read_b64_tr_b16 a[10:11], v13 offset:20480\n"
    "v_cvt_pk_bf16_f32 v192, v32, v33\n"
    "v_mfma_f32_16x16x32_bf16 v[72:75], a[16:19], a[56:59], v[72:75]\n"
    "ds_read_b64_tr_b16 a[12:13], v13 offset:24576\n"
    "v_cvt_pk_bf16_f32 v193, v34, v35\n"
    "v_mfma_f32_16x16x32_bf16 v[72:75], a[20:23], a[60:63], v[72:75]\n"
    "s_waitcnt lgkmcnt(7)\n"
    "ds_read_b64_tr_b16 a[14:15], v13 offset:28672\n"
    "v_mfma_f32_16x16x32_bf16 v[44:47], a[24:27], a[48:51], v[44:47]\n"
    "ds_read_b64_tr_b16 a[16:17], v14 offset:16384\n"
    "v_cvt_pk_bf16_f32 v194, v36, v37\n"
    "v_mfma_f32_16x16x32_bf16 v[44:47], a[28:31], a[52:55], v[44:47]\n"
    "ds_read_b64_tr_b16 a[18:19], v14 offset:20480\n"
    "v_cvt_pk_bf16_f32 v195, v38, v39\n"
    "v_mfma_f32_16x16x32_bf16 v[76:79], a[24:27], a[56:59], v[76:79]\n"
    "ds_read_b64_tr_b16 a[20:21], v14 offset:24576\n"
    "buffer_store_dwordx4 v[192:195], v3, s[60:63], s89 offen sc1\n"
    "v_mfma_f32_16x16x32_bf16 v[76:79], a[28:31], a[60:63], v[76:79]\n"
    "s_waitcnt lgkmcnt(7)\n"
    "ds_read_b64_tr_b16 a[22:23], v14 offset:28672\n"
    "v_mfma_f32_16x16x32_bf16 v[48:51], a[0:3], a[48:51], v[48:51]\n"
    "ds_read_b64_tr_b16 a[24:25], v15 offset:16384\n"
    "v_cvt_pk_bf16_f32 v208, v64, v65\n"
    "v_mfma_f32_16x16x32_bf16 v[48:51], a[4:7], a[52:55], v[48:51]\n"
    "ds_read_b64_tr_b16 a[26:27], v15 offset:20480\n"
    "v_cvt_pk_bf16_f32 v209, v66, v67\n"
    "v_cvt_pk_bf16_f32 v196, v40, v41\n"
    "v_mfma_f32_16x16x32_bf16 v[80:83], a[0:3], a[56:59], v[80:83]\n"
    "ds_read_b64_tr_b16 a[28:29], v15 offset:24576\n"
    "v_cvt_pk_bf16_f32 v210, v68, v69\n"
    "v_cvt_pk_bf16_f32 v197, v42, v43\n"
    "v_mfma_f32_16x16x32_bf16 v[80:83], a[4:7], a[60:63], v[80:83]\n"
    "s_waitcnt lgkmcnt(7)\n"
    "ds_read_b64_tr_b16 a[30:31], v15 offset:28672\n"
    "v_mfma_f32_16x16x32_bf16 v[52:55], a[8:11], a[48:51], v[52:55]\n"
    "v_cvt_pk_bf16_f32 v211, v70, v71\n"
    "v_cvt_pk_bf16_f32 v198, v44, v45\n"
    "v_mfma_f32_16x16x32_bf16 v[52:55], a[12:15], a[52:55], v[52:55]\n"
    "buffer_store_dwordx4 v[208:211], v3, s[60:63], s85 offen sc1\n"
    "v_mfma_f32_16x16x32_bf16 v[84:87], a[8:11], a[56:59], v[84:87]\n"
    "v_cvt_pk_bf16_f32 v199, v46, v47\n"
    "v_mfma_f32_16x16x32_bf16 v[84:87], a[12:15], a[60:63], v[84:87]\n"
    "s_waitcnt lgkmcnt(4)\n"
    "v_mfma_f32_16x16x32_bf16 v[56:59], a[16:19], a[48:51], v[56:59]\n"
    "buffer_store_dwordx4 v[196:199], v3, s[60:63], s89 offen offset:1024 sc1\n"
    "v_mfma_f32_16x16x32_bf16 v[56:59], a[20:23], a[52:55], v[56:59]\n"
    "v_cvt_pk_bf16_f32 v212, v72, v73\n"
    "v_cvt_pk_bf16_f32 v213, v74, v75\n"
    "v_mfma_f32_16x16x32_bf16 v[88:91], a[16:19], a[56:59], v[88:91]\n"
    "v_cvt_pk_bf16_f32 v214, v76, v77\n"
    "v_cvt_pk_bf16_f32 v215, v78, v79\n"
    "v_mfma_f32_16x16x32_bf16 v[88:91], a[20:23], a[60:63], v[88:91]\n"
    "s_waitcnt lgkmcnt(0)\n"
    "v_cvt_pk_bf16_f32 v200, v48, v49\n"
    "v_cvt_pk_bf16_f32 v201, v50, v51\n"
    "v_mfma_f32_16x16x32_bf16 v[60:63], a[24:27], a[48:51], v[60:63]\n"
    "buffer_store_dwordx4 v[212:215], v3, s[60:63], s85 offen offset:1024 sc1\n"
    "v_mfma_f32_16x16x32_bf16 v[60:63], a[28:31], a[52:55], v[60:63]\n"
    "v_cvt_pk_bf16_f32 v202, v52, v53\n"
    "v_cvt_pk_bf16_f32 v203, v54, v55\n"
    "v_mfma_f32_16x16x32_bf16 v[92:95], a[24:27], a[56:59], v[92:95]\n"
    "buffer_store_dwordx4 v[200:203], v3, s[60:63], s89 offen offset:2048 sc1\n"
    "v_mfma_f32_16x16x32_bf16 v[92:95], a[28:31], a[60:63], v[92:95]\n"
    "v_cvt_pk_bf16_f32 v216, v80, v81\n"
    "v_cvt_pk_bf16_f32 v217, v82, v83\n"
    "v_cvt_pk_bf16_f32 v218, v84, v85\n"
    "v_cvt_pk_bf16_f32 v219, v86, v87\n"
    "s_nop 7\n"
    "buffer_store_dwordx4 v[216:219], v3, s[60:63], s85 offen offset:2048 sc1\n"
    "v_cvt_pk_bf16_f32 v204, v56, v57\n"
    "v_cvt_pk_bf16_f32 v205, v58, v59\n"
    "v_cvt_pk_bf16_f32 v206, v60, v61\n"
    "v_cvt_pk_bf16_f32 v207, v62, v63\n"
    "buffer_store_dwordx4 v[204:207], v3, s[60:63], s89 offen offset:3072 sc1\n"
    "v_cvt_pk_bf16_f32 v220, v88, v89\n"
    "v_cvt_pk_bf16_f32 v221, v90, v91\n"
    "v_cvt_pk_bf16_f32 v222, v92, v93\n"
    "v_cvt_pk_bf16_f32 v223, v94, v95\n"
    "buffer_store_dwordx4 v[220:223], v3, s[60:63], s85 offen offset:3072 sc1\n"
    "v_mov_b32 v18, s94\n"
    "v_mov_b32 v19, s95\n"
    "s_waitcnt vmcnt(0)\n"
    "s_mov_b64 exec, 1\n buffer_store_dwordx2 v[18:19], off, s[64:67], s90 offset:0 sc1\n s_mov_b64 exec, -1\n"
    "s_endpgm\n"
    "k_ffn2_b:\n"
    "s_waitcnt lgkmcnt(8)\n"
    "v_mfma_f32_16x16x32_bf16 v[32:35], a[0:3], a[32:35], v[32:35]\n"
    "ds_read_b64_tr_b16 a[24:25], v11\n"
    "v_mfma_f32_16x16x32_bf16 v[32:35], a[4:7], a[36:39], v[32:35]\n"
    "ds_read_b64_tr_b16 a[26:27], v11 offset:4096\n"
    "buffer_load_dwordx4 a[80:83], v2, s[40:43], s72 offen sc1\n"
    "v_mfma_f32_16x16x32_bf16 v[64:67], a[0:3], a[40:43], v[64:67]\n"
    "s_add_u32 m0, s76, s87\n"
    "ds_read_b64_tr_b16 a[28:29], v11 offset:8192\n"
    "v_mfma_f32_16x16x32_bf16 v[64:67], a[4:7], a[44:47], v[64:67]\n"
    "s_waitcnt lgkmcnt(7)\n"
    "ds_read_b64_tr_b16 a[30:31], v11 offset:12288\n"
    "v_mfma_f32_16x16x32_bf16 v[36:39], a[8:11], a[32:35], v[36:39]\n"
    "ds_read_b64_tr_b16 a[0:1], v12\n"
    "buffer_load_dwordx4 v4, s[44:47], s73 offen lds\n"
    "v_mfma_f32_16x16x32_bf16 v[36:39], a[12:15], a[36:39], v[36:39]\n"
    "ds_read_b64_tr_b16 a[2:3], v12 offset:4096\n"
    "v_mfma_f32_16x16x32_bf16 v[68:71], a[8:11], a[40:43], v[68:71]\n"
    "ds_read_b64_tr_b16 a[4:5], v12 offset:8192\n"
    "v_mfma_f32_16x16x32_bf16 v[68:71], a[12:15], a[44:47], v[68:71]\n"
    "s_waitcnt lgkmcnt(7)\n"
    "ds_read_b64_tr_b16 a[6:7], v12 offset:12288\n"
    "v_mfma_f32_16x16x32_bf16 v[40:43], a[16:19], a[32:35], v[40:43]\n"
    "ds_read_b64_tr_b16 a[8:9], v13\n"
    "buffer_load_dwordx4 a[84:87], v2, s[40:43], s72 offen offset:1024 sc1\n"
    "v_mfma_f32_16x16x32_bf16 v[40:43], a[20:23], a[36:39], v[40:43]\n"
    "ds_read_b64_tr_b16 a[10:11], v13 offset:4096\n"
    "v_mfma_f32_16x16x32_bf16 v[72:75], a[16:19], a[40:43], v[72:75]\n"
    "ds_read_b64_tr_b16 a[12:13], v13 offset:8192\n"
    "v_mfma_f32_16x16x32_bf16 v[72:75], a[20:23], a[44:47], v[72:75]\n"
    "s_waitcnt lgkmcnt(7)\n"
    "ds_read_b64_tr_b16 a[14:15], v13 offset:12288\n"
    "v_mfma_f32_16x16x32_bf16 v[44:47], a[24:27], a[32:35], v[44:47]\n"
    "ds_read_b64_tr_b16 a[16:17], v14\n"
    "buffer_load_dwordx4 v5, s[44:47], s73 offen offset:1024 lds\n"
    "v_mfma_f32_16x16x32_bf16 v[44:47], a[28:31], a[36:39], v[44:47]\n"
    "ds_read_b64_tr_b16 a[18:19], v14 offset:4096\n"
    "v_mfma_f32_16x16x32_bf16 v[76:79], a[24:27], a[40:43], v[76:79]\n"
    "ds_read_b64_tr_b16 a[20:21], v14 offset:8192\n"
    "v_mfma_f32_16x16x32_bf16 v[76:79], a[28:31], a[44:47], v[76:79]\n"
    "s_waitcnt lgkmcnt(7)\n"
    "ds_read_b64_tr_b16 a[22:23], v14 offset:12288\n"
    "v_mfma_f32_16x16x32_bf16 v[48:51], a[0:3], a[32:35], v[48:51]\n"
    "ds_read_b64_tr_b16 a[24:25], v15\n"
    "buffer_load_dwordx4 a[88:91], v2, s[40:43], s72 offen offset:2048 sc1\n"
    "v_mfma_f32_16x16x32_bf16 v[48:51], a[4:7], a[36:39], v[48:51]\n"
    "ds_read_b64_tr_b16 a[26:27], v15 offset:4096\n"
    "v_mfma_f32_16x16x32_bf16 v[80:83], a[0:3], a[40:43], v[80:83]\n"
    "s_add_u32 s77, s77, 1\n"
    "ds_read_b64_tr_b16 a[28:29], v15 offset:8192\n"
    "v_mfma_f32_16x16x32_bf16 v[80:83], a[4:7], a[44:47], v[80:83]\n"
    "s_waitcnt lgkmcnt(7)\n"
    "ds_read_b64_tr_b16 a[30:31], v15 offset:12288\n"
    "v_mfma_f32_16x16x32_bf16 v[52:55], a[8:11], a[32:35], v[52:55]\n"
    "ds_read_b64_tr_b16 a[0:1], v8 offset:16384\n"
    "buffer_load_dwordx4 v6, s[44:47], s73 offen offset:2048 lds\n"
    "v_mfma_f32_16x16x32_bf16 v[52:55], a[12:15], a[36:39], v[52:55]\n"
    "s_cmp_lt_u32 s77, 5\n"
    "s_cselect_b32 s77, s77, 0\n"
    "s_cselect_b32 s78, 0x8000, s79\n"
    "ds_read_b64_tr_b16 a[2:3], v8 offset:20480\n"
    "v_mfma_f32_16x16x32_bf16 v[84:87], a[8:11], a[40:43], v[84:87]\n"
    "ds_read_b64_tr_b16 a[4:5], v8 offset:24576\n"
    "v_mfma_f32_16x16x32_bf16 v[84:87], a[12:15], a[44:47], v[84:87]\n"
    "s_waitcnt lgkmcnt(7)\n"
    "ds_read_b64_tr_b16 a[6:7], v8 offset:28672\n"
    "v_mfma_f32_16x16x32_bf16 v[56:59], a[16:19], a[32:35], v[56:59]\n"
    "ds_read_b64_tr_b16 a[8:9], v9 offset:16384\n"
    "v_add_u32 v8, s78, v8\n"
    "v_mfma_f32_16x16x32_bf16 v[56:59], a[20:23], a[36:39], v[56:59]\n"
    "ds_read_b64_tr_b16 a[10:11], v9 offset:20480\n"
    "buffer_load_dwordx4 a[92:95], v2, s[40:43], s72 offen offset:3072 sc1\n"
    "v_mfma_f32_16x16x32_bf16 v[88:91], a[16:19], a[40:43], v[88:91]\n"
    "s_add_u32 s72, s72, 0x40000\n"
    "ds_read_b64_tr_b16 a[12:13], v9 offset:24576\n"
    "v_mfma_f32_16x16x32_bf16 v[88:91], a[20:23], a[44:47], v[88:91]\n"
    "s_waitcnt lgkmcnt(7)\n"
    "ds_read_b64_tr_b16 a[14:15], v9 offset:28672\n"
    "v_mfma_f32_16x16x32_bf16 v[60:63], a[24:27], a[32:35], v[60:63]\n"
    "ds_read_b64_tr_b16 a[16:17], v10 offset:16384\n"
    "v_add_u32 v9, s78, v9\n"
    "v_mfma_f32_16x16x32_bf16 v[60:63], a[28:31], a[36:39], v[60:63]\n"
    "ds_read_b64_tr_b16 a[18:19], v10 offset:20480\n"
    "buffer_load_dwordx4 v7, s[44:47], s73 offen offset:3072 lds\n"
    "v_mfma_f32_16x16x32_bf16 v[92:95], a[24:27], a[40:43], v[92:95]\n"
    "ds_read_b64_tr_b16 a[20:21], v10 offset:24576\n"
    "v_mfma_f32_16x16x32_bf16 v[92:95], a[28:31], a[44:47], v[92:95]\n"
    "s_waitcnt vmcnt(17) lgkmcnt(7)\n"
    "ds_read_b64_tr_b16 a[22:23], v10 offset:28672\n"
    "v_mfma_f32_16x16x32_bf16 v[32:35], a[0:3], a[48:51], v[32:35]\n"
    "ds_read_b64_tr_b16 a[24:25], v11 offset:16384\n"
    "v_add_u32 v10, s78, v10\n"
    "v_mfma_f32_16x16x32_bf16 v[32:35], a[4:7], a[52:55], v[32:35]\n"
    "ds_read_b64_tr_b16 a[26:27], v11 offset:20480\n"
    "buffer_load_dwordx4 a[32:35], v2, s[40:43], s72 offen sc1\n"
    "v_mfma_f32_16x16x32_bf16 v[64:67], a[0:3], a[56:59], v[64:67]\n"
    "s_add_u32 m0, s76, s88\n"
    "ds_read_b64_tr_b16 a[28:29], v11 offset:24576\n"
    "v_mfma_f32_16x16x32_bf16 v[64:67], a[4:7], a[60:63], v[64:67]\n"
    "s_waitcnt lgkmcnt(7)\n"
    "ds_read_b64_tr_b16 a[30:31], v11 offset:28672\n"
    "v_mfma_f32_16x16x32_bf16 v[36:39], a[8:11], a[48:51], v[36:39]\n"
    "ds_read_b64_tr_b16 a[0:1], v12 offset:16384\n"
    "v_add_u32 v11, s78, v11\n"
    "v_mfma_f32_16x16x32_bf16 v[36:39], a[12:15], a[52:55], v[36:39]\n"
    "ds_read_b64_tr_b16 a[2:3], v12 offset:20480\n"
    "buffer_load_dwordx4 v4, s[44:47], s74 offen lds\n"
    "v_mfma_f32_16x16x32_bf16 v[68:71], a[8:11], a[56:59], v[68:71]\n"
    "ds_read_b64_tr_b16 a[4:5], v12 offset:24576\n"
    "v_mfma_f32_16x16x32_bf16 v[68:71], a[12:15], a[60:63], v[68:71]\n"
    "s_waitcnt lgkmcnt(7)\n"
    "ds_read_b64_tr_b16 a[6:7], v12 offset:28672\n"
    "v_mfma_f32_16x16x32_bf16 v[40:43], a[16:19], a[48:51], v[40:43]\n"
    "ds_read_b64_tr_b16 a[8:9], v13 offset:16384\n"
    "v_add_u32 v12, s78, v12\n"
    "v_mfma_f32_16x16x32_bf16 v[40:43], a[20:23], a[52:55], v[40:43]\n"
    "ds_read_b64_tr_b16 a[10:11], v13 offset:20480\n"
    "buffer_load_dwordx4 a[36:39], v2, s[40:43], s72 offen offset:1024 sc1\n"
    "v_mfma_f32_16x16x32_bf16 v[72:75], a[16:19], a[56:59], v[72:75]\n"
    "ds_read_b64_tr_b16 a[12:13], v13 offset:24576\n"
    "v_mfma_f32_16x16x32_bf16 v[72:75], a[20:23], a[60:63], v[72:75]\n"
    "s_waitcnt lgkmcnt(7)\n"
    "ds_read_b64_tr_b16 a[14:15], v13 offset:28672\n"
    "v_mfma_f32_16x16x32_bf16 v[44:47], a[24:27], a[48:51], v[44:47]\n"
    "ds_read_b64_tr_b16 a[16:17], v14 offset:16384\n"
    "v_add_u32 v13, s78, v13\n"
    "v_mfma_f32_16x16x32_bf16 v[44:47], a[28:31], a[52:55], v[44:47]\n"
    "ds_read_b64_tr_b16 a[18:19], v14 offset:20480\n"
    "buffer_load_dwordx4 v5, s[44:47], s74 offen offset:1024 lds\n"
    "v_mfma_f32_16x16x32_bf16 v[76:79], a[24:27], a[56:59], v[76:79]\n"
    "ds_read_b64_tr_b16 a[20:21], v14 offset:24576\n"
    "v_mfma_f32_16x16x32_bf16 v[76:79], a[28:31], a[60:63], v[76:79]\n"
    "s_waitcnt lgkmcnt(7)\n"
    "ds_read_b64_tr_b16 a[22:23], v14 offset:28672\n"
    "v_mfma_f32_16x16x32_bf16 v[48:51], a[0:3], a[48:51], v[48:51]\n"
    "ds_read_b64_tr_b16 a[24:25], v15 offset:16384\n"
    "v_add_u32 v14, s78, v14\n"
    "v_mfma_f32_16x16x32_bf16 v[48:51], a[4:7], a[52:55], v[48:51]\n"
    "ds_read_b64_tr_b16 a[26:27], v15 offset:20480\n"
    "buffer_load_dwordx4 a[40:43], v2, s[40:43], s72 offen offset:2048 sc1\n"
    "v_mfma_f32_16x16x32_bf16 v[80:83], a[0:3], a[56:59], v[80:83]\n"
    "ds_read_b64_tr_b16 a[28:29], v15 offset:24576\n"
    "v_mfma_f32_16x16x32_bf16 v[80:83], a[4:7], a[60:63], v[80:83]\n"
    "s_waitcnt vmcnt(13) lgkmcnt(7)\n"
    "s_barrier\n"
    "ds_read_b64_tr_b16 a[30:31], v15 offset:28672\n"
    "v_add_u32 v15, s78, v15\n"
    "v_mfma_f32_16x16x32_bf16 v[52:55], a[8:11], a[48:51], v[52:55]\n"
    "ds_read_b64_tr_b16 a[0:1], v8\n"
    "buffer_load_dwordx4 v6, s[44:47], s74 offen offset:2048 lds\n"
    "v_mfma_f32_16x16x32_bf16 v[52:55], a[12:15], a[52:55], v[52:55]\n"
    "ds_read_b64_tr_b16 a[2:3], v8 offset:4096\n"
    "v_mfma_f32_16x16x32_bf16 v[84:87], a[8:11], a[56:59], v[84:87]\n"
    "ds_read_b64_tr_b16 a[4:5], v8 offset:8192\n"
    "v_mfma_f32_16x16x32_bf16 v[84:87], a[12:15], a[60:63], v[84:87]\n"
    "s_waitcnt lgkmcnt(7)\n"
    "ds_read_b64_tr_b16 a[6:7], v8 offset:12288\n"
    "v_mfma_f32_16x16x32_bf16 v[56:59], a[16:19], a[48:51], v[56:59]\n"
    "ds_read_b64_tr_b16 a[8:9], v9\n"
    "buffer_load_dwordx4 a[44:47], v2, s[40:43], s72 offen offset:3072 sc1\n"
    "v_mfma_f32_16x16x32_bf16 v[56:59], a[20:23], a[52:55], v[56:59]\n"
    "s_add_u32 s72, s72, 0x40000\n"
    "ds_read_b64_tr_b16 a[10:11], v9 offset:4096\n"
    "v_mfma_f32_16x16x32_bf16 v[88:91], a[16:19], a[56:59], v[88:91]\n"
    "ds_read_b64_tr_b16 a[12:13], v9 offset:8192\n"
    "v_mfma_f32_16x16x32_bf16 v[88:91], a[20:23], a[60:63], v[88:91]\n"
    "s_waitcnt lgkmcnt(7)\n"
    "ds_read_b64_tr_b16 a[14:15], v9 offset:12288\n"
    "v_mfma_f32_16x16x32_bf16 v[60:63], a[24:27], a[48:51], v[60:63]\n"
    "ds_read_b64_tr_b16 a[16:17], v10\n"
    "buffer_load_dwordx4 v7, s[44:47], s74 offen offset:3072 lds\n"
    "v_mfma_f32_16x16x32_bf16 v[60:63], a[28:31], a[52:55], v[60:63]\n"
    "s_add_u32 s73, s73, 0x40000\n"
    "ds_read_b64_tr_b16 a[18:19], v10 offset:4096\n"
    "v_mfma_f32_16x16x32_bf16 v[92:95], a[24:27], a[56:59], v[92:95]\n"
    "s_add_u32 s74, s74, 0x40000\n"
    "ds_read_b64_tr_b16 a[20:21], v10 offset:8192\n"
    "v_mfma_f32_16x16x32_bf16 v[92:95], a[28:31], a[60:63], v[92:95]\n"
    "s_add_u32 s76, s76, 0x8000\n"
    "s_cmp_lt_u32 s76, 0x28000\n"
    "s_cselect_b32 s76, s76, 0\n"
    "ds_read_b64_tr_b16 a[22:23], v10 offset:12288\n"
    "s_add_u32 s48, s32, 0x100000\n"
    "s_addc_u32 s49, s33, 0\n"
    "s_mov_b32 s50, 0x400000\n"
    "s_mov_b32 s51, 0x20000\n"
    "s_lshl_b32 s85, s82, 19\n"
    "s_add_u32 s86, s85, 0x40000\n"
    "s_nop 1\n"
    "s_waitcnt lgkmcnt(8)\n"
    "v_mfma_f32_16x16x32_bf16 v[32:35], a[0:3], a[64:67], v[32:35]\n"
    "ds_read_b64_tr_b16 a[24:25], v11\n"
    "v_mfma_f32_16x16x32_bf16 v[32:35], a[4:7], a[68:71], v[32:35]\n"
    "ds_read_b64_tr_b16 a[26:27], v11 offset:4096\n"
    "buffer_load_dwordx4 a[48:51], v2, s[40:43], s72 offen sc1\n"
    "v_mfma_f32_16x16x32_bf16 v[64:67], a[0:3], a[72:75], v[64:67]\n"
    "ds_read_b64_tr_b16 a[28:29], v11 offset:8192\n"
    "v_mfma_f32_16x16x32_bf16 v[64:67], a[4:7], a[76:79], v[64:67]\n"
    "s_waitcnt lgkmcnt(7)\n"
    "ds_read_b64_tr_b16 a[30:31], v11 offset:12288\n"
    "v_mfma_f32_16x16x32_bf16 v[36:39], a[8:11], a[64:67], v[36:39]\n"
    "ds_read_b64_tr_b16 a[0:1], v12\n"
    "v_mfma_f32_16x16x32_bf16 v[36:39], a[12:15], a[68:71], v[36:39]\n"
    "ds_read_b64_tr_b16 a[2:3], v12 offset:4096\n"
    "v_mfma_f32_16x16x32_bf16 v[68:71], a[8:11], a[72:75], v[68:71]\n"
    "ds_read_b64_tr_b16 a[4:5], v12 offset:8192\n"
    "v_mfma_f32_16x16x32_bf16 v[68:71], a[12:15], a[76:79], v[68:71]\n"
    "s_waitcnt lgkmcnt(7)\n"
    "ds_read_b64_tr_b16 a[6:7], v12 offset:12288\n"
    "v_mfma_f32_16x16x32_bf16 v[40:43], a[16:19], a[64:67], v[40:43]\n"
    "ds_read_b64_tr_b16 a[8:9], v13\n"
    "buffer_load_dwordx4 a[52:55], v2, s[40:43], s72 offen offset:1024 sc1\n"
    "v_mfma_f32_16x16x32_bf16 v[40:43], a[20:23], a[68:71], v[40:43]\n"
    "ds_read_b64_tr_b16 a[10:11], v13 offset:4096\n"
    "v_mfma_f32_16x16x32_bf16 v[72:75], a[16:19], a[72:75], v[72:75]\n"
    "ds_read_b64_tr_b16 a[12:13], v13 offset:8192\n"
    "v_mfma_f32_16x16x32_bf16 v[72:75], a[20:23], a[76:79], v[72:75]\n"
    "s_waitcnt lgkmcnt(7)\n"
    "ds_read_b64_tr_b16 a[14:15], v13 offset:12288\n"
    "v_mfma_f32_16x16x32_bf16 v[44:47], a[24:27], a[64:67], v[44:47]\n"
    "ds_read_b64_tr_b16 a[16:17], v14\n"
    "v_mfma_f32_16x16x32_bf16 v[44:47], a[28:31], a[68:71], v[44:47]\n"
    "ds_read_b64_tr_b16 a[18:19], v14 offset:4096\n"
    "v_mfma_f32_16x16x32_bf16 v[76:79], a[24:27], a[72:75], v[76:79]\n"
    "ds_read_b64_tr_b16 a[20:21], v14 offset:8192\n"
    "v_mfma_f32_16x16x32_bf16 v[76:79], a[28:31], a[76:79], v[76:79]\n"
    "s_waitcnt lgkmcnt(7)\n"
    "ds_read_b64_tr_b16 a[22:23], v14 offset:12288\n"
    "v_mfma_f32_16x16x32_bf16 v[48:51], a[0:3], a[64:67], v[48:51]\n"
    "ds_read_b64_tr_b16 a[24:25], v15\n"
    "buffer_load_dwordx4 a[56:59], v2, s[40:43], s72 offen offset:2048 sc1\n"
    "v_mfma_f32_16x16x32_bf16 v[48:51], a[4:7], a[68:71], v[48:51]\n"
    "ds_read_b64_tr_b16 a[26:27], v15 offset:4096\n"
    "v_mfma_f32_16x16x32_bf16 v[80:83], a[0:3], a[72:75], v[80:83]\n"
    "s_add_u32 s77, s77, 1\n"
    "ds_read_b64_tr_b16 a[28:29], v15 offset:8192\n"
    "v_mfma_f32_16x16x32_bf16 v[80:83], a[4:7], a[76:79], v[80:83]\n"
    "s_waitcnt lgkmcnt(7)\n"
    "ds_read_b64_tr_b16 a[30:31], v15 offset:12288\n"
    "v_mfma_f32_16x16x32_bf16 v[52:55], a[8:11], a[64:67], v[52:55]\n"
    "ds_read_b64_tr_b16 a[0:1], v8 offset:16384\n"
    "v_mfma_f32_16x16x32_bf16 v[52:55], a[12:15], a[68:71], v[52:55]\n"
    "s_cmp_lt_u32 s77, 5\n"
    "s_cselect_b32 s77, s77, 0\n"
    "s_cselect_b32 s78, 0x8000, s79\n"
    "ds_read_b64_tr_b16 a[2:3], v8 offset:20480\n"
    "v_mfma_f32_16x16x32_bf16 v[84:87], a[8:11], a[72:75], v[84:87]\n"
    "ds_read_b64_tr_b16 a[4:5], v8 offset:24576\n"
    "buffer_load_dwordx4 a[60:63], v2, s[40:43], s72 offen offset:3072 sc1\n"
    "v_mfma_f32_16x16x32_bf16 v[84:87], a[12:15], a[76:79], v[84:87]\n"
    "s_waitcnt lgkmcnt(7)\n"
    "s_add_u32 s72, s72, 0x40000\n"
    "ds_read_b64_tr_b16 a[6:7], v8 offset:28672\n"
    "v_mfma_f32_16x16x32_bf16 v[56:59], a[16:19], a[64:67], v[56:59]\n"
    "buffer_load_dwordx2 v[18:19], off, s[64:67], s90 sc1\n"
    "ds_read_b64_tr_b16 a[8:9], v9 offset:16384\n"
    "v_mfma_f32_16x16x32_bf16 v[56:59], a[20:23], a[68:71], v[56:59]\n"
    "ds_read_b64_tr_b16 a[10:11], v9 offset:20480\n"
    "v_add_u32 v8, s78, v8\n"
    "v_mfma_f32_16x16x32_bf16 v[88:91], a[16:19], a[72:75], v[88:91]\n"
    "ds_read_b64_tr_b16 a[12:13], v9 offset:24576\n"
    "v_mfma_f32_16x16x32_bf16 v[88:91], a[20:23], a[76:79], v[88:91]\n"
    "s_waitcnt lgkmcnt(7)\n"
    "ds_read_b64_tr_b16 a[14:15], v9 offset:28672\n"
    "v_mfma_f32_16x16x32_bf16 v[60:63], a[24:27], a[64:67], v[60:63]\n"
    "ds_read_b64_tr_b16 a[16:17], v10 offset:16384\n"
    "v_add_u32 v9, s78, v9\n"
    "v_mfma_f32_16x16x32_bf16 v[60:63], a[28:31], a[68:71], v[60:63]\n"
    "ds_read_b64_tr_b16 a[18:19], v10 offset:20480\n"
    "buffer_load_dwordx4 v[96:99], v2, s[48:51], s85 offen sc1\n"
    "v_mfma_f32_16x16x32_bf16 v[92:95], a[24:27], a[72:75], v[92:95]\n"
    "ds_read_b64_tr_b16 a[20:21], v10 offset:24576\n"
    "v_mfma_f32_16x16x32_bf16 v[92:95], a[28:31], a[76:79], v[92:95]\n"
    "s_waitcnt vmcnt(15) lgkmcnt(7)\n"
    "ds_read_b64_tr_b16 a[22:23], v10 offset:28672\n"
    "v_mfma_f32_16x16x32_bf16 v[32:35], a[0:3], a[80:83], v[32:35]\n"
    "ds_read_b64_tr_b16 a[24:25], v11 offset:16384\n"
    "v_add_u32 v10, s78, v10\n"
    "v_mfma_f32_16x16x32_bf16 v[32:35], a[4:7], a[84:87], v[32:35]\n"
    "ds_read_b64_tr_b16 a[26:27], v11 offset:20480\n"
    "buffer_load_dwordx4 v[100:103], v2, s[48:51], s85 offen offset:1024 sc1\n"
    "v_mfma_f32_16x16x32_bf16 v[64:67], a[0:3], a[88:91], v[64:67]\n"
    "ds_read_b64_tr_b16 a[28:29], v11 offset:24576\n"
    "v_mfma_f32_16x16x32_bf16 v[64:67], a[4:7], a[92:95], v[64:67]\n"
    "s_waitcnt lgkmcnt(7)\n"
    "ds_read_b64_tr_b16 a[30:31], v11 offset:28672\n"
    "v_mfma_f32_16x16x32_bf16 v[36:39], a[8:11], a[80:83], v[36:39]\n"
    "ds_read_b64_tr_b16 a[0:1], v12 offset:16384\n"
    "v_add_u32 v11, s78, v11\n"
    "v_mfma_f32_16x16x32_bf16 v[36:39], a[12:15], a[84:87], v[36:39]\n"
    "ds_read_b64_tr_b16 a[2:3], v12 offset:20480\n"
    "buffer_load_dwordx4 v[104:107], v2, s[48:51], s86 offen sc1\n"
    "v_mfma_f32_16x16x32_bf16 v[68:71], a[8:11], a[88:91], v[68:71]\n"
    "ds_read_b64_tr_b16 a[4:5], v12 offset:24576\n"
    "v_mfma_f32_16x16x32_bf16 v[68:71], a[12:15], a[92:95], v[68:71]\n"
    "s_waitcnt lgkmcnt(7)\n"
    "ds_read_b64_tr_b16 a[6:7], v12 offset:28672\n"
    "v_mfma_f32_16x16x32_bf16 v[40:43], a[16:19], a[80:83], v[40:43]\n"
    "ds_read_b64_tr_b16 a[8:9], v13 offset:16384\n"
    "v_add_u32 v12, s78, v12\n"
    "v_mfma_f32_16x16x32_bf16 v[40:43], a[20:23], a[84:87], v[40:43]\n"
    "ds_read_b64_tr_b16 a[10:11], v13 offset:20480\n"
    "buffer_load_dwordx4 v[108:111], v2, s[48:51], s86 offen offset:1024 sc1\n"
    "v_mfma_f32_16x16x32_bf16 v[72:75], a[16:19], a[88:91], v[72:75]\n"
    "ds_read_b64_tr_b16 a[12:13], v13 offset:24576\n"
    "v_mfma_f32_16x16x32_bf16 v[72:75], a[20:23], a[92:95], v[72:75]\n"
    "s_waitcnt lgkmcnt(7)\n"
    "ds_read_b64_tr_b16 a[14:15], v13 offset:28672\n"
    "v_mfma_f32_16x16x32_bf16 v[44:47], a[24:27], a[80:83], v[44:47]\n"
    "ds_read_b64_tr_b16 a[16:17], v14 offset:16384\n"
    "v_add_u32 v13, s78, v13\n"
    "v_mfma_f32_16x16x32_bf16 v[44:47], a[28:31], a[84:87], v[44:47]\n"
    "ds_read_b64_tr_b16 a[18:19], v14 offset:20480\n"
    "buffer_load_dwordx4 v[112:115], v2, s[48:51], s85 offen offset:2048 sc1\n"
    "v_mfma_f32_16x16x32_bf16 v[76:79], a[24:27], a[88:91], v[76:79]\n"
    "ds_read_b64_tr_b16 a[20:21], v14 offset:24576\n"
    "v_mfma_f32_16x16x32_bf16 v[76:79], a[28:31], a[92:95], v[76:79]\n"
    "s_waitcnt lgkmcnt(7)\n"
    "ds_read_b64_tr_b16 a[22:23], v14 offset:28672\n"
    "v_mfma_f32_16x16x32_bf16 v[48:51], a[0:3], a[80:83], v[48:51]\n"
    "ds_read_b64_tr_b16 a[24:25], v15 offset:16384\n"
    "v_add_u32 v14, s78, v14\n"
    "v_mfma_f32_16x16x32_bf16 v[48:51], a[4:7], a[84:87], v[48:51]\n"
    "ds_read_b64_tr_b16 a[26:27], v15 offset:20480\n"
    "buffer_load_dwordx4 v[116:119], v2, s[48:51], s85 offen offset:3072 sc1\n"
    "v_mfma_f32_16x16x32_bf16 v[80:83], a[0:3], a[88:91], v[80:83]\n"
    "ds_read_b64_tr_b16 a[28:29], v15 offset:24576\n"
    "v_mfma_f32_16x16x32_bf16 v[80:83], a[4:7], a[92:95], v[80:83]\n"
    "s_waitcnt vmcnt(11) lgkmcnt(7)\n"
    "s_barrier\n"
    "ds_read_b64_tr_b16 a[30:31], v15 offset:28672\n"
    "v_add_u32 v15, s78, v15\n"
    "v_mfma_f32_16x16x32_bf16 v[52:55], a[8:11], a[80:83], v[52:55]\n"
    "ds_read_b64_tr_b16 a[0:1], v8\n"
    "v_mfma_f32_16x16x32_bf16 v[52:55], a[12:15], a[84:87], v[52:55]\n"
    "ds_read_b64_tr_b16 a[2:3], v8 offset:4096\n"
    "buffer_load_dwordx4 v[120:123], v2, s[48:51], s86 offen offset:2048 sc1\n"
    "v_mfma_f32_16x16x32_bf16 v[84:87], a[8:11], a[88:91], v[84:87]\n"
    "ds_read_b64_tr_b16 a[4:5], v8 offset:8192\n"
    "v_mfma_f32_16x16x32_bf16 v[84:87], a[12:15], a[92:95], v[84:87]\n"
    "s_waitcnt lgkmcnt(7)\n"
    "ds_read_b64_tr_b16 a[6:7], v8 offset:12288\n"
    "v_mfma_f32_16x16x32_bf16 v[56:59], a[16:19], a[80:83], v[56:59]\n"
    "ds_read_b64_tr_b16 a[8:9], v9\n"
    "v_mfma_f32_16x16x32_bf16 v[56:59], a[20:23], a[84:87], v[56:59]\n"
    "ds_read_b64_tr_b16 a[10:11], v9 offset:4096\n"
    "buffer_load_dwordx4 v[124:127], v2, s[48:51], s86 offen offset:3072 sc1\n"
    "v_mfma_f32_16x16x32_bf16 v[88:91], a[16:19], a[88:91], v[88:91]\n"
    "ds_read_b64_tr_b16 a[12:13], v9 offset:8192\n"
    "v_mfma_f32_16x16x32_bf16 v[88:91], a[20:23], a[92:95], v[88:91]\n"
    "s_waitcnt lgkmcnt(7)\n"
    "ds_read_b64_tr_b16 a[14:15], v9 offset:12288\n"
    "v_mfma_f32_16x16x32_bf16 v[60:63], a[24:27], a[80:83], v[60:63]\n"
    "ds_read_b64_tr_b16 a[16:17], v10\n"
    "v_mfma_f32_16x16x32_bf16 v[60:63], a[28:31], a[84:87], v[60:63]\n"
    "ds_read_b64_tr_b16 a[18:19], v10 offset:4096\n"
    "v_mfma_f32_16x16x32_bf16 v[92:95], a[24:27], a[88:91], v[92:95]\n"
    "ds_read_b64_tr_b16 a[20:21], v10 offset:8192\n"
    "v_mfma_f32_16x16x32_bf16 v[92:95], a[28:31], a[92:95], v[92:95]\n"
    "ds_read_b64_tr_b16 a[22:23], v10 offset:12288\n"
    "s_mov_b32 s91, 1048577\n"
    "s_waitcnt vmcnt(8)\n"
    "v_cmp_ne_u64 vcc, s[94:95], v[18:19]\n"
    "s_sub_u32 s91, s91, 1\n"
    "s_cmp_eq_u64 vcc, 0\n"
    "s_cbranch_scc1 k_ffn2_go\n"
    "k_ffn2_spin:\n"
    "buffer_load_dwordx2 v[18:19], off, s[64:67], s90 sc1\n"
    "s_waitcnt vmcnt(0)\n"
    "v_cmp_ne_u64 vcc, s[94:95], v[18:19]\n"
    "s_sub_u32 s91, s91, 1\n"
    "s_cmp_eq_u64 vcc, 0\n"
    "s_cbranch_scc1 k_ffn2_go\n"
    "s_sleep 2\n"
    "s_cmp_lg_u32 s91, 0\n"
    "s_cbranch_scc1 k_ffn2_spin\n"
    "k_ffn2_go:\n"
    "s_waitcnt lgkmcnt(8)\n"
    "v_mfma_f32_16x16x32_bf16 v[32:35], a[0:3], a[32:35], v[32:35]\n"
    "ds_read_b64_tr_b16 a[24:25], v11\n"
    "v_mfma_f32_16x16x32_bf16 v[32:35], a[4:7], a[36:39], v[32:35]\n"
    "ds_read_b64_tr_b16 a[26:27], v11 offset:4096\n"
    "buffer_load_dwordx4 v[128:131], v3, s[60:63], s89 offen sc1\n"
    "v_mfma_f32_16x16x32_bf16 v[64:67], a[0:3], a[40:43], v[64:67]\n"
    "ds_read_b64_tr_b16 a[28:29], v11 offset:8192\n"
    "v_mfma_f32_16x16x32_bf16 v[64:67], a[4:7], a[44:47], v[64:67]\n"
    "s_waitcnt lgkmcnt(7)\n"
    "ds_read_b64_tr_b16 a[30:31], v11 offset:12288\n"
    "v_mfma_f32_16x16x32_bf16 v[36:39], a[8:11], a[32:35], v[36:39]\n"
    "ds_read_b64_tr_b16 a[0:1], v12\n"
    "buffer_load_dwordx4 v[132:135], v3, s[60:63], s89 offen offset:1024 sc1\n"
    "v_mfma_f32_16x16x32_bf16 v[36:39], a[12:15], a[36:39], v[36:39]\n"
    "ds_read_b64_tr_b16 a[2:3], v12 offset:4096\n"
    "buffer_load_dwordx4 v[136:139], v3, s[60:63], s89 offen offset:2048 sc1\n"
    "v_mfma_f32_16x16x32_bf16 v[68:71], a[8:11], a[40:43], v[68:71]\n"
    "ds_read_b64_tr_b16 a[4:5], v12 offset:8192\n"
    "v_mfma_f32_16x16x32_bf16 v[68:71], a[12:15], a[44:47], v[68:71]\n"
    "s_waitcnt lgkmcnt(7)\n"
    "ds_read_b64_tr_b16 a[6:7], v12 offset:12288\n"
    "v_mfma_f32_16x16x32_bf16 v[40:43], a[16:19], a[32:35], v[40:43]\n"
    "ds_read_b64_tr_b16 a[8:9], v13\n"
    "buffer_load_dwordx4 v[140:143], v3, s[60:63], s89 offen offset:3072 sc1\n"
    "v_mfma_f32_16x16x32_bf16 v[40:43], a[20:23], a[36:39], v[40:43]\n"
    "ds_read_b64_tr_b16 a[10:11], v13 offset:4096\n"
    "v_mfma_f32_16x16x32_bf16 v[72:75], a[16:19], a[40:43], v[72:75]\n"
    "s_add_u32 s89, s89, 0x1000\n"
    "ds_read_b64_tr_b16 a[12:13], v13 offset:8192\n"
    "v_mfma_f32_16x16x32_bf16 v[72:75], a[20:23], a[44:47], v[72:75]\n"
    "s_waitcnt lgkmcnt(7)\n"
    "ds_read_b64_tr_b16 a[14:15], v13 offset:12288\n"
    "v_mfma_f32_16x16x32_bf16 v[44:47], a[24:27], a[32:35], v[44:47]\n"
    "ds_read_b64_tr_b16 a[16:17], v14\n"
    "buffer_load_dwordx4 v[144:147], v3, s[60:63], s89 offen sc1\n"
    "v_mfma_f32_16x16x32_bf16 v[44:47], a[28:31], a[36:39], v[44:47]\n"
    "ds_read_b64_tr_b16 a[18:19], v14 offset:4096\n"
    "v_mfma_f32_16x16x32_bf16 v[76:79], a[24:27], a[40:43], v[76:79]\n"
    "ds_read_b64_tr_b16 a[20:21], v14 offset:8192\n"
    "buffer_load_dwordx4 v[148:151], v3, s[60:63], s89 offen offset:1024 sc1\n"
    "v_mfma_f32_16x16x32_bf16 v[76:79], a[28:31], a[44:47], v[76:79]\n"
    "s_waitcnt lgkmcnt(7)\n"
    "ds_read_b64_tr_b16 a[22:23], v14 offset:12288\n"
    "v_mfma_f32_16x16x32_bf16 v[48:51], a[0:3], a[32:35], v[48:51]\n"
    "ds_read_b64_tr_b16 a[24:25], v15\n"
    "v_mfma_f32_16x16x32_bf16 v[48:51], a[4:7], a[36:39], v[48:51]\n"
    "ds_read_b64_tr_b16 a[26:27], v15 offset:4096\n"
    "buffer_load_dwordx4 v[152:155], v3, s[60:63], s89 offen offset:2048 sc1\n"
    "v_mfma_f32_16x16x32_bf16 v[80:83], a[0:3], a[40:43], v[80:83]\n"
    "ds_read_b64_tr_b16 a[28:29], v15 offset:8192\n"
    "v_mfma_f32_16x16x32_bf16 v[80:83], a[4:7], a[44:47], v[80:83]\n"
    "s_waitcnt lgkmcnt(7)\n"
    "ds_read_b64_tr_b16 a[30:31], v15 offset:12288\n"
    "v_mfma_f32_16x16x32_bf16 v[52:55], a[8:11], a[32:35], v[52:55]\n"
    "ds_read_b64_tr_b16 a[0:1], v8 offset:16384\n"
    "buffer_load_dwordx4 v[156:159], v3, s[60:63], s89 offen offset:3072 sc1\n"
    "v_mfma_f32_16x16x32_bf16 v[52:55], a[12:15], a[36:39], v[52:55]\n"
    "s_add_u32 s89, s89, 0x1000\n"
    "ds_read_b64_tr_b16 a[2:3], v8 offset:20480\n"
    "v_mfma_f32_16x16x32_bf16 v[84:87], a[8:11], a[40:43], v[84:87]\n"
    "ds_read_b64_tr_b16 a[4:5], v8 offset:24576\n"
    "v_mfma_f32_16x16x32_bf16 v[84:87], a[12:15], a[44:47], v[84:87]\n"
    "s_waitcnt lgkmcnt(7)\n"
    "ds_read_b64_tr_b16 a[6:7], v8 offset:28672\n"
    "v_mfma_f32_16x16x32_bf16 v[56:59], a[16:19], a[32:35], v[56:59]\n"
    "ds_read_b64_tr_b16 a[8:9], v9 offset:16384\n"
    "v_mfma_f32_16x16x32_bf16 v[56:59], a[20:23], a[36:39], v[56:59]\n"
    "ds_read_b64_tr_b16 a[10:11], v9 offset:20480\n"
    "v_mfma_f32_16x16x32_bf16 v[88:91], a[16:19], a[40:43], v[88:91]\n"
    "ds_read_b64_tr_b16 a[12:13], v9 offset:24576\n"
    "v_mfma_f32_16x16x32_bf16 v[88:91], a[20:23], a[44:47], v[88:91]\n"
    "s_waitcnt lgkmcnt(7)\n"
    "ds_read_b64_tr_b16 a[14:15], v9 offset:28672\n"
    "v_mfma_f32_16x16x32_bf16 v[60:63], a[24:27], a[32:35], v[60:63]\n"
    "ds_read_b64_tr_b16 a[16:17], v10 offset:16384\n"
    "v_mfma_f32_16x16x32_bf16 v[60:63], a[28:31], a[36:39], v[60:63]\n"
    "ds_read_b64_tr_b16 a[18:19], v10 offset:20480\n"
    "v_mfma_f32_16x16x32_bf16 v[92:95], a[24:27], a[40:43], v[92:95]\n"
    "s_mov_b32 s52, s36\n"
    "ds_read_b64_tr_b16 a[20:21], v10 offset:24576\n"
    "v_mfma_f32_16x16x32_bf16 v[92:95], a[28:31], a[44:47], v[92:95]\n"
    "s_waitcnt lgkmcnt(7)\n"
    "ds_read_b64_tr_b16 a[22:23], v10 offset:28672\n"
    "v_mfma_f32_16x16x32_bf16 v[32:35], a[0:3], a[48:51], v[32:35]\n"
    "s_mov_b32 s53, s37\n"
    "ds_read_b64_tr_b16 a[24:25], v11 offset:16384\n"
    "v_mfma_f32_16x16x32_bf16 v[32:35], a[4:7], a[52:55], v[32:35]\n"
    "s_mov_b32 s54, 0x400000\n"
    "ds_read_b64_tr_b16 a[26:27], v11 offset:20480\n"
    "v_mfma_f32_16x16x32_bf16 v[64:67], a[0:3], a[56:59], v[64:67]\n"
    "s_mov_b32 s55, 0x20000\n"
    "ds_read_b64_tr_b16 a[28:29], v11 offset:24576\n"
    "v_mfma_f32_16x16x32_bf16 v[64:67], a[4:7], a[60:63], v[64:67]\n"
    "s_waitcnt lgkmcnt(7)\n"
    "ds_read_b64_tr_b16 a[30:31], v11 offset:28672\n"
    "v_mfma_f32_16x16x32_bf16 v[36:39], a[8:11], a[48:51], v[36:39]\n"
    "ds_read_b64_tr_b16 a[0:1], v12 offset:16384\n"
    "v_and_b32 v18, 15, v1\n"
    "v_mfma_f32_16x16x32_bf16 v[36:39], a[12:15], a[52:55], v[36:39]\n"
    "ds_read_b64_tr_b16 a[2:3], v12 offset:20480\n"
    "v_lshrrev_b32 v19, 4, v1\n"
    "v_mfma_f32_16x16x32_bf16 v[68:71], a[8:11], a[56:59], v[68:71]\n"
    "ds_read_b64_tr_b16 a[4:5], v12 offset:24576\n"
    "v_mfma_f32_16x16x32_bf16 v[68:71], a[12:15], a[60:63], v[68:71]\n"
    "s_waitcnt lgkmcnt(7)\n"
    "ds_read_b64_tr_b16 a[6:7], v12 offset:28672\n"
    "v_mfma_f32_16x16x32_bf16 v[40:43], a[16:19], a[48:51], v[40:43]\n"
    "s_mul_i32 s85, s80, 0x2200\n"
    "ds_read_b64_tr_b16 a[8:9], v13 offset:16384\n"
    "v_mfma_f32_16x16x32_bf16 v[40:43], a[20:23], a[52:55], v[40:43]\n"
    "s_add_u32 s85, s85, 0x8000\n"
    "ds_read_b64_tr_b16 a[10:11], v13 offset:20480\n"
    "v_mfma_f32_16x16x32_bf16 v[72:75], a[16:19], a[56:59], v[72:75]\n"
    "ds_read_b64_tr_b16 a[12:13], v13 offset:24576\n"
    "v_mul_u32_u24 v20, 0x110, v18\n"
    "v_mfma_f32_16x16x32_bf16 v[72:75], a[20:23], a[60:63], v[72:75]\n"
    "s_waitcnt lgkmcnt(7)\n"
    "ds_read_b64_tr_b16 a[14:15], v13 offset:28672\n"
    "v_mfma_f32_16x16x32_bf16 v[44:47], a[24:27], a[48:51], v[44:47]\n"
    "ds_read_b64_tr_b16 a[16:17], v14 offset:16384\n"
    "v_lshl_add_u32 v20, v19, 3, v20\n"
    "v_mfma_f32_16x16x32_bf16 v[44:47], a[28:31], a[52:55], v[44:47]\n"
    "ds_read_b64_tr_b16 a[18:19], v14 offset:20480\n"
    "v_add_u32 v192, s85, v20\n"
    "v_mfma_f32_16x16x32_bf16 v[76:79], a[24:27], a[56:59], v[76:79]\n"
    "ds_read_b64_tr_b16 a[20:21], v14 offset:24576\n"
    "v_mul_u32_u24 v20, 0x110, v19\n"
    "v_mfma_f32_16x16x32_bf16 v[76:79], a[28:31], a[60:63], v[76:79]\n"
    "s_waitcnt lgkmcnt(7)\n"
    "ds_read_b64_tr_b16 a[22:23], v14 offset:28672\n"
    "v_mfma_f32_16x16x32_bf16 v[48:51], a[0:3], a[48:51], v[48:51]\n"
    "ds_read_b64_tr_b16 a[24:25], v15 offset:16384\n"
    "v_lshl_add_u32 v20, v18, 4, v20\n"
    "v_mfma_f32_16x16x32_bf16 v[48:51], a[4:7], a[52:55], v[48:51]\n"
    "ds_read_b64_tr_b16 a[26:27], v15 offset:20480\n"
    "v_add_u32 v193, s85, v20\n"
    "v_mfma_f32_16x16x32_bf16 v[80:83], a[0:3], a[56:59], v[80:83]\n"
    "ds_read_b64_tr_b16 a[28:29], v15 offset:24576\n"
    "v_mfma_f32_16x16x32_bf16 v[80:83], a[4:7], a[60:63], v[80:83]\n"
    "s_waitcnt lgkmcnt(7)\n"
    "ds_read_b64_tr_b16 a[30:31], v15 offset:28672\n"
    "v_mfma_f32_16x16x32_bf16 v[52:55], a[8:11], a[48:51], v[52:55]\n"
    "v_lshlrev_b32 v18, 4, v18\n"
    "v_lshl_add_u32 v18, v19, 11, v18\n"
    "v_mfma_f32_16x16x32_bf16 v[52:55], a[12:15], a[52:55], v[52:55]\n"
    "v_mfma_f32_16x16x32_bf16 v[84:87], a[8:11], a[56:59], v[84:87]\n"
    "s_lshl_b32 s86, s84, 15\n"
    "v_mfma_f32_16x16x32_bf16 v[84:87], a[12:15], a[60:63], v[84:87]\n"
    "s_waitcnt lgkmcnt(4)\n"
    "s_lshl_b32 s85, s82, 8\n"
    "v_mfma_f32_16x16x32_bf16 v[56:59], a[16:19], a[48:51], v[56:59]\n"
    "v_mfma_f32_16x16x32_bf16 v[56:59], a[20:23], a[52:55], v[56:59]\n"
    "s_add_u32 s85, s86, s85\n"
    "v_mfma_f32_16x16x32_bf16 v[88:91], a[16:19], a[56:59], v[88:91]\n"
    "v_add_u32 v16, s85, v18\n"
    "v_mfma_f32_16x16x32_bf16 v[88:91], a[20:23], a[60:63], v[88:91]\n"
    "s_waitcnt lgkmcnt(0)\n"
    "v_mfma_f32_16x16x32_bf16 v[60:63], a[24:27], a[48:51], v[60:63]\n"
    "v_mfma_f32_16x16x32_bf16 v[60:63], a[28:31], a[52:55], v[60:63]\n"
    "v_mfma_f32_16x16x32_bf16 v[92:95], a[24:27], a[56:59], v[92:95]\n"
    "v_mfma_f32_16x16x32_bf16 v[92:95], a[28:31], a[60:63], v[92:95]\n"
    "s_waitcnt vmcnt(8)\n"
    "v_mfma_f32_16x16x32_bf16 v[32:35], v[226:229], v[96:99], v[32:35]\n"
    "v_mfma_f32_16x16x32_bf16 v[36:39], v[224:227], v[96:99], v[36:39]\n"
    "v_mfma_f32_16x16x32_bf16 v[40:43], v[226:229], v[100:103], v[40:43]\n"
    "v_mfma_f32_16x16x32_bf16 v[44:47], v[224:227], v[100:103], v[44:47]\n"
    "v_mfma_f32_16x16x32_bf16 v[48:51], v[226:229], v[104:107], v[48:51]\n"
    "v_mfma_f32_16x16x32_bf16 v[52:55], v[224:227], v[104:107], v[52:55]\n"
    "v_mfma_f32_16x16x32_bf16 v[56:59], v[226:229], v[108:111], v[56:59]\n"
    "v_mfma_f32_16x16x32_bf16 v[60:63], v[224:227], v[108:111], v[60:63]\n"
    "v_mfma_f32_16x16x32_bf16 v[64:67], v[226:229], v[112:115], v[64:67]\n"
    "v_mfma_f32_16x16x32_bf16 v[68:71], v[224:227], v[112:115], v[68:71]\n"
    "v_mfma_f32_16x16x32_bf16 v[72:75], v[226:229], v[116:119], v[72:75]\n"
    "v_mfma_f32_16x16x32_bf16 v[76:79], v[224:227], v[116:119], v[76:79]\n"
    "v_mfma_f32_16x16x32_bf16 v[80:83], v[226:229], v[120:123], v[80:83]\n"
    "v_mfma_f32_16x16x32_bf16 v[84:87], v[224:227], v[120:123], v[84:87]\n"
    "v_mfma_f32_16x16x32_bf16 v[88:91], v[226:229], v[124:127], v[88:91]\n"
    "v_mfma_f32_16x16x32_bf16 v[92:95], v[224:227], v[124:127], v[92:95]\n"
    "s_waitcnt vmcnt(7)\n"
    "v_mfma_f32_16x16x32_bf16 v[32:35], v[226:229], v[128:131], v[32:35]\n"
    "v_mfma_f32_16x16x32_bf16 v[36:39], v[224:227], v[128:131], v[36:39]\n"
    "s_waitcnt vmcnt(6)\n"
    "v_mfma_f32_16x16x32_bf16 v[40:43], v[226:229], v[132:135], v[40:43]\n"
    "v_mfma_f32_16x16x32_bf16 v[44:47], v[224:227], v[132:135], v[44:47]\n"
    "s_waitcnt vmcnt(5)\n"
    "v_mfma_f32_16x16x32_bf16 v[48:51], v[226:229], v[136:139], v[48:51]\n"
    "v_mfma_f32_16x16x32_bf16 v[52:55], v[224:227], v[136:139], v[52:55]\n"
    "s_waitcnt vmcnt(4)\n"
    "v_mfma_f32_16x16x32_bf16 v[56:59], v[226:229], v[140:143], v[56:59]\n"
    "v_cvt_pk_bf16_f32 v96, v32, v33\n"
    "v_cvt_pk_bf16_f32 v97, v34, v35\n"
    "v_mfma_f32_16x16x32_bf16 v[60:63], v[224:227], v[140:143], v[60:63]\n"
    "s_waitcnt vmcnt(3)\n"
    "v_cvt_pk_bf16_f32 v98, v36, v37\n"
    "v_cvt_pk_bf16_f32 v99, v38, v39\n"
    "v_mfma_f32_16x16x32_bf16 v[64:67], v[226:229], v[144:147], v[64:67]\n"
    "ds_write_b64 v192, v[96:97] offset:0\n"
    "v_mfma_f32_16x16x32_bf16 v[68:71], v[224:227], v[144:147], v[68:71]\n"
    "s_waitcnt vmcnt(2)\n"
    "ds_write_b64 v192, v[98:99] offset:32\n"
    "v_mfma_f32_16x16x32_bf16 v[72:75], v[226:229], v[148:151], v[72:75]\n"
    "v_cvt_pk_bf16_f32 v100, v40, v41\n"
    "v_cvt_pk_bf16_f32 v101, v42, v43\n"
    "v_mfma_f32_16x16x32_bf16 v[76:79], v[224:227], v[148:151], v[76:79]\n"
    "s_waitcnt vmcnt(1)\n"
    "v_cvt_pk_bf16_f32 v102, v44, v45\n"
    "v_cvt_pk_bf16_f32 v103, v46, v47\n"
    "v_mfma_f32_16x16x32_bf16 v[80:83], v[226:229], v[152:155], v[80:83]\n"
    "ds_write_b64 v192, v[100:101] offset:64\n"
    "v_mfma_f32_16x16x32_bf16 v[84:87], v[224:227], v[152:155], v[84:87]\n"
    "s_waitcnt vmcnt(0)\n"
    "ds_write_b64 v192, v[102:103] offset:96\n"
    "v_mfma_f32_16x16x32_bf16 v[88:91], v[226:229], v[156:159], v[88:91]\n"
    "v_cvt_pk_bf16_f32 v104, v48, v49\n"
    "v_cvt_pk_bf16_f32 v105, v50, v51\n"
    "v_mfma_f32_16x16x32_bf16 v[92:95], v[224:227], v[156:159], v[92:95]\n"
    "v_cvt_pk_bf16_f32 v106, v52, v53\n"
    "v_cvt_pk_bf16_f32 v107, v54, v55\n"
    "s_nop 2\n"
    "ds_write_b64 v192, v[104:105] offset:128\n"
    "ds_write_b64 v192, v[106:107] offset:160\n"
    "v_cvt_pk_bf16_f32 v108, v56, v57\n"
    "v_cvt_pk_bf16_f32 v109, v58, v59\n"
    "v_cvt_pk_bf16_f32 v110, v60, v61\n"
    "v_cvt_pk_bf16_f32 v111, v62, v63\n"
    "ds_write_b64 v192, v[108:109] offset:192\n"
    "ds_write_b64 v192, v[110:111] offset:224\n"
    "v_cvt_pk_bf16_f32 v112, v64, v65\n"
    "v_cvt_pk_bf16_f32 v113, v66, v67\n"
    "v_cvt_pk_bf16_f32 v114, v68, v69\n"
    "v_cvt_pk_bf16_f32 v115, v70, v71\n"
    "ds_write_b64 v192, v[112:113] offset:4352\n"
    "ds_write_b64 v192, v[114:115] offset:4384\n"
    "s_waitcnt lgkmcnt(2)\n"
    "ds_read_b128 v[160:163], v193\n"
    "ds_read_b128 v[164:167], v193 offset:1088\n"
    "ds_read_b128 v[168:171], v193 offset:2176\n"
    "ds_read_b128 v[172:175], v193 offset:3264\n"
    "v_cvt_pk_bf16_f32 v116, v72, v73\n"
    "v_cvt_pk_bf16_f32 v117, v74, v75\n"
    "v_cvt_pk_bf16_f32 v118, v76, v77\n"
    "v_cvt_pk_bf16_f32 v119, v78, v79\n"
    "ds_write_b64 v192, v[116:117] offset:4416\n"
    "ds_write_b64 v192, v[118:119] offset:4448\n"
    "v_cvt_pk_bf16_f32 v120, v80, v81\n"
    "v_cvt_pk_bf16_f32 v121, v82, v83\n"
    "v_cvt_pk_bf16_f32 v122, v84, v85\n"
    "v_cvt_pk_bf16_f32 v123, v86, v87\n"
    "ds_write_b64 v192, v[120:121] offset:4480\n"
    "ds_write_b64 v192, v[122:123] offset:4512\n"
    "v_cvt_pk_bf16_f32 v124, v88, v89\n"
    "v_cvt_pk_bf16_f32 v125, v90, v91\n"
    "v_cvt_pk_bf16_f32 v126, v92, v93\n"
    "v_cvt_pk_bf16_f32 v127, v94, v95\n"
    "ds_write_b64 v192, v[124:125] offset:4544\n"
    "ds_write_b64 v192, v[126:127] offset:4576\n"
    "s_mov_b32 s86, 0\n"
    "s_waitcnt lgkmcnt(9)\n"
    "buffer_store_dwordx4 v[160:163], v16, s[52:55], s86 offen sc1\n"
    "s_mov_b32 s86, 0x2000\n"
    "s_waitcnt lgkmcnt(8)\n"
    "buffer_store_dwordx4 v[164:167], v16, s[52:55], s86 offen sc1\n"
    "s_mov_b32 s86, 0x4000\n"
    "s_waitcnt lgkmcnt(7)\n"
    "buffer_store_dwordx4 v[168:171], v16, s[52:55], s86 offen sc1\n"
    "s_mov_b32 s86, 0x6000\n"
    "s_waitcnt lgkmcnt(6)\n"
    "buffer_store_dwordx4 v[172:175], v16, s[52:55], s86 offen sc1\n"
    "s_waitcnt lgkmcnt(0)\n"
    "ds_read_b128 v[176:179], v193 offset:4352\n"
    "ds_read_b128 v[180:183], v193 offset:5440\n"
    "ds_read_b128 v[184:187], v193 offset:6528\n"
    "ds_read_b128 v[188:191], v193 offset:7616\n"
    "s_mov_b32 s86, 0x8000\n"
    "s_waitcnt lgkmcnt(3)\n"
    "buffer_store_dwordx4 v[176:179], v16, s[52:55], s86 offen sc1\n"
    "s_mov_b32 s86, 0xa000\n"
    "s_waitcnt lgkmcnt(2)\n"
    "buffer_store_dwordx4 v[180:183], v16, s[52:55], s86 offen sc1\n"
    "s_mov_b32 s86, 0xc000\n"
    "s_waitcnt lgkmcnt(1)\n"
    "buffer_store_dwordx4 v[184:187], v16, s[52:55], s86 offen sc1\n"
    "s_mov_b32 s86, 0xe000\n"
    "s_waitcnt lgkmcnt(0)\n"
    "buffer_store_dwordx4 v[188:191], v16, s[52:55], s86 offen sc1\n"
    "k_ffn2_code_end:\n"
    : : "s"((unsigned long long)__builtin_amdgcn_kernarg_segment_ptr()), "s"(blockIdx.x), "v"(threadIdx.x), "s"((unsigned)(size_t)lds)
    : "s8", "s9", "s10", "s11", "s12", "s13", "s14", "s15", "s16", "s17", "s18", "s19", "s20", "s21", "s22", "s23", "s24", "s25", "s26", "s27", "v1", "v2", "v3", "v4", "v5", "v6", "v7", "v8", "v9", "v10", "v11", "v12", "v13", "v14", "v15", "v16", "v17", "v18", "v19", "v20", "v21", "v22", "v23", "v24", "v25", "v26", "v27", "v28", "v29", "v30", "v31", "v32", "v33", "v34", "v35", "v36", "v37", "v38", "v39", "v40", "v41", "v42", "v43", "v44", "v45", "v46", "v47", "v48", "v49", "v50", "v51", "v52", "v53", "v54", "v55", "v56", "v57", "v58", "v59", "v60", "v61", "v62", "v63", "v64", "v65", "v66", "v67", "v68", "v69", "v70", "v71", "v72", "v73", "v74", "v75", "v76", "v77", "v78", "v79", "v80", "v81", "v82", "v83", "v84", "v85", "v86", "v87", "v88", "v89", "v90", "v91", "v92", "v93", "v94", "v95", "v96", "v97", "v98", "v99", "v100", "v101", "v102", "v103", "v104", "v105", "v106", "v107", "v108", "v109", "v110", "v111", "v112", "v113", "v114", "v115", "v116", "v117", "v118", "v119", "v120", "v121", "v122", "v123", "v124", "v125", "v126", "v127", "v128", "v129", "v130", "v131", "v132", "v133", "v134", "v135", "v136", "v137", "v138", "v139", "v140", "v141", "v142", "v143", "v144", "v145", "v146", "v147", "v148", "v149", "v150", "v151", "v152", "v153", "v154", "v155", "v156", "v157", "v158", "v159", "v160", "v161", "v162", "v163", "v164", "v165", "v166", "v167", "v168", "v169", "v170", "v171", "v172", "v173", "v174", "v175", "v176", "v177", "v178", "v179", "v180", "v181", "v182", "v183", "v184", "v185", "v186", "v187", "v188", "v189", "v190", "v191", "v192", "v193", "v194", "v195", "v196", "v197", "v198", "v199", "v200", "v201", "v202", "v203", "v204", "v205", "v206", "v207", "v208", "v209", "v210", "v211", "v212", "v213", "v214", "v215", "v216", "v217", "v218", "v219", "v220", "v221", "v222", "v223", "v224", "v225", "v226", "v227", "v228", "v229", "v230", "v231", "v232", "v233", "v234", "v235", "v236", "v237", "v238", "v239", "v240", "v241", "v242", "v243", "v244", "v245", "v246", "v247", "v248", "v249", "v250", "v251", "v252", "v253", "v254", "v255", "a0", "a1", "a2", "a3", "a4", "a5", "a6", "a7", "a8", "a9", "a10", "a11", "a12", "a13", "a14", "a15", "a16", "a17", "a18", "a19", "a20", "a21", "a22", "a23", "a24", "a25", "a26", "a27", "a28", "a29", "a30", "a31", "a32", "a33", "a34", "a35", "a36", "a37", "a38", "a39", "a40", "a41", "a42", "a43", "a44", "a45", "a46", "a47", "a48", "a49", "a50", "a51", "a52", "a53", "a54", "a55", "a56", "a57", "a58", "a59", "a60", "a61", "a62", "a63", "a64", "a65", "a66", "a67", "a68", "a69", "a70", "a71", "a72", "a73", "a74", "a75", "a76", "a77", "a78", "a79", "a80", "a81", "a82", "a83", "a84", "a85", "a86", "a87", "a88", "a89", "a90", "a91", "a92", "a93", "a94", "a95", "a96", "a97", "a98", "a99", "a100", "a101", "a102", "a103", "a104", "a105", "a106", "a107", "a108", "a109", "a110", "a111", "a112", "a113", "a114", "a115", "a116", "a117", "a118", "a119", "a120", "a121", "a122", "a123", "a124", "a125", "a126", "a127", "a128", "a129", "a130", "a131", "a132", "a133", "a134", "a135", "a136", "a137", "a138", "a139", "a140", "a141", "a142", "a143", "a144", "a145", "a146", "a147", "a148", "a149", "a150", "a151", "a152", "a153", "a154", "a155", "a156", "a157", "a158", "a159", "a160", "a161", "a162", "a163", "a164", "a165", "a166", "a167", "a168", "a169", "a170", "a171", "a172", "a173", "a174", "a175", "a176", "a177", "a178", "a179", "a180", "a181", "a182", "a183", "a184", "a185", "a186", "a187", "a188", "a189", "a190", "a191", "a192", "a193", "a194", "a195", "a196", "a197", "a198", "a199", "a200", "a201", "a202", "a203", "a204", "a205", "a206", "a207", "a208", "a209", "a210", "a211", "a212", "a213", "a214", "a215", "a216", "a217", "a218", "a219", "a220", "a221", "a222", "a223", "a224", "a225", "a226", "a227", "a228", "a229", "a230", "a231", "a232", "a233", "a234", "a235", "a236", "a237", "a238", "a239", "a240", "a241", "a242", "a243", "a244", "a245", "a246", "a247", "a248", "a249", "a250", "a251", "a252", "a253", "a254", "a255", "s30", "s31", "s32", "s33", "s34", "s35", "s36", "s37", "s38", "s39", "s40", "s41", "s42", "s43", "s44", "s45", "s46", "s47", "s48", "s49", "s50", "s51", "s52", "s53", "s54", "s55", "s56", "s57", "s58", "s59", "s60", "s61", "s62", "s63", "s64", "s65", "s66", "s67", "s68", "s69", "s70", "s71", "s72", "s73", "s74", "s75", "s76", "s77", "s78", "s79", "s80", "s81", "s82", "s83", "s84", "s85", "s86", "s87", "s88", "s89", "s90", "s91", "s92", "s93", "s94", "s95", "s96", "s97", "s98", "s99", "s100", "s101", "vcc", "scc", "memory");
}

static void launch_stages(int mask, void* const* d_in, void* d_out, void* d_ws, hipStream_t stream) {
  if (mask & 2) k_attn<<<256, 256, 0, stream>>>(d_ws, d_in[2], d_in[3], d_in[4], d_in[5], d_in[7], d_in[8], d_in[0], d_out, d_in[1]);
}
extern "C" void kernel_launch(void* const* d_in, const int* in_sizes, int n_in, void* d_out, int out_size, void* d_ws, size_t ws_size, hipStream_t stream) {
  launch_stages(31, d_in, d_out, d_ws, stream);
}
```
